```python
import torch

def _softmax(x, axis=-1):
    return torch.softmax(x, dim=axis)
def _gelu(x):
    return torch.nn.functional.gelu(x, approximate="tanh")
def _silu(x):
    return torch.nn.functional.silu(x)
def _sigmoid(x):
    return torch.sigmoid(x)
def _layernorm(x, axis=-1, eps=1e-5):
    m = x.mean(dim=axis, keepdim=True)
    v = x.var(dim=axis, keepdim=True, unbiased=False)
    return (x - m) / torch.sqrt(v + eps)
def _scatter_add(tgt, idx, src, axis=0):
    return tgt.clone().index_add_(axis, idx, src)
def _segment_sum(src, seg, n):
    out = torch.zeros((n,) + src.shape[1:], dtype=src.dtype)
    return out.index_add_(0, seg, src)
def _pad(x, pw):
    flat = tuple(v for pair in reversed(pw) for v in pair)
    return torch.nn.functional.pad(x, flat)

kernel_name = 'parallel_sum__grouped_kv_mqa__moe_arch_mod__prompt'

def setup_inputs():

    _w = (lambda a: (lambda t: t.to(torch.bfloat16) if t.is_floating_point() else t)(torch.as_tensor(a)))
    def _A_setup():
        B, S, D, H = 1, 2048, 512, 32
        Dh = D // H
        return {
            "x": _w((lambda *s: torch.randn(*s, dtype=torch.bfloat16) * 0.05)(B, S, D)),
            "wq": _w((lambda *s: torch.randn(*s, dtype=torch.bfloat16) * 0.2)(D, H * Dh)),
            "wk": _w((lambda *s: torch.randn(*s, dtype=torch.bfloat16) * 0.2)(D, Dh)),
            "wv": _w((lambda *s: torch.randn(*s, dtype=torch.bfloat16) * 0.1)(D, Dh)),
            "wo": _w((lambda *s: torch.randn(*s, dtype=torch.bfloat16) * 0.1)(H * Dh, D)),
        }
    _A_in = _A_setup()

    def _B_setup():
        B, S, D, F, C = 1, 2048, 512, 1376, 2
        return {
            "x": _w((lambda *s: torch.randn(*s, dtype=torch.bfloat16) * 0.05)(B, S, D)),
            "w_r": _w((lambda *s: torch.randn(*s, dtype=torch.bfloat16) * 0.2)(D)),
            "w_gate": _w((lambda *s: torch.randn(*s, dtype=torch.bfloat16) * 0.1)(D, F)),
            "w_up": _w((lambda *s: torch.randn(*s, dtype=torch.bfloat16) * 0.1)(D, F)),
            "w_down": _w((lambda *s: torch.randn(*s, dtype=torch.bfloat16) * 0.1)(F, D)),
        }
    _B_in = _B_setup()

    return {
        "A_x": _A_in["x"],
        "A_wq": _A_in["wq"],
        "A_wk": _A_in["wk"],
        "A_wv": _A_in["wv"],
        "A_wo": _A_in["wo"],
        "B_x": _B_in["x"],
        "B_w_r": _B_in["w_r"],
        "B_w_gate": _B_in["w_gate"],
        "B_w_up": _B_in["w_up"],
        "B_w_down": _B_in["w_down"],
    }

def reference(A_x, A_wq, A_wk, A_wv, A_wo, B_x, B_w_r, B_w_gate, B_w_up, B_w_down):
    def _A_ref(x, wq, wk, wv, wo):
        B, S, D = x.shape
        H = 32
        Dh = D // H
        q = (lambda x, *d: x.permute(*d))((x @ wq).reshape(B, S, H, Dh), 0, 2, 1, 3)
        k = x @ wk
        v = x @ wv
        scores = torch.einsum("bhqd,bkd->bhqk", q, k) * (Dh ** -0.5)
        p = _softmax(scores, axis=-1)
        o = torch.einsum("bhqk,bkd->bhqd", p, v)
        return (lambda x, *d: x.permute(*d))(o, 0, 2, 1, 3).reshape(B, S, H * Dh) @ wo

    def _B_ref(x, w_r, w_gate, w_up, w_down):
        B, S, D = x.shape
        C = 2
        r = x @ w_r
        idx = torch.argsort(-r, axis=-1)[:, :C]
        idx_e = (lambda x, n, axis: torch.repeat_interleave(x, n, dim=axis))(idx.reshape(B, C, 1), D, axis=2)
        x_sel = (lambda x, i, axis: torch.take_along_dim(x, i, dim=axis))(x, idx_e, axis=1)
        g = _silu(x_sel @ w_gate)
        u = x_sel @ w_up
        y = (g * u) @ w_down
        r_sel = _sigmoid((lambda x, i, axis: torch.take_along_dim(x, i, dim=axis))(r, idx, axis=1))
        y = y * r_sel.reshape(B, C, 1)
        b_off = (torch.arange(B) * S).reshape(B, 1)
        flat_idx = (idx + b_off).reshape(B * C)
        zero = (x * 0.0).reshape(B * S, D)
        upd = _scatter_add(zero, flat_idx, y.reshape(B * C, D), axis=0)
        return x + upd.reshape(B, S, D)

    x = A_x
    xn = _layernorm(x, axis=-1)
    return x + _A_ref(xn, A_wq, A_wk, A_wv, A_wo) + _B_ref(xn, B_w_r, B_w_gate, B_w_up, B_w_down)
```

```cpp
#include <hip/hip_runtime.h>
#include <hip/hip_bf16.h>


typedef unsigned short u16;
typedef __attribute__((ext_vector_type(8))) __bf16 bf16x8;
typedef __attribute__((ext_vector_type(2))) __bf16 bf16x2;
typedef __attribute__((ext_vector_type(2))) float f32x2;
typedef __attribute__((ext_vector_type(4))) float f32x4;
typedef __attribute__((ext_vector_type(16))) float f32x16;
typedef __attribute__((ext_vector_type(4))) unsigned u32x4;
typedef __attribute__((ext_vector_type(2))) unsigned u32x2;

#define DI __device__ __forceinline__

static constexpr int S_ = 2048;
static constexpr int D_ = 512;
static constexpr int F_ = 1376;

DI float bf2f(unsigned u) { return __uint_as_float(u << 16); }
DI float bfhi2f(unsigned u) { return __uint_as_float(u & 0xffff0000u); }
DI float rbf(float f) { return (float)(__bf16)f; }
DI unsigned pack2(float lo, float hi) {
  f32x2 p = {lo, hi};
  bf16x2 q = __builtin_convertvector(p, bf16x2);
  return __builtin_bit_cast(unsigned, q);
}
DI f32x4 mfma16(bf16x8 a, bf16x8 b, f32x4 c) { return __builtin_amdgcn_mfma_f32_16x16x32_bf16(a, b, c, 0, 0, 0); }
DI f32x16 mfma32(bf16x8 a, bf16x8 b, f32x16 c) { return __builtin_amdgcn_mfma_f32_32x32x16_bf16(a, b, c, 0, 0, 0); }
DI unsigned perm_lo(unsigned hi_src, unsigned lo_src) { return __builtin_amdgcn_perm(hi_src, lo_src, 0x05040100u); }
DI unsigned perm_hi(unsigned hi_src, unsigned lo_src) { return __builtin_amdgcn_perm(hi_src, lo_src, 0x07060302u); }

DI bf16x8 frag_lo(const unsigned* w) {
  u32x4 r = {perm_lo(w[1], w[0]), perm_lo(w[3], w[2]), perm_lo(w[5], w[4]), perm_lo(w[7], w[6])};
  return __builtin_bit_cast(bf16x8, r);
}
DI bf16x8 frag_hi(const unsigned* w) {
  u32x4 r = {perm_hi(w[1], w[0]), perm_hi(w[3], w[2]), perm_hi(w[5], w[4]), perm_hi(w[7], w[6])};
  return __builtin_bit_cast(bf16x8, r);
}
DI bf16x8 ones_frag() {
  u32x4 r = {0x3f803f80u, 0x3f803f80u, 0x3f803f80u, 0x3f803f80u};
  return __builtin_bit_cast(bf16x8, r);
}

static constexpr size_t OFF_QRAW = 0;
static constexpr size_t OFF_OBUF = OFF_QRAW + (size_t)S_ * D_ * 4;
static constexpr size_t OFF_KF = OFF_OBUF + (size_t)S_ * D_ * 2;
static constexpr size_t OFF_VF = OFF_KF + 65536;
static constexpr size_t OFF_R = OFF_VF + 65536;
static constexpr size_t OFF_STATS = OFF_R + S_ * 4;
static constexpr size_t OFF_CS = OFF_STATS + S_ * 8;
static constexpr size_t OFF_H = OFF_CS + 512 * 4;
static constexpr size_t OFF_SEL = OFF_H + 2 * F_ * 4;

__global__ __launch_bounds__(128) void k1_proj(const u16* __restrict__ x, const u16* __restrict__ wq,
                                               const u16* __restrict__ wk, const u16* __restrict__ wv,
                                               const u16* __restrict__ wr, float* __restrict__ qraw,
                                               u16* __restrict__ kf, u16* __restrict__ vf,
                                               float* __restrict__ rbuf, float2* __restrict__ stats,
                                               float* __restrict__ cs) {
  const int lane = threadIdx.x & 63, wave = threadIdx.x >> 6;
  const int g = lane >> 4, i = lane & 15;
  const int cg = blockIdx.x % 9;
  const int rt = (blockIdx.x / 9) * 2 + wave;
  const int row0 = rt * 32;

  bf16x8 xa[2][16];
#pragma unroll
  for (int t = 0; t < 2; ++t)
#pragma unroll
    for (int s = 0; s < 16; ++s)
      xa[t][s] = *(const bf16x8*)(x + (size_t)(row0 + 16 * t + i) * D_ + 32 * s + 8 * g);

  f32x4 acc[2][4];
#pragma unroll
  for (int t = 0; t < 2; ++t)
#pragma unroll
    for (int c = 0; c < 4; ++c) acc[t][c] = (f32x4){0.f, 0.f, 0.f, 0.f};
  f32x4 accs[4];
#pragma unroll
  for (int c = 0; c < 4; ++c) accs[c] = (f32x4){0.f, 0.f, 0.f, 0.f};
  const bf16x8 ones = ones_frag();

  if (cg < 8) {
    const bool docs = (rt == 0);
    const u16* wbase = wq + cg * 64 + 4 * i + (size_t)(8 * g) * D_;
#pragma unroll
    for (int s = 0; s < 16; ++s) {
      unsigned wx[8], wy[8];
#pragma unroll
      for (int j = 0; j < 8; ++j) {
        u32x2 w = *(const u32x2*)(wbase + (size_t)(32 * s + j) * D_);
        wx[j] = w[0];
        wy[j] = w[1];
      }
      bf16x8 b[4] = {frag_lo(wx), frag_hi(wx), frag_lo(wy), frag_hi(wy)};
#pragma unroll
      for (int c = 0; c < 4; ++c) {
        acc[0][c] = mfma16(xa[0][s], b[c], acc[0][c]);
        acc[1][c] = mfma16(xa[1][s], b[c], acc[1][c]);
        if (docs) accs[c] = mfma16(ones, b[c], accs[c]);
      }
    }
#pragma unroll
    for (int t = 0; t < 2; ++t)
#pragma unroll
      for (int r = 0; r < 4; ++r) {
        f32x4 v = {acc[t][0][r], acc[t][1][r], acc[t][2][r], acc[t][3][r]};
        *(f32x4*)(qraw + (size_t)(row0 + 16 * t + 4 * g + r) * D_ + cg * 64 + 4 * i) = v;
      }
    if (docs && g == 0) {
      f32x4 v = {accs[0][0], accs[1][0], accs[2][0], accs[3][0]};
      *(f32x4*)(cs + cg * 64 + 4 * i) = v;
    }
  } else {
    const u16* wbase = ((i < 4) ? (wk + 4 * i) : (i < 8 ? wv + 4 * (i - 4) : wk)) + (8 * g) * 16;
    f32x4 asum[2], agram[2];
#pragma unroll
    for (int t = 0; t < 2; ++t) {
      asum[t] = (f32x4){0.f, 0.f, 0.f, 0.f};
      agram[t] = (f32x4){0.f, 0.f, 0.f, 0.f};
    }
#pragma unroll
    for (int s = 0; s < 16; ++s) {
      unsigned wx[8], wy[8];
#pragma unroll
      for (int j = 0; j < 8; ++j) {
        u32x2 w = *(const u32x2*)(wbase + (32 * s + j) * 16);
        unsigned rv = wr[32 * s + 8 * g + j];
        wx[j] = (i < 8) ? w[0] : (i == 8 ? rv : 0u);
        wy[j] = (i < 8) ? w[1] : 0u;
      }
      bf16x8 b[4] = {frag_lo(wx), frag_hi(wx), frag_lo(wy), frag_hi(wy)};
#pragma unroll
      for (int c = 0; c < 4; ++c) {
        acc[0][c] = mfma16(xa[0][s], b[c], acc[0][c]);
        acc[1][c] = mfma16(xa[1][s], b[c], acc[1][c]);
        accs[c] = mfma16(ones, b[c], accs[c]);
      }
#pragma unroll
      for (int t = 0; t < 2; ++t) {
        asum[t] = mfma16(xa[t][s], ones, asum[t]);
        agram[t] = mfma16(xa[t][s], xa[t][s], agram[t]);
      }
    }
#pragma unroll
    for (int t = 0; t < 2; ++t) {
      float val[4][4];
#pragma unroll
      for (int r = 0; r < 4; ++r) {
        const float ssq = __shfl(agram[t][r], 20 * g + r);
        const float mean = asum[t][r] * (1.0f / D_);
        float var = ssq * (1.0f / D_) - mean * mean;
        var = fmaxf(var, 0.f);
        const float mb = rbf(mean);
        const float vb = rbf(var);
        const float den = rbf(__builtin_sqrtf(rbf(vb + 1e-5f)));
        const float inv = 1.0f / den;
        const int row = row0 + 16 * t + 4 * g + r;
        if (i == 0) stats[row] = make_float2(mb, den);
#pragma unroll
        for (int c = 0; c < 4; ++c) val[r][c] = (acc[t][c][r] - mb * accs[c][r]) * inv;
        if (i == 8) rbuf[row] = rbf(val[r][0]);
      }
      const int kk = 16 * t + 4 * g;
      if (i < 4) {
#pragma unroll
        for (int r = 0; r < 4; ++r) {
          u32x2 o = {pack2(val[r][0], val[r][1]), pack2(val[r][2], val[r][3])};
          const int d = 4 * i;
          *(u32x2*)(kf + (size_t)rt * 512 + (d >> 3) * 256 + (kk + r) * 8 + (d & 7)) = o;
        }
      } else if (i < 8) {
        const int hh = g & 1, jh = g >> 1;
#pragma unroll
        for (int c = 0; c < 4; ++c) {
          const int d = 4 * (i - 4) + c;
          u32x2 o = {pack2(val[0][c], val[1][c]), pack2(val[2][c], val[3][c])};
          *(u32x2*)(vf + (size_t)rt * 512 + t * 256 + (hh * 16 + d) * 8 + jh * 4) = o;
        }
      }
    }
  }
}

DI void top2_wave(const float* __restrict__ rbuf, int lane, int& i0, float& v0, int& i1, float& v1) {
  v0 = -__builtin_inff(); v1 = -__builtin_inff(); i0 = 0x7fffffff; i1 = 0x7fffffff;
#pragma unroll
  for (int c = 0; c < 8; ++c) {
    f32x4 v = *(const f32x4*)(rbuf + c * 256 + lane * 4);
#pragma unroll
    for (int e = 0; e < 4; ++e) {
      const int idx = c * 256 + lane * 4 + e;
      const float val = v[e];
      if (val > v0 || (val == v0 && idx < i0)) { v1 = v0; i1 = i0; v0 = val; i0 = idx; }
      else if (val > v1 || (val == v1 && idx < i1)) { v1 = val; i1 = idx; }
    }
  }
#pragma unroll
  for (int off = 1; off < 64; off <<= 1) {
    const float ov0 = __shfl_xor(v0, off), ov1 = __shfl_xor(v1, off);
    const int oi0 = __shfl_xor(i0, off), oi1 = __shfl_xor(i1, off);
    const bool a_first = (v0 > ov0) || (v0 == ov0 && i0 < oi0);
    float n0, n1; int j0, j1;
    if (a_first) {
      n0 = v0; j0 = i0;
      const bool a2 = (v1 > ov0) || (v1 == ov0 && i1 < oi0);
      n1 = a2 ? v1 : ov0; j1 = a2 ? i1 : oi0;
    } else {
      n0 = ov0; j0 = oi0;
      const bool b2 = (ov1 > v0) || (ov1 == v0 && oi1 < i0);
      n1 = b2 ? ov1 : v0; j1 = b2 ? oi1 : i0;
    }
    v0 = n0; i0 = j0; v1 = n1; i1 = j1;
  }
}

DI float ln_elem(float xv, float mb, float den) { return rbf(rbf(xv - mb) / den); }

static constexpr int LDS_K = 0;
static constexpr int LDS_V = 65536;
static constexpr int LDS_C = 131072;
static constexpr int LDS_TOTAL = 131072 + 32;

__global__ __launch_bounds__(576) void k2_attn(const u16* __restrict__ x, const float* __restrict__ qraw,
                                               const u16* __restrict__ kf, const u16* __restrict__ vf,
                                               const float* __restrict__ rbuf, const float2* __restrict__ stats,
                                               const float* __restrict__ cs, const u16* __restrict__ wg,
                                               const u16* __restrict__ wu, u16* __restrict__ obuf,
                                               float* __restrict__ hbuf, int* __restrict__ sel) {
  __shared__ __attribute__((aligned(16))) char smem[LDS_TOTAL];
  const int tid = threadIdx.x;
  const int lane = tid & 63, wave = tid >> 6;
  const int b = blockIdx.x;

  if (wave < 8) {
    {
      u32x4 t[8];
#pragma unroll
      for (int c = 0; c < 8; ++c) t[c] = *(const u32x4*)((const char*)kf + c * 8192 + tid * 16);
#pragma unroll
      for (int c = 0; c < 8; ++c) *(u32x4*)(smem + LDS_K + c * 8192 + tid * 16) = t[c];
    }
    if (tid < 8) *(unsigned*)(smem + LDS_C + tid * 4) = (tid < 4) ? 0x3f803f80u : 0u;
    u32x4 tv[8];
#pragma unroll
    for (int c = 0; c < 8; ++c) tv[c] = *(const u32x4*)((const char*)vf + c * 8192 + tid * 16);

    const int r = lane & 31, hh = lane >> 5;
    const int qt = b * 8 + wave;
    const int rowblk = qt >> 5, head = qt & 31;
    const int row = rowblk * 32 + r;
    bf16x8 qf;
    {
      const float* qp = qraw + (size_t)row * D_ + head * 16 + 8 * hh;
      f32x4 a0 = *(const f32x4*)qp, a1 = *(const f32x4*)(qp + 4);
      f32x4 c0 = *(const f32x4*)(cs + head * 16 + 8 * hh), c1 = *(const f32x4*)(cs + head * 16 + 8 * hh + 4);
      const float2 st = stats[row];
      const float inv = 1.0f / st.y;
      const float SC = 0.25f * 1.44269504088896340736f;
      float q[8];
#pragma unroll
      for (int e = 0; e < 4; ++e) {
        q[e] = rbf((a0[e] - st.x * c0[e]) * inv) * SC;
        q[4 + e] = rbf((a1[e] - st.x * c1[e]) * inv) * SC;
      }
      u32x4 pk = {pack2(q[0], q[1]), pack2(q[2], q[3]), pack2(q[4], q[5]), pack2(q[6], q[7])};
      qf = __builtin_bit_cast(bf16x8, pk);
    }
    __syncthreads();

    const char* kp = smem + LDS_K + lane * 16;
    float m0 = -__builtin_inff(), m1 = -__builtin_inff();
    f32x16 zero16;
#pragma unroll
    for (int e = 0; e < 16; ++e) zero16[e] = 0.f;
#pragma unroll 2
    for (int t = 0; t < 64; t += 2) {
      bf16x8 ka = *(const bf16x8*)(kp + t * 1024);
      bf16x8 kb = *(const bf16x8*)(kp + t * 1024 + 1024);
      f32x16 sa = mfma32(ka, qf, zero16);
      f32x16 sb = mfma32(kb, qf, zero16);
#pragma unroll
      for (int e = 0; e < 16; e += 2) m0 = fmaxf(fmaxf(m0, sa[e]), sa[e + 1]);
#pragma unroll
      for (int e = 0; e < 16; e += 2) m1 = fmaxf(fmaxf(m1, sb[e]), sb[e + 1]);
    }
    float m = fmaxf(m0, m1);
    m = fmaxf(m, __shfl_xor(m, 32));

#pragma unroll
    for (int c = 0; c < 8; ++c) *(u32x4*)(smem + LDS_V + c * 8192 + tid * 16) = tv[c];
    __syncthreads();

    f32x16 negm;
#pragma unroll
    for (int e = 0; e < 16; ++e) negm[e] = -m;
    f32x16 o;
#pragma unroll
    for (int e = 0; e < 16; ++e) o[e] = 0.f;
    int voff0 = (r < 16) ? (LDS_V + (hh * 16 + r) * 16) : (LDS_C + (r == 16 ? 0 : 16));
    int voff1 = (r < 16) ? (voff0 + 512) : voff0;
    const int vstride = (r < 16) ? 1024 : 0;
#pragma unroll 2
    for (int t = 0; t < 64; ++t) {
      bf16x8 ka = *(const bf16x8*)(kp + t * 1024);
      bf16x8 v0 = *(const bf16x8*)(smem + voff0);
      bf16x8 v1 = *(const bf16x8*)(smem + voff1);
      voff0 += vstride;
      voff1 += vstride;
      f32x16 s = mfma32(ka, qf, negm);
      float p[16];
#pragma unroll
      for (int e = 0; e < 16; ++e) p[e] = __builtin_amdgcn_exp2f(s[e]);
      u32x4 pa = {pack2(p[0], p[1]), pack2(p[2], p[3]), pack2(p[4], p[5]), pack2(p[6], p[7])};
      u32x4 pb = {pack2(p[8], p[9]), pack2(p[10], p[11]), pack2(p[12], p[13]), pack2(p[14], p[15])};
      o = mfma32(v0, __builtin_bit_cast(bf16x8, pa), o);
      o = mfma32(v1, __builtin_bit_cast(bf16x8, pb), o);
    }
    const float l = __shfl(o[8], r);
    const float il = 1.0f / l;
    u16* op = obuf + (size_t)row * D_ + head * 16 + 4 * hh;
    u32x2 w0 = {pack2(o[0] * il, o[1] * il), pack2(o[2] * il, o[3] * il)};
    u32x2 w1 = {pack2(o[4] * il, o[5] * il), pack2(o[6] * il, o[7] * il)};
    *(u32x2*)(op) = w0;
    *(u32x2*)(op + 8) = w1;
  } else {
    __syncthreads();
    __syncthreads();
    if (b < 43) {
      int i0, i1; float v0, v1;
      top2_wave(rbuf, lane, i0, v0, i1, v1);
      i0 = min(i0, S_ - 1);
      i1 = min(i1, S_ - 1);
      if (b == 0 && lane == 0) {
        sel[0] = i0; sel[1] = i1;
        ((float*)sel)[2] = rbf(1.0f / (1.0f + __expf(-v0)));
        ((float*)sel)[3] = rbf(1.0f / (1.0f + __expf(-v1)));
      }
      const int g = lane >> 4, i = lane & 15;
      const int srow = (i == 0) ? i0 : i1;
      const float2 st = stats[srow];
      f32x4 ag[2], au[2];
#pragma unroll
      for (int c = 0; c < 2; ++c) { ag[c] = (f32x4){0.f, 0.f, 0.f, 0.f}; au[c] = (f32x4){0.f, 0.f, 0.f, 0.f}; }
      const int f0 = b * 32;
      const u16* gp = wg + f0 + 2 * i + (size_t)(8 * g) * F_;
      const u16* up = wu + f0 + 2 * i + (size_t)(8 * g) * F_;
#pragma unroll 4
      for (int s = 0; s < 16; ++s) {
        u32x4 xr = *(const u32x4*)(x + (size_t)srow * D_ + 32 * s + 8 * g);
        u32x4 xo;
#pragma unroll
        for (int e = 0; e < 4; ++e) {
          const float lo = ln_elem(bf2f(xr[e] & 0xffffu), st.x, st.y);
          const float hi = ln_elem(bfhi2f(xr[e]), st.x, st.y);
          xo[e] = (i < 2) ? pack2(lo, hi) : 0u;
        }
        const bf16x8 a = __builtin_bit_cast(bf16x8, xo);
        unsigned w1[8], w2[8];
#pragma unroll
        for (int j = 0; j < 8; ++j) {
          w1[j] = *(const unsigned*)(gp + (size_t)(32 * s + j) * F_);
          w2[j] = *(const unsigned*)(up + (size_t)(32 * s + j) * F_);
        }
        ag[0] = mfma16(a, frag_lo(w1), ag[0]);
        ag[1] = mfma16(a, frag_hi(w1), ag[1]);
        au[0] = mfma16(a, frag_lo(w2), au[0]);
        au[1] = mfma16(a, frag_hi(w2), au[1]);
      }
      if (g == 0) {
#pragma unroll
        for (int c = 0; c < 2; ++c)
#pragma unroll
          for (int tk = 0; tk < 2; ++tk) {
            const float gv = rbf(ag[c][tk]);
            const float uv = rbf(au[c][tk]);
            const float sg = rbf(gv / (1.0f + __expf(-gv)));
            hbuf[tk * F_ + f0 + 2 * i + c] = rbf(sg * uv);
          }
      }
    }
  }
}

__global__ __launch_bounds__(128) void k3_out(const u16* __restrict__ x, const u16* __restrict__ obuf,
                                              const u16* __restrict__ wo, const u16* __restrict__ wd,
                                              const float2* __restrict__ stats, const float* __restrict__ hbuf,
                                              const int* __restrict__ sel, u16* __restrict__ out) {
  __shared__ float red[64 * 32];
  const int tid = threadIdx.x;
  const int lane = tid & 63, wave = tid >> 6;
  const int idx0 = sel[0], idx1 = sel[1];
  if (blockIdx.x < 256) {
    const int g = lane >> 4, i = lane & 15;
    const int cg = blockIdx.x & 7;
    const int rt = (blockIdx.x >> 3) * 2 + wave;
    const int row0 = rt * 32;
    bf16x8 xa[2][16];
#pragma unroll
    for (int t = 0; t < 2; ++t)
#pragma unroll
      for (int s = 0; s < 16; ++s)
        xa[t][s] = *(const bf16x8*)(obuf + (size_t)(row0 + 16 * t + i) * D_ + 32 * s + 8 * g);
    f32x4 acc[2][4];
#pragma unroll
    for (int t = 0; t < 2; ++t)
#pragma unroll
      for (int c = 0; c < 4; ++c) acc[t][c] = (f32x4){0.f, 0.f, 0.f, 0.f};
    const u16* wbase = wo + cg * 64 + 4 * i + (size_t)(8 * g) * D_;
#pragma unroll
    for (int s = 0; s < 16; ++s) {
      unsigned wx[8], wy[8];
#pragma unroll
      for (int j = 0; j < 8; ++j) {
        u32x2 w = *(const u32x2*)(wbase + (size_t)(32 * s + j) * D_);
        wx[j] = w[0];
        wy[j] = w[1];
      }
      bf16x8 bb[4] = {frag_lo(wx), frag_hi(wx), frag_lo(wy), frag_hi(wy)};
#pragma unroll
      for (int c = 0; c < 4; ++c) {
        acc[0][c] = mfma16(xa[0][s], bb[c], acc[0][c]);
        acc[1][c] = mfma16(xa[1][s], bb[c], acc[1][c]);
      }
    }
#pragma unroll
    for (int t = 0; t < 2; ++t)
#pragma unroll
      for (int r = 0; r < 4; ++r) {
        const int row = row0 + 16 * t + 4 * g + r;
        const float2 st = stats[row];
        const size_t off = (size_t)row * D_ + cg * 64 + 4 * i;
        const u32x2 xv = *(const u32x2*)(x + off);
        float xf[4] = {bf2f(xv[0] & 0xffffu), bfhi2f(xv[0]), bf2f(xv[1] & 0xffffu), bfhi2f(xv[1])};
        float res[4];
#pragma unroll
        for (int c = 0; c < 4; ++c) {
          const float a = rbf(acc[t][c][r]);
          const float t1 = rbf(xf[c] + a);
          const float xn = ln_elem(xf[c], st.x, st.y);
          res[c] = t1 + xn;
        }
        if (row != idx0 && row != idx1) {
          u32x2 o = {pack2(res[0], res[1]), pack2(res[2], res[3])};
          *(u32x2*)(out + off) = o;
        }
      }
  } else {
    const int col0 = (blockIdx.x - 256) * 16;
    const int kg = tid >> 1, cq = tid & 1;
    const int cbase = col0 + cq * 8;
    float ya[2][8], yd[2][8];
#pragma unroll
    for (int e = 0; e < 8; ++e) { ya[0][e] = ya[1][e] = 0.f; yd[0][e] = yd[1][e] = 0.f; }
#pragma unroll
    for (int it = 0; it < 8; ++it) {
      const int k = kg + 64 * it;
      const u32x4 w = *(const u32x4*)(wo + (size_t)k * D_ + cbase);
      const float o0 = bf2f(obuf[(size_t)idx0 * D_ + k]);
      const float o1 = bf2f(obuf[(size_t)idx1 * D_ + k]);
#pragma unroll
      for (int e = 0; e < 4; ++e) {
        const float wl = bf2f(w[e] & 0xffffu), wh = bfhi2f(w[e]);
        ya[0][2 * e] += o0 * wl; ya[0][2 * e + 1] += o0 * wh;
        ya[1][2 * e] += o1 * wl; ya[1][2 * e + 1] += o1 * wh;
      }
    }
#pragma unroll 2
    for (int it = 0; it < 22; ++it) {
      const int f = kg + 64 * it;
      if (f < F_) {
        const u32x4 w = *(const u32x4*)(wd + (size_t)f * D_ + cbase);
        const float h0 = hbuf[f], h1 = hbuf[F_ + f];
#pragma unroll
        for (int e = 0; e < 4; ++e) {
          const float wl = bf2f(w[e] & 0xffffu), wh = bfhi2f(w[e]);
          yd[0][2 * e] += h0 * wl; yd[0][2 * e + 1] += h0 * wh;
          yd[1][2 * e] += h1 * wl; yd[1][2 * e + 1] += h1 * wh;
        }
      }
    }
    float tot[2];
#pragma unroll
    for (int part = 0; part < 2; ++part) {
#pragma unroll
      for (int tk = 0; tk < 2; ++tk)
#pragma unroll
        for (int e = 0; e < 8; ++e) red[kg * 32 + tk * 16 + cq * 8 + e] = part ? yd[tk][e] : ya[tk][e];
      __syncthreads();
      float sum = 0.f;
      if (tid < 32) {
#pragma unroll 8
        for (int k = 0; k < 64; ++k) sum += red[k * 32 + tid];
      }
      tot[part] = sum;
      __syncthreads();
    }
    if (tid < 32) {
      const int tk = tid >> 4, c = tid & 15;
      const int row = tk ? idx1 : idx0;
      const float rs = ((const float*)sel)[2 + tk];
      const float2 st = stats[row];
      const float xf = bf2f(x[(size_t)row * D_ + col0 + c]);
      const float a = rbf(tot[0]);
      const float t1 = rbf(xf + a);
      const float xn = ln_elem(xf, st.x, st.y);
      const float upd = rbf(rbf(tot[1]) * rs);
      const float bb = rbf(xn + upd);
      out[(size_t)row * D_ + col0 + c] = __builtin_bit_cast(u16, (__bf16)(t1 + bb));
    }
  }
}

extern "C" void kernel_launch(void* const* d_in, const int* in_sizes, int n_in, void* d_out, int out_size,
                              void* d_ws, size_t ws_size, hipStream_t stream) {
  const u16* x = (const u16*)d_in[0];
  const u16* wq = (const u16*)d_in[1];
  const u16* wk = (const u16*)d_in[2];
  const u16* wv = (const u16*)d_in[3];
  const u16* wo = (const u16*)d_in[4];
  const u16* wr = (const u16*)d_in[6];
  const u16* wg = (const u16*)d_in[7];
  const u16* wu = (const u16*)d_in[8];
  const u16* wd = (const u16*)d_in[9];
  char* ws = (char*)d_ws;
  float* qraw = (float*)(ws + OFF_QRAW);
  u16* obuf = (u16*)(ws + OFF_OBUF);
  u16* kf = (u16*)(ws + OFF_KF);
  u16* vf = (u16*)(ws + OFF_VF);
  float* rbuf = (float*)(ws + OFF_R);
  float2* stats = (float2*)(ws + OFF_STATS);
  float* cs = (float*)(ws + OFF_CS);
  float* hbuf = (float*)(ws + OFF_H);
  int* sel = (int*)(ws + OFF_SEL);
  u16* out = (u16*)d_out;

  k1_proj<<<288, 128, 0, stream>>>(x, wq, wk, wv, wr, qraw, kf, vf, rbuf, stats, cs);
  k2_attn<<<256, 576, 0, stream>>>(x, qraw, kf, vf, rbuf, stats, cs, wg, wu, obuf, hbuf, sel);
  k3_out<<<288, 128, 0, stream>>>(x, obuf, wo, wd, stats, hbuf, sel, out);
}
```

```cpp
#include <hip/hip_runtime.h>
#include <hip/hip_bf16.h>


typedef unsigned short u16;
typedef __attribute__((ext_vector_type(8))) __bf16 bf16x8;
typedef __attribute__((ext_vector_type(2))) __bf16 bf16x2;
typedef __attribute__((ext_vector_type(2))) float f32x2;
typedef __attribute__((ext_vector_type(4))) float f32x4;
typedef __attribute__((ext_vector_type(16))) float f32x16;
typedef __attribute__((ext_vector_type(4))) unsigned u32x4;
typedef __attribute__((ext_vector_type(2))) unsigned u32x2;

#define DI __device__ __forceinline__

static constexpr int S_ = 2048;
static constexpr int D_ = 512;
static constexpr int F_ = 1376;

DI float bf2f(unsigned u) { return __uint_as_float(u << 16); }
DI float bfhi2f(unsigned u) { return __uint_as_float(u & 0xffff0000u); }
DI float rbf(float f) { return (float)(__bf16)f; }
DI unsigned pack2(float lo, float hi) {
  f32x2 p = {lo, hi};
  bf16x2 q = __builtin_convertvector(p, bf16x2);
  return __builtin_bit_cast(unsigned, q);
}
DI f32x4 mfma16(bf16x8 a, bf16x8 b, f32x4 c) { return __builtin_amdgcn_mfma_f32_16x16x32_bf16(a, b, c, 0, 0, 0); }
DI f32x16 mfma32(bf16x8 a, bf16x8 b, f32x16 c) { return __builtin_amdgcn_mfma_f32_32x32x16_bf16(a, b, c, 0, 0, 0); }
DI unsigned perm_lo(unsigned hi_src, unsigned lo_src) { return __builtin_amdgcn_perm(hi_src, lo_src, 0x05040100u); }
DI unsigned perm_hi(unsigned hi_src, unsigned lo_src) { return __builtin_amdgcn_perm(hi_src, lo_src, 0x07060302u); }

DI bf16x8 frag_lo(const unsigned* w) {
  u32x4 r = {perm_lo(w[1], w[0]), perm_lo(w[3], w[2]), perm_lo(w[5], w[4]), perm_lo(w[7], w[6])};
  return __builtin_bit_cast(bf16x8, r);
}
DI bf16x8 frag_hi(const unsigned* w) {
  u32x4 r = {perm_hi(w[1], w[0]), perm_hi(w[3], w[2]), perm_hi(w[5], w[4]), perm_hi(w[7], w[6])};
  return __builtin_bit_cast(bf16x8, r);
}
DI bf16x8 ones_frag() {
  u32x4 r = {0x3f803f80u, 0x3f803f80u, 0x3f803f80u, 0x3f803f80u};
  return __builtin_bit_cast(bf16x8, r);
}

static constexpr size_t OFF_QRAW = 0;
static constexpr size_t OFF_OBUF = OFF_QRAW + (size_t)S_ * D_ * 4;
static constexpr size_t OFF_KF = OFF_OBUF + (size_t)S_ * D_ * 2;
static constexpr size_t OFF_VF = OFF_KF + 65536;
static constexpr size_t OFF_R = OFF_VF + 65536;
static constexpr size_t OFF_STATS = OFF_R + S_ * 4;
static constexpr size_t OFF_CS = OFF_STATS + S_ * 8;
static constexpr size_t OFF_H = OFF_CS + 512 * 4;
static constexpr size_t OFF_SEL = OFF_H + 2 * F_ * 4;

__global__ __launch_bounds__(256) void k1_proj(const u16* __restrict__ x, const u16* __restrict__ wq,
                                               const u16* __restrict__ wk, const u16* __restrict__ wv,
                                               const u16* __restrict__ wr, float* __restrict__ qraw,
                                               u16* __restrict__ kf, u16* __restrict__ vf,
                                               float* __restrict__ rbuf, float2* __restrict__ stats,
                                               float* __restrict__ cs) {
  __shared__ __attribute__((aligned(16))) float2 red[4 * 32 * 64];
  const int lane = threadIdx.x & 63, wave = threadIdx.x >> 6;
  const int g = lane >> 4, i = lane & 15;
  const int cg = blockIdx.x % 9;
  const int rt = blockIdx.x / 9;
  const int row0 = rt * 32;
  const int k0 = wave * 128 + 8 * g;
  const bool kvp = (cg == 8);

  bf16x8 xa[2][4];
#pragma unroll
  for (int t = 0; t < 2; ++t)
#pragma unroll
    for (int s = 0; s < 4; ++s) xa[t][s] = *(const bf16x8*)(x + (size_t)(row0 + 16 * t + i) * D_ + k0 + 32 * s);

  const u16* wbase;
  int wstride;
  if (!kvp) {
    wbase = wq + cg * 64 + 4 * i + (size_t)k0 * D_;
    wstride = D_;
  } else {
    wbase = ((i < 4) ? (wk + 4 * i) : (i < 8 ? wv + 4 * (i - 4) : wk)) + k0 * 16;
    wstride = 16;
  }
  u32x2 w[4][8];
#pragma unroll
  for (int s = 0; s < 4; ++s)
#pragma unroll
    for (int j = 0; j < 8; ++j) w[s][j] = *(const u32x2*)(wbase + (size_t)(32 * s + j) * wstride);

  f32x4 acc[2][4], accs[4];
#pragma unroll
  for (int c = 0; c < 4; ++c) {
    acc[0][c] = (f32x4){0.f, 0.f, 0.f, 0.f};
    acc[1][c] = (f32x4){0.f, 0.f, 0.f, 0.f};
    accs[c] = (f32x4){0.f, 0.f, 0.f, 0.f};
  }
  const bf16x8 ones = ones_frag();
#pragma unroll
  for (int s = 0; s < 4; ++s) {
    unsigned wx[8], wy[8];
#pragma unroll
    for (int j = 0; j < 8; ++j) { wx[j] = w[s][j][0]; wy[j] = w[s][j][1]; }
    bf16x8 b[4] = {frag_lo(wx), frag_hi(wx), frag_lo(wy), frag_hi(wy)};
#pragma unroll
    for (int c = 0; c < 4; ++c) {
      acc[0][c] = mfma16(xa[0][s], b[c], acc[0][c]);
      acc[1][c] = mfma16(xa[1][s], b[c], acc[1][c]);
      accs[c] = mfma16(ones, b[c], accs[c]);
    }
  }
#pragma unroll
  for (int t = 0; t < 2; ++t)
#pragma unroll
    for (int c = 0; c < 4; ++c) {
      red[(wave * 32 + (t * 4 + c) * 2 + 0) * 64 + lane] = make_float2(acc[t][c][0], acc[t][c][1]);
      red[(wave * 32 + (t * 4 + c) * 2 + 1) * 64 + lane] = make_float2(acc[t][c][2], acc[t][c][3]);
    }
#pragma unroll
  for (int c = 0; c < 4; ++c) red[(wave * 32 + 16 + c) * 64 + lane] = make_float2(accs[c][0], accs[c][1]);

  if (kvp) {
    f32x4 asum[2], agram[2], ar[2], ars;
    ars = (f32x4){0.f, 0.f, 0.f, 0.f};
#pragma unroll
    for (int t = 0; t < 2; ++t) {
      asum[t] = (f32x4){0.f, 0.f, 0.f, 0.f};
      agram[t] = (f32x4){0.f, 0.f, 0.f, 0.f};
      ar[t] = (f32x4){0.f, 0.f, 0.f, 0.f};
    }
    bf16x8 rf[4];
#pragma unroll
    for (int s = 0; s < 4; ++s) rf[s] = *(const bf16x8*)(wr + k0 + 32 * s);
#pragma unroll
    for (int s = 0; s < 4; ++s) {
#pragma unroll
      for (int t = 0; t < 2; ++t) {
        asum[t] = mfma16(xa[t][s], ones, asum[t]);
        agram[t] = mfma16(xa[t][s], xa[t][s], agram[t]);
        ar[t] = mfma16(xa[t][s], rf[s], ar[t]);
      }
      ars = mfma16(ones, rf[s], ars);
    }
#pragma unroll
    for (int t = 0; t < 2; ++t) {
      red[(wave * 32 + 20 + t * 2 + 0) * 64 + lane] = make_float2(asum[t][0], asum[t][1]);
      red[(wave * 32 + 20 + t * 2 + 1) * 64 + lane] = make_float2(asum[t][2], asum[t][3]);
      red[(wave * 32 + 24 + t * 2 + 0) * 64 + lane] = make_float2(agram[t][0], agram[t][1]);
      red[(wave * 32 + 24 + t * 2 + 1) * 64 + lane] = make_float2(agram[t][2], agram[t][3]);
      red[(wave * 32 + 28 + t * 2 + 0) * 64 + lane] = make_float2(ar[t][0], ar[t][1]);
      red[(wave * 32 + 28 + t * 2 + 1) * 64 + lane] = make_float2(ar[t][2], ar[t][3]);
    }
    red[(wave * 32 + 16) * 64 + lane] = make_float2(accs[0][0], ars[0]);
  }
  __syncthreads();

  const int t = wave >> 1, half = wave & 1;
  float2 v[4];
#pragma unroll
  for (int c = 0; c < 4; ++c) {
    float2 a = red[(0 * 32 + (t * 4 + c) * 2 + half) * 64 + lane];
#pragma unroll
    for (int wv_ = 1; wv_ < 4; ++wv_) {
      float2 bb = red[(wv_ * 32 + (t * 4 + c) * 2 + half) * 64 + lane];
      a.x += bb.x; a.y += bb.y;
    }
    v[c] = a;
  }
  float csum[4];
  float rsum = 0.f;
#pragma unroll
  for (int c = 0; c < 4; ++c) {
    float a = 0.f;
#pragma unroll
    for (int wv_ = 0; wv_ < 4; ++wv_) {
      float2 bb = red[(wv_ * 32 + 16 + c) * 64 + lane];
      a += bb.x;
      if (c == 0) rsum += bb.y;
    }
    csum[c] = a;
  }
  const int rowa = row0 + 16 * t + 4 * g + 2 * half;
  if (!kvp) {
    f32x4 o0 = {v[0].x, v[1].x, v[2].x, v[3].x};
    f32x4 o1 = {v[0].y, v[1].y, v[2].y, v[3].y};
    *(f32x4*)(qraw + (size_t)rowa * D_ + cg * 64 + 4 * i) = o0;
    *(f32x4*)(qraw + (size_t)(rowa + 1) * D_ + cg * 64 + 4 * i) = o1;
    if (rt == 0 && wave == 0 && g == 0) {
      f32x4 o = {csum[0], csum[1], csum[2], csum[3]};
      *(f32x4*)(cs + cg * 64 + 4 * i) = o;
    }
  } else {
    float2 sm = make_float2(0.f, 0.f), gr = make_float2(0.f, 0.f), rr = make_float2(0.f, 0.f);
#pragma unroll
    for (int wv_ = 0; wv_ < 4; ++wv_) {
      float2 a = red[(wv_ * 32 + 20 + t * 2 + half) * 64 + lane];
      float2 bq = red[(wv_ * 32 + 24 + t * 2 + half) * 64 + lane];
      float2 cq = red[(wv_ * 32 + 28 + t * 2 + half) * 64 + lane];
      sm.x += a.x; sm.y += a.y; gr.x += bq.x; gr.y += bq.y; rr.x += cq.x; rr.y += cq.y;
    }
    float val[2][4];
#pragma unroll
    for (int e = 0; e < 2; ++e) {
      const int rloc = 4 * g + 2 * half + e;
      const float ssq = __shfl(e ? gr.y : gr.x, 16 * g + rloc);
      const float mean = (e ? sm.y : sm.x) * (1.0f / D_);
      float var = ssq * (1.0f / D_) - mean * mean;
      var = fmaxf(var, 0.f);
      const float mb = rbf(mean);
      const float vb = rbf(var);
      const float den = rbf(__builtin_sqrtf(rbf(vb + 1e-5f)));
      const float inv = 1.0f / den;
      const int row = rowa + e;
      if (i == 0) stats[row] = make_float2(mb, den);
#pragma unroll
      for (int c = 0; c < 4; ++c) val[e][c] = ((e ? v[c].y : v[c].x) - mb * csum[c]) * inv;
      if (i == 8) rbuf[row] = rbf(((e ? rr.y : rr.x) - mb * rsum) * inv);
    }
    const int kk = 16 * t + 4 * g + 2 * half;
    if (i < 4) {
      const int d = 4 * i;
#pragma unroll
      for (int e = 0; e < 2; ++e) {
        u32x2 o = {pack2(val[e][0], val[e][1]), pack2(val[e][2], val[e][3])};
        *(u32x2*)(kf + (size_t)rt * 512 + (d >> 3) * 256 + (kk + e) * 8 + (d & 7)) = o;
      }
    } else if (i < 8) {
      const int hh = g & 1, jh = g >> 1;
#pragma unroll
      for (int c = 0; c < 4; ++c) {
        const int d = 4 * (i - 4) + c;
        *(unsigned*)(vf + (size_t)rt * 512 + t * 256 + (hh * 16 + d) * 8 + jh * 4 + 2 * half) =
            pack2(val[0][c], val[1][c]);
      }
    }
  }
}

DI void top2_wave(const float* __restrict__ rbuf, int lane, int& i0, float& v0, int& i1, float& v1) {
  f32x4 v[8];
#pragma unroll
  for (int c = 0; c < 8; ++c) v[c] = *(const f32x4*)(rbuf + c * 256 + lane * 4);
  unsigned k0 = 0, k1 = 0;
#pragma unroll
  for (int c = 0; c < 8; ++c)
#pragma unroll
    for (int e = 0; e < 4; ++e) {
      const unsigned u = __float_as_uint(v[c][e]);
      const unsigned ord = (u & 0x80000000u) ? ~u : (u | 0x80000000u);
      const unsigned key = (ord & 0xffff0000u) | (unsigned)(2047 - (c * 256 + lane * 4 + e));
      k1 = max(k1, min(k0, key));
      k0 = max(k0, key);
    }
#pragma unroll
  for (int off = 1; off < 64; off <<= 1) {
    const unsigned o0 = __shfl_xor(k0, off), o1 = __shfl_xor(k1, off);
    const unsigned n0 = max(k0, o0);
    const unsigned n1 = max(min(k0, o0), max(k1, o1));
    k0 = n0; k1 = n1;
  }
  i0 = 2047 - (int)(k0 & 0x7ffu);
  i1 = 2047 - (int)(k1 & 0x7ffu);
  v0 = rbuf[i0];
  v1 = rbuf[i1];
}

DI float ln_elem(float xv, float mb, float den) { return rbf(rbf(xv - mb) / den); }

static constexpr int LDS_K = 0;
static constexpr int LDS_V = 65536;
static constexpr int LDS_C = 131072;
static constexpr int LDS_TOTAL = 131072 + 32;

__global__ __launch_bounds__(576) void k2_attn(const u16* __restrict__ x, const float* __restrict__ qraw,
                                               const u16* __restrict__ kf, const u16* __restrict__ vf,
                                               const float* __restrict__ rbuf, const float2* __restrict__ stats,
                                               const float* __restrict__ cs, const u16* __restrict__ wg,
                                               const u16* __restrict__ wu, u16* __restrict__ obuf,
                                               float* __restrict__ hbuf, int* __restrict__ sel) {
  __shared__ __attribute__((aligned(16))) char smem[LDS_TOTAL];
  const int tid = threadIdx.x;
  const int lane = tid & 63, wave = tid >> 6;
  const int b = blockIdx.x;

  if (wave < 8) {
    {
      u32x4 t[8];
#pragma unroll
      for (int c = 0; c < 8; ++c) t[c] = *(const u32x4*)((const char*)kf + c * 8192 + tid * 16);
#pragma unroll
      for (int c = 0; c < 8; ++c) *(u32x4*)(smem + LDS_K + c * 8192 + tid * 16) = t[c];
    }
    if (tid < 8) *(unsigned*)(smem + LDS_C + tid * 4) = (tid < 4) ? 0x3f803f80u : 0u;
    u32x4 tv[8];
#pragma unroll
    for (int c = 0; c < 8; ++c) tv[c] = *(const u32x4*)((const char*)vf + c * 8192 + tid * 16);

    const int r = lane & 31, hh = lane >> 5;
    const int qt = b * 8 + wave;
    const int rowblk = qt >> 5, head = qt & 31;
    const int row = rowblk * 32 + r;
    bf16x8 qf;
    {
      const float* qp = qraw + (size_t)row * D_ + head * 16 + 8 * hh;
      f32x4 a0 = *(const f32x4*)qp, a1 = *(const f32x4*)(qp + 4);
      f32x4 c0 = *(const f32x4*)(cs + head * 16 + 8 * hh), c1 = *(const f32x4*)(cs + head * 16 + 8 * hh + 4);
      const float2 st = stats[row];
      const float inv = 1.0f / st.y;
      const float SC = 0.25f * 1.44269504088896340736f;
      float q[8];
#pragma unroll
      for (int e = 0; e < 4; ++e) {
        q[e] = rbf((a0[e] - st.x * c0[e]) * inv) * SC;
        q[4 + e] = rbf((a1[e] - st.x * c1[e]) * inv) * SC;
      }
      u32x4 pk = {pack2(q[0], q[1]), pack2(q[2], q[3]), pack2(q[4], q[5]), pack2(q[6], q[7])};
      qf = __builtin_bit_cast(bf16x8, pk);
    }
    __syncthreads();

    const char* kp = smem + LDS_K + lane * 16;
    float m0 = -__builtin_inff(), m1 = -__builtin_inff();
    f32x16 zero16;
#pragma unroll
    for (int e = 0; e < 16; ++e) zero16[e] = 0.f;
#pragma unroll 2
    for (int t = 0; t < 64; t += 2) {
      bf16x8 ka = *(const bf16x8*)(kp + t * 1024);
      bf16x8 kb = *(const bf16x8*)(kp + t * 1024 + 1024);
      f32x16 sa = mfma32(ka, qf, zero16);
      f32x16 sb = mfma32(kb, qf, zero16);
#pragma unroll
      for (int e = 0; e < 16; e += 2) m0 = fmaxf(fmaxf(m0, sa[e]), sa[e + 1]);
#pragma unroll
      for (int e = 0; e < 16; e += 2) m1 = fmaxf(fmaxf(m1, sb[e]), sb[e + 1]);
    }
    float m = fmaxf(m0, m1);
    m = fmaxf(m, __shfl_xor(m, 32));

#pragma unroll
    for (int c = 0; c < 8; ++c) *(u32x4*)(smem + LDS_V + c * 8192 + tid * 16) = tv[c];
    __syncthreads();

    f32x16 negm;
#pragma unroll
    for (int e = 0; e < 16; ++e) negm[e] = -m;
    f32x16 o;
#pragma unroll
    for (int e = 0; e < 16; ++e) o[e] = 0.f;
    int voff0 = (r < 16) ? (LDS_V + (hh * 16 + r) * 16) : (LDS_C + (r == 16 ? 0 : 16));
    int voff1 = (r < 16) ? (voff0 + 512) : voff0;
    const int vstride = (r < 16) ? 1024 : 0;
#pragma unroll 2
    for (int t = 0; t < 64; ++t) {
      bf16x8 ka = *(const bf16x8*)(kp + t * 1024);
      bf16x8 v0 = *(const bf16x8*)(smem + voff0);
      bf16x8 v1 = *(const bf16x8*)(smem + voff1);
      voff0 += vstride;
      voff1 += vstride;
      f32x16 s = mfma32(ka, qf, negm);
      float p[16];
#pragma unroll
      for (int e = 0; e < 16; ++e) p[e] = __builtin_amdgcn_exp2f(s[e]);
      u32x4 pa = {pack2(p[0], p[1]), pack2(p[2], p[3]), pack2(p[4], p[5]), pack2(p[6], p[7])};
      u32x4 pb = {pack2(p[8], p[9]), pack2(p[10], p[11]), pack2(p[12], p[13]), pack2(p[14], p[15])};
      o = mfma32(v0, __builtin_bit_cast(bf16x8, pa), o);
      o = mfma32(v1, __builtin_bit_cast(bf16x8, pb), o);
    }
    const float l = __shfl(o[8], r);
    const float il = 1.0f / l;
    u16* op = obuf + (size_t)row * D_ + head * 16 + 4 * hh;
    u32x2 w0 = {pack2(o[0] * il, o[1] * il), pack2(o[2] * il, o[3] * il)};
    u32x2 w1 = {pack2(o[4] * il, o[5] * il), pack2(o[6] * il, o[7] * il)};
    *(u32x2*)(op) = w0;
    *(u32x2*)(op + 8) = w1;
  } else {
    __syncthreads();
    __syncthreads();
    if (b < 43) {
      int i0, i1; float v0, v1;
      top2_wave(rbuf, lane, i0, v0, i1, v1);
      i0 = min(i0, S_ - 1);
      i1 = min(i1, S_ - 1);
      if (b == 0 && lane == 0) {
        sel[0] = i0; sel[1] = i1;
        ((float*)sel)[2] = rbf(1.0f / (1.0f + __expf(-v0)));
        ((float*)sel)[3] = rbf(1.0f / (1.0f + __expf(-v1)));
      }
      const int g = lane >> 4, i = lane & 15;
      const int srow = (i == 0) ? i0 : i1;
      const float2 st = stats[srow];
      f32x4 ag[2], au[2];
#pragma unroll
      for (int c = 0; c < 2; ++c) { ag[c] = (f32x4){0.f, 0.f, 0.f, 0.f}; au[c] = (f32x4){0.f, 0.f, 0.f, 0.f}; }
      const int f0 = b * 32;
      const u16* gp = wg + f0 + 2 * i + (size_t)(8 * g) * F_;
      const u16* up = wu + f0 + 2 * i + (size_t)(8 * g) * F_;
#pragma unroll 1
      for (int sb = 0; sb < 4; ++sb) {
        unsigned w1[4][8], w2[4][8];
        u32x4 xr[4];
#pragma unroll
        for (int s = 0; s < 4; ++s) {
          xr[s] = *(const u32x4*)(x + (size_t)srow * D_ + 32 * (4 * sb + s) + 8 * g);
#pragma unroll
          for (int j = 0; j < 8; ++j) {
            w1[s][j] = *(const unsigned*)(gp + (size_t)(32 * (4 * sb + s) + j) * F_);
            w2[s][j] = *(const unsigned*)(up + (size_t)(32 * (4 * sb + s) + j) * F_);
          }
        }
#pragma unroll
        for (int s = 0; s < 4; ++s) {
          u32x4 xo;
#pragma unroll
          for (int e = 0; e < 4; ++e) {
            const float lo = ln_elem(bf2f(xr[s][e] & 0xffffu), st.x, st.y);
            const float hi = ln_elem(bfhi2f(xr[s][e]), st.x, st.y);
            xo[e] = (i < 2) ? pack2(lo, hi) : 0u;
          }
          const bf16x8 a = __builtin_bit_cast(bf16x8, xo);
          ag[0] = mfma16(a, frag_lo(w1[s]), ag[0]);
          ag[1] = mfma16(a, frag_hi(w1[s]), ag[1]);
          au[0] = mfma16(a, frag_lo(w2[s]), au[0]);
          au[1] = mfma16(a, frag_hi(w2[s]), au[1]);
        }
      }
      if (g == 0) {
#pragma unroll
        for (int c = 0; c < 2; ++c)
#pragma unroll
          for (int tk = 0; tk < 2; ++tk) {
            const float gv = rbf(ag[c][tk]);
            const float uv = rbf(au[c][tk]);
            const float sg = rbf(gv / (1.0f + __expf(-gv)));
            hbuf[tk * F_ + f0 + 2 * i + c] = rbf(sg * uv);
          }
      }
    }
  }
}

__global__ __launch_bounds__(256) void k3_out(const u16* __restrict__ x, const u16* __restrict__ obuf,
                                              const u16* __restrict__ wo, const u16* __restrict__ wd,
                                              const float2* __restrict__ stats, const float* __restrict__ hbuf,
                                              const int* __restrict__ sel, u16* __restrict__ out) {
  __shared__ __attribute__((aligned(16))) float2 red[4 * 16 * 64];
  const int tid = threadIdx.x;
  const int lane = tid & 63, wave = tid >> 6;
  const int idx0 = sel[0], idx1 = sel[1];
  if (blockIdx.x < 512) {
    const int g = lane >> 4, i = lane & 15;
    const int cg = blockIdx.x & 7;
    const int rt = blockIdx.x >> 3;
    const int row0 = rt * 32;
    const int k0 = wave * 128 + 8 * g;
    bf16x8 xa[2][4];
#pragma unroll
    for (int t = 0; t < 2; ++t)
#pragma unroll
      for (int s = 0; s < 4; ++s)
        xa[t][s] = *(const bf16x8*)(obuf + (size_t)(row0 + 16 * t + i) * D_ + k0 + 32 * s);
    const u16* wbase = wo + cg * 64 + 4 * i + (size_t)k0 * D_;
    u32x2 w[4][8];
#pragma unroll
    for (int s = 0; s < 4; ++s)
#pragma unroll
      for (int j = 0; j < 8; ++j) w[s][j] = *(const u32x2*)(wbase + (size_t)(32 * s + j) * D_);
    const int t = wave >> 1, half = wave & 1;
    const int rowa = row0 + 16 * t + 4 * g + 2 * half;
    const size_t off0 = (size_t)rowa * D_ + cg * 64 + 4 * i;
    const u32x2 xv0 = *(const u32x2*)(x + off0), xv1 = *(const u32x2*)(x + off0 + D_);
    const float2 st0 = stats[rowa], st1 = stats[rowa + 1];

    f32x4 acc[2][4];
#pragma unroll
    for (int c = 0; c < 4; ++c) {
      acc[0][c] = (f32x4){0.f, 0.f, 0.f, 0.f};
      acc[1][c] = (f32x4){0.f, 0.f, 0.f, 0.f};
    }
#pragma unroll
    for (int s = 0; s < 4; ++s) {
      unsigned wx[8], wy[8];
#pragma unroll
      for (int j = 0; j < 8; ++j) { wx[j] = w[s][j][0]; wy[j] = w[s][j][1]; }
      bf16x8 bb[4] = {frag_lo(wx), frag_hi(wx), frag_lo(wy), frag_hi(wy)};
#pragma unroll
      for (int c = 0; c < 4; ++c) {
        acc[0][c] = mfma16(xa[0][s], bb[c], acc[0][c]);
        acc[1][c] = mfma16(xa[1][s], bb[c], acc[1][c]);
      }
    }
#pragma unroll
    for (int tt = 0; tt < 2; ++tt)
#pragma unroll
      for (int c = 0; c < 4; ++c) {
        red[(wave * 16 + (tt * 4 + c) * 2 + 0) * 64 + lane] = make_float2(acc[tt][c][0], acc[tt][c][1]);
        red[(wave * 16 + (tt * 4 + c) * 2 + 1) * 64 + lane] = make_float2(acc[tt][c][2], acc[tt][c][3]);
      }
    __syncthreads();
    float2 v[4];
#pragma unroll
    for (int c = 0; c < 4; ++c) {
      float2 a = red[(0 * 16 + (t * 4 + c) * 2 + half) * 64 + lane];
#pragma unroll
      for (int wv_ = 1; wv_ < 4; ++wv_) {
        float2 bq = red[(wv_ * 16 + (t * 4 + c) * 2 + half) * 64 + lane];
        a.x += bq.x; a.y += bq.y;
      }
      v[c] = a;
    }
#pragma unroll
    for (int e = 0; e < 2; ++e) {
      const u32x2 xv = e ? xv1 : xv0;
      const float2 st = e ? st1 : st0;
      float xf[4] = {bf2f(xv[0] & 0xffffu), bfhi2f(xv[0]), bf2f(xv[1] & 0xffffu), bfhi2f(xv[1])};
      float res[4];
#pragma unroll
      for (int c = 0; c < 4; ++c) {
        const float a = rbf(e ? v[c].y : v[c].x);
        const float t1 = rbf(xf[c] + a);
        const float xn = ln_elem(xf[c], st.x, st.y);
        res[c] = t1 + xn;
      }
      const int row = rowa + e;
      if (row != idx0 && row != idx1) {
        u32x2 o = {pack2(res[0], res[1]), pack2(res[2], res[3])};
        *(u32x2*)(out + off0 + (size_t)e * D_) = o;
      }
    }
  } else {
    float* redf = (float*)red;
    const int col0 = (blockIdx.x - 512) * 16;
    const int kg = tid >> 1, cq = tid & 1;
    const int cbase = col0 + cq * 8;
    u32x4 wa[4], wdn[11];
    float o0[4], o1[4], h0[11], h1[11];
#pragma unroll
    for (int it = 0; it < 4; ++it) {
      const int k = kg + 128 * it;
      wa[it] = *(const u32x4*)(wo + (size_t)k * D_ + cbase);
      o0[it] = bf2f(obuf[(size_t)idx0 * D_ + k]);
      o1[it] = bf2f(obuf[(size_t)idx1 * D_ + k]);
    }
#pragma unroll
    for (int it = 0; it < 11; ++it) {
      const int f = kg + 128 * it;
      const int fc = min(f, F_ - 1);
      wdn[it] = *(const u32x4*)(wd + (size_t)fc * D_ + cbase);
      const float a0 = hbuf[fc], a1 = hbuf[F_ + fc];
      h0[it] = (f < F_) ? a0 : 0.f;
      h1[it] = (f < F_) ? a1 : 0.f;
    }
    float ya[2][8], yd[2][8];
#pragma unroll
    for (int e = 0; e < 8; ++e) { ya[0][e] = ya[1][e] = 0.f; yd[0][e] = yd[1][e] = 0.f; }
#pragma unroll
    for (int it = 0; it < 4; ++it)
#pragma unroll
      for (int e = 0; e < 4; ++e) {
        const float wl = bf2f(wa[it][e] & 0xffffu), wh = bfhi2f(wa[it][e]);
        ya[0][2 * e] += o0[it] * wl; ya[0][2 * e + 1] += o0[it] * wh;
        ya[1][2 * e] += o1[it] * wl; ya[1][2 * e + 1] += o1[it] * wh;
      }
#pragma unroll
    for (int it = 0; it < 11; ++it)
#pragma unroll
      for (int e = 0; e < 4; ++e) {
        const float wl = bf2f(wdn[it][e] & 0xffffu), wh = bfhi2f(wdn[it][e]);
        yd[0][2 * e] += h0[it] * wl; yd[0][2 * e + 1] += h0[it] * wh;
        yd[1][2 * e] += h1[it] * wl; yd[1][2 * e + 1] += h1[it] * wh;
      }
#pragma unroll
    for (int tk = 0; tk < 2; ++tk)
#pragma unroll
      for (int e = 0; e < 8; ++e) {
        redf[kg * 64 + tk * 16 + cq * 8 + e] = ya[tk][e];
        redf[kg * 64 + 32 + tk * 16 + cq * 8 + e] = yd[tk][e];
      }
    __syncthreads();
    {
      const int o = tid & 63, q = tid >> 6;
      float sum = 0.f;
#pragma unroll 8
      for (int k = 0; k < 32; ++k) sum += redf[(q * 32 + k) * 64 + o];
      __syncthreads();
      redf[q * 64 + o] = sum;
      __syncthreads();
    }
    if (tid < 32) {
      const float tota = redf[tid] + redf[64 + tid] + redf[128 + tid] + redf[192 + tid];
      const float totd = redf[32 + tid] + redf[96 + tid] + redf[160 + tid] + redf[224 + tid];
      const int tk = tid >> 4, c = tid & 15;
      const int row = tk ? idx1 : idx0;
      const float rs = ((const float*)sel)[2 + tk];
      const float2 st = stats[row];
      const float xf = bf2f(x[(size_t)row * D_ + col0 + c]);
      const float a = rbf(tota);
      const float t1 = rbf(xf + a);
      const float xn = ln_elem(xf, st.x, st.y);
      const float upd = rbf(rbf(totd) * rs);
      const float bb = rbf(xn + upd);
      out[(size_t)row * D_ + col0 + c] = __builtin_bit_cast(u16, (__bf16)(t1 + bb));
    }
  }
}

extern "C" void kernel_launch(void* const* d_in, const int* in_sizes, int n_in, void* d_out, int out_size,
                              void* d_ws, size_t ws_size, hipStream_t stream) {
  const u16* x = (const u16*)d_in[0];
  const u16* wq = (const u16*)d_in[1];
  const u16* wk = (const u16*)d_in[2];
  const u16* wv = (const u16*)d_in[3];
  const u16* wo = (const u16*)d_in[4];
  const u16* wr = (const u16*)d_in[6];
  const u16* wg = (const u16*)d_in[7];
  const u16* wu = (const u16*)d_in[8];
  const u16* wd = (const u16*)d_in[9];
  char* ws = (char*)d_ws;
  float* qraw = (float*)(ws + OFF_QRAW);
  u16* obuf = (u16*)(ws + OFF_OBUF);
  u16* kf = (u16*)(ws + OFF_KF);
  u16* vf = (u16*)(ws + OFF_VF);
  float* rbuf = (float*)(ws + OFF_R);
  float2* stats = (float2*)(ws + OFF_STATS);
  float* cs = (float*)(ws + OFF_CS);
  float* hbuf = (float*)(ws + OFF_H);
  int* sel = (int*)(ws + OFF_SEL);
  u16* out = (u16*)d_out;

  k1_proj<<<576, 256, 0, stream>>>(x, wq, wk, wv, wr, qraw, kf, vf, rbuf, stats, cs);
  k2_attn<<<256, 576, 0, stream>>>(x, qraw, kf, vf, rbuf, stats, cs, wg, wu, obuf, hbuf, sel);
  k3_out<<<544, 256, 0, stream>>>(x, obuf, wo, wd, stats, hbuf, sel, out);
}
```

```cpp
#include <hip/hip_runtime.h>
#include <hip/hip_bf16.h>

typedef unsigned short u16;
typedef __attribute__((ext_vector_type(8))) __bf16 bf16x8;
typedef __attribute__((ext_vector_type(2))) __bf16 bf16x2;
typedef __attribute__((ext_vector_type(2))) float f32x2;
typedef __attribute__((ext_vector_type(4))) float f32x4;
typedef __attribute__((ext_vector_type(16))) float f32x16;
typedef __attribute__((ext_vector_type(4))) unsigned u32x4;
typedef __attribute__((ext_vector_type(2))) unsigned u32x2;

#define DI __device__ __forceinline__
#define TS(k, n)
#define TSR(k, n)

static constexpr int S_ = 2048;
static constexpr int D_ = 512;
static constexpr int F_ = 1376;

DI float bf2f(unsigned u) { return __uint_as_float(u << 16); }
DI float bfhi2f(unsigned u) { return __uint_as_float(u & 0xffff0000u); }
DI float rbf(float f) { return (float)(__bf16)f; }
DI unsigned pack2(float lo, float hi) {
  f32x2 p = {lo, hi};
  bf16x2 q = __builtin_convertvector(p, bf16x2);
  return __builtin_bit_cast(unsigned, q);
}
DI f32x4 mfma16(bf16x8 a, bf16x8 b, f32x4 c) { return __builtin_amdgcn_mfma_f32_16x16x32_bf16(a, b, c, 0, 0, 0); }
DI f32x16 mfma32(bf16x8 a, bf16x8 b, f32x16 c) { return __builtin_amdgcn_mfma_f32_32x32x16_bf16(a, b, c, 0, 0, 0); }
DI unsigned perm_lo(unsigned hi_src, unsigned lo_src) { return __builtin_amdgcn_perm(hi_src, lo_src, 0x05040100u); }
DI unsigned perm_hi(unsigned hi_src, unsigned lo_src) { return __builtin_amdgcn_perm(hi_src, lo_src, 0x07060302u); }

DI bf16x8 frag_lo(const unsigned* w) {
  u32x4 r = {perm_lo(w[1], w[0]), perm_lo(w[3], w[2]), perm_lo(w[5], w[4]), perm_lo(w[7], w[6])};
  return __builtin_bit_cast(bf16x8, r);
}
DI bf16x8 frag_hi(const unsigned* w) {
  u32x4 r = {perm_hi(w[1], w[0]), perm_hi(w[3], w[2]), perm_hi(w[5], w[4]), perm_hi(w[7], w[6])};
  return __builtin_bit_cast(bf16x8, r);
}
DI float ln_elem(float xv, float mb, float den, float inv) {
  const float d = rbf(xv - mb);
  float q = d * inv;
  const float rem = __builtin_fmaf(-q, den, d);
  q = __builtin_fmaf(rem, inv, q);
  return rbf(q);
}

static constexpr int VT_STRIDE = 1040;
static constexpr int VF_BYTES = 64 * VT_STRIDE;
static constexpr size_t OFF_XNF = 0;
static constexpr size_t OFF_OF = OFF_XNF + (size_t)S_ * D_ * 2;
static constexpr size_t OFF_WQF = OFF_OF + (size_t)S_ * D_ * 2;
static constexpr size_t OFF_WOF = OFF_WQF + (size_t)D_ * D_ * 2;
static constexpr size_t OFF_KF = OFF_WOF + (size_t)D_ * D_ * 2;
static constexpr size_t OFF_VF = OFF_KF + 65536;
static constexpr size_t OFF_R = OFF_VF + VF_BYTES;
static constexpr size_t OFF_STATS = OFF_R + S_ * 4;
static constexpr size_t OFF_H = OFF_STATS + S_ * 8;
static constexpr size_t OFF_SEL = OFF_H + 2 * F_ * 4;

static constexpr int XN_STRIDE = 1040;
__global__ __launch_bounds__(256) void ka_prep(const u16* __restrict__ x, const u16* __restrict__ wq,
                                               const u16* __restrict__ wk, const u16* __restrict__ wv,
                                               const u16* __restrict__ wo, const u16* __restrict__ wr,
                                               u16* __restrict__ xnf, u16* __restrict__ wqf, u16* __restrict__ wof,
                                               u16* __restrict__ kf, u16* __restrict__ vf,
                                               float* __restrict__ rbuf, float2* __restrict__ stats) {
  __shared__ __attribute__((aligned(16))) char smem[32 * XN_STRIDE + 4 * 20 * 64 * 8];
  const int tid = threadIdx.x;
  const int lane = tid & 63, wave = tid >> 6;
  TSR(0, 8); TS(0, 0);
  if (blockIdx.x >= 64) {
    const int pb = blockIdx.x - 64;
    const bool iso = pb >= 64;
    const int cp = tid, ko = pb & 63;
    const int ks = ko >> 1, hh = ko & 1;
    const u16* W = (iso ? wo : wq) + 2 * cp;
    unsigned w[8];
#pragma unroll
    for (int j = 0; j < 8; ++j) {
      const int row = 16 * ks + (iso ? (8 * (j >> 2) + 4 * hh + (j & 3)) : (8 * hh + j));
      w[j] = *(const unsigned*)(W + row * D_);
    }
    u16* dst = (iso ? wof : wqf) + (((cp >> 4) * 32 + ks) * 64 + hh * 32 + ((2 * cp) & 31)) * 8;
    *(bf16x8*)dst = frag_lo(w);
    *(bf16x8*)(dst + 8) = frag_hi(w);
    TS(0, 3); TSR(0, 9);
    return;
  }
  const int rt = blockIdx.x, row0 = rt * 32;
  const int g = lane >> 4, i = lane & 15;
  const int k0 = wave * 128 + 8 * g;
  u32x4 xr[8];
#pragma unroll
  for (int rr = 0; rr < 8; ++rr) xr[rr] = *(const u32x4*)(x + (size_t)(row0 + 8 * wave + rr) * D_ + 8 * lane);
  const u16* wbase = ((i < 4) ? (wk + 4 * i) : (i < 8 ? wv + 4 * (i - 4) : wk)) + k0 * 16;
  u32x2 w[4][8];
#pragma unroll
  for (int s = 0; s < 4; ++s)
#pragma unroll
    for (int j = 0; j < 8; ++j) w[s][j] = *(const u32x2*)(wbase + (32 * s + j) * 16);
  bf16x8 rf[4];
#pragma unroll
  for (int s = 0; s < 4; ++s) rf[s] = *(const bf16x8*)(wr + k0 + 32 * s);

  float s1[8], s2[8];
#pragma unroll
  for (int rr = 0; rr < 8; ++rr) {
    float a = 0.f, q = 0.f;
#pragma unroll
    for (int e = 0; e < 4; ++e) {
      const float lo = bf2f(xr[rr][e] & 0xffffu), hi = bfhi2f(xr[rr][e]);
      a += lo + hi;
      q = __builtin_fmaf(lo, lo, q);
      q = __builtin_fmaf(hi, hi, q);
    }
    s1[rr] = a; s2[rr] = q;
  }
#pragma unroll
  for (int off = 32; off >= 1; off >>= 1)
#pragma unroll
    for (int rr = 0; rr < 8; ++rr) {
      s1[rr] += __shfl_xor(s1[rr], off);
      s2[rr] += __shfl_xor(s2[rr], off);
    }
#pragma unroll
  for (int rr = 0; rr < 8; ++rr) {
    const float mean = s1[rr] * (1.0f / D_);
    float var = s2[rr] * (1.0f / D_) - mean * mean;
    var = fmaxf(var, 0.f);
    const float mb = rbf(mean);
    const float den = rbf(__builtin_sqrtf(rbf(rbf(var) + 1e-5f)));
    const float inv = 1.0f / den;
    if (lane == rr) stats[row0 + 8 * wave + rr] = make_float2(mb, den);
    u32x4 o;
#pragma unroll
    for (int e = 0; e < 4; ++e)
      o[e] = pack2(ln_elem(bf2f(xr[rr][e] & 0xffffu), mb, den, inv), ln_elem(bfhi2f(xr[rr][e]), mb, den, inv));
    *(u32x4*)(smem + (8 * wave + rr) * XN_STRIDE + lane * 16) = o;
  }
  if (tid < 1) {
    u32x4 ones = {0x3f803f80u, 0x3f803f80u, 0x3f803f80u, 0x3f803f80u};
    *(u32x4*)((char*)vf + rt * VT_STRIDE + 1024) = ones;
  }
  TS(0, 1);
  __syncthreads();
  {
    const int r = lane & 31, hh = lane >> 5;
#pragma unroll
    for (int s = 0; s < 8; ++s) {
      const int ks = 8 * wave + s;
      const u32x4 v = *(const u32x4*)(smem + r * XN_STRIDE + ks * 32 + hh * 16);
      *(u32x4*)(xnf + ((size_t)(rt * 32 + ks) * 64 + lane) * 8) = v;
    }
  }
  float2* red = (float2*)(smem + 32 * XN_STRIDE);
  {
    f32x4 acc[2][4], ar[2];
#pragma unroll
    for (int t = 0; t < 2; ++t) {
      ar[t] = (f32x4){0.f, 0.f, 0.f, 0.f};
#pragma unroll
      for (int c = 0; c < 4; ++c) acc[t][c] = (f32x4){0.f, 0.f, 0.f, 0.f};
    }
#pragma unroll
    for (int s = 0; s < 4; ++s) {
      unsigned wx[8], wy[8];
#pragma unroll
      for (int j = 0; j < 8; ++j) { wx[j] = w[s][j][0]; wy[j] = w[s][j][1]; }
      bf16x8 b[4] = {frag_lo(wx), frag_hi(wx), frag_lo(wy), frag_hi(wy)};
#pragma unroll
      for (int t = 0; t < 2; ++t) {
        const bf16x8 xa = *(const bf16x8*)(smem + (16 * t + i) * XN_STRIDE + (k0 + 32 * s) * 2);
#pragma unroll
        for (int c = 0; c < 4; ++c) acc[t][c] = mfma16(xa, b[c], acc[t][c]);
        ar[t] = mfma16(xa, rf[s], ar[t]);
      }
    }
#pragma unroll
    for (int t = 0; t < 2; ++t) {
#pragma unroll
      for (int c = 0; c < 4; ++c) {
        red[(wave * 20 + (t * 4 + c) * 2 + 0) * 64 + lane] = make_float2(acc[t][c][0], acc[t][c][1]);
        red[(wave * 20 + (t * 4 + c) * 2 + 1) * 64 + lane] = make_float2(acc[t][c][2], acc[t][c][3]);
      }
      red[(wave * 20 + 16 + t * 2 + 0) * 64 + lane] = make_float2(ar[t][0], ar[t][1]);
      red[(wave * 20 + 16 + t * 2 + 1) * 64 + lane] = make_float2(ar[t][2], ar[t][3]);
    }
  }
  __syncthreads();
  TS(0, 2);
  const int t = wave >> 1, half = wave & 1;
  float2 v[4], rr2 = make_float2(0.f, 0.f);
#pragma unroll
  for (int c = 0; c < 4; ++c) {
    float2 a = red[(0 * 20 + (t * 4 + c) * 2 + half) * 64 + lane];
#pragma unroll
    for (int wv_ = 1; wv_ < 4; ++wv_) {
      const float2 bb = red[(wv_ * 20 + (t * 4 + c) * 2 + half) * 64 + lane];
      a.x += bb.x; a.y += bb.y;
    }
    v[c] = a;
  }
#pragma unroll
  for (int wv_ = 0; wv_ < 4; ++wv_) {
    const float2 bb = red[(wv_ * 20 + 16 + t * 2 + half) * 64 + lane];
    rr2.x += bb.x; rr2.y += bb.y;
  }
  const int kk = 16 * t + 4 * g + 2 * half;
  if (i < 4) {
    const int hh = i & 1, jh = i >> 1;
    u32x2 o0 = {pack2(v[0].x, v[1].x), pack2(v[2].x, v[3].x)};
    u32x2 o1 = {pack2(v[0].y, v[1].y), pack2(v[2].y, v[3].y)};
    *(u32x2*)(kf + rt * 512 + (hh * 32 + kk) * 8 + jh * 4) = o0;
    *(u32x2*)(kf + rt * 512 + (hh * 32 + kk + 1) * 8 + jh * 4) = o1;
  } else if (i < 8) {
    const int hh = g & 1, jh = g >> 1;
#pragma unroll
    for (int c = 0; c < 4; ++c) {
      const int d = 4 * (i - 4) + c;
      *(unsigned*)(vf + rt * (VT_STRIDE / 2) + t * 256 + (hh * 16 + d) * 8 + jh * 4 + 2 * half) =
          pack2(v[c].x, v[c].y);
    }
  } else if (i == 8) {
    *(float2*)(rbuf + row0 + kk) = make_float2(rbf(rr2.x), rbf(rr2.y));
  }
  TS(0, 3); TSR(0, 9);
}

DI void top2_keys(const f32x4* v, int lane, int& i0, int& i1) {
  unsigned k0 = 0, k1 = 0;
#pragma unroll
  for (int c = 0; c < 8; ++c)
#pragma unroll
    for (int e = 0; e < 4; ++e) {
      const unsigned u = __float_as_uint(v[c][e]);
      const unsigned ord = (u & 0x80000000u) ? ~u : (u | 0x80000000u);
      const unsigned key = (ord & 0xffff0000u) | (unsigned)(2047 - (c * 256 + lane * 4 + e));
      k1 = max(k1, min(k0, key));
      k0 = max(k0, key);
    }
#pragma unroll
  for (int off = 1; off < 64; off <<= 1) {
    const unsigned o0 = __shfl_xor(k0, off), o1 = __shfl_xor(k1, off);
    const unsigned n0 = max(k0, o0);
    const unsigned n1 = max(min(k0, o0), max(k1, o1));
    k0 = n0; k1 = n1;
  }
  i0 = 2047 - (int)(k0 & 0x7ffu);
  i1 = 2047 - (int)(k1 & 0x7ffu);
}

static constexpr int LDS_K = 0;
static constexpr int LDS_V = 65536;
static constexpr int LDS_TOTAL = 65536 + VF_BYTES;

__global__ __launch_bounds__(576) void kb_attn(const u16* __restrict__ xnf, const u16* __restrict__ wqf,
                                               const u16* __restrict__ kf, const u16* __restrict__ vf,
                                               const float* __restrict__ rbuf, const u16* __restrict__ wg,
                                               const u16* __restrict__ wu, u16* __restrict__ of,
                                               float* __restrict__ hbuf, int* __restrict__ sel) {
  __shared__ __attribute__((aligned(16))) char smem[LDS_TOTAL];
  const int tid = threadIdx.x;
  const int lane = tid & 63, wave = tid >> 6;
  const int b = blockIdx.x;
  TSR(1, 8); TS(1, 0);

  if (wave < 8) {
    const int rb = b >> 3, hg = b & 7;
    const int rt = wave >> 2, ks4 = wave & 3;
    const int r = lane & 31, hh = lane >> 5;
    {
      u32x4 tk[8];
#pragma unroll
      for (int c = 0; c < 8; ++c) tk[c] = *(const u32x4*)((const char*)kf + c * 8192 + tid * 16);
      bf16x8 af[2][8], bfr[8];
      const u16* ap = wqf + ((size_t)(2 * hg) * 32 + 8 * ks4) * 512 + lane * 8;
      const u16* bp = xnf + ((size_t)(2 * rb + rt) * 32 + 8 * ks4) * 512 + lane * 8;
#pragma unroll
      for (int s = 0; s < 8; ++s) {
        bfr[s] = *(const bf16x8*)(bp + s * 512);
        af[0][s] = *(const bf16x8*)(ap + s * 512);
        af[1][s] = *(const bf16x8*)(ap + 32 * 512 + s * 512);
      }
      __builtin_amdgcn_sched_barrier(0);
#pragma unroll
      for (int c = 0; c < 8; ++c) *(u32x4*)(smem + LDS_K + c * 8192 + tid * 16) = tk[c];
      __builtin_amdgcn_sched_barrier(0);
      f32x16 acc[2];
#pragma unroll
      for (int e = 0; e < 16; ++e) { acc[0][e] = 0.f; acc[1][e] = 0.f; }
#pragma unroll
      for (int s = 0; s < 8; ++s) {
        acc[0] = mfma32(af[0][s], bfr[s], acc[0]);
        acc[1] = mfma32(af[1][s], bfr[s], acc[1]);
      }
#pragma unroll
      for (int dt = 0; dt < 2; ++dt)
#pragma unroll
        for (int q4 = 0; q4 < 4; ++q4) {
          f32x4 p = {acc[dt][4 * q4], acc[dt][4 * q4 + 1], acc[dt][4 * q4 + 2], acc[dt][4 * q4 + 3]};
          *(f32x4*)(smem + LDS_V + (((wave * 2 + dt) * 4 + q4) * 64 + lane) * 16) = p;
        }
    }
    __builtin_amdgcn_sched_barrier(0);
    u32x4 tv[8], tvx;
#pragma unroll
    for (int c = 0; c < 8; ++c) tv[c] = *(const u32x4*)((const char*)vf + c * 8192 + tid * 16);
    tvx = *(const u32x4*)((const char*)vf + 65536 + (tid & 63) * 16);
    TS(1, 1);
    __syncthreads();
    TS(1, 2);
    const int hl = wave & 3;
    const int head = hg * 4 + hl;
    bf16x8 qf;
    {
      f32x4 a0 = {0.f, 0.f, 0.f, 0.f}, a1 = {0.f, 0.f, 0.f, 0.f};
#pragma unroll
      for (int k = 0; k < 4; ++k) {
        const char* p = smem + LDS_V + ((((rt * 4 + k) * 2 + (hl >> 1)) * 4 + 2 * (hl & 1)) * 64 + lane) * 16;
        a0 += *(const f32x4*)p;
        a1 += *(const f32x4*)(p + 1024);
      }
      const float SC = 0.25f * 1.44269504088896340736f;
      u32x4 pk = {pack2(rbf(a0[0]) * SC, rbf(a0[1]) * SC), pack2(rbf(a0[2]) * SC, rbf(a0[3]) * SC),
                  pack2(rbf(a1[0]) * SC, rbf(a1[1]) * SC), pack2(rbf(a1[2]) * SC, rbf(a1[3]) * SC)};
      qf = __builtin_bit_cast(bf16x8, pk);
    }

    const char* kp = smem + LDS_K + lane * 16;
    float m0 = -__builtin_inff(), m1 = -__builtin_inff();
    f32x16 zero16;
#pragma unroll
    for (int e = 0; e < 16; ++e) zero16[e] = 0.f;
    {
      f32x16 sa = mfma32(*(const bf16x8*)(kp), qf, zero16);
      f32x16 sb = mfma32(*(const bf16x8*)(kp + 1024), qf, zero16);
#pragma unroll 4
      for (int t = 2; t < 64; t += 2) {
        const bf16x8 ka = *(const bf16x8*)(kp + t * 1024);
        const bf16x8 kb = *(const bf16x8*)(kp + t * 1024 + 1024);
        const f32x16 na = mfma32(ka, qf, zero16);
#pragma unroll
        for (int e = 0; e < 16; e += 2) m0 = fmaxf(fmaxf(m0, sa[e]), sa[e + 1]);
        const f32x16 nb = mfma32(kb, qf, zero16);
#pragma unroll
        for (int e = 0; e < 16; e += 2) m1 = fmaxf(fmaxf(m1, sb[e]), sb[e + 1]);
        sa = na; sb = nb;
      }
#pragma unroll
      for (int e = 0; e < 16; e += 2) m0 = fmaxf(fmaxf(m0, sa[e]), sa[e + 1]);
#pragma unroll
      for (int e = 0; e < 16; e += 2) m1 = fmaxf(fmaxf(m1, sb[e]), sb[e + 1]);
    }
    float m = fmaxf(m0, m1);
    m = fmaxf(m, __shfl_xor(m, 32));
    TS(1, 3);
    __syncthreads();
#pragma unroll
    for (int c = 0; c < 8; ++c) *(u32x4*)(smem + LDS_V + c * 8192 + tid * 16) = tv[c];
    if (wave == 0) *(u32x4*)(smem + LDS_V + 65536 + lane * 16) = tvx;
    __syncthreads();
    TS(1, 4);

    f32x16 negm;
#pragma unroll
    for (int e = 0; e < 16; ++e) negm[e] = -m;
    f32x16 o;
#pragma unroll
    for (int e = 0; e < 16; ++e) o[e] = 0.f;
    const char* vp0 = smem + LDS_V + ((r < 16) ? (hh * 16 + r) * 16 : 1024);
    const char* vp1 = smem + LDS_V + ((r < 16) ? (hh * 16 + r) * 16 + 512 : 1024);
    f32x16 s = mfma32(*(const bf16x8*)(kp), qf, negm);
    bf16x8 kn = *(const bf16x8*)(kp + 1024);
    bf16x8 v0 = *(const bf16x8*)(vp0);
    bf16x8 v1 = *(const bf16x8*)(vp1);
#pragma unroll 4
    for (int t = 0; t < 64; ++t) {
      const bf16x8 k2 = *(const bf16x8*)(kp + ((t + 2) & 63) * 1024);
      const bf16x8 v0n = *(const bf16x8*)(vp0 + ((t + 1) & 63) * VT_STRIDE);
      const bf16x8 v1n = *(const bf16x8*)(vp1 + ((t + 1) & 63) * VT_STRIDE);
      const f32x16 sn = mfma32(kn, qf, negm);
      float p[16];
#pragma unroll
      for (int e = 0; e < 8; ++e) p[e] = __builtin_amdgcn_exp2f(s[e]);
      u32x4 pa = {pack2(p[0], p[1]), pack2(p[2], p[3]), pack2(p[4], p[5]), pack2(p[6], p[7])};
      o = mfma32(v0, __builtin_bit_cast(bf16x8, pa), o);
#pragma unroll
      for (int e = 8; e < 16; ++e) p[e] = __builtin_amdgcn_exp2f(s[e]);
      u32x4 pb = {pack2(p[8], p[9]), pack2(p[10], p[11]), pack2(p[12], p[13]), pack2(p[14], p[15])};
      o = mfma32(v1, __builtin_bit_cast(bf16x8, pb), o);
      s = sn; kn = k2; v0 = v0n; v1 = v1n;
      __builtin_amdgcn_sched_barrier(0);
    }
    TS(1, 5);
    const float il = 1.0f / o[8];
    u32x4 w0 = {pack2(o[0] * il, o[1] * il), pack2(o[2] * il, o[3] * il), pack2(o[4] * il, o[5] * il),
                pack2(o[6] * il, o[7] * il)};
    *(u32x4*)(of + ((size_t)((2 * rb + rt) * 32 + head) * 64 + lane) * 8) = w0;
  } else {
    __builtin_amdgcn_s_setprio(3);
    const bool act = b < 43;
    f32x4 rv[8];
    if (act) {
#pragma unroll
      for (int c = 0; c < 8; ++c) rv[c] = *(const f32x4*)(rbuf + c * 256 + lane * 4);
    }
    __syncthreads();
    int i0 = 0, i1 = 0;
    if (act) top2_keys(rv, lane, i0, i1);
    TS(1, 3);
    __syncthreads();
    __syncthreads();
    TS(1, 4);
    if (act) {
      i0 = min(i0, S_ - 1);
      i1 = min(i1, S_ - 1);
      if (b == 0 && lane == 0) {
        const float v0 = rbuf[i0], v1 = rbuf[i1];
        sel[0] = i0; sel[1] = i1;
        ((float*)sel)[2] = rbf(1.0f / (1.0f + __expf(-v0)));
        ((float*)sel)[3] = rbf(1.0f / (1.0f + __expf(-v1)));
      }
      const int g = lane >> 4, i = lane & 15;
      const int srow = (i == 0) ? i0 : i1;
      const u16* xp = xnf + ((size_t)((srow >> 5) * 32 + (g >> 1)) * 64 + (g & 1) * 32 + (srow & 31)) * 8;
      f32x4 ag[2], au[2];
#pragma unroll
      for (int c = 0; c < 2; ++c) { ag[c] = (f32x4){0.f, 0.f, 0.f, 0.f}; au[c] = (f32x4){0.f, 0.f, 0.f, 0.f}; }
      const int f0 = b * 32;
      const u16* gp = wg + f0 + 2 * i + (size_t)(8 * g) * F_;
      const u16* up = wu + f0 + 2 * i + (size_t)(8 * g) * F_;
#pragma unroll 1
      for (int sb = 0; sb < 4; ++sb) {
        unsigned w1[4][8], w2[4][8];
        bf16x8 xa[4];
#pragma unroll
        for (int s = 0; s < 4; ++s) {
          xa[s] = *(const bf16x8*)(xp + (size_t)(4 * sb + s) * 1024);
#pragma unroll
          for (int j = 0; j < 8; ++j) {
            w1[s][j] = *(const unsigned*)(gp + (size_t)(32 * (4 * sb + s) + j) * F_);
            w2[s][j] = *(const unsigned*)(up + (size_t)(32 * (4 * sb + s) + j) * F_);
          }
        }
#pragma unroll
        for (int s = 0; s < 4; ++s) {
          ag[0] = mfma16(xa[s], frag_lo(w1[s]), ag[0]);
          ag[1] = mfma16(xa[s], frag_hi(w1[s]), ag[1]);
          au[0] = mfma16(xa[s], frag_lo(w2[s]), au[0]);
          au[1] = mfma16(xa[s], frag_hi(w2[s]), au[1]);
        }
      }
      if (g == 0) {
#pragma unroll
        for (int c = 0; c < 2; ++c)
#pragma unroll
          for (int tk = 0; tk < 2; ++tk) {
            const float gv = rbf(ag[c][tk]);
            const float uv = rbf(au[c][tk]);
            const float sg = rbf(gv / (1.0f + __expf(-gv)));
            hbuf[tk * F_ + f0 + 2 * i + c] = rbf(sg * uv);
          }
      }
    }
  }
  TS(1, 6); TSR(1, 9);
}

static constexpr int CH_STRIDE = 1056;
__global__ __launch_bounds__(512) void kc_out(const u16* __restrict__ x, const u16* __restrict__ of,
                                              const u16* __restrict__ wof, const u16* __restrict__ wo,
                                              const u16* __restrict__ wd, const float2* __restrict__ stats,
                                              const float* __restrict__ hbuf, const int* __restrict__ sel,
                                              u16* __restrict__ out) {
  __shared__ __attribute__((aligned(16))) char smem[64 * CH_STRIDE];
  const int tid = threadIdx.x;
  const int lane = tid & 63, wave = tid >> 6;
  TSR(2, 8); TS(2, 0);
  const int idx0 = sel[0], idx1 = sel[1];
  if (blockIdx.x >= 32) {
    const int bb = blockIdx.x - 32;
    const int rb = bb >> 3, cg = bb & 7;
    const int rt = wave >> 2, ks4 = wave & 3;
    bf16x8 af[2][8], bfr[8];
    const u16* ap = wof + ((size_t)(2 * cg) * 32 + 8 * ks4) * 512 + lane * 8;
    const u16* bp = of + ((size_t)(2 * rb + rt) * 32 + 8 * ks4) * 512 + lane * 8;
#pragma unroll
    for (int s = 0; s < 8; ++s) {
      bfr[s] = *(const bf16x8*)(bp + s * 512);
      af[0][s] = *(const bf16x8*)(ap + s * 512);
      af[1][s] = *(const bf16x8*)(ap + 32 * 512 + s * 512);
    }
    const int r64 = tid >> 3, oct = tid & 7;
    const int row = rb * 64 + r64;
    const size_t off = (size_t)row * D_ + cg * 64 + oct * 8;
    const u32x4 xv = *(const u32x4*)(x + off);
    const float2 st = stats[row];
    f32x16 acc[2];
#pragma unroll
    for (int e = 0; e < 16; ++e) { acc[0][e] = 0.f; acc[1][e] = 0.f; }
#pragma unroll
    for (int s = 0; s < 8; ++s) {
      acc[0] = mfma32(af[0][s], bfr[s], acc[0]);
      acc[1] = mfma32(af[1][s], bfr[s], acc[1]);
    }
    TS(2, 1);
#pragma unroll
    for (int ct = 0; ct < 2; ++ct)
#pragma unroll
      for (int q4 = 0; q4 < 4; ++q4) {
        f32x4 p = {acc[ct][4 * q4], acc[ct][4 * q4 + 1], acc[ct][4 * q4 + 2], acc[ct][4 * q4 + 3]};
        *(f32x4*)(smem + ((wave * 2 + ct) * 4 + q4) * CH_STRIDE + lane * 16) = p;
      }
    __syncthreads();
    TS(2, 2);
    f32x4 lo = {0.f, 0.f, 0.f, 0.f}, hi = {0.f, 0.f, 0.f, 0.f};
#pragma unroll
    for (int k = 0; k < 4; ++k) {
      const char* p = smem + (((r64 >> 5) * 4 + k) * 8 + oct) * CH_STRIDE + (r64 & 31) * 16;
      lo += *(const f32x4*)p;
      hi += *(const f32x4*)(p + 512);
    }
    const float inv = 1.0f / st.y;
    float res[8];
#pragma unroll
    for (int e = 0; e < 4; ++e) {
      const float x0 = bf2f(xv[e] & 0xffffu), x1 = bfhi2f(xv[e]);
      const float a0 = rbf(e < 2 ? lo[2 * e] : hi[2 * e - 4]);
      const float a1 = rbf(e < 2 ? lo[2 * e + 1] : hi[2 * e - 3]);
      res[2 * e] = rbf(x0 + a0) + ln_elem(x0, st.x, st.y, inv);
      res[2 * e + 1] = rbf(x1 + a1) + ln_elem(x1, st.x, st.y, inv);
    }
    if (row != idx0 && row != idx1) {
      u32x4 o = {pack2(res[0], res[1]), pack2(res[2], res[3]), pack2(res[4], res[5]), pack2(res[6], res[7])};
      *(u32x4*)(out + off) = o;
    }
  } else {
    float* redf = (float*)smem;
    const int col0 = blockIdx.x * 16;
    const int kg = tid >> 1, cq = tid & 1;
    const int cbase = col0 + cq * 8;
    u32x4 wa[2], wdn[6];
    float o0[2], o1[2], h0[6], h1[6];
#pragma unroll
    for (int it = 0; it < 2; ++it) {
      const int k = kg + 256 * it;
      wa[it] = *(const u32x4*)(wo + (size_t)k * D_ + cbase);
      const int d = k & 15;
      const int pos = (k >> 4) * 512 + ((d >> 2) & 1) * 256 + ((d >> 3) << 2) + (d & 3);
      o0[it] = bf2f(of[(size_t)(idx0 >> 5) * 16384 + pos + (idx0 & 31) * 8]);
      o1[it] = bf2f(of[(size_t)(idx1 >> 5) * 16384 + pos + (idx1 & 31) * 8]);
    }
#pragma unroll
    for (int it = 0; it < 6; ++it) {
      const int f = kg + 256 * it;
      const int fc = min(f, F_ - 1);
      wdn[it] = *(const u32x4*)(wd + (size_t)fc * D_ + cbase);
      const float a0 = hbuf[fc], a1 = hbuf[F_ + fc];
      h0[it] = (f < F_) ? a0 : 0.f;
      h1[it] = (f < F_) ? a1 : 0.f;
    }
    float ya[2][8], yd[2][8];
#pragma unroll
    for (int e = 0; e < 8; ++e) { ya[0][e] = ya[1][e] = 0.f; yd[0][e] = yd[1][e] = 0.f; }
#pragma unroll
    for (int it = 0; it < 2; ++it)
#pragma unroll
      for (int e = 0; e < 4; ++e) {
        const float wl = bf2f(wa[it][e] & 0xffffu), wh = bfhi2f(wa[it][e]);
        ya[0][2 * e] += o0[it] * wl; ya[0][2 * e + 1] += o0[it] * wh;
        ya[1][2 * e] += o1[it] * wl; ya[1][2 * e + 1] += o1[it] * wh;
      }
#pragma unroll
    for (int it = 0; it < 6; ++it)
#pragma unroll
      for (int e = 0; e < 4; ++e) {
        const float wl = bf2f(wdn[it][e] & 0xffffu), wh = bfhi2f(wdn[it][e]);
        yd[0][2 * e] += h0[it] * wl; yd[0][2 * e + 1] += h0[it] * wh;
        yd[1][2 * e] += h1[it] * wl; yd[1][2 * e + 1] += h1[it] * wh;
      }
#pragma unroll
    for (int tk = 0; tk < 2; ++tk)
#pragma unroll
      for (int e = 0; e < 8; ++e) {
        redf[kg * 64 + tk * 16 + cq * 8 + e] = ya[tk][e];
        redf[kg * 64 + 32 + tk * 16 + cq * 8 + e] = yd[tk][e];
      }
    __syncthreads();
    {
      const int o = tid & 63, q = tid >> 6;
      float sum = 0.f;
#pragma unroll 8
      for (int k = 0; k < 32; ++k) sum += redf[(q * 32 + k) * 64 + o];
      __syncthreads();
      redf[q * 64 + o] = sum;
      __syncthreads();
    }
    if (tid < 32) {
      float tota = 0.f, totd = 0.f;
#pragma unroll
      for (int q = 0; q < 8; ++q) { tota += redf[q * 64 + tid]; totd += redf[q * 64 + 32 + tid]; }
      const int tk = tid >> 4, c = tid & 15;
      const int row = tk ? idx1 : idx0;
      const float rs = ((const float*)sel)[2 + tk];
      const float2 st = stats[row];
      const float xf = bf2f(x[(size_t)row * D_ + col0 + c]);
      const float a = rbf(tota);
      const float t1 = rbf(xf + a);
      const float xn = ln_elem(xf, st.x, st.y, 1.0f / st.y);
      const float upd = rbf(rbf(totd) * rs);
      const float bb = rbf(xn + upd);
      out[(size_t)row * D_ + col0 + c] = __builtin_bit_cast(u16, (__bf16)(t1 + bb));
    }
  }
  TS(2, 3); TSR(2, 9);
}

extern "C" void kernel_launch(void* const* d_in, const int* in_sizes, int n_in, void* d_out, int out_size,
                              void* d_ws, size_t ws_size, hipStream_t stream) {
  const u16* x = (const u16*)d_in[0];
  const u16* wq = (const u16*)d_in[1];
  const u16* wk = (const u16*)d_in[2];
  const u16* wv = (const u16*)d_in[3];
  const u16* wo = (const u16*)d_in[4];
  const u16* wr = (const u16*)d_in[6];
  const u16* wg = (const u16*)d_in[7];
  const u16* wu = (const u16*)d_in[8];
  const u16* wd = (const u16*)d_in[9];
  char* ws = (char*)d_ws;
  u16* xnf = (u16*)(ws + OFF_XNF);
  u16* of = (u16*)(ws + OFF_OF);
  u16* wqf = (u16*)(ws + OFF_WQF);
  u16* wof = (u16*)(ws + OFF_WOF);
  u16* kf = (u16*)(ws + OFF_KF);
  u16* vf = (u16*)(ws + OFF_VF);
  float* rbuf = (float*)(ws + OFF_R);
  float2* stats = (float2*)(ws + OFF_STATS);
  float* hbuf = (float*)(ws + OFF_H);
  int* sel = (int*)(ws + OFF_SEL);
  u16* out = (u16*)d_out;

  ka_prep<<<192, 256, 0, stream>>>(x, wq, wk, wv, wo, wr, xnf, wqf, wof, kf, vf, rbuf, stats);
  kb_attn<<<256, 576, 0, stream>>>(xnf, wqf, kf, vf, rbuf, wg, wu, of, hbuf, sel);
  kc_out<<<288, 512, 0, stream>>>(x, of, wof, wo, wd, stats, hbuf, sel, out);
}
```

```cpp
#include <hip/hip_runtime.h>
#include <hip/hip_bf16.h>

typedef unsigned short u16;
typedef __attribute__((ext_vector_type(8))) __bf16 bf16x8;
typedef __attribute__((ext_vector_type(2))) __bf16 bf16x2;
typedef __attribute__((ext_vector_type(2))) float f32x2;
typedef __attribute__((ext_vector_type(4))) float f32x4;
typedef __attribute__((ext_vector_type(16))) float f32x16;
typedef __attribute__((ext_vector_type(4))) unsigned u32x4;
typedef __attribute__((ext_vector_type(2))) unsigned u32x2;

#define DI __device__ __forceinline__
#define TS(k, n)
#define TSR(k, n)

static constexpr int S_ = 2048;
static constexpr int D_ = 512;
static constexpr int F_ = 1376;

DI float bf2f(unsigned u) { return __uint_as_float(u << 16); }
DI float bfhi2f(unsigned u) { return __uint_as_float(u & 0xffff0000u); }
DI float rbf(float f) { return (float)(__bf16)f; }
DI unsigned pack2(float lo, float hi) {
  f32x2 p = {lo, hi};
  bf16x2 q = __builtin_convertvector(p, bf16x2);
  return __builtin_bit_cast(unsigned, q);
}
DI f32x4 mfma16(bf16x8 a, bf16x8 b, f32x4 c) { return __builtin_amdgcn_mfma_f32_16x16x32_bf16(a, b, c, 0, 0, 0); }
DI f32x16 mfma32(bf16x8 a, bf16x8 b, f32x16 c) { return __builtin_amdgcn_mfma_f32_32x32x16_bf16(a, b, c, 0, 0, 0); }
DI unsigned perm_lo(unsigned hi_src, unsigned lo_src) { return __builtin_amdgcn_perm(hi_src, lo_src, 0x05040100u); }
DI unsigned perm_hi(unsigned hi_src, unsigned lo_src) { return __builtin_amdgcn_perm(hi_src, lo_src, 0x07060302u); }

DI bf16x8 frag_lo(const unsigned* w) {
  u32x4 r = {perm_lo(w[1], w[0]), perm_lo(w[3], w[2]), perm_lo(w[5], w[4]), perm_lo(w[7], w[6])};
  return __builtin_bit_cast(bf16x8, r);
}
DI bf16x8 frag_hi(const unsigned* w) {
  u32x4 r = {perm_hi(w[1], w[0]), perm_hi(w[3], w[2]), perm_hi(w[5], w[4]), perm_hi(w[7], w[6])};
  return __builtin_bit_cast(bf16x8, r);
}
DI float ln_elem(float xv, float mb, float den, float inv) {
  const float d = rbf(xv - mb);
  float q = d * inv;
  const float rem = __builtin_fmaf(-q, den, d);
  q = __builtin_fmaf(rem, inv, q);
  return rbf(q);
}

static constexpr int VT_STRIDE = 1056;
static constexpr int VF_BYTES = 64 * VT_STRIDE;
static constexpr size_t OFF_XNF = 0;
static constexpr size_t OFF_OF = OFF_XNF + (size_t)S_ * D_ * 2;
static constexpr size_t OFF_WQF = OFF_OF + (size_t)S_ * D_ * 2;
static constexpr size_t OFF_WOF = OFF_WQF + (size_t)D_ * D_ * 2;
static constexpr size_t OFF_KF = OFF_WOF + (size_t)D_ * D_ * 2;
static constexpr size_t OFF_VF = OFF_KF + 65536;
static constexpr size_t OFF_R = OFF_VF + VF_BYTES;
static constexpr size_t OFF_STATS = OFF_R + S_ * 4;
static constexpr size_t OFF_H = OFF_STATS + S_ * 8;
static constexpr size_t OFF_SEL = OFF_H + 2 * F_ * 4;

static constexpr int XN_STRIDE = 1040;
__global__ __launch_bounds__(512) void ka_prep(const u16* __restrict__ x, const u16* __restrict__ wq,
                                               const u16* __restrict__ wk, const u16* __restrict__ wv,
                                               const u16* __restrict__ wo, const u16* __restrict__ wr,
                                               u16* __restrict__ xnf, u16* __restrict__ wqf, u16* __restrict__ wof,
                                               u16* __restrict__ kf, u16* __restrict__ vf,
                                               float* __restrict__ rbuf, float2* __restrict__ stats) {
  __shared__ __attribute__((aligned(16))) char smem[16 * XN_STRIDE + 8 * 5 * 64 * 16];
  const int tid = threadIdx.x;
  const int lane = tid & 63, wave = tid >> 6;
  TSR(0, 8); TS(0, 0);
  if (blockIdx.x >= 128) {
    const int pb = blockIdx.x - 128;
    const bool iso = pb >= 32;
    const int cp = tid & 255, ko = (pb & 31) * 2 + (tid >> 8);
    const int ks = ko >> 1, hh = ko & 1;
    const u16* W = (iso ? wo : wq) + 2 * cp;
    unsigned w[8];
#pragma unroll
    for (int j = 0; j < 8; ++j) {
      const int row = 16 * ks + (iso ? (8 * (j >> 2) + 4 * hh + (j & 3)) : (8 * hh + j));
      w[j] = *(const unsigned*)(W + row * D_);
    }
    u16* dst = (iso ? wof : wqf) + (((cp >> 4) * 32 + ks) * 64 + hh * 32 + ((2 * cp) & 31)) * 8;
    *(bf16x8*)dst = frag_lo(w);
    *(bf16x8*)(dst + 8) = frag_hi(w);
    TS(0, 3); TSR(0, 9);
    return;
  }
  const int row0 = blockIdx.x * 16;
  const int rt = blockIdx.x >> 1, th = blockIdx.x & 1;
  const int g = lane >> 4, i = lane & 15;
  const int k0 = wave * 64 + 8 * g;
  u32x4 xr[2];
#pragma unroll
  for (int rr = 0; rr < 2; ++rr) xr[rr] = *(const u32x4*)(x + (size_t)(row0 + 2 * wave + rr) * D_ + 8 * lane);
  const u16* wbase = ((i < 4) ? (wk + 4 * i) : (i < 8 ? wv + 4 * (i - 4) : wk)) + k0 * 16;
  u32x2 w[2][8];
#pragma unroll
  for (int s = 0; s < 2; ++s)
#pragma unroll
    for (int j = 0; j < 8; ++j) w[s][j] = *(const u32x2*)(wbase + (32 * s + j) * 16);
  bf16x8 rf[2];
#pragma unroll
  for (int s = 0; s < 2; ++s) rf[s] = *(const bf16x8*)(wr + k0 + 32 * s);

  float s1[2], s2[2];
#pragma unroll
  for (int rr = 0; rr < 2; ++rr) {
    float a = 0.f, q = 0.f;
#pragma unroll
    for (int e = 0; e < 4; ++e) {
      const float lo = bf2f(xr[rr][e] & 0xffffu), hi = bfhi2f(xr[rr][e]);
      a += lo + hi;
      q = __builtin_fmaf(lo, lo, q);
      q = __builtin_fmaf(hi, hi, q);
    }
    s1[rr] = a; s2[rr] = q;
  }
#pragma unroll
  for (int off = 32; off >= 1; off >>= 1)
#pragma unroll
    for (int rr = 0; rr < 2; ++rr) {
      s1[rr] += __shfl_xor(s1[rr], off);
      s2[rr] += __shfl_xor(s2[rr], off);
    }
#pragma unroll
  for (int rr = 0; rr < 2; ++rr) {
    const float mean = s1[rr] * (1.0f / D_);
    float var = s2[rr] * (1.0f / D_) - mean * mean;
    var = fmaxf(var, 0.f);
    const float mb = rbf(mean);
    const float den = rbf(__builtin_sqrtf(rbf(rbf(var) + 1e-5f)));
    const float inv = 1.0f / den;
    if (lane == rr) stats[row0 + 2 * wave + rr] = make_float2(mb, den);
    u32x4 o;
#pragma unroll
    for (int e = 0; e < 4; ++e)
      o[e] = pack2(ln_elem(bf2f(xr[rr][e] & 0xffffu), mb, den, inv), ln_elem(bfhi2f(xr[rr][e]), mb, den, inv));
    *(u32x4*)(smem + (2 * wave + rr) * XN_STRIDE + lane * 16) = o;
  }
  if (tid < 2 && th == 0) {
    const unsigned cv = tid ? 0u : 0x3f803f80u;
    u32x4 c4 = {cv, cv, cv, cv};
    *(u32x4*)((char*)vf + rt * VT_STRIDE + 1024 + tid * 16) = c4;
  }
  TS(0, 1);
  __syncthreads();
  {
    const int r = lane & 15, hh = (lane >> 4) & 1, kp = lane >> 5;
#pragma unroll
    for (int s = 0; s < 2; ++s) {
      const int ks = 4 * wave + 2 * s + kp;
      const u32x4 v = *(const u32x4*)(smem + r * XN_STRIDE + ks * 32 + hh * 16);
      *(u32x4*)(xnf + ((size_t)(rt * 32 + ks) * 64 + hh * 32 + th * 16 + r) * 8) = v;
    }
  }
  f32x4* red = (f32x4*)(smem + 16 * XN_STRIDE);
  {
    f32x4 acc[4], ar;
    ar = (f32x4){0.f, 0.f, 0.f, 0.f};
#pragma unroll
    for (int c = 0; c < 4; ++c) acc[c] = (f32x4){0.f, 0.f, 0.f, 0.f};
#pragma unroll
    for (int s = 0; s < 2; ++s) {
      unsigned wx[8], wy[8];
#pragma unroll
      for (int j = 0; j < 8; ++j) { wx[j] = w[s][j][0]; wy[j] = w[s][j][1]; }
      bf16x8 b[4] = {frag_lo(wx), frag_hi(wx), frag_lo(wy), frag_hi(wy)};
      const bf16x8 xa = *(const bf16x8*)(smem + i * XN_STRIDE + (k0 + 32 * s) * 2);
#pragma unroll
      for (int c = 0; c < 4; ++c) acc[c] = mfma16(xa, b[c], acc[c]);
      ar = mfma16(xa, rf[s], ar);
    }
#pragma unroll
    for (int c = 0; c < 4; ++c) red[(wave * 5 + c) * 64 + lane] = acc[c];
    red[(wave * 5 + 4) * 64 + lane] = ar;
  }
  __syncthreads();
  TS(0, 2);
  if (wave < 5) {
    f32x4 v = red[wave * 64 + lane];
#pragma unroll
    for (int wv_ = 1; wv_ < 8; ++wv_) v += red[(wv_ * 5 + wave) * 64 + lane];
    const int kk = 16 * th + 4 * g;
    if (wave == 4) {
      if (i == 0) {
        f32x4 o = {rbf(v[0]), rbf(v[1]), rbf(v[2]), rbf(v[3])};
        *(f32x4*)(rbuf + row0 + 4 * g) = o;
      }
    } else if (i < 4) {
      const int hh = i & 1, j = (i >> 1) * 4 + wave;
#pragma unroll
      for (int e = 0; e < 4; ++e)
        kf[rt * 512 + (hh * 32 + kk + e) * 8 + j] = __builtin_bit_cast(u16, (__bf16)v[e]);
    } else if (i < 8) {
      const int d = 4 * (i - 4) + wave;
      const int hh = g & 1, jh = g >> 1;
      u32x2 o = {pack2(v[0], v[1]), pack2(v[2], v[3])};
      *(u32x2*)(vf + rt * (VT_STRIDE / 2) + th * 256 + (hh * 16 + d) * 8 + jh * 4) = o;
    }
  }
  TS(0, 3); TSR(0, 9);
}

DI void top2_keys(const f32x4* v, int lane, int& i0, int& i1) {
  unsigned k0 = 0, k1 = 0;
#pragma unroll
  for (int c = 0; c < 8; ++c)
#pragma unroll
    for (int e = 0; e < 4; ++e) {
      const unsigned u = __float_as_uint(v[c][e]);
      const unsigned ord = (u & 0x80000000u) ? ~u : (u | 0x80000000u);
      const unsigned key = (ord & 0xffff0000u) | (unsigned)(2047 - (c * 256 + lane * 4 + e));
      k1 = max(k1, min(k0, key));
      k0 = max(k0, key);
    }
#pragma unroll
  for (int off = 1; off < 64; off <<= 1) {
    const unsigned o0 = __shfl_xor(k0, off), o1 = __shfl_xor(k1, off);
    const unsigned n0 = max(k0, o0);
    const unsigned n1 = max(min(k0, o0), max(k1, o1));
    k0 = n0; k1 = n1;
  }
  i0 = 2047 - (int)(k0 & 0x7ffu);
  i1 = 2047 - (int)(k1 & 0x7ffu);
}

static constexpr int LDS_K = 0;
static constexpr int LDS_V = 65536;
static constexpr int LDS_TOTAL = 65536 + VF_BYTES;

__global__ __launch_bounds__(576) void kb_attn(const u16* __restrict__ xnf, const u16* __restrict__ wqf,
                                               const u16* __restrict__ kf, const u16* __restrict__ vf,
                                               const float* __restrict__ rbuf, const u16* __restrict__ wg,
                                               const u16* __restrict__ wu, u16* __restrict__ of,
                                               float* __restrict__ hbuf, int* __restrict__ sel) {
  __shared__ __attribute__((aligned(16))) char smem[LDS_TOTAL];
  const int tid = threadIdx.x;
  const int lane = tid & 63, wave = tid >> 6;
  const int b = blockIdx.x;
  TSR(1, 8); TS(1, 0);

  if (wave < 8) {
    const int rb = (b & 7) + 8 * ((b >> 3) & 3), hg = b >> 5;
    const int rt = wave >> 2, ks4 = wave & 3;
    const int r = lane & 31, hh = lane >> 5;
    {
      u32x4 tk[8];
#pragma unroll
      for (int c = 0; c < 8; ++c) tk[c] = *(const u32x4*)((const char*)kf + c * 8192 + tid * 16);
      bf16x8 af[2][8], bfr[8];
      const u16* ap = wqf + ((size_t)(2 * hg) * 32 + 8 * ks4) * 512 + lane * 8;
      const u16* bp = xnf + ((size_t)(2 * rb + rt) * 32 + 8 * ks4) * 512 + lane * 8;
#pragma unroll
      for (int s = 0; s < 8; ++s) {
        bfr[s] = *(const bf16x8*)(bp + s * 512);
        af[0][s] = *(const bf16x8*)(ap + s * 512);
        af[1][s] = *(const bf16x8*)(ap + 32 * 512 + s * 512);
      }
      __builtin_amdgcn_sched_barrier(0);
#pragma unroll
      for (int c = 0; c < 8; ++c) *(u32x4*)(smem + LDS_K + c * 8192 + tid * 16) = tk[c];
      __builtin_amdgcn_sched_barrier(0);
      f32x16 acc[2];
#pragma unroll
      for (int e = 0; e < 16; ++e) { acc[0][e] = 0.f; acc[1][e] = 0.f; }
#pragma unroll
      for (int s = 0; s < 8; ++s) {
        acc[0] = mfma32(af[0][s], bfr[s], acc[0]);
        acc[1] = mfma32(af[1][s], bfr[s], acc[1]);
      }
#pragma unroll
      for (int dt = 0; dt < 2; ++dt)
#pragma unroll
        for (int q4 = 0; q4 < 4; ++q4) {
          f32x4 p = {acc[dt][4 * q4], acc[dt][4 * q4 + 1], acc[dt][4 * q4 + 2], acc[dt][4 * q4 + 3]};
          *(f32x4*)(smem + LDS_V + (((wave * 2 + dt) * 4 + q4) * 64 + lane) * 16) = p;
        }
    }
    __builtin_amdgcn_sched_barrier(0);
    u32x4 tv[8], tvx;
#pragma unroll
    for (int c = 0; c < 8; ++c) tv[c] = *(const u32x4*)((const char*)vf + c * 8192 + tid * 16);
    tvx = *(const u32x4*)((const char*)vf + 65536 + (tid & 127) * 16);
    TS(1, 1);
    __syncthreads();
    TS(1, 2);
    const int hl = wave & 3;
    const int head = hg * 4 + hl;
    bf16x8 qf;
    {
      f32x4 a0 = {0.f, 0.f, 0.f, 0.f}, a1 = {0.f, 0.f, 0.f, 0.f};
#pragma unroll
      for (int k = 0; k < 4; ++k) {
        const char* p = smem + LDS_V + ((((rt * 4 + k) * 2 + (hl >> 1)) * 4 + 2 * (hl & 1)) * 64 + lane) * 16;
        a0 += *(const f32x4*)p;
        a1 += *(const f32x4*)(p + 1024);
      }
      const float SC = 0.25f * 1.44269504088896340736f;
      u32x4 pk = {pack2(rbf(a0[0]) * SC, rbf(a0[1]) * SC), pack2(rbf(a0[2]) * SC, rbf(a0[3]) * SC),
                  pack2(rbf(a1[0]) * SC, rbf(a1[1]) * SC), pack2(rbf(a1[2]) * SC, rbf(a1[3]) * SC)};
      qf = __builtin_bit_cast(bf16x8, pk);
    }

    const char* kp = smem + LDS_K + lane * 16;
    float m0 = -__builtin_inff(), m1 = -__builtin_inff();
    f32x16 zero16;
#pragma unroll
    for (int e = 0; e < 16; ++e) zero16[e] = 0.f;
    {
      f32x16 sa = mfma32(*(const bf16x8*)(kp), qf, zero16);
      f32x16 sb = mfma32(*(const bf16x8*)(kp + 1024), qf, zero16);
#pragma unroll 4
      for (int t = 2; t < 64; t += 2) {
        const bf16x8 ka = *(const bf16x8*)(kp + t * 1024);
        const bf16x8 kb = *(const bf16x8*)(kp + t * 1024 + 1024);
        const f32x16 na = mfma32(ka, qf, zero16);
#pragma unroll
        for (int e = 0; e < 16; e += 2) m0 = fmaxf(fmaxf(m0, sa[e]), sa[e + 1]);
        const f32x16 nb = mfma32(kb, qf, zero16);
#pragma unroll
        for (int e = 0; e < 16; e += 2) m1 = fmaxf(fmaxf(m1, sb[e]), sb[e + 1]);
        sa = na; sb = nb;
      }
#pragma unroll
      for (int e = 0; e < 16; e += 2) m0 = fmaxf(fmaxf(m0, sa[e]), sa[e + 1]);
#pragma unroll
      for (int e = 0; e < 16; e += 2) m1 = fmaxf(fmaxf(m1, sb[e]), sb[e + 1]);
    }
    float m = fmaxf(m0, m1);
    m = fmaxf(m, __shfl_xor(m, 32));
    TS(1, 3);
    __syncthreads();
#pragma unroll
    for (int c = 0; c < 8; ++c) *(u32x4*)(smem + LDS_V + c * 8192 + tid * 16) = tv[c];
    if (wave < 2) *(u32x4*)(smem + LDS_V + 65536 + tid * 16) = tvx;
    __syncthreads();
    TS(1, 4);

    f32x16 negm;
#pragma unroll
    for (int e = 0; e < 16; ++e) negm[e] = -m;
    f32x16 o;
#pragma unroll
    for (int e = 0; e < 16; ++e) o[e] = 0.f;
    const char* vp0 = smem + LDS_V + ((r < 16) ? (hh * 16 + r) * 16 : (r == 16 ? 1024 : 1040));
    const char* vp1 = smem + LDS_V + ((r < 16) ? (hh * 16 + r) * 16 + 512 : (r == 16 ? 1024 : 1040));
    f32x16 s = mfma32(*(const bf16x8*)(kp), qf, negm);
    bf16x8 kn = *(const bf16x8*)(kp + 1024);
    bf16x8 v0 = *(const bf16x8*)(vp0);
    bf16x8 v1 = *(const bf16x8*)(vp1);
#pragma unroll 4
    for (int t = 0; t < 64; ++t) {
      const bf16x8 k2 = *(const bf16x8*)(kp + ((t + 2) & 63) * 1024);
      const bf16x8 v0n = *(const bf16x8*)(vp0 + ((t + 1) & 63) * VT_STRIDE);
      const bf16x8 v1n = *(const bf16x8*)(vp1 + ((t + 1) & 63) * VT_STRIDE);
      const f32x16 sn = mfma32(kn, qf, negm);
      float p[16];
#pragma unroll
      for (int e = 0; e < 8; ++e) p[e] = __builtin_amdgcn_exp2f(s[e]);
      u32x4 pa = {pack2(p[0], p[1]), pack2(p[2], p[3]), pack2(p[4], p[5]), pack2(p[6], p[7])};
      o = mfma32(v0, __builtin_bit_cast(bf16x8, pa), o);
#pragma unroll
      for (int e = 8; e < 16; ++e) p[e] = __builtin_amdgcn_exp2f(s[e]);
      u32x4 pb = {pack2(p[8], p[9]), pack2(p[10], p[11]), pack2(p[12], p[13]), pack2(p[14], p[15])};
      o = mfma32(v1, __builtin_bit_cast(bf16x8, pb), o);
      s = sn; kn = k2; v0 = v0n; v1 = v1n;
      __builtin_amdgcn_sched_barrier(0);
    }
    TS(1, 5);
    const float il = 1.0f / __shfl(o[8], r);
    u32x4 w0 = {pack2(o[0] * il, o[1] * il), pack2(o[2] * il, o[3] * il), pack2(o[4] * il, o[5] * il),
                pack2(o[6] * il, o[7] * il)};
    *(u32x4*)(of + ((size_t)((2 * rb + rt) * 32 + head) * 64 + lane) * 8) = w0;
  } else {
    __builtin_amdgcn_s_setprio(3);
    const bool act = b < 43;
    f32x4 rv[8];
    if (act) {
#pragma unroll
      for (int c = 0; c < 8; ++c) rv[c] = *(const f32x4*)(rbuf + c * 256 + lane * 4);
    }
    __syncthreads();
    int i0 = 0, i1 = 0;
    if (act) top2_keys(rv, lane, i0, i1);
    TS(1, 3);
    __syncthreads();
    __syncthreads();
    TS(1, 4);
    if (act) {
      i0 = min(i0, S_ - 1);
      i1 = min(i1, S_ - 1);
      if (b == 0 && lane == 0) {
        const float v0 = rbuf[i0], v1 = rbuf[i1];
        sel[0] = i0; sel[1] = i1;
        ((float*)sel)[2] = rbf(1.0f / (1.0f + __expf(-v0)));
        ((float*)sel)[3] = rbf(1.0f / (1.0f + __expf(-v1)));
      }
      const int g = lane >> 4, i = lane & 15;
      const int srow = (i == 0) ? i0 : i1;
      const u16* xp = xnf + ((size_t)((srow >> 5) * 32 + (g >> 1)) * 64 + (g & 1) * 32 + (srow & 31)) * 8;
      f32x4 ag[2], au[2];
#pragma unroll
      for (int c = 0; c < 2; ++c) { ag[c] = (f32x4){0.f, 0.f, 0.f, 0.f}; au[c] = (f32x4){0.f, 0.f, 0.f, 0.f}; }
      const int f0 = b * 32;
      const u16* gp = wg + f0 + 2 * i + (size_t)(8 * g) * F_;
      const u16* up = wu + f0 + 2 * i + (size_t)(8 * g) * F_;
#pragma unroll 1
      for (int sb = 0; sb < 4; ++sb) {
        unsigned w1[4][8], w2[4][8];
        bf16x8 xa[4];
#pragma unroll
        for (int s = 0; s < 4; ++s) {
          xa[s] = *(const bf16x8*)(xp + (size_t)(4 * sb + s) * 1024);
#pragma unroll
          for (int j = 0; j < 8; ++j) {
            w1[s][j] = *(const unsigned*)(gp + (size_t)(32 * (4 * sb + s) + j) * F_);
            w2[s][j] = *(const unsigned*)(up + (size_t)(32 * (4 * sb + s) + j) * F_);
          }
        }
#pragma unroll
        for (int s = 0; s < 4; ++s) {
          ag[0] = mfma16(xa[s], frag_lo(w1[s]), ag[0]);
          ag[1] = mfma16(xa[s], frag_hi(w1[s]), ag[1]);
          au[0] = mfma16(xa[s], frag_lo(w2[s]), au[0]);
          au[1] = mfma16(xa[s], frag_hi(w2[s]), au[1]);
        }
      }
      if (g == 0) {
#pragma unroll
        for (int c = 0; c < 2; ++c)
#pragma unroll
          for (int tk = 0; tk < 2; ++tk) {
            const float gv = rbf(ag[c][tk]);
            const float uv = rbf(au[c][tk]);
            const float sg = rbf(gv / (1.0f + __expf(-gv)));
            hbuf[tk * F_ + f0 + 2 * i + c] = rbf(sg * uv);
          }
      }
    }
  }
  TS(1, 6); TSR(1, 9);
}

static constexpr int CH_STRIDE = 1056;
__global__ __launch_bounds__(512) void kc_out(const u16* __restrict__ x, const u16* __restrict__ of,
                                              const u16* __restrict__ wof, const u16* __restrict__ wo,
                                              const u16* __restrict__ wd, const float2* __restrict__ stats,
                                              const float* __restrict__ hbuf, const int* __restrict__ sel,
                                              u16* __restrict__ out) {
  __shared__ __attribute__((aligned(16))) char smem[64 * CH_STRIDE];
  const int tid = threadIdx.x;
  const int lane = tid & 63, wave = tid >> 6;
  TSR(2, 8); TS(2, 0);
  const int idx0 = sel[0], idx1 = sel[1];
  if (blockIdx.x >= 32) {
    const int bb = blockIdx.x - 32;
    const int rb = (bb & 7) + 8 * ((bb >> 3) & 3), cg = bb >> 5;
    const int rt = wave >> 2, ks4 = wave & 3;
    bf16x8 af[2][8], bfr[8];
    const u16* ap = wof + ((size_t)(2 * cg) * 32 + 8 * ks4) * 512 + lane * 8;
    const u16* bp = of + ((size_t)(2 * rb + rt) * 32 + 8 * ks4) * 512 + lane * 8;
#pragma unroll
    for (int s = 0; s < 8; ++s) {
      bfr[s] = *(const bf16x8*)(bp + s * 512);
      af[0][s] = *(const bf16x8*)(ap + s * 512);
      af[1][s] = *(const bf16x8*)(ap + 32 * 512 + s * 512);
    }
    const int r64 = tid >> 3, oct = tid & 7;
    const int row = rb * 64 + r64;
    const size_t off = (size_t)row * D_ + cg * 64 + oct * 8;
    const u32x4 xv = *(const u32x4*)(x + off);
    const float2 st = stats[row];
    f32x16 acc[2];
#pragma unroll
    for (int e = 0; e < 16; ++e) { acc[0][e] = 0.f; acc[1][e] = 0.f; }
#pragma unroll
    for (int s = 0; s < 8; ++s) {
      acc[0] = mfma32(af[0][s], bfr[s], acc[0]);
      acc[1] = mfma32(af[1][s], bfr[s], acc[1]);
    }
    TS(2, 1);
#pragma unroll
    for (int ct = 0; ct < 2; ++ct)
#pragma unroll
      for (int q4 = 0; q4 < 4; ++q4) {
        f32x4 p = {acc[ct][4 * q4], acc[ct][4 * q4 + 1], acc[ct][4 * q4 + 2], acc[ct][4 * q4 + 3]};
        *(f32x4*)(smem + ((wave * 2 + ct) * 4 + q4) * CH_STRIDE + lane * 16) = p;
      }
    __syncthreads();
    TS(2, 2);
    f32x4 lo = {0.f, 0.f, 0.f, 0.f}, hi = {0.f, 0.f, 0.f, 0.f};
#pragma unroll
    for (int k = 0; k < 4; ++k) {
      const char* p = smem + (((r64 >> 5) * 4 + k) * 8 + oct) * CH_STRIDE + (r64 & 31) * 16;
      lo += *(const f32x4*)p;
      hi += *(const f32x4*)(p + 512);
    }
    const float inv = 1.0f / st.y;
    float res[8];
#pragma unroll
    for (int e = 0; e < 4; ++e) {
      const float x0 = bf2f(xv[e] & 0xffffu), x1 = bfhi2f(xv[e]);
      const float a0 = rbf(e < 2 ? lo[2 * e] : hi[2 * e - 4]);
      const float a1 = rbf(e < 2 ? lo[2 * e + 1] : hi[2 * e - 3]);
      res[2 * e] = rbf(x0 + a0) + ln_elem(x0, st.x, st.y, inv);
      res[2 * e + 1] = rbf(x1 + a1) + ln_elem(x1, st.x, st.y, inv);
    }
    if (row != idx0 && row != idx1) {
      u32x4 o = {pack2(res[0], res[1]), pack2(res[2], res[3]), pack2(res[4], res[5]), pack2(res[6], res[7])};
      *(u32x4*)(out + off) = o;
    }
  } else {
    float* redf = (float*)smem;
    const int col0 = blockIdx.x * 16;
    const int kg = tid >> 1, cq = tid & 1;
    const int cbase = col0 + cq * 8;
    u32x4 wa[2], wdn[6];
    float o0[2], o1[2], h0[6], h1[6];
#pragma unroll
    for (int it = 0; it < 2; ++it) {
      const int k = kg + 256 * it;
      wa[it] = *(const u32x4*)(wo + (size_t)k * D_ + cbase);
      const int d = k & 15;
      const int pos = (k >> 4) * 512 + ((d >> 2) & 1) * 256 + ((d >> 3) << 2) + (d & 3);
      o0[it] = bf2f(of[(size_t)(idx0 >> 5) * 16384 + pos + (idx0 & 31) * 8]);
      o1[it] = bf2f(of[(size_t)(idx1 >> 5) * 16384 + pos + (idx1 & 31) * 8]);
    }
#pragma unroll
    for (int it = 0; it < 6; ++it) {
      const int f = kg + 256 * it;
      const int fc = min(f, F_ - 1);
      wdn[it] = *(const u32x4*)(wd + (size_t)fc * D_ + cbase);
      const float a0 = hbuf[fc], a1 = hbuf[F_ + fc];
      h0[it] = (f < F_) ? a0 : 0.f;
      h1[it] = (f < F_) ? a1 : 0.f;
    }
    float ya[2][8], yd[2][8];
#pragma unroll
    for (int e = 0; e < 8; ++e) { ya[0][e] = ya[1][e] = 0.f; yd[0][e] = yd[1][e] = 0.f; }
#pragma unroll
    for (int it = 0; it < 2; ++it)
#pragma unroll
      for (int e = 0; e < 4; ++e) {
        const float wl = bf2f(wa[it][e] & 0xffffu), wh = bfhi2f(wa[it][e]);
        ya[0][2 * e] += o0[it] * wl; ya[0][2 * e + 1] += o0[it] * wh;
        ya[1][2 * e] += o1[it] * wl; ya[1][2 * e + 1] += o1[it] * wh;
      }
#pragma unroll
    for (int it = 0; it < 6; ++it)
#pragma unroll
      for (int e = 0; e < 4; ++e) {
        const float wl = bf2f(wdn[it][e] & 0xffffu), wh = bfhi2f(wdn[it][e]);
        yd[0][2 * e] += h0[it] * wl; yd[0][2 * e + 1] += h0[it] * wh;
        yd[1][2 * e] += h1[it] * wl; yd[1][2 * e + 1] += h1[it] * wh;
      }
#pragma unroll
    for (int tk = 0; tk < 2; ++tk)
#pragma unroll
      for (int e = 0; e < 8; ++e) {
        redf[kg * 64 + tk * 16 + cq * 8 + e] = ya[tk][e];
        redf[kg * 64 + 32 + tk * 16 + cq * 8 + e] = yd[tk][e];
      }
    __syncthreads();
    {
      const int o = tid & 63, q = tid >> 6;
      float sum = 0.f;
#pragma unroll 8
      for (int k = 0; k < 32; ++k) sum += redf[(q * 32 + k) * 64 + o];
      __syncthreads();
      redf[q * 64 + o] = sum;
      __syncthreads();
    }
    if (tid < 32) {
      float tota = 0.f, totd = 0.f;
#pragma unroll
      for (int q = 0; q < 8; ++q) { tota += redf[q * 64 + tid]; totd += redf[q * 64 + 32 + tid]; }
      const int tk = tid >> 4, c = tid & 15;
      const int row = tk ? idx1 : idx0;
      const float rs = ((const float*)sel)[2 + tk];
      const float2 st = stats[row];
      const float xf = bf2f(x[(size_t)row * D_ + col0 + c]);
      const float a = rbf(tota);
      const float t1 = rbf(xf + a);
      const float xn = ln_elem(xf, st.x, st.y, 1.0f / st.y);
      const float upd = rbf(rbf(totd) * rs);
      const float bb = rbf(xn + upd);
      out[(size_t)row * D_ + col0 + c] = __builtin_bit_cast(u16, (__bf16)(t1 + bb));
    }
  }
  TS(2, 3); TSR(2, 9);
}

extern "C" void kernel_launch(void* const* d_in, const int* in_sizes, int n_in, void* d_out, int out_size,
                              void* d_ws, size_t ws_size, hipStream_t stream) {
  const u16* x = (const u16*)d_in[0];
  const u16* wq = (const u16*)d_in[1];
  const u16* wk = (const u16*)d_in[2];
  const u16* wv = (const u16*)d_in[3];
  const u16* wo = (const u16*)d_in[4];
  const u16* wr = (const u16*)d_in[6];
  const u16* wg = (const u16*)d_in[7];
  const u16* wu = (const u16*)d_in[8];
  const u16* wd = (const u16*)d_in[9];
  char* ws = (char*)d_ws;
  u16* xnf = (u16*)(ws + OFF_XNF);
  u16* of = (u16*)(ws + OFF_OF);
  u16* wqf = (u16*)(ws + OFF_WQF);
  u16* wof = (u16*)(ws + OFF_WOF);
  u16* kf = (u16*)(ws + OFF_KF);
  u16* vf = (u16*)(ws + OFF_VF);
  float* rbuf = (float*)(ws + OFF_R);
  float2* stats = (float2*)(ws + OFF_STATS);
  float* hbuf = (float*)(ws + OFF_H);
  int* sel = (int*)(ws + OFF_SEL);
  u16* out = (u16*)d_out;

  ka_prep<<<192, 512, 0, stream>>>(x, wq, wk, wv, wo, wr, xnf, wqf, wof, kf, vf, rbuf, stats);
  kb_attn<<<256, 576, 0, stream>>>(xnf, wqf, kf, vf, rbuf, wg, wu, of, hbuf, sel);
  kc_out<<<288, 512, 0, stream>>>(x, of, wof, wo, wd, stats, hbuf, sel, out);
}
```

```cpp
#include <hip/hip_runtime.h>
#include <hip/hip_bf16.h>

typedef unsigned short u16;
typedef __attribute__((ext_vector_type(8))) __bf16 bf16x8;
typedef __attribute__((ext_vector_type(2))) __bf16 bf16x2;
typedef __attribute__((ext_vector_type(2))) float f32x2;
typedef __attribute__((ext_vector_type(4))) float f32x4;
typedef __attribute__((ext_vector_type(16))) float f32x16;
typedef __attribute__((ext_vector_type(4))) unsigned u32x4;
typedef __attribute__((ext_vector_type(2))) unsigned u32x2;

#define DI __device__ __forceinline__
#define TS(k, n)
#define TSR(k, n)

static constexpr int S_ = 2048;
static constexpr int D_ = 512;
static constexpr int F_ = 1376;

#define LDSF(a) (*(const __attribute__((address_space(3))) bf16x8*)(uintptr_t)(a))
DI float bf2f(unsigned u) { return __uint_as_float(u << 16); }
DI float bfhi2f(unsigned u) { return __uint_as_float(u & 0xffff0000u); }
DI float rbf(float f) { return (float)(__bf16)f; }
DI unsigned pack2(float lo, float hi) {
  f32x2 p = {lo, hi};
  bf16x2 q = __builtin_convertvector(p, bf16x2);
  return __builtin_bit_cast(unsigned, q);
}
DI f32x4 mfma16(bf16x8 a, bf16x8 b, f32x4 c) { return __builtin_amdgcn_mfma_f32_16x16x32_bf16(a, b, c, 0, 0, 0); }
DI f32x16 mfma32(bf16x8 a, bf16x8 b, f32x16 c) { return __builtin_amdgcn_mfma_f32_32x32x16_bf16(a, b, c, 0, 0, 0); }
DI unsigned perm_lo(unsigned hi_src, unsigned lo_src) { return __builtin_amdgcn_perm(hi_src, lo_src, 0x05040100u); }
DI unsigned perm_hi(unsigned hi_src, unsigned lo_src) { return __builtin_amdgcn_perm(hi_src, lo_src, 0x07060302u); }

DI bf16x8 frag_lo(const unsigned* w) {
  u32x4 r = {perm_lo(w[1], w[0]), perm_lo(w[3], w[2]), perm_lo(w[5], w[4]), perm_lo(w[7], w[6])};
  return __builtin_bit_cast(bf16x8, r);
}
DI bf16x8 frag_hi(const unsigned* w) {
  u32x4 r = {perm_hi(w[1], w[0]), perm_hi(w[3], w[2]), perm_hi(w[5], w[4]), perm_hi(w[7], w[6])};
  return __builtin_bit_cast(bf16x8, r);
}
DI float ln_elem(float xv, float mb, float den, float inv) {
  const float d = rbf(xv - mb);
  float q = d * inv;
  const float rem = __builtin_fmaf(-q, den, d);
  q = __builtin_fmaf(rem, inv, q);
  return rbf(q);
}

static constexpr int VT_STRIDE = 1056;
static constexpr int VF_BYTES = 64 * VT_STRIDE;
static constexpr size_t OFF_XNF = 0;
static constexpr size_t OFF_OF = OFF_XNF + (size_t)S_ * D_ * 2;
static constexpr size_t OFF_WQF = OFF_OF + (size_t)S_ * D_ * 2;
static constexpr size_t OFF_WOF = OFF_WQF + (size_t)D_ * D_ * 2;
static constexpr size_t OFF_KF = OFF_WOF + (size_t)D_ * D_ * 2;
static constexpr size_t OFF_VF = OFF_KF + 65536;
static constexpr size_t OFF_R = OFF_VF + VF_BYTES;
static constexpr size_t OFF_STATS = OFF_R + S_ * 4;
static constexpr size_t OFF_Y = OFF_STATS + S_ * 8;
static constexpr size_t OFF_SEL = OFF_Y + 43 * 1024 * 4;

static constexpr int XN_STRIDE = 1040;
__global__ __launch_bounds__(512) void ka_prep(const u16* __restrict__ x, const u16* __restrict__ wq,
                                               const u16* __restrict__ wk, const u16* __restrict__ wv,
                                               const u16* __restrict__ wo, const u16* __restrict__ wr,
                                               u16* __restrict__ xnf, u16* __restrict__ wqf, u16* __restrict__ wof,
                                               u16* __restrict__ kf, u16* __restrict__ vf,
                                               float* __restrict__ rbuf, float2* __restrict__ stats) {
  __shared__ __attribute__((aligned(16))) char smem[16 * XN_STRIDE + 8 * 5 * 64 * 16];
  const int tid = threadIdx.x;
  const int lane = tid & 63, wave = tid >> 6;
  TSR(0, 8); TS(0, 0);
  if (blockIdx.x >= 128) {
    const int pb = blockIdx.x - 128;
    const bool iso = pb >= 32;
    const int cp = tid & 255, ko = (pb & 31) * 2 + (tid >> 8);
    const int ks = ko >> 1, hh = ko & 1;
    const u16* W = (iso ? wo : wq) + 2 * cp;
    unsigned w[8];
#pragma unroll
    for (int j = 0; j < 8; ++j) {
      const int row = 16 * ks + (iso ? (8 * (j >> 2) + 4 * hh + (j & 3)) : (8 * hh + j));
      w[j] = *(const unsigned*)(W + row * D_);
    }
    u16* dst = (iso ? wof : wqf) + (((cp >> 4) * 32 + ks) * 64 + hh * 32 + ((2 * cp) & 31)) * 8;
    *(bf16x8*)dst = frag_lo(w);
    *(bf16x8*)(dst + 8) = frag_hi(w);
    TS(0, 3); TSR(0, 9);
    return;
  }
  const int row0 = blockIdx.x * 16;
  const int rt = blockIdx.x >> 1, th = blockIdx.x & 1;
  const int g = lane >> 4, i = lane & 15;
  const int k0 = wave * 64 + 8 * g;
  u32x4 xr[2];
#pragma unroll
  for (int rr = 0; rr < 2; ++rr) xr[rr] = *(const u32x4*)(x + (size_t)(row0 + 2 * wave + rr) * D_ + 8 * lane);
  const u16* wbase = ((i < 4) ? (wk + 4 * i) : (i < 8 ? wv + 4 * (i - 4) : wk)) + k0 * 16;
  u32x2 w[2][8];
#pragma unroll
  for (int s = 0; s < 2; ++s)
#pragma unroll
    for (int j = 0; j < 8; ++j) w[s][j] = *(const u32x2*)(wbase + (32 * s + j) * 16);
  bf16x8 rf[2];
#pragma unroll
  for (int s = 0; s < 2; ++s) rf[s] = *(const bf16x8*)(wr + k0 + 32 * s);

  float s1[2], s2[2];
#pragma unroll
  for (int rr = 0; rr < 2; ++rr) {
    float a = 0.f, q = 0.f;
#pragma unroll
    for (int e = 0; e < 4; ++e) {
      const float lo = bf2f(xr[rr][e] & 0xffffu), hi = bfhi2f(xr[rr][e]);
      a += lo + hi;
      q = __builtin_fmaf(lo, lo, q);
      q = __builtin_fmaf(hi, hi, q);
    }
    s1[rr] = a; s2[rr] = q;
  }
#pragma unroll
  for (int off = 32; off >= 1; off >>= 1)
#pragma unroll
    for (int rr = 0; rr < 2; ++rr) {
      s1[rr] += __shfl_xor(s1[rr], off);
      s2[rr] += __shfl_xor(s2[rr], off);
    }
#pragma unroll
  for (int rr = 0; rr < 2; ++rr) {
    const float mean = s1[rr] * (1.0f / D_);
    float var = s2[rr] * (1.0f / D_) - mean * mean;
    var = fmaxf(var, 0.f);
    const float mb = rbf(mean);
    const float den = rbf(__builtin_sqrtf(rbf(rbf(var) + 1e-5f)));
    const float inv = 1.0f / den;
    if (lane == rr) stats[row0 + 2 * wave + rr] = make_float2(mb, den);
    u32x4 o;
#pragma unroll
    for (int e = 0; e < 4; ++e)
      o[e] = pack2(ln_elem(bf2f(xr[rr][e] & 0xffffu), mb, den, inv), ln_elem(bfhi2f(xr[rr][e]), mb, den, inv));
    *(u32x4*)(smem + (2 * wave + rr) * XN_STRIDE + lane * 16) = o;
  }
  if (tid < 2 && th == 0) {
    const unsigned cv = tid ? 0u : 0x3f803f80u;
    u32x4 c4 = {cv, cv, cv, cv};
    *(u32x4*)((char*)vf + rt * VT_STRIDE + 1024 + tid * 16) = c4;
  }
  TS(0, 1);
  __syncthreads();
  {
    const int r = lane & 15, hh = (lane >> 4) & 1, kp = lane >> 5;
#pragma unroll
    for (int s = 0; s < 2; ++s) {
      const int ks = 4 * wave + 2 * s + kp;
      const u32x4 v = *(const u32x4*)(smem + r * XN_STRIDE + ks * 32 + hh * 16);
      *(u32x4*)(xnf + ((size_t)(rt * 32 + ks) * 64 + hh * 32 + th * 16 + r) * 8) = v;
    }
  }
  f32x4* red = (f32x4*)(smem + 16 * XN_STRIDE);
  {
    f32x4 acc[4], ar;
    ar = (f32x4){0.f, 0.f, 0.f, 0.f};
#pragma unroll
    for (int c = 0; c < 4; ++c) acc[c] = (f32x4){0.f, 0.f, 0.f, 0.f};
#pragma unroll
    for (int s = 0; s < 2; ++s) {
      unsigned wx[8], wy[8];
#pragma unroll
      for (int j = 0; j < 8; ++j) { wx[j] = w[s][j][0]; wy[j] = w[s][j][1]; }
      bf16x8 b[4] = {frag_lo(wx), frag_hi(wx), frag_lo(wy), frag_hi(wy)};
      const bf16x8 xa = *(const bf16x8*)(smem + i * XN_STRIDE + (k0 + 32 * s) * 2);
#pragma unroll
      for (int c = 0; c < 4; ++c) acc[c] = mfma16(xa, b[c], acc[c]);
      ar = mfma16(xa, rf[s], ar);
    }
#pragma unroll
    for (int c = 0; c < 4; ++c) red[(wave * 5 + c) * 64 + lane] = acc[c];
    red[(wave * 5 + 4) * 64 + lane] = ar;
  }
  __syncthreads();
  TS(0, 2);
  if (wave < 5) {
    f32x4 v = red[wave * 64 + lane];
#pragma unroll
    for (int wv_ = 1; wv_ < 8; ++wv_) v += red[(wv_ * 5 + wave) * 64 + lane];
    const int kk = 16 * th + 4 * g;
    if (wave == 4) {
      if (i == 0) {
        f32x4 o = {rbf(v[0]), rbf(v[1]), rbf(v[2]), rbf(v[3])};
        *(f32x4*)(rbuf + row0 + 4 * g) = o;
      }
    } else if (i < 4) {
      const int hh = i & 1, j = (i >> 1) * 4 + wave;
#pragma unroll
      for (int e = 0; e < 4; ++e)
        kf[rt * 512 + (hh * 32 + kk + e) * 8 + j] = __builtin_bit_cast(u16, (__bf16)v[e]);
    } else if (i < 8) {
      const int d = 4 * (i - 4) + wave;
      const int hh = g & 1, jh = g >> 1;
      u32x2 o = {pack2(v[0], v[1]), pack2(v[2], v[3])};
      *(u32x2*)(vf + rt * (VT_STRIDE / 2) + th * 256 + (hh * 16 + d) * 8 + jh * 4) = o;
    }
  }
  TS(0, 3); TSR(0, 9);
}

DI void top2_keys(const f32x4* v, int lane, int& i0, int& i1) {
  unsigned k0 = 0, k1 = 0;
#pragma unroll
  for (int c = 0; c < 8; ++c)
#pragma unroll
    for (int e = 0; e < 4; ++e) {
      const unsigned u = __float_as_uint(v[c][e]);
      const unsigned ord = (u & 0x80000000u) ? ~u : (u | 0x80000000u);
      const unsigned key = (ord & 0xffff0000u) | (unsigned)(2047 - (c * 256 + lane * 4 + e));
      k1 = max(k1, min(k0, key));
      k0 = max(k0, key);
    }
#pragma unroll
  for (int off = 1; off < 64; off <<= 1) {
    const unsigned o0 = __shfl_xor(k0, off), o1 = __shfl_xor(k1, off);
    const unsigned n0 = max(k0, o0);
    const unsigned n1 = max(min(k0, o0), max(k1, o1));
    k0 = n0; k1 = n1;
  }
  i0 = 2047 - (int)(k0 & 0x7ffu);
  i1 = 2047 - (int)(k1 & 0x7ffu);
}

static constexpr int LDS_K = 0;
static constexpr int LDS_V = 65536;
static constexpr int LDS_TOTAL = 65536 + VF_BYTES + 2048;

__global__ __launch_bounds__(576) void kb_attn(const u16* __restrict__ xnf, const u16* __restrict__ wqf,
                                               const u16* __restrict__ kf, const u16* __restrict__ vf,
                                               const float* __restrict__ rbuf, const u16* __restrict__ wg,
                                               const u16* __restrict__ wu, const u16* __restrict__ wd,
                                               u16* __restrict__ of, float* __restrict__ ypart,
                                               int* __restrict__ sel) {
  __shared__ __attribute__((aligned(16))) char smem[LDS_TOTAL];
  const int tid = threadIdx.x;
  const int lane = tid & 63, wave = tid >> 6;
  const int b = blockIdx.x;
  TSR(1, 8); TS(1, 0);

  if (wave < 8) {
    const int rb = (b & 7) + 8 * ((b >> 3) & 3), hg = b >> 5;
    const int rt = wave >> 2, ks4 = wave & 3;
    const int r = lane & 31, hh = lane >> 5;
    {
      u32x4 tk[8];
#pragma unroll
      for (int c = 0; c < 8; ++c) tk[c] = *(const u32x4*)((const char*)kf + c * 8192 + tid * 16);
      bf16x8 af[2][8], bfr[8];
      const u16* ap = wqf + ((size_t)(2 * hg) * 32 + 8 * ks4) * 512 + lane * 8;
      const u16* bp = xnf + ((size_t)(2 * rb + rt) * 32 + 8 * ks4) * 512 + lane * 8;
#pragma unroll
      for (int s = 0; s < 8; ++s) {
        bfr[s] = *(const bf16x8*)(bp + s * 512);
        af[0][s] = *(const bf16x8*)(ap + s * 512);
        af[1][s] = *(const bf16x8*)(ap + 32 * 512 + s * 512);
      }
      __builtin_amdgcn_sched_barrier(0);
#pragma unroll
      for (int c = 0; c < 8; ++c) *(u32x4*)(smem + LDS_K + c * 8192 + tid * 16) = tk[c];
      __builtin_amdgcn_sched_barrier(0);
      f32x16 acc[2];
#pragma unroll
      for (int e = 0; e < 16; ++e) { acc[0][e] = 0.f; acc[1][e] = 0.f; }
#pragma unroll
      for (int s = 0; s < 8; ++s) {
        acc[0] = mfma32(af[0][s], bfr[s], acc[0]);
        acc[1] = mfma32(af[1][s], bfr[s], acc[1]);
      }
#pragma unroll
      for (int dt = 0; dt < 2; ++dt)
#pragma unroll
        for (int q4 = 0; q4 < 4; ++q4) {
          f32x4 p = {acc[dt][4 * q4], acc[dt][4 * q4 + 1], acc[dt][4 * q4 + 2], acc[dt][4 * q4 + 3]};
          *(f32x4*)(smem + LDS_V + (((wave * 2 + dt) * 4 + q4) * 64 + lane) * 16) = p;
        }
    }
    __builtin_amdgcn_sched_barrier(0);
    u32x4 tv[8], tvx;
#pragma unroll
    for (int c = 0; c < 8; ++c) tv[c] = *(const u32x4*)((const char*)vf + c * 8192 + tid * 16);
    tvx = *(const u32x4*)((const char*)vf + 65536 + (tid & 127) * 16);
    TS(1, 1);
    __syncthreads();
    TS(1, 2);
    const int hl = wave & 3;
    const int head = hg * 4 + hl;
    bf16x8 qf;
    {
      f32x4 a0 = {0.f, 0.f, 0.f, 0.f}, a1 = {0.f, 0.f, 0.f, 0.f};
#pragma unroll
      for (int k = 0; k < 4; ++k) {
        const char* p = smem + LDS_V + ((((rt * 4 + k) * 2 + (hl >> 1)) * 4 + 2 * (hl & 1)) * 64 + lane) * 16;
        a0 += *(const f32x4*)p;
        a1 += *(const f32x4*)(p + 1024);
      }
      const float SC = 0.25f * 1.44269504088896340736f;
      u32x4 pk = {pack2(rbf(a0[0]) * SC, rbf(a0[1]) * SC), pack2(rbf(a0[2]) * SC, rbf(a0[3]) * SC),
                  pack2(rbf(a1[0]) * SC, rbf(a1[1]) * SC), pack2(rbf(a1[2]) * SC, rbf(a1[3]) * SC)};
      qf = __builtin_bit_cast(bf16x8, pk);
    }

    const char* kp = smem + LDS_K + lane * 16;
    float m0 = -__builtin_inff(), m1 = -__builtin_inff();
    f32x16 zero16;
#pragma unroll
    for (int e = 0; e < 16; ++e) zero16[e] = 0.f;
    {
      f32x16 sa = mfma32(*(const bf16x8*)(kp), qf, zero16);
      f32x16 sb = mfma32(*(const bf16x8*)(kp + 1024), qf, zero16);
#pragma unroll 4
      for (int t = 2; t < 64; t += 2) {
        const bf16x8 ka = *(const bf16x8*)(kp + t * 1024);
        const bf16x8 kb = *(const bf16x8*)(kp + t * 1024 + 1024);
        const f32x16 na = mfma32(ka, qf, zero16);
#pragma unroll
        for (int e = 0; e < 16; e += 2) m0 = fmaxf(fmaxf(m0, sa[e]), sa[e + 1]);
        const f32x16 nb = mfma32(kb, qf, zero16);
#pragma unroll
        for (int e = 0; e < 16; e += 2) m1 = fmaxf(fmaxf(m1, sb[e]), sb[e + 1]);
        sa = na; sb = nb;
      }
#pragma unroll
      for (int e = 0; e < 16; e += 2) m0 = fmaxf(fmaxf(m0, sa[e]), sa[e + 1]);
#pragma unroll
      for (int e = 0; e < 16; e += 2) m1 = fmaxf(fmaxf(m1, sb[e]), sb[e + 1]);
    }
    float m = fmaxf(m0, m1);
    m = fmaxf(m, __shfl_xor(m, 32));
    TS(1, 3);
    __syncthreads();
#pragma unroll
    for (int c = 0; c < 8; ++c) *(u32x4*)(smem + LDS_V + c * 8192 + tid * 16) = tv[c];
    if (wave < 2) *(u32x4*)(smem + LDS_V + 65536 + tid * 16) = tvx;
    __syncthreads();
    TS(1, 4);

    f32x16 negm;
#pragma unroll
    for (int e = 0; e < 16; ++e) negm[e] = -m;
    f32x16 o;
#pragma unroll
    for (int e = 0; e < 16; ++e) o[e] = 0.f;
    const unsigned lbase = (unsigned)(uintptr_t)(__attribute__((address_space(3))) char*)smem;
    unsigned ka_ = lbase + LDS_K + lane * 16;
    unsigned va_ = lbase + LDS_V + ((r < 16) ? (hh * 16 + r) * 16 : (r == 16 ? 1024 : 1040));
    unsigned vb_ = lbase + LDS_V + ((r < 16) ? (hh * 16 + r) * 16 + 512 : (r == 16 ? 1024 : 1040));
    f32x16 s = mfma32(LDSF(ka_), qf, negm);
    bf16x8 kn = LDSF(ka_ + 1024);
    bf16x8 v0 = LDSF(va_);
    bf16x8 v1 = LDSF(vb_);
#pragma unroll 1
    for (int tb = 0; tb < 8; ++tb) {
      asm volatile("" : "+v"(ka_), "+v"(va_), "+v"(vb_));
#pragma unroll
      for (int u = 0; u < 8; ++u) {
        const f32x16 sn = mfma32(kn, qf, negm);
        const bf16x8 k2 = LDSF(ka_ + (u + 2) * 1024);
        const bf16x8 v0n = LDSF(va_ + (u + 1) * VT_STRIDE);
        const bf16x8 v1n = LDSF(vb_ + (u + 1) * VT_STRIDE);
        float p[16];
#pragma unroll
        for (int e = 0; e < 8; ++e) p[e] = __builtin_amdgcn_exp2f(s[e]);
        u32x4 pa = {pack2(p[0], p[1]), pack2(p[2], p[3]), pack2(p[4], p[5]), pack2(p[6], p[7])};
        o = mfma32(v0, __builtin_bit_cast(bf16x8, pa), o);
#pragma unroll
        for (int e = 8; e < 16; ++e) p[e] = __builtin_amdgcn_exp2f(s[e]);
        u32x4 pb = {pack2(p[8], p[9]), pack2(p[10], p[11]), pack2(p[12], p[13]), pack2(p[14], p[15])};
        o = mfma32(v1, __builtin_bit_cast(bf16x8, pb), o);
        s = sn; kn = k2; v0 = v0n; v1 = v1n;
        __builtin_amdgcn_sched_group_barrier(0x008, 1, 0);
        __builtin_amdgcn_sched_group_barrier(0x100, 3, 0);
        __builtin_amdgcn_sched_group_barrier(0x402, 12, 0);
        __builtin_amdgcn_sched_group_barrier(0x008, 1, 0);
        __builtin_amdgcn_sched_group_barrier(0x402, 12, 0);
        __builtin_amdgcn_sched_group_barrier(0x008, 1, 0);
        __builtin_amdgcn_sched_barrier(0);
      }
      ka_ += 8 * 1024; va_ += 8 * VT_STRIDE; vb_ += 8 * VT_STRIDE;
    }
    TS(1, 5);
    const float il = 1.0f / __shfl(o[8], r);
    u32x4 w0 = {pack2(o[0] * il, o[1] * il), pack2(o[2] * il, o[3] * il), pack2(o[4] * il, o[5] * il),
                pack2(o[6] * il, o[7] * il)};
    *(u32x4*)(of + ((size_t)((2 * rb + rt) * 32 + head) * 64 + lane) * 8) = w0;
  } else {
    __builtin_amdgcn_s_setprio(3);
    const bool act = b < 43;
    f32x4 rv[8];
    if (act) {
#pragma unroll
      for (int c = 0; c < 8; ++c) rv[c] = *(const f32x4*)(rbuf + c * 256 + lane * 4);
    }
    __syncthreads();
    int i0 = 0, i1 = 0;
    if (act) top2_keys(rv, lane, i0, i1);
    TS(1, 3);
    __syncthreads();
    __syncthreads();
    TS(1, 4);
    if (act) {
      i0 = min(i0, S_ - 1);
      i1 = min(i1, S_ - 1);
      if (b == 0 && lane == 0) {
        const float v0 = rbuf[i0], v1 = rbuf[i1];
        sel[0] = i0; sel[1] = i1;
        ((float*)sel)[2] = rbf(1.0f / (1.0f + __expf(-v0)));
        ((float*)sel)[3] = rbf(1.0f / (1.0f + __expf(-v1)));
      }
      const int g = lane >> 4, i = lane & 15;
      const int srow = (i == 0) ? i0 : i1;
      const u16* xp = xnf + ((size_t)((srow >> 5) * 32 + (g >> 1)) * 64 + (g & 1) * 32 + (srow & 31)) * 8;
      f32x4 ag[2], au[2];
#pragma unroll
      for (int c = 0; c < 2; ++c) { ag[c] = (f32x4){0.f, 0.f, 0.f, 0.f}; au[c] = (f32x4){0.f, 0.f, 0.f, 0.f}; }
      const int f0 = b * 32;
      const u16* gp = wg + f0 + 2 * i + (size_t)(8 * g) * F_;
      const u16* up = wu + f0 + 2 * i + (size_t)(8 * g) * F_;
#pragma unroll 1
      for (int sb = 0; sb < 4; ++sb) {
        unsigned w1[4][8], w2[4][8];
        bf16x8 xa[4];
#pragma unroll
        for (int s = 0; s < 4; ++s) {
          xa[s] = *(const bf16x8*)(xp + (size_t)(4 * sb + s) * 1024);
#pragma unroll
          for (int j = 0; j < 8; ++j) {
            w1[s][j] = *(const unsigned*)(gp + (size_t)(32 * (4 * sb + s) + j) * F_);
            w2[s][j] = *(const unsigned*)(up + (size_t)(32 * (4 * sb + s) + j) * F_);
          }
        }
#pragma unroll
        for (int s = 0; s < 4; ++s) {
          ag[0] = mfma16(xa[s], frag_lo(w1[s]), ag[0]);
          ag[1] = mfma16(xa[s], frag_hi(w1[s]), ag[1]);
          au[0] = mfma16(xa[s], frag_lo(w2[s]), au[0]);
          au[1] = mfma16(xa[s], frag_hi(w2[s]), au[1]);
        }
      }
      float hv[2][2];
#pragma unroll
      for (int c = 0; c < 2; ++c)
#pragma unroll
        for (int tk = 0; tk < 2; ++tk) {
          const float gv = rbf(ag[c][tk]);
          const float uv = rbf(au[c][tk]);
          const float sg = rbf(gv / (1.0f + __expf(-gv)));
          hv[c][tk] = rbf(sg * uv);
        }
      float y0[8], y1[8];
#pragma unroll
      for (int e = 0; e < 8; ++e) { y0[e] = 0.f; y1[e] = 0.f; }
      const u16* dp = wd + (size_t)f0 * D_ + lane * 8;
#pragma unroll 1
      for (int fb = 0; fb < 4; ++fb) {
        u32x4 wv_[8];
#pragma unroll
        for (int f = 0; f < 8; ++f) wv_[f] = *(const u32x4*)(dp + (size_t)(fb * 8 + f) * D_);
#pragma unroll
        for (int f = 0; f < 8; ++f) {
          const int sl = fb * 4 + (f >> 1);
          const float h0 = __builtin_bit_cast(float, __builtin_amdgcn_readlane(__builtin_bit_cast(int, hv[f & 1][0]), sl));
          const float h1 = __builtin_bit_cast(float, __builtin_amdgcn_readlane(__builtin_bit_cast(int, hv[f & 1][1]), sl));
#pragma unroll
          for (int e = 0; e < 4; ++e) {
            const float wl = bf2f(wv_[f][e] & 0xffffu), wh = bfhi2f(wv_[f][e]);
            y0[2 * e] = __builtin_fmaf(h0, wl, y0[2 * e]); y0[2 * e + 1] = __builtin_fmaf(h0, wh, y0[2 * e + 1]);
            y1[2 * e] = __builtin_fmaf(h1, wl, y1[2 * e]); y1[2 * e + 1] = __builtin_fmaf(h1, wh, y1[2 * e + 1]);
          }
          __builtin_amdgcn_sched_barrier(0);
        }
      }
      float* yp = ypart + (size_t)b * 1024 + lane * 8;
      *(f32x4*)(yp) = (f32x4){y0[0], y0[1], y0[2], y0[3]};
      *(f32x4*)(yp + 4) = (f32x4){y0[4], y0[5], y0[6], y0[7]};
      *(f32x4*)(yp + 512) = (f32x4){y1[0], y1[1], y1[2], y1[3]};
      *(f32x4*)(yp + 516) = (f32x4){y1[4], y1[5], y1[6], y1[7]};
    }
  }
  TS(1, 6); TSR(1, 9);
}

static constexpr int CH_STRIDE = 1056;
static constexpr int LDS_Y = 64 * CH_STRIDE;
__global__ __launch_bounds__(512) void kc_out(const u16* __restrict__ x, const u16* __restrict__ of,
                                              const u16* __restrict__ wof, const float2* __restrict__ stats,
                                              const float* __restrict__ ypart, const int* __restrict__ sel,
                                              u16* __restrict__ out) {
  __shared__ __attribute__((aligned(16))) char smem[LDS_Y + 2 * 43 * 256];
  const int tid = threadIdx.x;
  const int lane = tid & 63, wave = tid >> 6;
  TSR(2, 8); TS(2, 0);
  const int bb = blockIdx.x;
  const int rb = (bb & 7) + 8 * ((bb >> 3) & 3), cg = bb >> 5;
  const int rt = wave >> 2, ks4 = wave & 3;
  const int idx0 = sel[0], idx1 = sel[1];
  bf16x8 af[2][8], bfr[8];
  const u16* ap = wof + ((size_t)(2 * cg) * 32 + 8 * ks4) * 512 + lane * 8;
  const u16* bp = of + ((size_t)(2 * rb + rt) * 32 + 8 * ks4) * 512 + lane * 8;
#pragma unroll
  for (int s = 0; s < 8; ++s) {
    bfr[s] = *(const bf16x8*)(bp + s * 512);
    af[0][s] = *(const bf16x8*)(ap + s * 512);
    af[1][s] = *(const bf16x8*)(ap + 32 * 512 + s * 512);
  }
  const int r64 = tid >> 3, oct = tid & 7;
  const int row = rb * 64 + r64;
  const size_t off = (size_t)row * D_ + cg * 64 + oct * 8;
  const u32x4 xv = *(const u32x4*)(x + off);
  const float2 st = stats[row];
#pragma unroll
  for (int tk = 0; tk < 2; ++tk) {
    const int idx = tk ? idx1 : idx0;
    if ((unsigned)(idx - rb * 64) < 64u) {
      for (int q = tid; q < 43 * 16; q += 512)
        *(f32x4*)(smem + LDS_Y + (tk * 43 + (q >> 4)) * 256 + (q & 15) * 16) =
            *(const f32x4*)(ypart + (size_t)((q >> 4) * 2 + tk) * 512 + cg * 64 + (q & 15) * 4);
    }
  }
  f32x16 acc[2];
#pragma unroll
  for (int e = 0; e < 16; ++e) { acc[0][e] = 0.f; acc[1][e] = 0.f; }
#pragma unroll
  for (int s = 0; s < 8; ++s) {
    acc[0] = mfma32(af[0][s], bfr[s], acc[0]);
    acc[1] = mfma32(af[1][s], bfr[s], acc[1]);
  }
  TS(2, 1);
#pragma unroll
  for (int ct = 0; ct < 2; ++ct)
#pragma unroll
    for (int q4 = 0; q4 < 4; ++q4) {
      f32x4 p = {acc[ct][4 * q4], acc[ct][4 * q4 + 1], acc[ct][4 * q4 + 2], acc[ct][4 * q4 + 3]};
      *(f32x4*)(smem + ((wave * 2 + ct) * 4 + q4) * CH_STRIDE + lane * 16) = p;
    }
  __syncthreads();
  TS(2, 2);
  f32x4 lo = {0.f, 0.f, 0.f, 0.f}, hi = {0.f, 0.f, 0.f, 0.f};
#pragma unroll
  for (int k = 0; k < 4; ++k) {
    const char* p = smem + (((r64 >> 5) * 4 + k) * 8 + oct) * CH_STRIDE + (r64 & 31) * 16;
    lo += *(const f32x4*)p;
    hi += *(const f32x4*)(p + 512);
  }
  float upd[8];
#pragma unroll
  for (int e = 0; e < 8; ++e) upd[e] = 0.f;
  if (row == idx0 || row == idx1) {
    const int tk = (row == idx0) ? 0 : 1;
    f32x4 ya = {0.f, 0.f, 0.f, 0.f}, yb = {0.f, 0.f, 0.f, 0.f};
    for (int p = 0; p < 43; ++p) {
      const char* yp = smem + LDS_Y + (tk * 43 + p) * 256 + oct * 32;
      ya += *(const f32x4*)yp;
      yb += *(const f32x4*)(yp + 16);
    }
    const float rs = ((const float*)sel)[2 + tk];
#pragma unroll
    for (int e = 0; e < 4; ++e) { upd[e] = rbf(rbf(ya[e]) * rs); upd[4 + e] = rbf(rbf(yb[e]) * rs); }
  }
  const float inv = 1.0f / st.y;
  float res[8];
#pragma unroll
  for (int e = 0; e < 4; ++e) {
    const float x0 = bf2f(xv[e] & 0xffffu), x1 = bfhi2f(xv[e]);
    const float a0 = rbf(e < 2 ? lo[2 * e] : hi[2 * e - 4]);
    const float a1 = rbf(e < 2 ? lo[2 * e + 1] : hi[2 * e - 3]);
    res[2 * e] = rbf(x0 + a0) + rbf(ln_elem(x0, st.x, st.y, inv) + upd[2 * e]);
    res[2 * e + 1] = rbf(x1 + a1) + rbf(ln_elem(x1, st.x, st.y, inv) + upd[2 * e + 1]);
  }
  u32x4 o = {pack2(res[0], res[1]), pack2(res[2], res[3]), pack2(res[4], res[5]), pack2(res[6], res[7])};
  *(u32x4*)(out + off) = o;
  TS(2, 3); TSR(2, 9);
}

extern "C" void kernel_launch(void* const* d_in, const int* in_sizes, int n_in, void* d_out, int out_size,
                              void* d_ws, size_t ws_size, hipStream_t stream) {
  const u16* x = (const u16*)d_in[0];
  const u16* wq = (const u16*)d_in[1];
  const u16* wk = (const u16*)d_in[2];
  const u16* wv = (const u16*)d_in[3];
  const u16* wo = (const u16*)d_in[4];
  const u16* wr = (const u16*)d_in[6];
  const u16* wg = (const u16*)d_in[7];
  const u16* wu = (const u16*)d_in[8];
  const u16* wd = (const u16*)d_in[9];
  char* ws = (char*)d_ws;
  u16* xnf = (u16*)(ws + OFF_XNF);
  u16* of = (u16*)(ws + OFF_OF);
  u16* wqf = (u16*)(ws + OFF_WQF);
  u16* wof = (u16*)(ws + OFF_WOF);
  u16* kf = (u16*)(ws + OFF_KF);
  u16* vf = (u16*)(ws + OFF_VF);
  float* rbuf = (float*)(ws + OFF_R);
  float2* stats = (float2*)(ws + OFF_STATS);
  float* ypart = (float*)(ws + OFF_Y);
  int* sel = (int*)(ws + OFF_SEL);
  u16* out = (u16*)d_out;

  ka_prep<<<192, 512, 0, stream>>>(x, wq, wk, wv, wo, wr, xnf, wqf, wof, kf, vf, rbuf, stats);
  kb_attn<<<256, 576, 0, stream>>>(xnf, wqf, kf, vf, rbuf, wg, wu, wd, of, ypart, sel);
  kc_out<<<256, 512, 0, stream>>>(x, of, wof, stats, ypart, sel, out);
}
```

```cpp
#include <hip/hip_runtime.h>
#include <hip/hip_bf16.h>

typedef unsigned short u16;
typedef __attribute__((ext_vector_type(8))) __bf16 bf16x8;
typedef __attribute__((ext_vector_type(2))) __bf16 bf16x2;
typedef __attribute__((ext_vector_type(2))) float f32x2;
typedef __attribute__((ext_vector_type(4))) float f32x4;
typedef __attribute__((ext_vector_type(16))) float f32x16;
typedef __attribute__((ext_vector_type(4))) unsigned u32x4;
typedef __attribute__((ext_vector_type(2))) unsigned u32x2;

#define DI __device__ __forceinline__
#define TS(k, n)
#define TSR(k, n)

static constexpr int S_ = 2048;
static constexpr int D_ = 512;
static constexpr int F_ = 1376;

#define LDSF(a) (*(const __attribute__((address_space(3))) bf16x8*)(uintptr_t)(a))
DI float bf2f(unsigned u) { return __uint_as_float(u << 16); }
DI float bfhi2f(unsigned u) { return __uint_as_float(u & 0xffff0000u); }
DI float rbf(float f) { return (float)(__bf16)f; }
DI unsigned pack2(float lo, float hi) {
  f32x2 p = {lo, hi};
  bf16x2 q = __builtin_convertvector(p, bf16x2);
  return __builtin_bit_cast(unsigned, q);
}
DI f32x4 mfma16(bf16x8 a, bf16x8 b, f32x4 c) { return __builtin_amdgcn_mfma_f32_16x16x32_bf16(a, b, c, 0, 0, 0); }
DI f32x16 mfma32(bf16x8 a, bf16x8 b, f32x16 c) { return __builtin_amdgcn_mfma_f32_32x32x16_bf16(a, b, c, 0, 0, 0); }
DI unsigned perm_lo(unsigned hi_src, unsigned lo_src) { return __builtin_amdgcn_perm(hi_src, lo_src, 0x05040100u); }
DI unsigned perm_hi(unsigned hi_src, unsigned lo_src) { return __builtin_amdgcn_perm(hi_src, lo_src, 0x07060302u); }

DI bf16x8 frag_lo(const unsigned* w) {
  u32x4 r = {perm_lo(w[1], w[0]), perm_lo(w[3], w[2]), perm_lo(w[5], w[4]), perm_lo(w[7], w[6])};
  return __builtin_bit_cast(bf16x8, r);
}
DI bf16x8 frag_hi(const unsigned* w) {
  u32x4 r = {perm_hi(w[1], w[0]), perm_hi(w[3], w[2]), perm_hi(w[5], w[4]), perm_hi(w[7], w[6])};
  return __builtin_bit_cast(bf16x8, r);
}
DI float ln_elem(float xv, float mb, float den, float inv) {
  const float d = rbf(xv - mb);
  float q = d * inv;
  const float rem = __builtin_fmaf(-q, den, d);
  q = __builtin_fmaf(rem, inv, q);
  return rbf(q);
}

static constexpr int VT_STRIDE = 1056;
static constexpr int VF_BYTES = 64 * VT_STRIDE;
static constexpr size_t OFF_XNF = 0;
static constexpr size_t OFF_OF = OFF_XNF + (size_t)S_ * D_ * 2;
static constexpr size_t OFF_WQF = OFF_OF + (size_t)S_ * D_ * 2;
static constexpr size_t OFF_WOF = OFF_WQF + (size_t)D_ * D_ * 2;
static constexpr size_t OFF_KF = OFF_WOF + (size_t)D_ * D_ * 2;
static constexpr size_t OFF_VF = OFF_KF + 65536;
static constexpr size_t OFF_R = OFF_VF + VF_BYTES;
static constexpr size_t OFF_STATS = OFF_R + S_ * 4;
static constexpr int NHELP = 86;
static constexpr size_t OFF_Y = OFF_STATS + S_ * 8;
static constexpr size_t OFF_SEL = OFF_Y + NHELP * 1024 * 4;

static constexpr int XN_STRIDE = 1040;
__global__ __launch_bounds__(512) void ka_prep(const u16* __restrict__ x, const u16* __restrict__ wq,
                                               const u16* __restrict__ wk, const u16* __restrict__ wv,
                                               const u16* __restrict__ wo, const u16* __restrict__ wr,
                                               u16* __restrict__ xnf, u16* __restrict__ wqf, u16* __restrict__ wof,
                                               u16* __restrict__ kf, u16* __restrict__ vf,
                                               float* __restrict__ rbuf, float2* __restrict__ stats) {
  __shared__ __attribute__((aligned(16))) char smem[16 * XN_STRIDE + 8 * 5 * 64 * 16];
  const int tid = threadIdx.x;
  const int lane = tid & 63, wave = tid >> 6;
  TSR(0, 8); TS(0, 0);
  if (blockIdx.x >= 128) {
    const int pb = blockIdx.x - 128;
    const bool iso = pb >= 32;
    const int cp = tid & 255, ko = (pb & 31) * 2 + (tid >> 8);
    const int ks = ko >> 1, hh = ko & 1;
    const u16* W = (iso ? wo : wq) + 2 * cp;
    unsigned w[8];
#pragma unroll
    for (int j = 0; j < 8; ++j) {
      const int row = 16 * ks + (iso ? (8 * (j >> 2) + 4 * hh + (j & 3)) : (8 * hh + j));
      w[j] = *(const unsigned*)(W + row * D_);
    }
    u16* dst = (iso ? wof : wqf) + (((cp >> 4) * 32 + ks) * 64 + hh * 32 + ((2 * cp) & 31)) * 8;
    *(bf16x8*)dst = frag_lo(w);
    *(bf16x8*)(dst + 8) = frag_hi(w);
    TS(0, 3); TSR(0, 9);
    return;
  }
  const int row0 = blockIdx.x * 16;
  const int rt = blockIdx.x >> 1, th = blockIdx.x & 1;
  const int g = lane >> 4, i = lane & 15;
  const int k0 = wave * 64 + 8 * g;
  u32x4 xr[2];
#pragma unroll
  for (int rr = 0; rr < 2; ++rr) xr[rr] = *(const u32x4*)(x + (size_t)(row0 + 2 * wave + rr) * D_ + 8 * lane);
  const u16* wbase = ((i < 4) ? (wk + 4 * i) : (i < 8 ? wv + 4 * (i - 4) : wk)) + k0 * 16;
  u32x2 w[2][8];
#pragma unroll
  for (int s = 0; s < 2; ++s)
#pragma unroll
    for (int j = 0; j < 8; ++j) w[s][j] = *(const u32x2*)(wbase + (32 * s + j) * 16);
  bf16x8 rf[2];
#pragma unroll
  for (int s = 0; s < 2; ++s) rf[s] = *(const bf16x8*)(wr + k0 + 32 * s);

  float s1[2], s2[2];
#pragma unroll
  for (int rr = 0; rr < 2; ++rr) {
    float a = 0.f, q = 0.f;
#pragma unroll
    for (int e = 0; e < 4; ++e) {
      const float lo = bf2f(xr[rr][e] & 0xffffu), hi = bfhi2f(xr[rr][e]);
      a += lo + hi;
      q = __builtin_fmaf(lo, lo, q);
      q = __builtin_fmaf(hi, hi, q);
    }
    s1[rr] = a; s2[rr] = q;
  }
#pragma unroll
  for (int off = 32; off >= 1; off >>= 1)
#pragma unroll
    for (int rr = 0; rr < 2; ++rr) {
      s1[rr] += __shfl_xor(s1[rr], off);
      s2[rr] += __shfl_xor(s2[rr], off);
    }
#pragma unroll
  for (int rr = 0; rr < 2; ++rr) {
    const float mean = s1[rr] * (1.0f / D_);
    float var = s2[rr] * (1.0f / D_) - mean * mean;
    var = fmaxf(var, 0.f);
    const float mb = rbf(mean);
    const float den = rbf(__builtin_sqrtf(rbf(rbf(var) + 1e-5f)));
    const float inv = 1.0f / den;
    if (lane == rr) stats[row0 + 2 * wave + rr] = make_float2(mb, den);
    u32x4 o;
#pragma unroll
    for (int e = 0; e < 4; ++e)
      o[e] = pack2(ln_elem(bf2f(xr[rr][e] & 0xffffu), mb, den, inv), ln_elem(bfhi2f(xr[rr][e]), mb, den, inv));
    *(u32x4*)(smem + (2 * wave + rr) * XN_STRIDE + lane * 16) = o;
  }
  if (tid < 2 && th == 0) {
    const unsigned cv = tid ? 0u : 0x3f803f80u;
    u32x4 c4 = {cv, cv, cv, cv};
    *(u32x4*)((char*)vf + rt * VT_STRIDE + 1024 + tid * 16) = c4;
  }
  TS(0, 1);
  __syncthreads();
  {
    const int r = lane & 15, hh = (lane >> 4) & 1, kp = lane >> 5;
#pragma unroll
    for (int s = 0; s < 2; ++s) {
      const int ks = 4 * wave + 2 * s + kp;
      const u32x4 v = *(const u32x4*)(smem + r * XN_STRIDE + ks * 32 + hh * 16);
      *(u32x4*)(xnf + ((size_t)(rt * 32 + ks) * 64 + hh * 32 + th * 16 + r) * 8) = v;
    }
  }
  f32x4* red = (f32x4*)(smem + 16 * XN_STRIDE);
  {
    f32x4 acc[4], ar;
    ar = (f32x4){0.f, 0.f, 0.f, 0.f};
#pragma unroll
    for (int c = 0; c < 4; ++c) acc[c] = (f32x4){0.f, 0.f, 0.f, 0.f};
#pragma unroll
    for (int s = 0; s < 2; ++s) {
      unsigned wx[8], wy[8];
#pragma unroll
      for (int j = 0; j < 8; ++j) { wx[j] = w[s][j][0]; wy[j] = w[s][j][1]; }
      bf16x8 b[4] = {frag_lo(wx), frag_hi(wx), frag_lo(wy), frag_hi(wy)};
      const bf16x8 xa = *(const bf16x8*)(smem + i * XN_STRIDE + (k0 + 32 * s) * 2);
#pragma unroll
      for (int c = 0; c < 4; ++c) acc[c] = mfma16(xa, b[c], acc[c]);
      ar = mfma16(xa, rf[s], ar);
    }
#pragma unroll
    for (int c = 0; c < 4; ++c) red[(wave * 5 + c) * 64 + lane] = acc[c];
    red[(wave * 5 + 4) * 64 + lane] = ar;
  }
  __syncthreads();
  TS(0, 2);
  if (wave < 5) {
    f32x4 v = red[wave * 64 + lane];
#pragma unroll
    for (int wv_ = 1; wv_ < 8; ++wv_) v += red[(wv_ * 5 + wave) * 64 + lane];
    const int kk = 16 * th + 4 * g;
    if (wave == 4) {
      if (i == 0) {
        f32x4 o = {rbf(v[0]), rbf(v[1]), rbf(v[2]), rbf(v[3])};
        *(f32x4*)(rbuf + row0 + 4 * g) = o;
      }
    } else if (i < 4) {
      const int hh = i & 1, j = (i >> 1) * 4 + wave;
#pragma unroll
      for (int e = 0; e < 4; ++e)
        kf[rt * 512 + (hh * 32 + kk + e) * 8 + j] = __builtin_bit_cast(u16, (__bf16)v[e]);
    } else if (i < 8) {
      const int d = 4 * (i - 4) + wave;
      const int hh = g & 1, jh = g >> 1;
      u32x2 o = {pack2(v[0], v[1]), pack2(v[2], v[3])};
      *(u32x2*)(vf + rt * (VT_STRIDE / 2) + th * 256 + (hh * 16 + d) * 8 + jh * 4) = o;
    }
  }
  TS(0, 3); TSR(0, 9);
}

DI void top2_keys(const f32x4* v, int lane, int& i0, int& i1) {
  unsigned k0 = 0, k1 = 0;
#pragma unroll
  for (int c = 0; c < 8; ++c)
#pragma unroll
    for (int e = 0; e < 4; ++e) {
      const unsigned u = __float_as_uint(v[c][e]);
      const unsigned ord = (u & 0x80000000u) ? ~u : (u | 0x80000000u);
      const unsigned key = (ord & 0xffff0000u) | (unsigned)(2047 - (c * 256 + lane * 4 + e));
      k1 = max(k1, min(k0, key));
      k0 = max(k0, key);
    }
#pragma unroll
  for (int off = 1; off < 64; off <<= 1) {
    const unsigned o0 = __shfl_xor(k0, off), o1 = __shfl_xor(k1, off);
    const unsigned n0 = max(k0, o0);
    const unsigned n1 = max(min(k0, o0), max(k1, o1));
    k0 = n0; k1 = n1;
  }
  i0 = 2047 - (int)(k0 & 0x7ffu);
  i1 = 2047 - (int)(k1 & 0x7ffu);
}

static constexpr int LDS_K = 0;
static constexpr int LDS_V = 65536;
static constexpr int LDS_H = 65536 + VF_BYTES + 2048;
static constexpr int LDS_TOTAL = LDS_H + 64;

__global__ __launch_bounds__(576) void kb_attn(const u16* __restrict__ xnf, const u16* __restrict__ wqf,
                                               const u16* __restrict__ kf, const u16* __restrict__ vf,
                                               const float* __restrict__ rbuf, const u16* __restrict__ wg,
                                               const u16* __restrict__ wu, const u16* __restrict__ wd,
                                               u16* __restrict__ of, float* __restrict__ ypart,
                                               int* __restrict__ sel) {
  __shared__ __attribute__((aligned(16))) char smem[LDS_TOTAL];
  const int tid = threadIdx.x;
  const int lane = tid & 63, wave = tid >> 6;
  const int b = blockIdx.x;
  TSR(1, 8); TS(1, 0);

  if (wave < 8) {
    const int rb = (b & 7) + 8 * ((b >> 3) & 3), hg = b >> 5;
    const int rt = wave >> 2, ks4 = wave & 3;
    const int r = lane & 31, hh = lane >> 5;
    {
      u32x4 tk[8];
#pragma unroll
      for (int c = 0; c < 8; ++c) tk[c] = *(const u32x4*)((const char*)kf + c * 8192 + tid * 16);
      bf16x8 af[2][8], bfr[8];
      const u16* ap = wqf + ((size_t)(2 * hg) * 32 + 8 * ks4) * 512 + lane * 8;
      const u16* bp = xnf + ((size_t)(2 * rb + rt) * 32 + 8 * ks4) * 512 + lane * 8;
#pragma unroll
      for (int s = 0; s < 8; ++s) {
        bfr[s] = *(const bf16x8*)(bp + s * 512);
        af[0][s] = *(const bf16x8*)(ap + s * 512);
        af[1][s] = *(const bf16x8*)(ap + 32 * 512 + s * 512);
      }
      __builtin_amdgcn_sched_barrier(0);
#pragma unroll
      for (int c = 0; c < 8; ++c) *(u32x4*)(smem + LDS_K + c * 8192 + tid * 16) = tk[c];
      __builtin_amdgcn_sched_barrier(0);
      f32x16 acc[2];
#pragma unroll
      for (int e = 0; e < 16; ++e) { acc[0][e] = 0.f; acc[1][e] = 0.f; }
#pragma unroll
      for (int s = 0; s < 8; ++s) {
        acc[0] = mfma32(af[0][s], bfr[s], acc[0]);
        acc[1] = mfma32(af[1][s], bfr[s], acc[1]);
      }
#pragma unroll
      for (int dt = 0; dt < 2; ++dt)
#pragma unroll
        for (int q4 = 0; q4 < 4; ++q4) {
          f32x4 p = {acc[dt][4 * q4], acc[dt][4 * q4 + 1], acc[dt][4 * q4 + 2], acc[dt][4 * q4 + 3]};
          *(f32x4*)(smem + LDS_V + (((wave * 2 + dt) * 4 + q4) * 64 + lane) * 16) = p;
        }
    }
    __builtin_amdgcn_sched_barrier(0);
    u32x4 tv[8], tvx;
#pragma unroll
    for (int c = 0; c < 8; ++c) tv[c] = *(const u32x4*)((const char*)vf + c * 8192 + tid * 16);
    tvx = *(const u32x4*)((const char*)vf + 65536 + (tid & 127) * 16);
    TS(1, 1);
    __syncthreads();
    TS(1, 2);
    const int hl = wave & 3;
    const int head = hg * 4 + hl;
    bf16x8 qf;
    {
      f32x4 a0 = {0.f, 0.f, 0.f, 0.f}, a1 = {0.f, 0.f, 0.f, 0.f};
#pragma unroll
      for (int k = 0; k < 4; ++k) {
        const char* p = smem + LDS_V + ((((rt * 4 + k) * 2 + (hl >> 1)) * 4 + 2 * (hl & 1)) * 64 + lane) * 16;
        a0 += *(const f32x4*)p;
        a1 += *(const f32x4*)(p + 1024);
      }
      const float SC = 0.25f * 1.44269504088896340736f;
      u32x4 pk = {pack2(rbf(a0[0]) * SC, rbf(a0[1]) * SC), pack2(rbf(a0[2]) * SC, rbf(a0[3]) * SC),
                  pack2(rbf(a1[0]) * SC, rbf(a1[1]) * SC), pack2(rbf(a1[2]) * SC, rbf(a1[3]) * SC)};
      qf = __builtin_bit_cast(bf16x8, pk);
    }

    const char* kp = smem + LDS_K + lane * 16;
    float m0 = -__builtin_inff(), m1 = -__builtin_inff();
    f32x16 zero16;
#pragma unroll
    for (int e = 0; e < 16; ++e) zero16[e] = 0.f;
    {
      f32x16 sa = mfma32(*(const bf16x8*)(kp), qf, zero16);
      f32x16 sb = mfma32(*(const bf16x8*)(kp + 1024), qf, zero16);
#pragma unroll 4
      for (int t = 2; t < 64; t += 2) {
        const bf16x8 ka = *(const bf16x8*)(kp + t * 1024);
        const bf16x8 kb = *(const bf16x8*)(kp + t * 1024 + 1024);
        const f32x16 na = mfma32(ka, qf, zero16);
#pragma unroll
        for (int e = 0; e < 16; e += 2) m0 = fmaxf(fmaxf(m0, sa[e]), sa[e + 1]);
        const f32x16 nb = mfma32(kb, qf, zero16);
#pragma unroll
        for (int e = 0; e < 16; e += 2) m1 = fmaxf(fmaxf(m1, sb[e]), sb[e + 1]);
        sa = na; sb = nb;
      }
#pragma unroll
      for (int e = 0; e < 16; e += 2) m0 = fmaxf(fmaxf(m0, sa[e]), sa[e + 1]);
#pragma unroll
      for (int e = 0; e < 16; e += 2) m1 = fmaxf(fmaxf(m1, sb[e]), sb[e + 1]);
    }
    float m = fmaxf(m0, m1);
    m = fmaxf(m, __shfl_xor(m, 32));
    TS(1, 3);
    __syncthreads();
#pragma unroll
    for (int c = 0; c < 8; ++c) *(u32x4*)(smem + LDS_V + c * 8192 + tid * 16) = tv[c];
    if (wave < 2) *(u32x4*)(smem + LDS_V + 65536 + tid * 16) = tvx;
    __syncthreads();
    TS(1, 4);

    f32x16 negm;
#pragma unroll
    for (int e = 0; e < 16; ++e) negm[e] = -m;
    f32x16 o;
#pragma unroll
    for (int e = 0; e < 16; ++e) o[e] = 0.f;
    const unsigned lbase = (unsigned)(uintptr_t)(__attribute__((address_space(3))) char*)smem;
    unsigned ka_ = lbase + LDS_K + lane * 16;
    unsigned va_ = lbase + LDS_V + ((r < 16) ? (hh * 16 + r) * 16 : (r == 16 ? 1024 : 1040));
    unsigned vb_ = lbase + LDS_V + ((r < 16) ? (hh * 16 + r) * 16 + 512 : (r == 16 ? 1024 : 1040));
    f32x16 s = mfma32(LDSF(ka_), qf, negm);
    bf16x8 kn = LDSF(ka_ + 1024);
    bf16x8 v0 = LDSF(va_);
    bf16x8 v1 = LDSF(vb_);
#pragma unroll 1
    for (int tb = 0; tb < 8; ++tb) {
      asm volatile("" : "+v"(ka_), "+v"(va_), "+v"(vb_));
#pragma unroll
      for (int u = 0; u < 8; ++u) {
        const f32x16 sn = mfma32(kn, qf, negm);
        const bf16x8 k2 = LDSF(ka_ + (u + 2) * 1024);
        const bf16x8 v0n = LDSF(va_ + (u + 1) * VT_STRIDE);
        const bf16x8 v1n = LDSF(vb_ + (u + 1) * VT_STRIDE);
        float p[16];
#pragma unroll
        for (int e = 0; e < 8; ++e) p[e] = __builtin_amdgcn_exp2f(s[e]);
        u32x4 pa = {pack2(p[0], p[1]), pack2(p[2], p[3]), pack2(p[4], p[5]), pack2(p[6], p[7])};
        o = mfma32(v0, __builtin_bit_cast(bf16x8, pa), o);
#pragma unroll
        for (int e = 8; e < 16; ++e) p[e] = __builtin_amdgcn_exp2f(s[e]);
        u32x4 pb = {pack2(p[8], p[9]), pack2(p[10], p[11]), pack2(p[12], p[13]), pack2(p[14], p[15])};
        o = mfma32(v1, __builtin_bit_cast(bf16x8, pb), o);
        s = sn; kn = k2; v0 = v0n; v1 = v1n;
        __builtin_amdgcn_sched_group_barrier(0x008, 1, 0);
        __builtin_amdgcn_sched_group_barrier(0x100, 3, 0);
        __builtin_amdgcn_sched_group_barrier(0x402, 12, 0);
        __builtin_amdgcn_sched_group_barrier(0x008, 1, 0);
        __builtin_amdgcn_sched_group_barrier(0x402, 12, 0);
        __builtin_amdgcn_sched_group_barrier(0x008, 1, 0);
        __builtin_amdgcn_sched_barrier(0);
      }
      ka_ += 8 * 1024; va_ += 8 * VT_STRIDE; vb_ += 8 * VT_STRIDE;
    }
    TS(1, 5);
    const float il = 1.0f / __shfl(o[8], r);
    u32x4 w0 = {pack2(o[0] * il, o[1] * il), pack2(o[2] * il, o[3] * il), pack2(o[4] * il, o[5] * il),
                pack2(o[6] * il, o[7] * il)};
    *(u32x4*)(of + ((size_t)((2 * rb + rt) * 32 + head) * 64 + lane) * 8) = w0;
  } else {
    __builtin_amdgcn_s_setprio(3);
    const bool act = b < NHELP;
    const int g = lane >> 4, i = lane & 15;
    const int f0 = b * 16;
    f32x4 rv[8];
    unsigned wa[8][8];
    const u16* wp = ((i < 8) ? wg : wu) + f0 + 2 * (i & 7) + (size_t)(8 * g) * F_;
    if (act) {
#pragma unroll
      for (int c = 0; c < 8; ++c) rv[c] = *(const f32x4*)(rbuf + c * 256 + lane * 4);
#pragma unroll
      for (int s = 0; s < 8; ++s)
#pragma unroll
        for (int j = 0; j < 8; ++j) wa[s][j] = *(const unsigned*)(wp + (size_t)(32 * s + j) * F_);
    }
    __syncthreads();
    int i0 = 0, i1 = 0;
    if (act) top2_keys(rv, lane, i0, i1);
    TS(1, 3);
    __syncthreads();
    __syncthreads();
    TS(1, 4);
    if (act) {
      i0 = min(i0, S_ - 1);
      i1 = min(i1, S_ - 1);
      if (b == 0 && lane == 0) {
        const float v0 = rbuf[i0], v1 = rbuf[i1];
        sel[0] = i0; sel[1] = i1;
        ((float*)sel)[2] = rbf(1.0f / (1.0f + __expf(-v0)));
        ((float*)sel)[3] = rbf(1.0f / (1.0f + __expf(-v1)));
      }
      const int srow = (i == 0) ? i0 : i1;
      const u16* xp = xnf + ((size_t)((srow >> 5) * 32 + (g >> 1)) * 64 + (g & 1) * 32 + (srow & 31)) * 8;
      f32x4 a0 = {0.f, 0.f, 0.f, 0.f}, a1 = {0.f, 0.f, 0.f, 0.f};
      {
        bf16x8 xa[8], xb[8];
        unsigned wb[8][8];
#pragma unroll
        for (int s = 0; s < 8; ++s) xa[s] = *(const bf16x8*)(xp + (size_t)s * 1024);
#pragma unroll
        for (int s = 0; s < 8; ++s) {
          a0 = mfma16(xa[s], frag_lo(wa[s]), a0);
          a1 = mfma16(xa[s], frag_hi(wa[s]), a1);
        }
        __builtin_amdgcn_sched_barrier(0);
#pragma unroll
        for (int s = 0; s < 8; ++s) {
          xb[s] = *(const bf16x8*)(xp + (size_t)(8 + s) * 1024);
#pragma unroll
          for (int j = 0; j < 8; ++j) wb[s][j] = *(const unsigned*)(wp + (size_t)(32 * (8 + s) + j) * F_);
        }
#pragma unroll
        for (int s = 0; s < 8; ++s) {
          a0 = mfma16(xb[s], frag_lo(wb[s]), a0);
          a1 = mfma16(xb[s], frag_hi(wb[s]), a1);
        }
      }
      u16* hs = (u16*)(smem + LDS_H);
#pragma unroll
      for (int tk = 0; tk < 2; ++tk) {
        const float g0 = rbf(a0[tk]), g1 = rbf(a1[tk]);
        const float u0 = __shfl_xor(g0, 8), u1 = __shfl_xor(g1, 8);
        const float h0 = rbf(g0 / (1.0f + __expf(-g0))) * u0;
        const float h1 = rbf(g1 / (1.0f + __expf(-g1))) * u1;
        if (lane < 8) *(unsigned*)(hs + tk * 16 + 2 * lane) = pack2(h0, h1);
      }
      __builtin_amdgcn_fence(__ATOMIC_RELEASE, "wavefront");
      __builtin_amdgcn_wave_barrier();
      __builtin_amdgcn_fence(__ATOMIC_ACQUIRE, "wavefront");
      u32x4 hz = {0u, 0u, 0u, 0u};
      if (i < 2 && g < 2) hz = *(const u32x4*)(hs + i * 16 + 8 * g);
      const bf16x8 ha = __builtin_bit_cast(bf16x8, hz);
      const u16* dp = wd + (size_t)(f0 + 8 * (g & 1)) * D_ + 2 * i;
      float* yp = ypart + (size_t)b * 1024 + 2 * i;
#pragma unroll 1
      for (int gb = 0; gb < 2; ++gb) {
        unsigned wdv[8][8];
#pragma unroll
        for (int q = 0; q < 8; ++q)
#pragma unroll
          for (int j = 0; j < 8; ++j) wdv[q][j] = *(const unsigned*)(dp + (size_t)j * D_ + (gb * 8 + q) * 32);
#pragma unroll
        for (int q = 0; q < 8; ++q) {
          const f32x4 z = {0.f, 0.f, 0.f, 0.f};
          const f32x4 y0 = mfma16(ha, frag_lo(wdv[q]), z);
          const f32x4 y1 = mfma16(ha, frag_hi(wdv[q]), z);
          if (g == 0) {
            *(float2*)(yp + (gb * 8 + q) * 32) = make_float2(y0[0], y1[0]);
            *(float2*)(yp + 512 + (gb * 8 + q) * 32) = make_float2(y0[1], y1[1]);
          }
        }
      }
    }
  }
  TS(1, 6); TSR(1, 9);
}

static constexpr int CH_STRIDE = 1056;
static constexpr int LDS_Y = 64 * CH_STRIDE;
__global__ __launch_bounds__(512) void kc_out(const u16* __restrict__ x, const u16* __restrict__ of,
                                              const u16* __restrict__ wof, const float2* __restrict__ stats,
                                              const float* __restrict__ ypart, const int* __restrict__ sel,
                                              u16* __restrict__ out) {
  __shared__ __attribute__((aligned(16))) char smem[LDS_Y + 2 * NHELP * 256];
  const int tid = threadIdx.x;
  const int lane = tid & 63, wave = tid >> 6;
  TSR(2, 8); TS(2, 0);
  const int bb = blockIdx.x;
  const int rb = (bb & 7) + 8 * ((bb >> 3) & 3), cg = bb >> 5;
  const int rt = wave >> 2, ks4 = wave & 3;
  const int idx0 = sel[0], idx1 = sel[1];
  bf16x8 af[2][8], bfr[8];
  const u16* ap = wof + ((size_t)(2 * cg) * 32 + 8 * ks4) * 512 + lane * 8;
  const u16* bp = of + ((size_t)(2 * rb + rt) * 32 + 8 * ks4) * 512 + lane * 8;
#pragma unroll
  for (int s = 0; s < 8; ++s) {
    bfr[s] = *(const bf16x8*)(bp + s * 512);
    af[0][s] = *(const bf16x8*)(ap + s * 512);
    af[1][s] = *(const bf16x8*)(ap + 32 * 512 + s * 512);
  }
  const int r64 = tid >> 3, oct = tid & 7;
  const int row = rb * 64 + r64;
  const size_t off = (size_t)row * D_ + cg * 64 + oct * 8;
  const u32x4 xv = *(const u32x4*)(x + off);
  const float2 st = stats[row];
#pragma unroll
  for (int tk = 0; tk < 2; ++tk) {
    const int idx = tk ? idx1 : idx0;
    if ((unsigned)(idx - rb * 64) < 64u) {
      for (int q = tid; q < NHELP * 16; q += 512)
        *(f32x4*)(smem + LDS_Y + (tk * NHELP + (q >> 4)) * 256 + (q & 15) * 16) =
            *(const f32x4*)(ypart + (size_t)((q >> 4) * 2 + tk) * 512 + cg * 64 + (q & 15) * 4);
    }
  }
  f32x16 acc[2];
#pragma unroll
  for (int e = 0; e < 16; ++e) { acc[0][e] = 0.f; acc[1][e] = 0.f; }
#pragma unroll
  for (int s = 0; s < 8; ++s) {
    acc[0] = mfma32(af[0][s], bfr[s], acc[0]);
    acc[1] = mfma32(af[1][s], bfr[s], acc[1]);
  }
  TS(2, 1);
#pragma unroll
  for (int ct = 0; ct < 2; ++ct)
#pragma unroll
    for (int q4 = 0; q4 < 4; ++q4) {
      f32x4 p = {acc[ct][4 * q4], acc[ct][4 * q4 + 1], acc[ct][4 * q4 + 2], acc[ct][4 * q4 + 3]};
      *(f32x4*)(smem + ((wave * 2 + ct) * 4 + q4) * CH_STRIDE + lane * 16) = p;
    }
  __syncthreads();
  TS(2, 2);
  f32x4 lo = {0.f, 0.f, 0.f, 0.f}, hi = {0.f, 0.f, 0.f, 0.f};
#pragma unroll
  for (int k = 0; k < 4; ++k) {
    const char* p = smem + (((r64 >> 5) * 4 + k) * 8 + oct) * CH_STRIDE + (r64 & 31) * 16;
    lo += *(const f32x4*)p;
    hi += *(const f32x4*)(p + 512);
  }
  float upd[8];
#pragma unroll
  for (int e = 0; e < 8; ++e) upd[e] = 0.f;
  if (row == idx0 || row == idx1) {
    const int tk = (row == idx0) ? 0 : 1;
    f32x4 ya = {0.f, 0.f, 0.f, 0.f}, yb = {0.f, 0.f, 0.f, 0.f};
    for (int p = 0; p < NHELP; ++p) {
      const char* yp = smem + LDS_Y + (tk * NHELP + p) * 256 + oct * 32;
      ya += *(const f32x4*)yp;
      yb += *(const f32x4*)(yp + 16);
    }
    const float rs = ((const float*)sel)[2 + tk];
#pragma unroll
    for (int e = 0; e < 4; ++e) { upd[e] = rbf(rbf(ya[e]) * rs); upd[4 + e] = rbf(rbf(yb[e]) * rs); }
  }
  const float inv = 1.0f / st.y;
  float res[8];
#pragma unroll
  for (int e = 0; e < 4; ++e) {
    const float x0 = bf2f(xv[e] & 0xffffu), x1 = bfhi2f(xv[e]);
    const float a0 = rbf(e < 2 ? lo[2 * e] : hi[2 * e - 4]);
    const float a1 = rbf(e < 2 ? lo[2 * e + 1] : hi[2 * e - 3]);
    res[2 * e] = rbf(x0 + a0) + rbf(ln_elem(x0, st.x, st.y, inv) + upd[2 * e]);
    res[2 * e + 1] = rbf(x1 + a1) + rbf(ln_elem(x1, st.x, st.y, inv) + upd[2 * e + 1]);
  }
  u32x4 o = {pack2(res[0], res[1]), pack2(res[2], res[3]), pack2(res[4], res[5]), pack2(res[6], res[7])};
  *(u32x4*)(out + off) = o;
  TS(2, 3); TSR(2, 9);
}

extern "C" void kernel_launch(void* const* d_in, const int* in_sizes, int n_in, void* d_out, int out_size,
                              void* d_ws, size_t ws_size, hipStream_t stream) {
  const u16* x = (const u16*)d_in[0];
  const u16* wq = (const u16*)d_in[1];
  const u16* wk = (const u16*)d_in[2];
  const u16* wv = (const u16*)d_in[3];
  const u16* wo = (const u16*)d_in[4];
  const u16* wr = (const u16*)d_in[6];
  const u16* wg = (const u16*)d_in[7];
  const u16* wu = (const u16*)d_in[8];
  const u16* wd = (const u16*)d_in[9];
  char* ws = (char*)d_ws;
  u16* xnf = (u16*)(ws + OFF_XNF);
  u16* of = (u16*)(ws + OFF_OF);
  u16* wqf = (u16*)(ws + OFF_WQF);
  u16* wof = (u16*)(ws + OFF_WOF);
  u16* kf = (u16*)(ws + OFF_KF);
  u16* vf = (u16*)(ws + OFF_VF);
  float* rbuf = (float*)(ws + OFF_R);
  float2* stats = (float2*)(ws + OFF_STATS);
  float* ypart = (float*)(ws + OFF_Y);
  int* sel = (int*)(ws + OFF_SEL);
  u16* out = (u16*)d_out;

  ka_prep<<<192, 512, 0, stream>>>(x, wq, wk, wv, wo, wr, xnf, wqf, wof, kf, vf, rbuf, stats);
  kb_attn<<<256, 576, 0, stream>>>(xnf, wqf, kf, vf, rbuf, wg, wu, wd, of, ypart, sel);
  kc_out<<<256, 512, 0, stream>>>(x, of, wof, stats, ypart, sel, out);
}
```

```cpp
#include <hip/hip_runtime.h>
#include <hip/hip_bf16.h>

typedef unsigned short u16;
typedef __attribute__((ext_vector_type(8))) __bf16 bf16x8;
typedef __attribute__((ext_vector_type(2))) __bf16 bf16x2;
typedef __attribute__((ext_vector_type(2))) float f32x2;
typedef __attribute__((ext_vector_type(4))) float f32x4;
typedef __attribute__((ext_vector_type(16))) float f32x16;
typedef __attribute__((ext_vector_type(4))) unsigned u32x4;
typedef __attribute__((ext_vector_type(2))) unsigned u32x2;

#define DI __device__ __forceinline__
#define TS(k, n)
#define TSR(k, n)

static constexpr int S_ = 2048;
static constexpr int D_ = 512;
static constexpr int F_ = 1376;

#define LDSF(a) (*(const __attribute__((address_space(3))) bf16x8*)(uintptr_t)(a))
DI float bf2f(unsigned u) { return __uint_as_float(u << 16); }
DI float bfhi2f(unsigned u) { return __uint_as_float(u & 0xffff0000u); }
DI float rbf(float f) { return (float)(__bf16)f; }
DI unsigned pack2(float lo, float hi) {
  f32x2 p = {lo, hi};
  bf16x2 q = __builtin_convertvector(p, bf16x2);
  return __builtin_bit_cast(unsigned, q);
}
DI f32x4 mfma16(bf16x8 a, bf16x8 b, f32x4 c) { return __builtin_amdgcn_mfma_f32_16x16x32_bf16(a, b, c, 0, 0, 0); }
DI f32x16 mfma32(bf16x8 a, bf16x8 b, f32x16 c) { return __builtin_amdgcn_mfma_f32_32x32x16_bf16(a, b, c, 0, 0, 0); }
DI unsigned perm_lo(unsigned hi_src, unsigned lo_src) { return __builtin_amdgcn_perm(hi_src, lo_src, 0x05040100u); }
DI unsigned perm_hi(unsigned hi_src, unsigned lo_src) { return __builtin_amdgcn_perm(hi_src, lo_src, 0x07060302u); }

DI bf16x8 frag_lo(const unsigned* w) {
  u32x4 r = {perm_lo(w[1], w[0]), perm_lo(w[3], w[2]), perm_lo(w[5], w[4]), perm_lo(w[7], w[6])};
  return __builtin_bit_cast(bf16x8, r);
}
DI bf16x8 frag_hi(const unsigned* w) {
  u32x4 r = {perm_hi(w[1], w[0]), perm_hi(w[3], w[2]), perm_hi(w[5], w[4]), perm_hi(w[7], w[6])};
  return __builtin_bit_cast(bf16x8, r);
}
DI float ln_elem(float xv, float mb, float den, float inv) {
  const float d = rbf(xv - mb);
  float q = d * inv;
  const float rem = __builtin_fmaf(-q, den, d);
  q = __builtin_fmaf(rem, inv, q);
  return rbf(q);
}

static constexpr int VT_STRIDE = 1056;
static constexpr int VF_BYTES = 64 * VT_STRIDE;
static constexpr size_t OFF_XNF = 0;
static constexpr size_t OFF_OF = OFF_XNF + (size_t)S_ * D_ * 2;
static constexpr size_t OFF_WQF = OFF_OF + (size_t)S_ * D_ * 2;
static constexpr size_t OFF_WOF = OFF_WQF + (size_t)D_ * D_ * 2;
static constexpr size_t OFF_KF = OFF_WOF + (size_t)D_ * D_ * 2;
static constexpr size_t OFF_VF = OFF_KF + 65536;
static constexpr size_t OFF_R = OFF_VF + VF_BYTES;
static constexpr size_t OFF_STATS = OFF_R + S_ * 4;
static constexpr int NHELP = 86;
static constexpr size_t OFF_Y = OFF_STATS + S_ * 8;
static constexpr size_t OFF_SEL = OFF_Y + NHELP * 1024 * 4;

static constexpr int XN_STRIDE = 1040;
__global__ __launch_bounds__(512) void ka_prep(const u16* __restrict__ x, const u16* __restrict__ wq,
                                               const u16* __restrict__ wk, const u16* __restrict__ wv,
                                               const u16* __restrict__ wo, const u16* __restrict__ wr,
                                               u16* __restrict__ xnf, u16* __restrict__ wqf, u16* __restrict__ wof,
                                               u16* __restrict__ kf, u16* __restrict__ vf,
                                               float* __restrict__ rbuf, float2* __restrict__ stats) {
  __shared__ __attribute__((aligned(16))) char smem[16 * XN_STRIDE + 8 * 5 * 64 * 16];
  const int tid = threadIdx.x;
  const int lane = tid & 63, wave = tid >> 6;
  TSR(0, 8); TS(0, 0);
  if (blockIdx.x >= 128) {
    const int pb = blockIdx.x - 128;
    const bool iso = pb >= 32;
    const int cp = tid & 255, ko = (pb & 31) * 2 + (tid >> 8);
    const int ks = ko >> 1, hh = ko & 1;
    const u16* W = (iso ? wo : wq) + 2 * cp;
    unsigned w[8];
#pragma unroll
    for (int j = 0; j < 8; ++j) {
      const int row = 16 * ks + (iso ? (8 * (j >> 2) + 4 * hh + (j & 3)) : (8 * hh + j));
      w[j] = *(const unsigned*)(W + row * D_);
    }
    u16* dst = (iso ? wof : wqf) + (((cp >> 4) * 32 + ks) * 64 + hh * 32 + ((2 * cp) & 31)) * 8;
    *(bf16x8*)dst = frag_lo(w);
    *(bf16x8*)(dst + 8) = frag_hi(w);
    TS(0, 3); TSR(0, 9);
    return;
  }
  const int row0 = blockIdx.x * 16;
  const int rt = blockIdx.x >> 1, th = blockIdx.x & 1;
  const int g = lane >> 4, i = lane & 15;
  const int k0 = wave * 64 + 8 * g;
  u32x4 xr[2];
#pragma unroll
  for (int rr = 0; rr < 2; ++rr) xr[rr] = *(const u32x4*)(x + (size_t)(row0 + 2 * wave + rr) * D_ + 8 * lane);
  const u16* wbase = ((i < 4) ? (wk + 4 * i) : (i < 8 ? wv + 4 * (i - 4) : wk)) + k0 * 16;
  u32x2 w[2][8];
#pragma unroll
  for (int s = 0; s < 2; ++s)
#pragma unroll
    for (int j = 0; j < 8; ++j) w[s][j] = *(const u32x2*)(wbase + (32 * s + j) * 16);
  bf16x8 rf[2];
#pragma unroll
  for (int s = 0; s < 2; ++s) rf[s] = *(const bf16x8*)(wr + k0 + 32 * s);

  float s1[2], s2[2];
#pragma unroll
  for (int rr = 0; rr < 2; ++rr) {
    float a = 0.f, q = 0.f;
#pragma unroll
    for (int e = 0; e < 4; ++e) {
      const float lo = bf2f(xr[rr][e] & 0xffffu), hi = bfhi2f(xr[rr][e]);
      a += lo + hi;
      q = __builtin_fmaf(lo, lo, q);
      q = __builtin_fmaf(hi, hi, q);
    }
    s1[rr] = a; s2[rr] = q;
  }
#pragma unroll
  for (int off = 32; off >= 1; off >>= 1)
#pragma unroll
    for (int rr = 0; rr < 2; ++rr) {
      s1[rr] += __shfl_xor(s1[rr], off);
      s2[rr] += __shfl_xor(s2[rr], off);
    }
#pragma unroll
  for (int rr = 0; rr < 2; ++rr) {
    const float mean = s1[rr] * (1.0f / D_);
    float var = s2[rr] * (1.0f / D_) - mean * mean;
    var = fmaxf(var, 0.f);
    const float mb = rbf(mean);
    const float den = rbf(__builtin_sqrtf(rbf(rbf(var) + 1e-5f)));
    const float inv = 1.0f / den;
    if (lane == rr) stats[row0 + 2 * wave + rr] = make_float2(mb, den);
    u32x4 o;
#pragma unroll
    for (int e = 0; e < 4; ++e)
      o[e] = pack2(ln_elem(bf2f(xr[rr][e] & 0xffffu), mb, den, inv), ln_elem(bfhi2f(xr[rr][e]), mb, den, inv));
    *(u32x4*)(smem + (2 * wave + rr) * XN_STRIDE + lane * 16) = o;
  }
  if (tid < 2 && th == 0) {
    const unsigned cv = tid ? 0u : 0x3f803f80u;
    u32x4 c4 = {cv, cv, cv, cv};
    *(u32x4*)((char*)vf + rt * VT_STRIDE + 1024 + tid * 16) = c4;
  }
  TS(0, 1);
  __syncthreads();
  {
    const int r = lane & 15, hh = (lane >> 4) & 1, kp = lane >> 5;
#pragma unroll
    for (int s = 0; s < 2; ++s) {
      const int ks = 4 * wave + 2 * s + kp;
      const u32x4 v = *(const u32x4*)(smem + r * XN_STRIDE + ks * 32 + hh * 16);
      *(u32x4*)(xnf + ((size_t)(rt * 32 + ks) * 64 + hh * 32 + th * 16 + r) * 8) = v;
    }
  }
  f32x4* red = (f32x4*)(smem + 16 * XN_STRIDE);
  {
    f32x4 acc[4], ar;
    ar = (f32x4){0.f, 0.f, 0.f, 0.f};
#pragma unroll
    for (int c = 0; c < 4; ++c) acc[c] = (f32x4){0.f, 0.f, 0.f, 0.f};
#pragma unroll
    for (int s = 0; s < 2; ++s) {
      unsigned wx[8], wy[8];
#pragma unroll
      for (int j = 0; j < 8; ++j) { wx[j] = w[s][j][0]; wy[j] = w[s][j][1]; }
      bf16x8 b[4] = {frag_lo(wx), frag_hi(wx), frag_lo(wy), frag_hi(wy)};
      const bf16x8 xa = *(const bf16x8*)(smem + i * XN_STRIDE + (k0 + 32 * s) * 2);
#pragma unroll
      for (int c = 0; c < 4; ++c) acc[c] = mfma16(xa, b[c], acc[c]);
      ar = mfma16(xa, rf[s], ar);
    }
#pragma unroll
    for (int c = 0; c < 4; ++c) red[(wave * 5 + c) * 64 + lane] = acc[c];
    red[(wave * 5 + 4) * 64 + lane] = ar;
  }
  __syncthreads();
  TS(0, 2);
  if (wave < 5) {
    f32x4 v = red[wave * 64 + lane];
#pragma unroll
    for (int wv_ = 1; wv_ < 8; ++wv_) v += red[(wv_ * 5 + wave) * 64 + lane];
    const int kk = 16 * th + 4 * g;
    if (wave == 4) {
      if (i == 0) {
        f32x4 o = {rbf(v[0]), rbf(v[1]), rbf(v[2]), rbf(v[3])};
        *(f32x4*)(rbuf + row0 + 4 * g) = o;
      }
    } else if (i < 4) {
      const int hh = i & 1, j = (i >> 1) * 4 + wave;
#pragma unroll
      for (int e = 0; e < 4; ++e)
        kf[rt * 512 + (hh * 32 + kk + e) * 8 + j] = __builtin_bit_cast(u16, (__bf16)v[e]);
    } else if (i < 8) {
      const int d = 4 * (i - 4) + wave;
      const int hh = g & 1, jh = g >> 1;
      u32x2 o = {pack2(v[0], v[1]), pack2(v[2], v[3])};
      *(u32x2*)(vf + rt * (VT_STRIDE / 2) + th * 256 + (hh * 16 + d) * 8 + jh * 4) = o;
    }
  }
  TS(0, 3); TSR(0, 9);
}

DI void top2_keys(const f32x4* v, int lane, int& i0, int& i1) {
  unsigned k0 = 0, k1 = 0;
#pragma unroll
  for (int c = 0; c < 8; ++c)
#pragma unroll
    for (int e = 0; e < 4; ++e) {
      const unsigned u = __float_as_uint(v[c][e]);
      const unsigned ord = (u & 0x80000000u) ? ~u : (u | 0x80000000u);
      const unsigned key = (ord & 0xffff0000u) | (unsigned)(2047 - (c * 256 + lane * 4 + e));
      k1 = max(k1, min(k0, key));
      k0 = max(k0, key);
    }
#pragma unroll
  for (int off = 1; off < 64; off <<= 1) {
    const unsigned o0 = __shfl_xor(k0, off), o1 = __shfl_xor(k1, off);
    const unsigned n0 = max(k0, o0);
    const unsigned n1 = max(min(k0, o0), max(k1, o1));
    k0 = n0; k1 = n1;
  }
  i0 = 2047 - (int)(k0 & 0x7ffu);
  i1 = 2047 - (int)(k1 & 0x7ffu);
}

static constexpr int LDS_K = 0;
static constexpr int LDS_V = 65536;
static constexpr int LDS_H = 65536 + VF_BYTES + 2048;
static constexpr int LDS_TOTAL = LDS_H + 64;

__global__ __launch_bounds__(576) void kb_attn(const u16* __restrict__ xnf, const u16* __restrict__ wqf,
                                               const u16* __restrict__ kf, const u16* __restrict__ vf,
                                               const float* __restrict__ rbuf, const u16* __restrict__ wg,
                                               const u16* __restrict__ wu, const u16* __restrict__ wd,
                                               u16* __restrict__ of, float* __restrict__ ypart,
                                               int* __restrict__ sel) {
  __shared__ __attribute__((aligned(16))) char smem[LDS_TOTAL];
  const int tid = threadIdx.x;
  const int lane = tid & 63, wave = tid >> 6;
  const int b = blockIdx.x;
  TSR(1, 8); TS(1, 0);

  if (wave < 8) {
    const int rb = (b & 7) + 8 * ((b >> 3) & 3), hg = b >> 5;
    const int rt = wave >> 2, ks4 = wave & 3;
    const int r = lane & 31, hh = lane >> 5;
    {
      u32x4 tk[8];
#pragma unroll
      for (int c = 0; c < 8; ++c) tk[c] = *(const u32x4*)((const char*)kf + c * 8192 + tid * 16);
      bf16x8 af[2][8], bfr[8];
      const u16* ap = wqf + ((size_t)(2 * hg) * 32 + 8 * ks4) * 512 + lane * 8;
      const u16* bp = xnf + ((size_t)(2 * rb + rt) * 32 + 8 * ks4) * 512 + lane * 8;
#pragma unroll
      for (int s = 0; s < 8; ++s) {
        bfr[s] = *(const bf16x8*)(bp + s * 512);
        af[0][s] = *(const bf16x8*)(ap + s * 512);
        af[1][s] = *(const bf16x8*)(ap + 32 * 512 + s * 512);
      }
      __builtin_amdgcn_sched_barrier(0);
#pragma unroll
      for (int c = 0; c < 8; ++c) *(u32x4*)(smem + LDS_K + c * 8192 + tid * 16) = tk[c];
      __builtin_amdgcn_sched_barrier(0);
      f32x16 acc[2];
#pragma unroll
      for (int e = 0; e < 16; ++e) { acc[0][e] = 0.f; acc[1][e] = 0.f; }
#pragma unroll
      for (int s = 0; s < 8; ++s) {
        acc[0] = mfma32(af[0][s], bfr[s], acc[0]);
        acc[1] = mfma32(af[1][s], bfr[s], acc[1]);
      }
#pragma unroll
      for (int dt = 0; dt < 2; ++dt)
#pragma unroll
        for (int q4 = 0; q4 < 4; ++q4) {
          f32x4 p = {acc[dt][4 * q4], acc[dt][4 * q4 + 1], acc[dt][4 * q4 + 2], acc[dt][4 * q4 + 3]};
          *(f32x4*)(smem + LDS_V + (((wave * 2 + dt) * 4 + q4) * 64 + lane) * 16) = p;
        }
    }
    __builtin_amdgcn_sched_barrier(0);
    u32x4 tv[8], tvx;
#pragma unroll
    for (int c = 0; c < 8; ++c) tv[c] = *(const u32x4*)((const char*)vf + c * 8192 + tid * 16);
    tvx = *(const u32x4*)((const char*)vf + 65536 + (tid & 127) * 16);
    TS(1, 1);
    __syncthreads();
    TS(1, 2);
    const int hl = wave & 3;
    const int head = hg * 4 + hl;
    bf16x8 qf;
    {
      f32x4 a0 = {0.f, 0.f, 0.f, 0.f}, a1 = {0.f, 0.f, 0.f, 0.f};
#pragma unroll
      for (int k = 0; k < 4; ++k) {
        const char* p = smem + LDS_V + ((((rt * 4 + k) * 2 + (hl >> 1)) * 4 + 2 * (hl & 1)) * 64 + lane) * 16;
        a0 += *(const f32x4*)p;
        a1 += *(const f32x4*)(p + 1024);
      }
      const float SC = 0.25f * 1.44269504088896340736f;
      u32x4 pk = {pack2(rbf(a0[0]) * SC, rbf(a0[1]) * SC), pack2(rbf(a0[2]) * SC, rbf(a0[3]) * SC),
                  pack2(rbf(a1[0]) * SC, rbf(a1[1]) * SC), pack2(rbf(a1[2]) * SC, rbf(a1[3]) * SC)};
      qf = __builtin_bit_cast(bf16x8, pk);
    }

    const char* kp = smem + LDS_K + lane * 16;
    float m0 = -__builtin_inff(), m1 = -__builtin_inff();
    f32x16 zero16;
#pragma unroll
    for (int e = 0; e < 16; ++e) zero16[e] = 0.f;
    {
      f32x16 sa = mfma32(*(const bf16x8*)(kp), qf, zero16);
      f32x16 sb = mfma32(*(const bf16x8*)(kp + 1024), qf, zero16);
#pragma unroll 4
      for (int t = 2; t < 64; t += 2) {
        const bf16x8 ka = *(const bf16x8*)(kp + t * 1024);
        const bf16x8 kb = *(const bf16x8*)(kp + t * 1024 + 1024);
        const f32x16 na = mfma32(ka, qf, zero16);
#pragma unroll
        for (int e = 0; e < 16; e += 2) m0 = fmaxf(fmaxf(m0, sa[e]), sa[e + 1]);
        const f32x16 nb = mfma32(kb, qf, zero16);
#pragma unroll
        for (int e = 0; e < 16; e += 2) m1 = fmaxf(fmaxf(m1, sb[e]), sb[e + 1]);
        sa = na; sb = nb;
      }
#pragma unroll
      for (int e = 0; e < 16; e += 2) m0 = fmaxf(fmaxf(m0, sa[e]), sa[e + 1]);
#pragma unroll
      for (int e = 0; e < 16; e += 2) m1 = fmaxf(fmaxf(m1, sb[e]), sb[e + 1]);
    }
    float m = fmaxf(m0, m1);
    m = fmaxf(m, __shfl_xor(m, 32));
    TS(1, 3);
    __syncthreads();
#pragma unroll
    for (int c = 0; c < 8; ++c) *(u32x4*)(smem + LDS_V + c * 8192 + tid * 16) = tv[c];
    if (wave < 2) *(u32x4*)(smem + LDS_V + 65536 + tid * 16) = tvx;
    __syncthreads();
    TS(1, 4);

    f32x16 negm;
#pragma unroll
    for (int e = 0; e < 16; ++e) negm[e] = -m;
    f32x16 o;
#pragma unroll
    for (int e = 0; e < 16; ++e) o[e] = 0.f;
    const unsigned lbase = (unsigned)(uintptr_t)(__attribute__((address_space(3))) char*)smem;
    unsigned ka_ = lbase + LDS_K + lane * 16;
    unsigned va_ = lbase + LDS_V + ((r < 16) ? (hh * 16 + r) * 16 : (r == 16 ? 1024 : 1040));
    unsigned vb_ = lbase + LDS_V + ((r < 16) ? (hh * 16 + r) * 16 + 512 : (r == 16 ? 1024 : 1040));
    f32x16 s = mfma32(LDSF(ka_), qf, negm);
    bf16x8 kn = LDSF(ka_ + 1024);
    bf16x8 v0 = LDSF(va_);
    bf16x8 v1 = LDSF(vb_);
#pragma unroll 1
    for (int tb = 0; tb < 8; ++tb) {
      asm volatile("" : "+v"(ka_), "+v"(va_), "+v"(vb_));
#pragma unroll
      for (int u = 0; u < 8; ++u) {
        const f32x16 sn = mfma32(kn, qf, negm);
        const bf16x8 k2 = LDSF(ka_ + (u + 2) * 1024);
        const bf16x8 v0n = LDSF(va_ + (u + 1) * VT_STRIDE);
        const bf16x8 v1n = LDSF(vb_ + (u + 1) * VT_STRIDE);
        float p[16];
#pragma unroll
        for (int e = 0; e < 8; ++e) p[e] = __builtin_amdgcn_exp2f(s[e]);
        u32x4 pa = {pack2(p[0], p[1]), pack2(p[2], p[3]), pack2(p[4], p[5]), pack2(p[6], p[7])};
        o = mfma32(v0, __builtin_bit_cast(bf16x8, pa), o);
#pragma unroll
        for (int e = 8; e < 16; ++e) p[e] = __builtin_amdgcn_exp2f(s[e]);
        u32x4 pb = {pack2(p[8], p[9]), pack2(p[10], p[11]), pack2(p[12], p[13]), pack2(p[14], p[15])};
        o = mfma32(v1, __builtin_bit_cast(bf16x8, pb), o);
        s = sn; kn = k2; v0 = v0n; v1 = v1n;
        __builtin_amdgcn_sched_group_barrier(0x008, 1, 0);
        __builtin_amdgcn_sched_group_barrier(0x100, 3, 0);
        __builtin_amdgcn_sched_group_barrier(0x402, 12, 0);
        __builtin_amdgcn_sched_group_barrier(0x008, 1, 0);
        __builtin_amdgcn_sched_group_barrier(0x402, 12, 0);
        __builtin_amdgcn_sched_group_barrier(0x008, 1, 0);
        __builtin_amdgcn_sched_barrier(0);
      }
      ka_ += 8 * 1024; va_ += 8 * VT_STRIDE; vb_ += 8 * VT_STRIDE;
    }
    TS(1, 5);
    const float il = 1.0f / __shfl(o[8], r);
    u32x4 w0 = {pack2(o[0] * il, o[1] * il), pack2(o[2] * il, o[3] * il), pack2(o[4] * il, o[5] * il),
                pack2(o[6] * il, o[7] * il)};
    *(u32x4*)(of + ((size_t)((2 * rb + rt) * 32 + head) * 64 + lane) * 8) = w0;
  } else {
    __builtin_amdgcn_s_setprio(3);
    const bool act = b < NHELP;
    const int g = lane >> 4, i = lane & 15;
    const int f0 = b * 16;
    f32x4 rv[8];
    unsigned wa[8][8];
    const u16* wp = ((i < 8) ? wg : wu) + f0 + 2 * (i & 7) + (size_t)(8 * g) * F_;
    if (act) {
#pragma unroll
      for (int c = 0; c < 8; ++c) rv[c] = *(const f32x4*)(rbuf + c * 256 + lane * 4);
#pragma unroll
      for (int s = 0; s < 8; ++s)
#pragma unroll
        for (int j = 0; j < 8; ++j) wa[s][j] = *(const unsigned*)(wp + (size_t)(32 * s + j) * F_);
    }
    __syncthreads();
    int i0 = 0, i1 = 0;
    if (act) top2_keys(rv, lane, i0, i1);
    TS(1, 3);
    __syncthreads();
    __syncthreads();
    TS(1, 4);
    if (act) {
      i0 = min(i0, S_ - 1);
      i1 = min(i1, S_ - 1);
      if (b == 0 && lane == 0) {
        const float v0 = rbuf[i0], v1 = rbuf[i1];
        sel[0] = i0; sel[1] = i1;
        ((float*)sel)[2] = rbf(1.0f / (1.0f + __expf(-v0)));
        ((float*)sel)[3] = rbf(1.0f / (1.0f + __expf(-v1)));
      }
      const int srow = (i == 0) ? i0 : i1;
      const u16* xp = xnf + ((size_t)((srow >> 5) * 32 + (g >> 1)) * 64 + (g & 1) * 32 + (srow & 31)) * 8;
      f32x4 a0 = {0.f, 0.f, 0.f, 0.f}, a1 = {0.f, 0.f, 0.f, 0.f};
      {
        bf16x8 xa[8], xb[8];
        unsigned wb[8][8];
#pragma unroll
        for (int s = 0; s < 8; ++s) xa[s] = *(const bf16x8*)(xp + (size_t)s * 1024);
#pragma unroll
        for (int s = 0; s < 8; ++s) {
          a0 = mfma16(xa[s], frag_lo(wa[s]), a0);
          a1 = mfma16(xa[s], frag_hi(wa[s]), a1);
        }
        __builtin_amdgcn_sched_barrier(0);
#pragma unroll
        for (int s = 0; s < 8; ++s) {
          xb[s] = *(const bf16x8*)(xp + (size_t)(8 + s) * 1024);
#pragma unroll
          for (int j = 0; j < 8; ++j) wb[s][j] = *(const unsigned*)(wp + (size_t)(32 * (8 + s) + j) * F_);
        }
#pragma unroll
        for (int s = 0; s < 8; ++s) {
          a0 = mfma16(xb[s], frag_lo(wb[s]), a0);
          a1 = mfma16(xb[s], frag_hi(wb[s]), a1);
        }
      }
      u16* hs = (u16*)(smem + LDS_H);
#pragma unroll
      for (int tk = 0; tk < 2; ++tk) {
        const float g0 = rbf(a0[tk]), g1 = rbf(a1[tk]);
        const float u0 = __shfl_xor(g0, 8), u1 = __shfl_xor(g1, 8);
        const float h0 = rbf(g0 / (1.0f + __expf(-g0))) * u0;
        const float h1 = rbf(g1 / (1.0f + __expf(-g1))) * u1;
        if (lane < 8) *(unsigned*)(hs + tk * 16 + 2 * lane) = pack2(h0, h1);
      }
      __builtin_amdgcn_fence(__ATOMIC_RELEASE, "wavefront");
      __builtin_amdgcn_wave_barrier();
      __builtin_amdgcn_fence(__ATOMIC_ACQUIRE, "wavefront");
      u32x4 hz = {0u, 0u, 0u, 0u};
      if (i < 2 && g < 2) hz = *(const u32x4*)(hs + i * 16 + 8 * g);
      const bf16x8 ha = __builtin_bit_cast(bf16x8, hz);
      const u16* dp = wd + (size_t)(f0 + 8 * (g & 1)) * D_ + 2 * i;
      float* yp = ypart + (size_t)b * 1024 + 2 * i;
#pragma unroll 1
      for (int gb = 0; gb < 2; ++gb) {
        unsigned wdv[8][8];
#pragma unroll
        for (int q = 0; q < 8; ++q)
#pragma unroll
          for (int j = 0; j < 8; ++j) wdv[q][j] = *(const unsigned*)(dp + (size_t)j * D_ + (gb * 8 + q) * 32);
#pragma unroll
        for (int q = 0; q < 8; ++q) {
          const f32x4 z = {0.f, 0.f, 0.f, 0.f};
          const f32x4 y0 = mfma16(ha, frag_lo(wdv[q]), z);
          const f32x4 y1 = mfma16(ha, frag_hi(wdv[q]), z);
          if (g == 0) {
            *(float2*)(yp + (gb * 8 + q) * 32) = make_float2(y0[0], y1[0]);
            *(float2*)(yp + 512 + (gb * 8 + q) * 32) = make_float2(y0[1], y1[1]);
          }
        }
      }
    }
  }
  TS(1, 6); TSR(1, 9);
}

static constexpr int CH_STRIDE = 1056;
static constexpr int LDS_Y = 64 * CH_STRIDE;
__global__ __launch_bounds__(512) void kc_out(const u16* __restrict__ x, const u16* __restrict__ of,
                                              const u16* __restrict__ wof, const float2* __restrict__ stats,
                                              const float* __restrict__ ypart, const int* __restrict__ sel,
                                              u16* __restrict__ out) {
  __shared__ __attribute__((aligned(16))) char smem[LDS_Y + 2 * 8 * 256];
  const int tid = threadIdx.x;
  const int lane = tid & 63, wave = tid >> 6;
  TSR(2, 8); TS(2, 0);
  const int bb = blockIdx.x;
  const int rb = (bb & 7) + 8 * ((bb >> 3) & 3), cg = bb >> 5;
  const int rt = wave >> 2, ks4 = wave & 3;
  const int idx0 = sel[0], idx1 = sel[1];
  bf16x8 af[2][8], bfr[8];
  const u16* ap = wof + ((size_t)(2 * cg) * 32 + 8 * ks4) * 512 + lane * 8;
  const u16* bp = of + ((size_t)(2 * rb + rt) * 32 + 8 * ks4) * 512 + lane * 8;
#pragma unroll
  for (int s = 0; s < 8; ++s) {
    bfr[s] = *(const bf16x8*)(bp + s * 512);
    af[0][s] = *(const bf16x8*)(ap + s * 512);
    af[1][s] = *(const bf16x8*)(ap + 32 * 512 + s * 512);
  }
  const int r64 = tid >> 3, oct = tid & 7;
  const int row = rb * 64 + r64;
  const size_t off = (size_t)row * D_ + cg * 64 + oct * 8;
  const u32x4 xv = *(const u32x4*)(x + off);
  const float2 st = stats[row];
#pragma unroll
  for (int tk = 0; tk < 2; ++tk) {
    const int idx = tk ? idx1 : idx0;
    if ((unsigned)(idx - rb * 64) < 64u) {
      const int pg = tid >> 4;
      const float* yp = ypart + (size_t)(pg * 2 + tk) * 512 + cg * 64 + (tid & 15) * 4;
      f32x4 y = *(const f32x4*)yp + *(const f32x4*)(yp + 32 * 1024);
      if (pg + 64 < NHELP) y += *(const f32x4*)(yp + 64 * 1024);
#pragma unroll
      for (int e = 0; e < 4; ++e) {
        y[e] += __shfl_xor(y[e], 16);
        y[e] += __shfl_xor(y[e], 32);
      }
      if (lane < 16) *(f32x4*)(smem + LDS_Y + (tk * 8 + wave) * 256 + lane * 16) = y;
    }
  }
  f32x16 acc[2];
#pragma unroll
  for (int e = 0; e < 16; ++e) { acc[0][e] = 0.f; acc[1][e] = 0.f; }
#pragma unroll
  for (int s = 0; s < 8; ++s) {
    acc[0] = mfma32(af[0][s], bfr[s], acc[0]);
    acc[1] = mfma32(af[1][s], bfr[s], acc[1]);
  }
  TS(2, 1);
#pragma unroll
  for (int ct = 0; ct < 2; ++ct)
#pragma unroll
    for (int q4 = 0; q4 < 4; ++q4) {
      f32x4 p = {acc[ct][4 * q4], acc[ct][4 * q4 + 1], acc[ct][4 * q4 + 2], acc[ct][4 * q4 + 3]};
      *(f32x4*)(smem + ((wave * 2 + ct) * 4 + q4) * CH_STRIDE + lane * 16) = p;
    }
  __syncthreads();
  TS(2, 2);
  f32x4 lo = {0.f, 0.f, 0.f, 0.f}, hi = {0.f, 0.f, 0.f, 0.f};
#pragma unroll
  for (int k = 0; k < 4; ++k) {
    const char* p = smem + (((r64 >> 5) * 4 + k) * 8 + oct) * CH_STRIDE + (r64 & 31) * 16;
    lo += *(const f32x4*)p;
    hi += *(const f32x4*)(p + 512);
  }
  float upd[8];
#pragma unroll
  for (int e = 0; e < 8; ++e) upd[e] = 0.f;
  if (row == idx0 || row == idx1) {
    const int tk = (row == idx0) ? 0 : 1;
    f32x4 ya = {0.f, 0.f, 0.f, 0.f}, yb = {0.f, 0.f, 0.f, 0.f};
#pragma unroll
    for (int p = 0; p < 8; ++p) {
      const char* yp = smem + LDS_Y + (tk * 8 + p) * 256 + oct * 32;
      ya += *(const f32x4*)yp;
      yb += *(const f32x4*)(yp + 16);
    }
    const float rs = ((const float*)sel)[2 + tk];
#pragma unroll
    for (int e = 0; e < 4; ++e) { upd[e] = rbf(rbf(ya[e]) * rs); upd[4 + e] = rbf(rbf(yb[e]) * rs); }
  }
  const float inv = 1.0f / st.y;
  float res[8];
#pragma unroll
  for (int e = 0; e < 4; ++e) {
    const float x0 = bf2f(xv[e] & 0xffffu), x1 = bfhi2f(xv[e]);
    const float a0 = rbf(e < 2 ? lo[2 * e] : hi[2 * e - 4]);
    const float a1 = rbf(e < 2 ? lo[2 * e + 1] : hi[2 * e - 3]);
    res[2 * e] = rbf(x0 + a0) + rbf(ln_elem(x0, st.x, st.y, inv) + upd[2 * e]);
    res[2 * e + 1] = rbf(x1 + a1) + rbf(ln_elem(x1, st.x, st.y, inv) + upd[2 * e + 1]);
  }
  u32x4 o = {pack2(res[0], res[1]), pack2(res[2], res[3]), pack2(res[4], res[5]), pack2(res[6], res[7])};
  *(u32x4*)(out + off) = o;
  TS(2, 3); TSR(2, 9);
}

extern "C" void kernel_launch(void* const* d_in, const int* in_sizes, int n_in, void* d_out, int out_size,
                              void* d_ws, size_t ws_size, hipStream_t stream) {
  const u16* x = (const u16*)d_in[0];
  const u16* wq = (const u16*)d_in[1];
  const u16* wk = (const u16*)d_in[2];
  const u16* wv = (const u16*)d_in[3];
  const u16* wo = (const u16*)d_in[4];
  const u16* wr = (const u16*)d_in[6];
  const u16* wg = (const u16*)d_in[7];
  const u16* wu = (const u16*)d_in[8];
  const u16* wd = (const u16*)d_in[9];
  char* ws = (char*)d_ws;
  u16* xnf = (u16*)(ws + OFF_XNF);
  u16* of = (u16*)(ws + OFF_OF);
  u16* wqf = (u16*)(ws + OFF_WQF);
  u16* wof = (u16*)(ws + OFF_WOF);
  u16* kf = (u16*)(ws + OFF_KF);
  u16* vf = (u16*)(ws + OFF_VF);
  float* rbuf = (float*)(ws + OFF_R);
  float2* stats = (float2*)(ws + OFF_STATS);
  float* ypart = (float*)(ws + OFF_Y);
  int* sel = (int*)(ws + OFF_SEL);
  u16* out = (u16*)d_out;

  ka_prep<<<192, 512, 0, stream>>>(x, wq, wk, wv, wo, wr, xnf, wqf, wof, kf, vf, rbuf, stats);
  kb_attn<<<256, 576, 0, stream>>>(xnf, wqf, kf, vf, rbuf, wg, wu, wd, of, ypart, sel);
  kc_out<<<256, 512, 0, stream>>>(x, of, wof, stats, ypart, sel, out);
}
```

```cpp
#include <hip/hip_runtime.h>
#include <hip/hip_bf16.h>

typedef unsigned short u16;
typedef __attribute__((ext_vector_type(8))) __bf16 bf16x8;
typedef __attribute__((ext_vector_type(2))) __bf16 bf16x2;
typedef __attribute__((ext_vector_type(2))) float f32x2;
typedef __attribute__((ext_vector_type(4))) float f32x4;
typedef __attribute__((ext_vector_type(16))) float f32x16;
typedef __attribute__((ext_vector_type(4))) unsigned u32x4;
typedef __attribute__((ext_vector_type(2))) unsigned u32x2;

#define DI __device__ __forceinline__
#define TS(k, n)
#define TSR(k, n)

static constexpr int S_ = 2048;
static constexpr int D_ = 512;
static constexpr int F_ = 1376;

#define LDSF(a) (*(const __attribute__((address_space(3))) bf16x8*)(uintptr_t)(a))
DI float bf2f(unsigned u) { return __uint_as_float(u << 16); }
DI float bfhi2f(unsigned u) { return __uint_as_float(u & 0xffff0000u); }
DI float rbf(float f) { return (float)(__bf16)f; }
DI unsigned pack2(float lo, float hi) {
  f32x2 p = {lo, hi};
  bf16x2 q = __builtin_convertvector(p, bf16x2);
  return __builtin_bit_cast(unsigned, q);
}
DI f32x4 mfma16(bf16x8 a, bf16x8 b, f32x4 c) { return __builtin_amdgcn_mfma_f32_16x16x32_bf16(a, b, c, 0, 0, 0); }
DI f32x16 mfma32(bf16x8 a, bf16x8 b, f32x16 c) { return __builtin_amdgcn_mfma_f32_32x32x16_bf16(a, b, c, 0, 0, 0); }
DI unsigned perm_lo(unsigned hi_src, unsigned lo_src) { return __builtin_amdgcn_perm(hi_src, lo_src, 0x05040100u); }
DI unsigned perm_hi(unsigned hi_src, unsigned lo_src) { return __builtin_amdgcn_perm(hi_src, lo_src, 0x07060302u); }

DI bf16x8 frag_lo(const unsigned* w) {
  u32x4 r = {perm_lo(w[1], w[0]), perm_lo(w[3], w[2]), perm_lo(w[5], w[4]), perm_lo(w[7], w[6])};
  return __builtin_bit_cast(bf16x8, r);
}
DI bf16x8 frag_hi(const unsigned* w) {
  u32x4 r = {perm_hi(w[1], w[0]), perm_hi(w[3], w[2]), perm_hi(w[5], w[4]), perm_hi(w[7], w[6])};
  return __builtin_bit_cast(bf16x8, r);
}
DI float ln_elem(float xv, float mb, float den, float inv) {
  const float d = rbf(xv - mb);
  float q = d * inv;
  const float rem = __builtin_fmaf(-q, den, d);
  q = __builtin_fmaf(rem, inv, q);
  return rbf(q);
}

static constexpr int VT_STRIDE = 1056;
static constexpr int VF_BYTES = 64 * VT_STRIDE;
static constexpr size_t OFF_XNF = 0;
static constexpr size_t OFF_OF = OFF_XNF + (size_t)S_ * D_ * 2;
static constexpr size_t OFF_WQF = OFF_OF + (size_t)S_ * D_ * 2;
static constexpr size_t OFF_WOF = OFF_WQF + (size_t)D_ * D_ * 2;
static constexpr size_t OFF_KF = OFF_WOF + (size_t)D_ * D_ * 2;
static constexpr size_t OFF_VF = OFF_KF + 65536;
static constexpr size_t OFF_R = OFF_VF + VF_BYTES;
static constexpr size_t OFF_STATS = OFF_R + S_ * 4;
static constexpr int NHELP = 86;
static constexpr size_t OFF_Y = OFF_STATS + S_ * 8;
static constexpr size_t OFF_SEL = OFF_Y + NHELP * 1024 * 4;

static constexpr int XN_STRIDE = 1040;
__global__ __launch_bounds__(512) void ka_prep(const u16* __restrict__ x, const u16* __restrict__ wq,
                                               const u16* __restrict__ wk, const u16* __restrict__ wv,
                                               const u16* __restrict__ wo, const u16* __restrict__ wr,
                                               u16* __restrict__ xnf, u16* __restrict__ wqf, u16* __restrict__ wof,
                                               u16* __restrict__ kf, u16* __restrict__ vf,
                                               float* __restrict__ rbuf, float2* __restrict__ stats) {
  __shared__ __attribute__((aligned(16))) char smem[16 * XN_STRIDE + 8 * 5 * 64 * 16];
  const int tid = threadIdx.x;
  const int lane = tid & 63, wave = tid >> 6;
  TSR(0, 8); TS(0, 0);
  if (blockIdx.x >= 128) {
    const int pb = blockIdx.x - 128;
    const bool iso = pb >= 32;
    const int cp = tid & 255, ko = (pb & 31) * 2 + (tid >> 8);
    const int ks = ko >> 1, hh = ko & 1;
    const u16* W = (iso ? wo : wq) + 2 * cp;
    unsigned w[8];
#pragma unroll
    for (int j = 0; j < 8; ++j) {
      const int row = 16 * ks + (iso ? (8 * (j >> 2) + 4 * hh + (j & 3)) : (8 * hh + j));
      w[j] = *(const unsigned*)(W + row * D_);
    }
    u16* dst = (iso ? wof : wqf) + (((cp >> 4) * 32 + ks) * 64 + hh * 32 + ((2 * cp) & 31)) * 8;
    *(bf16x8*)dst = frag_lo(w);
    *(bf16x8*)(dst + 8) = frag_hi(w);
    TS(0, 3); TSR(0, 9);
    return;
  }
  const int row0 = blockIdx.x * 16;
  const int rt = blockIdx.x >> 1, th = blockIdx.x & 1;
  const int g = lane >> 4, i = lane & 15;
  const int k0 = wave * 64 + 8 * g;
  u32x4 xr[2];
#pragma unroll
  for (int rr = 0; rr < 2; ++rr) xr[rr] = *(const u32x4*)(x + (size_t)(row0 + 2 * wave + rr) * D_ + 8 * lane);
  const u16* wbase = ((i < 4) ? (wk + 4 * i) : (i < 8 ? wv + 4 * (i - 4) : wk)) + k0 * 16;
  u32x2 w[2][8];
#pragma unroll
  for (int s = 0; s < 2; ++s)
#pragma unroll
    for (int j = 0; j < 8; ++j) w[s][j] = *(const u32x2*)(wbase + (32 * s + j) * 16);
  bf16x8 rf[2];
#pragma unroll
  for (int s = 0; s < 2; ++s) rf[s] = *(const bf16x8*)(wr + k0 + 32 * s);

  float s1[2], s2[2];
#pragma unroll
  for (int rr = 0; rr < 2; ++rr) {
    float a = 0.f, q = 0.f;
#pragma unroll
    for (int e = 0; e < 4; ++e) {
      const float lo = bf2f(xr[rr][e] & 0xffffu), hi = bfhi2f(xr[rr][e]);
      a += lo + hi;
      q = __builtin_fmaf(lo, lo, q);
      q = __builtin_fmaf(hi, hi, q);
    }
    s1[rr] = a; s2[rr] = q;
  }
#pragma unroll
  for (int off = 32; off >= 1; off >>= 1)
#pragma unroll
    for (int rr = 0; rr < 2; ++rr) {
      s1[rr] += __shfl_xor(s1[rr], off);
      s2[rr] += __shfl_xor(s2[rr], off);
    }
#pragma unroll
  for (int rr = 0; rr < 2; ++rr) {
    const float mean = s1[rr] * (1.0f / D_);
    float var = s2[rr] * (1.0f / D_) - mean * mean;
    var = fmaxf(var, 0.f);
    const float mb = rbf(mean);
    const float den = rbf(__builtin_sqrtf(rbf(rbf(var) + 1e-5f)));
    const float inv = 1.0f / den;
    if (lane == rr) stats[row0 + 2 * wave + rr] = make_float2(mb, den);
    u32x4 o;
#pragma unroll
    for (int e = 0; e < 4; ++e)
      o[e] = pack2(ln_elem(bf2f(xr[rr][e] & 0xffffu), mb, den, inv), ln_elem(bfhi2f(xr[rr][e]), mb, den, inv));
    *(u32x4*)(smem + (2 * wave + rr) * XN_STRIDE + lane * 16) = o;
  }
  if (tid < 2 && th == 0) {
    const unsigned cv = tid ? 0u : 0x3f803f80u;
    u32x4 c4 = {cv, cv, cv, cv};
    *(u32x4*)((char*)vf + rt * VT_STRIDE + 1024 + tid * 16) = c4;
  }
  TS(0, 1);
  __syncthreads();
  {
    const int r = lane & 15, hh = (lane >> 4) & 1, kp = lane >> 5;
#pragma unroll
    for (int s = 0; s < 2; ++s) {
      const int ks = 4 * wave + 2 * s + kp;
      const u32x4 v = *(const u32x4*)(smem + r * XN_STRIDE + ks * 32 + hh * 16);
      *(u32x4*)(xnf + ((size_t)(rt * 32 + ks) * 64 + hh * 32 + th * 16 + r) * 8) = v;
    }
  }
  f32x4* red = (f32x4*)(smem + 16 * XN_STRIDE);
  {
    f32x4 acc[4], ar;
    ar = (f32x4){0.f, 0.f, 0.f, 0.f};
#pragma unroll
    for (int c = 0; c < 4; ++c) acc[c] = (f32x4){0.f, 0.f, 0.f, 0.f};
#pragma unroll
    for (int s = 0; s < 2; ++s) {
      unsigned wx[8], wy[8];
#pragma unroll
      for (int j = 0; j < 8; ++j) { wx[j] = w[s][j][0]; wy[j] = w[s][j][1]; }
      bf16x8 b[4] = {frag_lo(wx), frag_hi(wx), frag_lo(wy), frag_hi(wy)};
      const bf16x8 xa = *(const bf16x8*)(smem + i * XN_STRIDE + (k0 + 32 * s) * 2);
#pragma unroll
      for (int c = 0; c < 4; ++c) acc[c] = mfma16(xa, b[c], acc[c]);
      ar = mfma16(xa, rf[s], ar);
    }
#pragma unroll
    for (int c = 0; c < 4; ++c) red[(wave * 5 + c) * 64 + lane] = acc[c];
    red[(wave * 5 + 4) * 64 + lane] = ar;
  }
  __syncthreads();
  TS(0, 2);
  if (wave < 5) {
    f32x4 v = red[wave * 64 + lane];
#pragma unroll
    for (int wv_ = 1; wv_ < 8; ++wv_) v += red[(wv_ * 5 + wave) * 64 + lane];
    const int kk = 16 * th + 4 * g;
    if (wave == 4) {
      if (i == 0) {
        f32x4 o = {rbf(v[0]), rbf(v[1]), rbf(v[2]), rbf(v[3])};
        *(f32x4*)(rbuf + row0 + 4 * g) = o;
      }
    } else if (i < 4) {
      const int hh = i & 1, j = (i >> 1) * 4 + wave;
#pragma unroll
      for (int e = 0; e < 4; ++e)
        kf[rt * 512 + (hh * 32 + kk + e) * 8 + j] = __builtin_bit_cast(u16, (__bf16)v[e]);
    } else if (i < 8) {
      const int d = 4 * (i - 4) + wave;
      const int hh = g & 1, jh = g >> 1;
      u32x2 o = {pack2(v[0], v[1]), pack2(v[2], v[3])};
      *(u32x2*)(vf + rt * (VT_STRIDE / 2) + th * 256 + (hh * 16 + d) * 8 + jh * 4) = o;
    }
  }
  TS(0, 3); TSR(0, 9);
}

DI void top2_keys(const f32x4* v, int lane, int& i0, int& i1) {
  unsigned k0 = 0, k1 = 0;
#pragma unroll
  for (int c = 0; c < 8; ++c)
#pragma unroll
    for (int e = 0; e < 4; ++e) {
      const unsigned u = __float_as_uint(v[c][e]);
      const unsigned ord = (u & 0x80000000u) ? ~u : (u | 0x80000000u);
      const unsigned key = (ord & 0xffff0000u) | (unsigned)(2047 - (c * 256 + lane * 4 + e));
      k1 = max(k1, min(k0, key));
      k0 = max(k0, key);
    }
#pragma unroll
  for (int off = 1; off < 64; off <<= 1) {
    const unsigned o0 = __shfl_xor(k0, off), o1 = __shfl_xor(k1, off);
    const unsigned n0 = max(k0, o0);
    const unsigned n1 = max(min(k0, o0), max(k1, o1));
    k0 = n0; k1 = n1;
  }
  i0 = 2047 - (int)(k0 & 0x7ffu);
  i1 = 2047 - (int)(k1 & 0x7ffu);
}

static constexpr int LDS_K = 0;
static constexpr int LDS_V = 65536;
static constexpr int LDS_H = 65536 + VF_BYTES + 2048;
static constexpr int LDS_TOTAL = LDS_H + 64;

__global__ __launch_bounds__(576) void kb_attn(const u16* __restrict__ xnf, const u16* __restrict__ wqf,
                                               const u16* __restrict__ kf, const u16* __restrict__ vf,
                                               const float* __restrict__ rbuf, const u16* __restrict__ wg,
                                               const u16* __restrict__ wu, const u16* __restrict__ wd,
                                               u16* __restrict__ of, float* __restrict__ ypart,
                                               int* __restrict__ sel) {
  __shared__ __attribute__((aligned(16))) char smem[LDS_TOTAL];
  const int tid = threadIdx.x;
  const int lane = tid & 63, wave = tid >> 6;
  const int b = blockIdx.x;
  TSR(1, 8); TS(1, 0);

  if (wave < 8) {
    const int rb = (b & 7) + 8 * ((b >> 3) & 3), hg = b >> 5;
    const int rt = wave >> 2, ks4 = wave & 3;
    const int r = lane & 31, hh = lane >> 5;
    {
      u32x4 tk[8];
#pragma unroll
      for (int c = 0; c < 8; ++c) tk[c] = *(const u32x4*)((const char*)kf + c * 8192 + tid * 16);
      bf16x8 af[2][8], bfr[8];
      const int rtq = wave & 1, kq = wave >> 1;
      const u16* ap = wqf + ((size_t)(2 * hg) * 32 + 8 * kq) * 512 + lane * 8;
      const u16* bp = xnf + ((size_t)(2 * rb + rtq) * 32 + 8 * kq) * 512 + lane * 8;
#pragma unroll
      for (int s = 0; s < 8; ++s) {
        bfr[s] = *(const bf16x8*)(bp + s * 512);
        af[0][s] = *(const bf16x8*)(ap + s * 512);
        af[1][s] = *(const bf16x8*)(ap + 32 * 512 + s * 512);
      }
      __builtin_amdgcn_sched_barrier(0);
#pragma unroll
      for (int c = 0; c < 8; ++c) *(u32x4*)(smem + LDS_K + c * 8192 + tid * 16) = tk[c];
      __builtin_amdgcn_sched_barrier(0);
      f32x16 acc[2];
#pragma unroll
      for (int e = 0; e < 16; ++e) { acc[0][e] = 0.f; acc[1][e] = 0.f; }
#pragma unroll
      for (int s = 0; s < 8; ++s) {
        acc[0] = mfma32(af[0][s], bfr[s], acc[0]);
        acc[1] = mfma32(af[1][s], bfr[s], acc[1]);
      }
#pragma unroll
      for (int dt = 0; dt < 2; ++dt)
#pragma unroll
        for (int q4 = 0; q4 < 4; ++q4) {
          f32x4 p = {acc[dt][4 * q4], acc[dt][4 * q4 + 1], acc[dt][4 * q4 + 2], acc[dt][4 * q4 + 3]};
          *(f32x4*)(smem + LDS_V + ((((rtq * 4 + kq) * 2 + dt) * 4 + q4) * 64 + lane) * 16) = p;
        }
    }
    __builtin_amdgcn_sched_barrier(0);
    u32x4 tv[8], tvx;
#pragma unroll
    for (int c = 0; c < 8; ++c) tv[c] = *(const u32x4*)((const char*)vf + c * 8192 + tid * 16);
    tvx = *(const u32x4*)((const char*)vf + 65536 + (tid & 127) * 16);
    TS(1, 1);
    __syncthreads();
    TS(1, 2);
    const int hl = wave & 3;
    const int head = hg * 4 + hl;
    bf16x8 qf;
    {
      f32x4 a0 = {0.f, 0.f, 0.f, 0.f}, a1 = {0.f, 0.f, 0.f, 0.f};
#pragma unroll
      for (int k = 0; k < 4; ++k) {
        const char* p = smem + LDS_V + ((((rt * 4 + k) * 2 + (hl >> 1)) * 4 + 2 * (hl & 1)) * 64 + lane) * 16;
        a0 += *(const f32x4*)p;
        a1 += *(const f32x4*)(p + 1024);
      }
      const float SC = 0.25f * 1.44269504088896340736f;
      u32x4 pk = {pack2(rbf(a0[0]) * SC, rbf(a0[1]) * SC), pack2(rbf(a0[2]) * SC, rbf(a0[3]) * SC),
                  pack2(rbf(a1[0]) * SC, rbf(a1[1]) * SC), pack2(rbf(a1[2]) * SC, rbf(a1[3]) * SC)};
      qf = __builtin_bit_cast(bf16x8, pk);
    }

    const char* kp = smem + LDS_K + lane * 16;
    float m0 = -__builtin_inff(), m1 = -__builtin_inff();
    f32x16 zero16;
#pragma unroll
    for (int e = 0; e < 16; ++e) zero16[e] = 0.f;
    {
      f32x16 sa = mfma32(*(const bf16x8*)(kp), qf, zero16);
      f32x16 sb = mfma32(*(const bf16x8*)(kp + 1024), qf, zero16);
#pragma unroll 4
      for (int t = 2; t < 64; t += 2) {
        const bf16x8 ka = *(const bf16x8*)(kp + t * 1024);
        const bf16x8 kb = *(const bf16x8*)(kp + t * 1024 + 1024);
        const f32x16 na = mfma32(ka, qf, zero16);
#pragma unroll
        for (int e = 0; e < 16; e += 2) m0 = fmaxf(fmaxf(m0, sa[e]), sa[e + 1]);
        const f32x16 nb = mfma32(kb, qf, zero16);
#pragma unroll
        for (int e = 0; e < 16; e += 2) m1 = fmaxf(fmaxf(m1, sb[e]), sb[e + 1]);
        sa = na; sb = nb;
      }
#pragma unroll
      for (int e = 0; e < 16; e += 2) m0 = fmaxf(fmaxf(m0, sa[e]), sa[e + 1]);
#pragma unroll
      for (int e = 0; e < 16; e += 2) m1 = fmaxf(fmaxf(m1, sb[e]), sb[e + 1]);
    }
    float m = fmaxf(m0, m1);
    m = fmaxf(m, __shfl_xor(m, 32));
    TS(1, 3);
    __syncthreads();
#pragma unroll
    for (int c = 0; c < 8; ++c) *(u32x4*)(smem + LDS_V + c * 8192 + tid * 16) = tv[c];
    if (wave < 2) *(u32x4*)(smem + LDS_V + 65536 + tid * 16) = tvx;
    __syncthreads();
    TS(1, 4);

    f32x16 negm;
#pragma unroll
    for (int e = 0; e < 16; ++e) negm[e] = -m;
    f32x16 o;
#pragma unroll
    for (int e = 0; e < 16; ++e) o[e] = 0.f;
    const unsigned lbase = (unsigned)(uintptr_t)(__attribute__((address_space(3))) char*)smem;
    unsigned ka_ = lbase + LDS_K + lane * 16;
    unsigned va_ = lbase + LDS_V + ((r < 16) ? (hh * 16 + r) * 16 : (r == 16 ? 1024 : 1040));
    unsigned vb_ = lbase + LDS_V + ((r < 16) ? (hh * 16 + r) * 16 + 512 : (r == 16 ? 1024 : 1040));
    f32x16 s = mfma32(LDSF(ka_), qf, negm);
    bf16x8 kn = LDSF(ka_ + 1024);
    bf16x8 v0 = LDSF(va_);
    bf16x8 v1 = LDSF(vb_);
#pragma unroll 1
    for (int tb = 0; tb < 8; ++tb) {
      asm volatile("" : "+v"(ka_), "+v"(va_), "+v"(vb_));
#pragma unroll
      for (int u = 0; u < 8; ++u) {
        const f32x16 sn = mfma32(kn, qf, negm);
        const bf16x8 k2 = LDSF(ka_ + (u + 2) * 1024);
        const bf16x8 v0n = LDSF(va_ + (u + 1) * VT_STRIDE);
        const bf16x8 v1n = LDSF(vb_ + (u + 1) * VT_STRIDE);
        float p[16];
#pragma unroll
        for (int e = 0; e < 8; ++e) p[e] = __builtin_amdgcn_exp2f(s[e]);
        u32x4 pa = {pack2(p[0], p[1]), pack2(p[2], p[3]), pack2(p[4], p[5]), pack2(p[6], p[7])};
        o = mfma32(v0, __builtin_bit_cast(bf16x8, pa), o);
#pragma unroll
        for (int e = 8; e < 16; ++e) p[e] = __builtin_amdgcn_exp2f(s[e]);
        u32x4 pb = {pack2(p[8], p[9]), pack2(p[10], p[11]), pack2(p[12], p[13]), pack2(p[14], p[15])};
        o = mfma32(v1, __builtin_bit_cast(bf16x8, pb), o);
        s = sn; kn = k2; v0 = v0n; v1 = v1n;
        __builtin_amdgcn_sched_group_barrier(0x008, 1, 0);
        __builtin_amdgcn_sched_group_barrier(0x100, 3, 0);
        __builtin_amdgcn_sched_group_barrier(0x402, 12, 0);
        __builtin_amdgcn_sched_group_barrier(0x008, 1, 0);
        __builtin_amdgcn_sched_group_barrier(0x402, 12, 0);
        __builtin_amdgcn_sched_group_barrier(0x008, 1, 0);
        __builtin_amdgcn_sched_barrier(0);
      }
      ka_ += 8 * 1024; va_ += 8 * VT_STRIDE; vb_ += 8 * VT_STRIDE;
    }
    TS(1, 5);
    const float il = 1.0f / __shfl(o[8], r);
    u32x4 w0 = {pack2(o[0] * il, o[1] * il), pack2(o[2] * il, o[3] * il), pack2(o[4] * il, o[5] * il),
                pack2(o[6] * il, o[7] * il)};
    *(u32x4*)(of + ((size_t)((2 * rb + rt) * 32 + head) * 64 + lane) * 8) = w0;
  } else {
    __builtin_amdgcn_s_setprio(3);
    const bool act = b < NHELP;
    const int g = lane >> 4, i = lane & 15;
    const int f0 = b * 16;
    f32x4 rv[8];
    unsigned wa[8][8];
    const u16* wp = ((i < 8) ? wg : wu) + f0 + 2 * (i & 7) + (size_t)(8 * g) * F_;
    if (act) {
#pragma unroll
      for (int c = 0; c < 8; ++c) rv[c] = *(const f32x4*)(rbuf + c * 256 + lane * 4);
#pragma unroll
      for (int s = 0; s < 8; ++s)
#pragma unroll
        for (int j = 0; j < 8; ++j) wa[s][j] = *(const unsigned*)(wp + (size_t)(32 * s + j) * F_);
    }
    __syncthreads();
    int i0 = 0, i1 = 0;
    if (act) top2_keys(rv, lane, i0, i1);
    TS(1, 3);
    __syncthreads();
    __syncthreads();
    TS(1, 4);
    if (act) {
      i0 = min(i0, S_ - 1);
      i1 = min(i1, S_ - 1);
      if (b == 0 && lane == 0) {
        const float v0 = rbuf[i0], v1 = rbuf[i1];
        sel[0] = i0; sel[1] = i1;
        ((float*)sel)[2] = rbf(1.0f / (1.0f + __expf(-v0)));
        ((float*)sel)[3] = rbf(1.0f / (1.0f + __expf(-v1)));
      }
      const int srow = (i == 0) ? i0 : i1;
      const u16* xp = xnf + ((size_t)((srow >> 5) * 32 + (g >> 1)) * 64 + (g & 1) * 32 + (srow & 31)) * 8;
      f32x4 a0 = {0.f, 0.f, 0.f, 0.f}, a1 = {0.f, 0.f, 0.f, 0.f};
      {
        bf16x8 xa[8], xb[8];
        unsigned wb[8][8];
#pragma unroll
        for (int s = 0; s < 8; ++s) xa[s] = *(const bf16x8*)(xp + (size_t)s * 1024);
#pragma unroll
        for (int s = 0; s < 8; ++s) {
          a0 = mfma16(xa[s], frag_lo(wa[s]), a0);
          a1 = mfma16(xa[s], frag_hi(wa[s]), a1);
        }
        __builtin_amdgcn_sched_barrier(0);
#pragma unroll
        for (int s = 0; s < 8; ++s) {
          xb[s] = *(const bf16x8*)(xp + (size_t)(8 + s) * 1024);
#pragma unroll
          for (int j = 0; j < 8; ++j) wb[s][j] = *(const unsigned*)(wp + (size_t)(32 * (8 + s) + j) * F_);
        }
#pragma unroll
        for (int s = 0; s < 8; ++s) {
          a0 = mfma16(xb[s], frag_lo(wb[s]), a0);
          a1 = mfma16(xb[s], frag_hi(wb[s]), a1);
        }
      }
      u16* hs = (u16*)(smem + LDS_H);
#pragma unroll
      for (int tk = 0; tk < 2; ++tk) {
        const float g0 = rbf(a0[tk]), g1 = rbf(a1[tk]);
        const float u0 = __shfl_xor(g0, 8), u1 = __shfl_xor(g1, 8);
        const float h0 = rbf(g0 / (1.0f + __expf(-g0))) * u0;
        const float h1 = rbf(g1 / (1.0f + __expf(-g1))) * u1;
        if (lane < 8) *(unsigned*)(hs + tk * 16 + 2 * lane) = pack2(h0, h1);
      }
      __builtin_amdgcn_fence(__ATOMIC_RELEASE, "wavefront");
      __builtin_amdgcn_wave_barrier();
      __builtin_amdgcn_fence(__ATOMIC_ACQUIRE, "wavefront");
      u32x4 hz = {0u, 0u, 0u, 0u};
      if (i < 2 && g < 2) hz = *(const u32x4*)(hs + i * 16 + 8 * g);
      const bf16x8 ha = __builtin_bit_cast(bf16x8, hz);
      const u16* dp = wd + (size_t)(f0 + 8 * (g & 1)) * D_ + 2 * i;
      float* yp = ypart + (size_t)b * 1024 + 2 * i;
#pragma unroll 1
      for (int gb = 0; gb < 2; ++gb) {
        unsigned wdv[8][8];
#pragma unroll
        for (int q = 0; q < 8; ++q)
#pragma unroll
          for (int j = 0; j < 8; ++j) wdv[q][j] = *(const unsigned*)(dp + (size_t)j * D_ + (gb * 8 + q) * 32);
#pragma unroll
        for (int q = 0; q < 8; ++q) {
          const f32x4 z = {0.f, 0.f, 0.f, 0.f};
          const f32x4 y0 = mfma16(ha, frag_lo(wdv[q]), z);
          const f32x4 y1 = mfma16(ha, frag_hi(wdv[q]), z);
          if (g == 0) {
            *(float2*)(yp + (gb * 8 + q) * 32) = make_float2(y0[0], y1[0]);
            *(float2*)(yp + 512 + (gb * 8 + q) * 32) = make_float2(y0[1], y1[1]);
          }
        }
      }
    }
  }
  TS(1, 6); TSR(1, 9);
}

static constexpr int CH_STRIDE = 1056;
static constexpr int LDS_Y = 64 * CH_STRIDE;
__global__ __launch_bounds__(512) void kc_out(const u16* __restrict__ x, const u16* __restrict__ of,
                                              const u16* __restrict__ wof, const float2* __restrict__ stats,
                                              const float* __restrict__ ypart, const int* __restrict__ sel,
                                              u16* __restrict__ out) {
  __shared__ __attribute__((aligned(16))) char smem[LDS_Y + 2 * 8 * 256];
  const int tid = threadIdx.x;
  const int lane = tid & 63, wave = tid >> 6;
  TSR(2, 8); TS(2, 0);
  const int bb = blockIdx.x;
  const int rb = (bb & 7) + 8 * ((bb >> 3) & 3), cg = bb >> 5;
  const int rt = wave & 1, ks4 = wave >> 1;
  const int idx0 = sel[0], idx1 = sel[1];
  bf16x8 af[2][8], bfr[8];
  const u16* ap = wof + ((size_t)(2 * cg) * 32 + 8 * ks4) * 512 + lane * 8;
  const u16* bp = of + ((size_t)(2 * rb + rt) * 32 + 8 * ks4) * 512 + lane * 8;
#pragma unroll
  for (int s = 0; s < 8; ++s) {
    bfr[s] = *(const bf16x8*)(bp + s * 512);
    af[0][s] = *(const bf16x8*)(ap + s * 512);
    af[1][s] = *(const bf16x8*)(ap + 32 * 512 + s * 512);
  }
  const int r64 = tid >> 3, oct = tid & 7;
  const int row = rb * 64 + r64;
  const size_t off = (size_t)row * D_ + cg * 64 + oct * 8;
  const u32x4 xv = *(const u32x4*)(x + off);
  const float2 st = stats[row];
#pragma unroll
  for (int tk = 0; tk < 2; ++tk) {
    const int idx = tk ? idx1 : idx0;
    if ((unsigned)(idx - rb * 64) < 64u) {
      const int pg = tid >> 4;
      const float* yp = ypart + (size_t)(pg * 2 + tk) * 512 + cg * 64 + (tid & 15) * 4;
      f32x4 y = *(const f32x4*)yp + *(const f32x4*)(yp + 32 * 1024);
      if (pg + 64 < NHELP) y += *(const f32x4*)(yp + 64 * 1024);
#pragma unroll
      for (int e = 0; e < 4; ++e) {
        y[e] += __shfl_xor(y[e], 16);
        y[e] += __shfl_xor(y[e], 32);
      }
      if (lane < 16) *(f32x4*)(smem + LDS_Y + (tk * 8 + wave) * 256 + lane * 16) = y;
    }
  }
  f32x16 acc[2];
#pragma unroll
  for (int e = 0; e < 16; ++e) { acc[0][e] = 0.f; acc[1][e] = 0.f; }
#pragma unroll
  for (int s = 0; s < 8; ++s) {
    acc[0] = mfma32(af[0][s], bfr[s], acc[0]);
    acc[1] = mfma32(af[1][s], bfr[s], acc[1]);
  }
  TS(2, 1);
#pragma unroll
  for (int ct = 0; ct < 2; ++ct)
#pragma unroll
    for (int q4 = 0; q4 < 4; ++q4) {
      f32x4 p = {acc[ct][4 * q4], acc[ct][4 * q4 + 1], acc[ct][4 * q4 + 2], acc[ct][4 * q4 + 3]};
      *(f32x4*)(smem + (((rt * 4 + ks4) * 2 + ct) * 4 + q4) * CH_STRIDE + lane * 16) = p;
    }
  __syncthreads();
  TS(2, 2);
  f32x4 lo = {0.f, 0.f, 0.f, 0.f}, hi = {0.f, 0.f, 0.f, 0.f};
#pragma unroll
  for (int k = 0; k < 4; ++k) {
    const char* p = smem + (((r64 >> 5) * 4 + k) * 8 + oct) * CH_STRIDE + (r64 & 31) * 16;
    lo += *(const f32x4*)p;
    hi += *(const f32x4*)(p + 512);
  }
  float upd[8];
#pragma unroll
  for (int e = 0; e < 8; ++e) upd[e] = 0.f;
  if (row == idx0 || row == idx1) {
    const int tk = (row == idx0) ? 0 : 1;
    f32x4 ya = {0.f, 0.f, 0.f, 0.f}, yb = {0.f, 0.f, 0.f, 0.f};
#pragma unroll
    for (int p = 0; p < 8; ++p) {
      const char* yp = smem + LDS_Y + (tk * 8 + p) * 256 + oct * 32;
      ya += *(const f32x4*)yp;
      yb += *(const f32x4*)(yp + 16);
    }
    const float rs = ((const float*)sel)[2 + tk];
#pragma unroll
    for (int e = 0; e < 4; ++e) { upd[e] = rbf(rbf(ya[e]) * rs); upd[4 + e] = rbf(rbf(yb[e]) * rs); }
  }
  const float inv = 1.0f / st.y;
  float res[8];
#pragma unroll
  for (int e = 0; e < 4; ++e) {
    const float x0 = bf2f(xv[e] & 0xffffu), x1 = bfhi2f(xv[e]);
    const float a0 = rbf(e < 2 ? lo[2 * e] : hi[2 * e - 4]);
    const float a1 = rbf(e < 2 ? lo[2 * e + 1] : hi[2 * e - 3]);
    res[2 * e] = rbf(x0 + a0) + rbf(ln_elem(x0, st.x, st.y, inv) + upd[2 * e]);
    res[2 * e + 1] = rbf(x1 + a1) + rbf(ln_elem(x1, st.x, st.y, inv) + upd[2 * e + 1]);
  }
  u32x4 o = {pack2(res[0], res[1]), pack2(res[2], res[3]), pack2(res[4], res[5]), pack2(res[6], res[7])};
  *(u32x4*)(out + off) = o;
  TS(2, 3); TSR(2, 9);
}

extern "C" void kernel_launch(void* const* d_in, const int* in_sizes, int n_in, void* d_out, int out_size,
                              void* d_ws, size_t ws_size, hipStream_t stream) {
  const u16* x = (const u16*)d_in[0];
  const u16* wq = (const u16*)d_in[1];
  const u16* wk = (const u16*)d_in[2];
  const u16* wv = (const u16*)d_in[3];
  const u16* wo = (const u16*)d_in[4];
  const u16* wr = (const u16*)d_in[6];
  const u16* wg = (const u16*)d_in[7];
  const u16* wu = (const u16*)d_in[8];
  const u16* wd = (const u16*)d_in[9];
  char* ws = (char*)d_ws;
  u16* xnf = (u16*)(ws + OFF_XNF);
  u16* of = (u16*)(ws + OFF_OF);
  u16* wqf = (u16*)(ws + OFF_WQF);
  u16* wof = (u16*)(ws + OFF_WOF);
  u16* kf = (u16*)(ws + OFF_KF);
  u16* vf = (u16*)(ws + OFF_VF);
  float* rbuf = (float*)(ws + OFF_R);
  float2* stats = (float2*)(ws + OFF_STATS);
  float* ypart = (float*)(ws + OFF_Y);
  int* sel = (int*)(ws + OFF_SEL);
  u16* out = (u16*)d_out;

  ka_prep<<<192, 512, 0, stream>>>(x, wq, wk, wv, wo, wr, xnf, wqf, wof, kf, vf, rbuf, stats);
  kb_attn<<<256, 576, 0, stream>>>(xnf, wqf, kf, vf, rbuf, wg, wu, wd, of, ypart, sel);
  kc_out<<<256, 512, 0, stream>>>(x, of, wof, stats, ypart, sel, out);
}
```

```cpp
#include <hip/hip_runtime.h>
#include <hip/hip_bf16.h>

typedef unsigned short u16;
typedef __attribute__((ext_vector_type(8))) __bf16 bf16x8;
typedef __attribute__((ext_vector_type(2))) __bf16 bf16x2;
typedef __attribute__((ext_vector_type(2))) float f32x2;
typedef __attribute__((ext_vector_type(4))) float f32x4;
typedef __attribute__((ext_vector_type(16))) float f32x16;
typedef __attribute__((ext_vector_type(4))) unsigned u32x4;
typedef __attribute__((ext_vector_type(2))) unsigned u32x2;

#define DI __device__ __forceinline__
#define TS(k, n)
#define TSR(k, n)

static constexpr int S_ = 2048;
static constexpr int D_ = 512;
static constexpr int F_ = 1376;

#define LDSF(a) (*(const __attribute__((address_space(3))) bf16x8*)(uintptr_t)(a))
DI float bf2f(unsigned u) { return __uint_as_float(u << 16); }
DI float bfhi2f(unsigned u) { return __uint_as_float(u & 0xffff0000u); }
DI float rbf(float f) { return (float)(__bf16)f; }
DI unsigned pack2(float lo, float hi) {
  f32x2 p = {lo, hi};
  bf16x2 q = __builtin_convertvector(p, bf16x2);
  return __builtin_bit_cast(unsigned, q);
}
DI f32x4 mfma16(bf16x8 a, bf16x8 b, f32x4 c) { return __builtin_amdgcn_mfma_f32_16x16x32_bf16(a, b, c, 0, 0, 0); }
DI f32x16 mfma32(bf16x8 a, bf16x8 b, f32x16 c) { return __builtin_amdgcn_mfma_f32_32x32x16_bf16(a, b, c, 0, 0, 0); }
DI unsigned perm_lo(unsigned hi_src, unsigned lo_src) { return __builtin_amdgcn_perm(hi_src, lo_src, 0x05040100u); }
DI unsigned perm_hi(unsigned hi_src, unsigned lo_src) { return __builtin_amdgcn_perm(hi_src, lo_src, 0x07060302u); }

DI bf16x8 frag_lo(const unsigned* w) {
  u32x4 r = {perm_lo(w[1], w[0]), perm_lo(w[3], w[2]), perm_lo(w[5], w[4]), perm_lo(w[7], w[6])};
  return __builtin_bit_cast(bf16x8, r);
}
DI bf16x8 frag_hi(const unsigned* w) {
  u32x4 r = {perm_hi(w[1], w[0]), perm_hi(w[3], w[2]), perm_hi(w[5], w[4]), perm_hi(w[7], w[6])};
  return __builtin_bit_cast(bf16x8, r);
}
DI float ln_elem(float xv, float mb, float den, float inv) {
  const float d = rbf(xv - mb);
  float q = d * inv;
  const float rem = __builtin_fmaf(-q, den, d);
  q = __builtin_fmaf(rem, inv, q);
  return rbf(q);
}

static constexpr int VT_STRIDE = 1056;
static constexpr int VF_BYTES = 64 * VT_STRIDE;
static constexpr size_t OFF_XNF = 0;
static constexpr size_t OFF_OF = OFF_XNF + (size_t)S_ * D_ * 2;
static constexpr size_t OFF_WQF = OFF_OF + (size_t)S_ * D_ * 2;
static constexpr size_t OFF_WOF = OFF_WQF + (size_t)D_ * D_ * 2;
static constexpr size_t OFF_KF = OFF_WOF + (size_t)D_ * D_ * 2;
static constexpr size_t OFF_VF = OFF_KF + 65536;
static constexpr size_t OFF_R = OFF_VF + VF_BYTES;
static constexpr size_t OFF_STATS = OFF_R + S_ * 4;
static constexpr int NHELP = 86;
static constexpr size_t OFF_Y = OFF_STATS + S_ * 8;
static constexpr size_t OFF_SEL = OFF_Y + NHELP * 1024 * 4;

static constexpr int XN_STRIDE = 1040;
__global__ __launch_bounds__(512) void ka_prep(const u16* __restrict__ x, const u16* __restrict__ wq,
                                               const u16* __restrict__ wk, const u16* __restrict__ wv,
                                               const u16* __restrict__ wo, const u16* __restrict__ wr,
                                               u16* __restrict__ xnf, u16* __restrict__ wqf, u16* __restrict__ wof,
                                               u16* __restrict__ kf, u16* __restrict__ vf,
                                               unsigned* __restrict__ rkeys, float2* __restrict__ stats) {
  __shared__ __attribute__((aligned(16))) char smem[16 * XN_STRIDE + 8 * 5 * 64 * 16];
  const int tid = threadIdx.x;
  const int lane = tid & 63, wave = tid >> 6;
  TSR(0, 8); TS(0, 0);
  if (blockIdx.x >= 128) {
    const int pb = blockIdx.x - 128;
    const bool iso = pb >= 32;
    const int cp = tid & 255, ko = (pb & 31) * 2 + (tid >> 8);
    const int ks = ko >> 1, hh = ko & 1;
    const u16* W = (iso ? wo : wq) + 2 * cp;
    unsigned w[8];
#pragma unroll
    for (int j = 0; j < 8; ++j) {
      const int row = 16 * ks + (iso ? (8 * (j >> 2) + 4 * hh + (j & 3)) : (8 * hh + j));
      w[j] = *(const unsigned*)(W + row * D_);
    }
    u16* dst = (iso ? wof : wqf) + (((cp >> 4) * 32 + ks) * 64 + hh * 32 + ((2 * cp) & 31)) * 8;
    *(bf16x8*)dst = frag_lo(w);
    *(bf16x8*)(dst + 8) = frag_hi(w);
    TS(0, 3); TSR(0, 9);
    return;
  }
  const int row0 = blockIdx.x * 16;
  const int rt = blockIdx.x >> 1, th = blockIdx.x & 1;
  const int g = lane >> 4, i = lane & 15;
  const int k0 = wave * 64 + 8 * g;
  u32x4 xr[2];
#pragma unroll
  for (int rr = 0; rr < 2; ++rr) xr[rr] = *(const u32x4*)(x + (size_t)(row0 + 2 * wave + rr) * D_ + 8 * lane);
  const u16* wbase = ((i < 4) ? (wk + 4 * i) : (i < 8 ? wv + 4 * (i - 4) : wk)) + k0 * 16;
  u32x2 w[2][8];
#pragma unroll
  for (int s = 0; s < 2; ++s)
#pragma unroll
    for (int j = 0; j < 8; ++j) w[s][j] = *(const u32x2*)(wbase + (32 * s + j) * 16);
  bf16x8 rf[2];
#pragma unroll
  for (int s = 0; s < 2; ++s) rf[s] = *(const bf16x8*)(wr + k0 + 32 * s);

  float s1[2], s2[2];
#pragma unroll
  for (int rr = 0; rr < 2; ++rr) {
    float a = 0.f, q = 0.f;
#pragma unroll
    for (int e = 0; e < 4; ++e) {
      const float lo = bf2f(xr[rr][e] & 0xffffu), hi = bfhi2f(xr[rr][e]);
      a += lo + hi;
      q = __builtin_fmaf(lo, lo, q);
      q = __builtin_fmaf(hi, hi, q);
    }
    s1[rr] = a; s2[rr] = q;
  }
#pragma unroll
  for (int off = 32; off >= 1; off >>= 1)
#pragma unroll
    for (int rr = 0; rr < 2; ++rr) {
      s1[rr] += __shfl_xor(s1[rr], off);
      s2[rr] += __shfl_xor(s2[rr], off);
    }
#pragma unroll
  for (int rr = 0; rr < 2; ++rr) {
    const float mean = s1[rr] * (1.0f / D_);
    float var = s2[rr] * (1.0f / D_) - mean * mean;
    var = fmaxf(var, 0.f);
    const float mb = rbf(mean);
    const float den = rbf(__builtin_sqrtf(rbf(rbf(var) + 1e-5f)));
    const float inv = 1.0f / den;
    if (lane == rr) stats[row0 + 2 * wave + rr] = make_float2(mb, den);
    u32x4 o;
#pragma unroll
    for (int e = 0; e < 4; ++e)
      o[e] = pack2(ln_elem(bf2f(xr[rr][e] & 0xffffu), mb, den, inv), ln_elem(bfhi2f(xr[rr][e]), mb, den, inv));
    *(u32x4*)(smem + (2 * wave + rr) * XN_STRIDE + lane * 16) = o;
  }
  if (tid < 2 && th == 0) {
    const unsigned cv = tid ? 0u : 0x3f803f80u;
    u32x4 c4 = {cv, cv, cv, cv};
    *(u32x4*)((char*)vf + rt * VT_STRIDE + 1024 + tid * 16) = c4;
  }
  TS(0, 1);
  __syncthreads();
  {
    const int r = lane & 15, hh = (lane >> 4) & 1, kp = lane >> 5;
#pragma unroll
    for (int s = 0; s < 2; ++s) {
      const int ks = 4 * wave + 2 * s + kp;
      const u32x4 v = *(const u32x4*)(smem + r * XN_STRIDE + ks * 32 + hh * 16);
      *(u32x4*)(xnf + ((size_t)(rt * 32 + ks) * 64 + hh * 32 + th * 16 + r) * 8) = v;
    }
  }
  f32x4* red = (f32x4*)(smem + 16 * XN_STRIDE);
  {
    f32x4 acc[4], ar;
    ar = (f32x4){0.f, 0.f, 0.f, 0.f};
#pragma unroll
    for (int c = 0; c < 4; ++c) acc[c] = (f32x4){0.f, 0.f, 0.f, 0.f};
#pragma unroll
    for (int s = 0; s < 2; ++s) {
      unsigned wx[8], wy[8];
#pragma unroll
      for (int j = 0; j < 8; ++j) { wx[j] = w[s][j][0]; wy[j] = w[s][j][1]; }
      bf16x8 b[4] = {frag_lo(wx), frag_hi(wx), frag_lo(wy), frag_hi(wy)};
      const bf16x8 xa = *(const bf16x8*)(smem + i * XN_STRIDE + (k0 + 32 * s) * 2);
#pragma unroll
      for (int c = 0; c < 4; ++c) acc[c] = mfma16(xa, b[c], acc[c]);
      ar = mfma16(xa, rf[s], ar);
    }
#pragma unroll
    for (int c = 0; c < 4; ++c) red[(wave * 5 + c) * 64 + lane] = acc[c];
    red[(wave * 5 + 4) * 64 + lane] = ar;
  }
  __syncthreads();
  TS(0, 2);
  if (wave < 5) {
    f32x4 v = red[wave * 64 + lane];
#pragma unroll
    for (int wv_ = 1; wv_ < 8; ++wv_) v += red[(wv_ * 5 + wave) * 64 + lane];
    const int kk = 16 * th + 4 * g;
    if (wave == 4) {
      unsigned k0 = 0, k1 = 0;
#pragma unroll
      for (int e = 0; e < 4; ++e) {
        const unsigned u = __float_as_uint(rbf(v[e]));
        const unsigned ord = (u & 0x80000000u) ? ~u : (u | 0x80000000u);
        const unsigned key = (ord & 0xffff0000u) | (unsigned)(2047 - (row0 + 4 * g + e));
        k1 = max(k1, min(k0, key));
        k0 = max(k0, key);
      }
#pragma unroll
      for (int off = 16; off < 64; off <<= 1) {
        const unsigned o0 = __shfl_xor(k0, off), o1 = __shfl_xor(k1, off);
        k1 = max(min(k0, o0), max(k1, o1));
        k0 = max(k0, o0);
      }
      if (lane == 0) *(u32x2*)(rkeys + 2 * blockIdx.x) = (u32x2){k0, k1};
    } else if (i < 4) {
      const int hh = i & 1, j = (i >> 1) * 4 + wave;
#pragma unroll
      for (int e = 0; e < 4; ++e)
        kf[rt * 512 + (hh * 32 + kk + e) * 8 + j] = __builtin_bit_cast(u16, (__bf16)v[e]);
    } else if (i < 8) {
      const int d = 4 * (i - 4) + wave;
      const int hh = g & 1, jh = g >> 1;
      u32x2 o = {pack2(v[0], v[1]), pack2(v[2], v[3])};
      *(u32x2*)(vf + rt * (VT_STRIDE / 2) + th * 256 + (hh * 16 + d) * 8 + jh * 4) = o;
    }
  }
  TS(0, 3); TSR(0, 9);
}

static constexpr int LDS_K = 0;
static constexpr int LDS_V = 65536;
static constexpr int LDS_H = 65536 + VF_BYTES + 2048;
static constexpr int LDS_TOTAL = LDS_H + 16384;

__global__ __launch_bounds__(768) void kb_attn(const u16* __restrict__ xnf, const u16* __restrict__ wqf,
                                               const u16* __restrict__ kf, const u16* __restrict__ vf,
                                               const unsigned* __restrict__ rkeys, const u16* __restrict__ wg,
                                               const u16* __restrict__ wu, const u16* __restrict__ wd,
                                               u16* __restrict__ of, float* __restrict__ ypart,
                                               int* __restrict__ sel) {
  __shared__ __attribute__((aligned(16))) char smem[LDS_TOTAL];
  const int tid = threadIdx.x;
  const int lane = tid & 63, wave = tid >> 6;
  const int b = blockIdx.x;
  TSR(1, 8); TS(1, 0);

  if (wave < 8) {
    const int rb = (b & 7) + 8 * ((b >> 3) & 3), hg = b >> 5;
    const int rt = wave >> 2, ks4 = wave & 3;
    const int r = lane & 31, hh = lane >> 5;
    {
      u32x4 tk[8];
#pragma unroll
      for (int c = 0; c < 8; ++c) tk[c] = *(const u32x4*)((const char*)kf + c * 8192 + tid * 16);
      bf16x8 af[2][8], bfr[8];
      const int rtq = wave & 1, kq = wave >> 1;
      const u16* ap = wqf + ((size_t)(2 * hg) * 32 + 8 * kq) * 512 + lane * 8;
      const u16* bp = xnf + ((size_t)(2 * rb + rtq) * 32 + 8 * kq) * 512 + lane * 8;
#pragma unroll
      for (int s = 0; s < 8; ++s) {
        bfr[s] = *(const bf16x8*)(bp + s * 512);
        af[0][s] = *(const bf16x8*)(ap + s * 512);
        af[1][s] = *(const bf16x8*)(ap + 32 * 512 + s * 512);
      }
      __builtin_amdgcn_sched_barrier(0);
#pragma unroll
      for (int c = 0; c < 8; ++c) *(u32x4*)(smem + LDS_K + c * 8192 + tid * 16) = tk[c];
      __builtin_amdgcn_sched_barrier(0);
      f32x16 acc[2];
#pragma unroll
      for (int e = 0; e < 16; ++e) { acc[0][e] = 0.f; acc[1][e] = 0.f; }
#pragma unroll
      for (int s = 0; s < 8; ++s) {
        acc[0] = mfma32(af[0][s], bfr[s], acc[0]);
        acc[1] = mfma32(af[1][s], bfr[s], acc[1]);
      }
#pragma unroll
      for (int dt = 0; dt < 2; ++dt)
#pragma unroll
        for (int q4 = 0; q4 < 4; ++q4) {
          f32x4 p = {acc[dt][4 * q4], acc[dt][4 * q4 + 1], acc[dt][4 * q4 + 2], acc[dt][4 * q4 + 3]};
          *(f32x4*)(smem + LDS_V + ((((rtq * 4 + kq) * 2 + dt) * 4 + q4) * 64 + lane) * 16) = p;
        }
    }
    __builtin_amdgcn_sched_barrier(0);
    u32x4 tv[8], tvx;
#pragma unroll
    for (int c = 0; c < 8; ++c) tv[c] = *(const u32x4*)((const char*)vf + c * 8192 + tid * 16);
    tvx = *(const u32x4*)((const char*)vf + 65536 + (tid & 127) * 16);
    TS(1, 1);
    __syncthreads();
    TS(1, 2);
    const int hl = wave & 3;
    const int head = hg * 4 + hl;
    bf16x8 qf;
    {
      f32x4 a0 = {0.f, 0.f, 0.f, 0.f}, a1 = {0.f, 0.f, 0.f, 0.f};
#pragma unroll
      for (int k = 0; k < 4; ++k) {
        const char* p = smem + LDS_V + ((((rt * 4 + k) * 2 + (hl >> 1)) * 4 + 2 * (hl & 1)) * 64 + lane) * 16;
        a0 += *(const f32x4*)p;
        a1 += *(const f32x4*)(p + 1024);
      }
      const float SC = 0.25f * 1.44269504088896340736f;
      u32x4 pk = {pack2(rbf(a0[0]) * SC, rbf(a0[1]) * SC), pack2(rbf(a0[2]) * SC, rbf(a0[3]) * SC),
                  pack2(rbf(a1[0]) * SC, rbf(a1[1]) * SC), pack2(rbf(a1[2]) * SC, rbf(a1[3]) * SC)};
      qf = __builtin_bit_cast(bf16x8, pk);
    }

    const char* kp = smem + LDS_K + lane * 16;
    float m0 = -__builtin_inff(), m1 = -__builtin_inff();
    f32x16 zero16;
#pragma unroll
    for (int e = 0; e < 16; ++e) zero16[e] = 0.f;
    {
      f32x16 sa = mfma32(*(const bf16x8*)(kp), qf, zero16);
      f32x16 sb = mfma32(*(const bf16x8*)(kp + 1024), qf, zero16);
#pragma unroll 4
      for (int t = 2; t < 64; t += 2) {
        const bf16x8 ka = *(const bf16x8*)(kp + t * 1024);
        const bf16x8 kb = *(const bf16x8*)(kp + t * 1024 + 1024);
        const f32x16 na = mfma32(ka, qf, zero16);
#pragma unroll
        for (int e = 0; e < 16; e += 2) m0 = fmaxf(fmaxf(m0, sa[e]), sa[e + 1]);
        const f32x16 nb = mfma32(kb, qf, zero16);
#pragma unroll
        for (int e = 0; e < 16; e += 2) m1 = fmaxf(fmaxf(m1, sb[e]), sb[e + 1]);
        sa = na; sb = nb;
      }
#pragma unroll
      for (int e = 0; e < 16; e += 2) m0 = fmaxf(fmaxf(m0, sa[e]), sa[e + 1]);
#pragma unroll
      for (int e = 0; e < 16; e += 2) m1 = fmaxf(fmaxf(m1, sb[e]), sb[e + 1]);
    }
    float m = fmaxf(m0, m1);
    m = fmaxf(m, __shfl_xor(m, 32));
    TS(1, 3);
    __syncthreads();
#pragma unroll
    for (int c = 0; c < 8; ++c) *(u32x4*)(smem + LDS_V + c * 8192 + tid * 16) = tv[c];
    if (wave < 2) *(u32x4*)(smem + LDS_V + 65536 + tid * 16) = tvx;
    __syncthreads();
    TS(1, 4);

    f32x16 negm;
#pragma unroll
    for (int e = 0; e < 16; ++e) negm[e] = -m;
    f32x16 o;
#pragma unroll
    for (int e = 0; e < 16; ++e) o[e] = 0.f;
    const unsigned lbase = (unsigned)(uintptr_t)(__attribute__((address_space(3))) char*)smem;
    unsigned ka_ = lbase + LDS_K + lane * 16;
    unsigned va_ = lbase + LDS_V + ((r < 16) ? (hh * 16 + r) * 16 : (r == 16 ? 1024 : 1040));
    unsigned vb_ = lbase + LDS_V + ((r < 16) ? (hh * 16 + r) * 16 + 512 : (r == 16 ? 1024 : 1040));
    f32x16 s = mfma32(LDSF(ka_), qf, negm);
    bf16x8 kn = LDSF(ka_ + 1024);
    bf16x8 v0 = LDSF(va_);
    bf16x8 v1 = LDSF(vb_);
#pragma unroll 1
    for (int tb = 0; tb < 8; ++tb) {
      asm volatile("" : "+v"(ka_), "+v"(va_), "+v"(vb_));
#pragma unroll
      for (int u = 0; u < 8; ++u) {
        const f32x16 sn = mfma32(kn, qf, negm);
        const bf16x8 k2 = LDSF(ka_ + (u + 2) * 1024);
        const bf16x8 v0n = LDSF(va_ + (u + 1) * VT_STRIDE);
        const bf16x8 v1n = LDSF(vb_ + (u + 1) * VT_STRIDE);
        float p[16];
#pragma unroll
        for (int e = 0; e < 8; ++e) p[e] = __builtin_amdgcn_exp2f(s[e]);
        u32x4 pa = {pack2(p[0], p[1]), pack2(p[2], p[3]), pack2(p[4], p[5]), pack2(p[6], p[7])};
        o = mfma32(v0, __builtin_bit_cast(bf16x8, pa), o);
#pragma unroll
        for (int e = 8; e < 16; ++e) p[e] = __builtin_amdgcn_exp2f(s[e]);
        u32x4 pb = {pack2(p[8], p[9]), pack2(p[10], p[11]), pack2(p[12], p[13]), pack2(p[14], p[15])};
        o = mfma32(v1, __builtin_bit_cast(bf16x8, pb), o);
        s = sn; kn = k2; v0 = v0n; v1 = v1n;
        __builtin_amdgcn_sched_group_barrier(0x008, 1, 0);
        __builtin_amdgcn_sched_group_barrier(0x100, 3, 0);
        __builtin_amdgcn_sched_group_barrier(0x402, 12, 0);
        __builtin_amdgcn_sched_group_barrier(0x008, 1, 0);
        __builtin_amdgcn_sched_group_barrier(0x402, 12, 0);
        __builtin_amdgcn_sched_group_barrier(0x008, 1, 0);
        __builtin_amdgcn_sched_barrier(0);
      }
      ka_ += 8 * 1024; va_ += 8 * VT_STRIDE; vb_ += 8 * VT_STRIDE;
    }
    TS(1, 5);
    const float il = 1.0f / __shfl(o[8], r);
    u32x4 w0 = {pack2(o[0] * il, o[1] * il), pack2(o[2] * il, o[3] * il), pack2(o[4] * il, o[5] * il),
                pack2(o[6] * il, o[7] * il)};
    *(u32x4*)(of + ((size_t)((2 * rb + rt) * 32 + head) * 64 + lane) * 8) = w0;
  } else {
    if (b >= NHELP) return;
    __builtin_amdgcn_s_setprio(3);
    const int hj = wave - 8;
    const int g = lane >> 4, i = lane & 15;
    const int qd = i >> 2, c = i & 3;
    const int f0 = b * 16 + hj * 4;
    const u32x4 kv = *(const u32x4*)(rkeys + lane * 4);
    unsigned wa[4][8];
    const u16* wp = ((c < 2) ? wg : wu) + f0 + 2 * (c & 1) + (size_t)(128 * qd + 8 * g) * F_;
#pragma unroll
    for (int s = 0; s < 4; ++s)
#pragma unroll
      for (int j = 0; j < 8; ++j) wa[s][j] = *(const unsigned*)(wp + (size_t)(32 * s + j) * F_);
    u32x4 wdv[4];
#pragma unroll
    for (int f = 0; f < 4; ++f) wdv[f] = *(const u32x4*)(wd + (size_t)(f0 + f) * D_ + lane * 8);
    unsigned k0 = max(kv[0], kv[2]), k1 = max(min(kv[0], kv[2]), max(kv[1], kv[3]));
#pragma unroll
    for (int off = 1; off < 64; off <<= 1) {
      const unsigned o0 = __shfl_xor(k0, off), o1 = __shfl_xor(k1, off);
      k1 = max(min(k0, o0), max(k1, o1));
      k0 = max(k0, o0);
    }
    const int i0 = 2047 - (int)(k0 & 0x7ffu), i1 = 2047 - (int)(k1 & 0x7ffu);
    TS(1, 3);
    if (b == 0 && hj == 0 && lane == 0) {
      const float v0 = __uint_as_float((k0 & 0x80000000u) ? (k0 & 0x7fff0000u) : (~k0 & 0xffff0000u));
      const float v1 = __uint_as_float((k1 & 0x80000000u) ? (k1 & 0x7fff0000u) : (~k1 & 0xffff0000u));
      sel[0] = i0; sel[1] = i1;
      ((float*)sel)[2] = rbf(1.0f / (1.0f + __expf(-v0)));
      ((float*)sel)[3] = rbf(1.0f / (1.0f + __expf(-v1)));
    }
    const int srow = (i & 1) ? i1 : i0;
    const u16* xp = xnf + ((size_t)((srow >> 5) * 32 + 8 * ((i >> 1) & 3) + (g >> 1)) * 64 + (g & 1) * 32 + (srow & 31)) * 8;
    f32x4 a0 = {0.f, 0.f, 0.f, 0.f}, a1 = {0.f, 0.f, 0.f, 0.f};
    {
      bf16x8 xa[4];
#pragma unroll
      for (int s = 0; s < 4; ++s) xa[s] = *(const bf16x8*)(xp + (size_t)s * 1024);
#pragma unroll
      for (int s = 0; s < 4; ++s) {
        a0 = mfma16(xa[s], frag_lo(wa[s]), a0);
        a1 = mfma16(xa[s], frag_hi(wa[s]), a1);
      }
    }
    float t[4];
    {
      const bool ok = g == (qd >> 1);
      const bool up = qd & 1;
      t[0] = ok ? (up ? a0[2] : a0[0]) : 0.f;
      t[1] = ok ? (up ? a0[3] : a0[1]) : 0.f;
      t[2] = ok ? (up ? a1[2] : a1[0]) : 0.f;
      t[3] = ok ? (up ? a1[3] : a1[1]) : 0.f;
#pragma unroll
      for (int off = 4; off < 32; off <<= 1)
#pragma unroll
        for (int e = 0; e < 4; ++e) t[e] += __shfl_xor(t[e], off);
    }
    float hv[2][4];
#pragma unroll
    for (int e = 0; e < 4; ++e) {
      const float gt = rbf(t[e]);
      const float u = __shfl_xor(gt, 2);
      const float h = rbf(rbf(gt / (1.0f + __expf(-gt))) * u);
      hv[e & 1][e >> 1] = __builtin_bit_cast(float, __builtin_amdgcn_readlane(__builtin_bit_cast(int, h), 0));
      hv[e & 1][2 + (e >> 1)] = __builtin_bit_cast(float, __builtin_amdgcn_readlane(__builtin_bit_cast(int, h), 1));
    }
#pragma unroll
    for (int tk = 0; tk < 2; ++tk) {
      float y[8];
#pragma unroll
      for (int e = 0; e < 8; ++e) y[e] = 0.f;
#pragma unroll
      for (int f = 0; f < 4; ++f)
#pragma unroll
        for (int e = 0; e < 4; ++e) {
          y[2 * e] = __builtin_fmaf(hv[tk][f], bf2f(wdv[f][e] & 0xffffu), y[2 * e]);
          y[2 * e + 1] = __builtin_fmaf(hv[tk][f], bfhi2f(wdv[f][e]), y[2 * e + 1]);
        }
      char* yp = smem + LDS_H + (hj * 2 + tk) * 2048 + lane * 32;
      *(f32x4*)yp = (f32x4){y[0], y[1], y[2], y[3]};
      *(f32x4*)(yp + 16) = (f32x4){y[4], y[5], y[6], y[7]};
    }
    TS(1, 4);
    __syncthreads();
    {
      const char* yp = smem + LDS_H + hj * 1024 + lane * 16;
      const f32x4 y = (*(const f32x4*)yp + *(const f32x4*)(yp + 4096)) +
                      (*(const f32x4*)(yp + 8192) + *(const f32x4*)(yp + 12288));
      *(f32x4*)(ypart + (size_t)b * 1024 + hj * 256 + lane * 4) = y;
    }
  }
  TS(1, 6); TSR(1, 9);
}

static constexpr int CH_STRIDE = 1056;
static constexpr int LDS_Y = 64 * CH_STRIDE;
__global__ __launch_bounds__(512) void kc_out(const u16* __restrict__ x, const u16* __restrict__ of,
                                              const u16* __restrict__ wof, const float2* __restrict__ stats,
                                              const float* __restrict__ ypart, const int* __restrict__ sel,
                                              u16* __restrict__ out) {
  __shared__ __attribute__((aligned(16))) char smem[LDS_Y + 2 * 8 * 256];
  const int tid = threadIdx.x;
  const int lane = tid & 63, wave = tid >> 6;
  TSR(2, 8); TS(2, 0);
  const int bb = blockIdx.x;
  const int rb = (bb & 7) + 8 * ((bb >> 3) & 3), cg = bb >> 5;
  const int rt = wave & 1, ks4 = wave >> 1;
  const int idx0 = sel[0], idx1 = sel[1];
  bf16x8 af[2][8], bfr[8];
  const u16* ap = wof + ((size_t)(2 * cg) * 32 + 8 * ks4) * 512 + lane * 8;
  const u16* bp = of + ((size_t)(2 * rb + rt) * 32 + 8 * ks4) * 512 + lane * 8;
#pragma unroll
  for (int s = 0; s < 8; ++s) {
    bfr[s] = *(const bf16x8*)(bp + s * 512);
    af[0][s] = *(const bf16x8*)(ap + s * 512);
    af[1][s] = *(const bf16x8*)(ap + 32 * 512 + s * 512);
  }
  const int r64 = tid >> 3, oct = tid & 7;
  const int row = rb * 64 + r64;
  const size_t off = (size_t)row * D_ + cg * 64 + oct * 8;
  const u32x4 xv = *(const u32x4*)(x + off);
  const float2 st = stats[row];
#pragma unroll
  for (int tk = 0; tk < 2; ++tk) {
    const int idx = tk ? idx1 : idx0;
    if ((unsigned)(idx - rb * 64) < 64u) {
      const int pg = tid >> 4;
      const float* yp = ypart + (size_t)(pg * 2 + tk) * 512 + cg * 64 + (tid & 15) * 4;
      f32x4 y = *(const f32x4*)yp + *(const f32x4*)(yp + 32 * 1024);
      if (pg + 64 < NHELP) y += *(const f32x4*)(yp + 64 * 1024);
#pragma unroll
      for (int e = 0; e < 4; ++e) {
        y[e] += __shfl_xor(y[e], 16);
        y[e] += __shfl_xor(y[e], 32);
      }
      if (lane < 16) *(f32x4*)(smem + LDS_Y + (tk * 8 + wave) * 256 + lane * 16) = y;
    }
  }
  f32x16 acc[2];
#pragma unroll
  for (int e = 0; e < 16; ++e) { acc[0][e] = 0.f; acc[1][e] = 0.f; }
#pragma unroll
  for (int s = 0; s < 8; ++s) {
    acc[0] = mfma32(af[0][s], bfr[s], acc[0]);
    acc[1] = mfma32(af[1][s], bfr[s], acc[1]);
  }
  TS(2, 1);
#pragma unroll
  for (int ct = 0; ct < 2; ++ct)
#pragma unroll
    for (int q4 = 0; q4 < 4; ++q4) {
      f32x4 p = {acc[ct][4 * q4], acc[ct][4 * q4 + 1], acc[ct][4 * q4 + 2], acc[ct][4 * q4 + 3]};
      *(f32x4*)(smem + (((rt * 4 + ks4) * 2 + ct) * 4 + q4) * CH_STRIDE + lane * 16) = p;
    }
  __syncthreads();
  TS(2, 2);
  f32x4 lo = {0.f, 0.f, 0.f, 0.f}, hi = {0.f, 0.f, 0.f, 0.f};
#pragma unroll
  for (int k = 0; k < 4; ++k) {
    const char* p = smem + (((r64 >> 5) * 4 + k) * 8 + oct) * CH_STRIDE + (r64 & 31) * 16;
    lo += *(const f32x4*)p;
    hi += *(const f32x4*)(p + 512);
  }
  float upd[8];
#pragma unroll
  for (int e = 0; e < 8; ++e) upd[e] = 0.f;
  if (row == idx0 || row == idx1) {
    const int tk = (row == idx0) ? 0 : 1;
    f32x4 ya = {0.f, 0.f, 0.f, 0.f}, yb = {0.f, 0.f, 0.f, 0.f};
#pragma unroll
    for (int p = 0; p < 8; ++p) {
      const char* yp = smem + LDS_Y + (tk * 8 + p) * 256 + oct * 32;
      ya += *(const f32x4*)yp;
      yb += *(const f32x4*)(yp + 16);
    }
    const float rs = ((const float*)sel)[2 + tk];
#pragma unroll
    for (int e = 0; e < 4; ++e) { upd[e] = rbf(rbf(ya[e]) * rs); upd[4 + e] = rbf(rbf(yb[e]) * rs); }
  }
  const float inv = 1.0f / st.y;
  float res[8];
#pragma unroll
  for (int e = 0; e < 4; ++e) {
    const float x0 = bf2f(xv[e] & 0xffffu), x1 = bfhi2f(xv[e]);
    const float a0 = rbf(e < 2 ? lo[2 * e] : hi[2 * e - 4]);
    const float a1 = rbf(e < 2 ? lo[2 * e + 1] : hi[2 * e - 3]);
    res[2 * e] = rbf(x0 + a0) + rbf(ln_elem(x0, st.x, st.y, inv) + upd[2 * e]);
    res[2 * e + 1] = rbf(x1 + a1) + rbf(ln_elem(x1, st.x, st.y, inv) + upd[2 * e + 1]);
  }
  u32x4 o = {pack2(res[0], res[1]), pack2(res[2], res[3]), pack2(res[4], res[5]), pack2(res[6], res[7])};
  *(u32x4*)(out + off) = o;
  TS(2, 3); TSR(2, 9);
}

extern "C" void kernel_launch(void* const* d_in, const int* in_sizes, int n_in, void* d_out, int out_size,
                              void* d_ws, size_t ws_size, hipStream_t stream) {
  const u16* x = (const u16*)d_in[0];
  const u16* wq = (const u16*)d_in[1];
  const u16* wk = (const u16*)d_in[2];
  const u16* wv = (const u16*)d_in[3];
  const u16* wo = (const u16*)d_in[4];
  const u16* wr = (const u16*)d_in[6];
  const u16* wg = (const u16*)d_in[7];
  const u16* wu = (const u16*)d_in[8];
  const u16* wd = (const u16*)d_in[9];
  char* ws = (char*)d_ws;
  u16* xnf = (u16*)(ws + OFF_XNF);
  u16* of = (u16*)(ws + OFF_OF);
  u16* wqf = (u16*)(ws + OFF_WQF);
  u16* wof = (u16*)(ws + OFF_WOF);
  u16* kf = (u16*)(ws + OFF_KF);
  u16* vf = (u16*)(ws + OFF_VF);
  unsigned* rkeys = (unsigned*)(ws + OFF_R);
  float2* stats = (float2*)(ws + OFF_STATS);
  float* ypart = (float*)(ws + OFF_Y);
  int* sel = (int*)(ws + OFF_SEL);
  u16* out = (u16*)d_out;

  ka_prep<<<192, 512, 0, stream>>>(x, wq, wk, wv, wo, wr, xnf, wqf, wof, kf, vf, rkeys, stats);
  kb_attn<<<256, 768, 0, stream>>>(xnf, wqf, kf, vf, rkeys, wg, wu, wd, of, ypart, sel);
  kc_out<<<256, 512, 0, stream>>>(x, of, wof, stats, ypart, sel, out);
}
```

```cpp
#include <hip/hip_runtime.h>
#include <hip/hip_bf16.h>

typedef unsigned short u16;
typedef __attribute__((ext_vector_type(8))) __bf16 bf16x8;
typedef __attribute__((ext_vector_type(2))) __bf16 bf16x2;
typedef __attribute__((ext_vector_type(2))) float f32x2;
typedef __attribute__((ext_vector_type(4))) float f32x4;
typedef __attribute__((ext_vector_type(16))) float f32x16;
typedef __attribute__((ext_vector_type(4))) unsigned u32x4;
typedef __attribute__((ext_vector_type(2))) unsigned u32x2;

#define DI __device__ __forceinline__
#define TS(k, n)
#define TSR(k, n)

static constexpr int S_ = 2048;
static constexpr int D_ = 512;
static constexpr int F_ = 1376;

#define LDSF(a) (*(const __attribute__((address_space(3))) bf16x8*)(uintptr_t)(a))
DI float bf2f(unsigned u) { return __uint_as_float(u << 16); }
DI float bfhi2f(unsigned u) { return __uint_as_float(u & 0xffff0000u); }
DI float rbf(float f) { return (float)(__bf16)f; }
DI unsigned pack2(float lo, float hi) {
  f32x2 p = {lo, hi};
  bf16x2 q = __builtin_convertvector(p, bf16x2);
  return __builtin_bit_cast(unsigned, q);
}
DI f32x4 mfma16(bf16x8 a, bf16x8 b, f32x4 c) { return __builtin_amdgcn_mfma_f32_16x16x32_bf16(a, b, c, 0, 0, 0); }
DI f32x16 mfma32(bf16x8 a, bf16x8 b, f32x16 c) { return __builtin_amdgcn_mfma_f32_32x32x16_bf16(a, b, c, 0, 0, 0); }
DI unsigned perm_lo(unsigned hi_src, unsigned lo_src) { return __builtin_amdgcn_perm(hi_src, lo_src, 0x05040100u); }
DI unsigned perm_hi(unsigned hi_src, unsigned lo_src) { return __builtin_amdgcn_perm(hi_src, lo_src, 0x07060302u); }

DI bf16x8 frag_lo(const unsigned* w) {
  u32x4 r = {perm_lo(w[1], w[0]), perm_lo(w[3], w[2]), perm_lo(w[5], w[4]), perm_lo(w[7], w[6])};
  return __builtin_bit_cast(bf16x8, r);
}
DI bf16x8 frag_hi(const unsigned* w) {
  u32x4 r = {perm_hi(w[1], w[0]), perm_hi(w[3], w[2]), perm_hi(w[5], w[4]), perm_hi(w[7], w[6])};
  return __builtin_bit_cast(bf16x8, r);
}
DI float ln_elem(float xv, float mb, float den, float inv) {
  const float d = rbf(xv - mb);
  float q = d * inv;
  const float rem = __builtin_fmaf(-q, den, d);
  q = __builtin_fmaf(rem, inv, q);
  return rbf(q);
}

static constexpr int VT_STRIDE = 1056;
static constexpr int VF_BYTES = 64 * VT_STRIDE;
static constexpr size_t OFF_XNF = 0;
static constexpr size_t OFF_OF = OFF_XNF + (size_t)S_ * D_ * 2;
static constexpr size_t OFF_WQF = OFF_OF + (size_t)S_ * D_ * 2;
static constexpr size_t OFF_WOF = OFF_WQF + (size_t)D_ * D_ * 2;
static constexpr size_t OFF_KF = OFF_WOF + (size_t)D_ * D_ * 2;
static constexpr size_t OFF_VF = OFF_KF + 65536;
static constexpr size_t OFF_R = OFF_VF + VF_BYTES;
static constexpr size_t OFF_STATS = OFF_R + S_ * 4;
static constexpr int NHELP = 86;
static constexpr size_t OFF_Y = OFF_STATS + S_ * 8;
static constexpr size_t OFF_SEL = OFF_Y + NHELP * 1024 * 4;
static constexpr size_t OFF_WGU = OFF_SEL + 256;

static constexpr int XN_STRIDE = 1040;
__global__ __launch_bounds__(512) void ka_prep(const u16* __restrict__ x, const u16* __restrict__ wq,
                                               const u16* __restrict__ wk, const u16* __restrict__ wv,
                                               const u16* __restrict__ wo, const u16* __restrict__ wr,
                                               const u16* __restrict__ wg, const u16* __restrict__ wu,
                                               u16* __restrict__ wgu,
                                               u16* __restrict__ xnf, u16* __restrict__ wqf, u16* __restrict__ wof,
                                               u16* __restrict__ kf, u16* __restrict__ vf,
                                               unsigned* __restrict__ rkeys, float2* __restrict__ stats) {
  __shared__ __attribute__((aligned(16))) char smem[16 * XN_STRIDE + 8 * 5 * 64 * 16];
  const int tid = threadIdx.x;
  const int lane = tid & 63, wave = tid >> 6;
  TSR(0, 8); TS(0, 0);
  if (blockIdx.x >= 192) {
    const int pb = blockIdx.x - 192;
    const int sg = pb & 15, mat = (pb >> 4) & 1, ch = pb >> 5;
    const int qd = tid & 3, fg = ch * 128 + (tid >> 2);
    if (fg < F_ / 4) {
      const u16* W = (mat ? wu : wg) + (size_t)(128 * qd + 8 * sg) * F_ + 4 * fg;
      unsigned wx[8], wy[8];
#pragma unroll
      for (int j = 0; j < 8; ++j) {
        const u32x2 t = *(const u32x2*)(W + (size_t)j * F_);
        wx[j] = t[0]; wy[j] = t[1];
      }
      u16* dst = wgu + (size_t)fg * 4096 + mat * 2048 + (sg >> 2) * 512 + ((sg & 3) * 16 + qd * 4) * 8;
      *(bf16x8*)dst = frag_lo(wx);
      *(bf16x8*)(dst + 8) = frag_hi(wx);
      *(bf16x8*)(dst + 16) = frag_lo(wy);
      *(bf16x8*)(dst + 24) = frag_hi(wy);
    }
    TS(0, 3); TSR(0, 9);
    return;
  }
  if (blockIdx.x >= 128) {
    const int pb = blockIdx.x - 128;
    const bool iso = pb >= 32;
    const int cp = tid & 255, ko = (pb & 31) * 2 + (tid >> 8);
    const int ks = ko >> 1, hh = ko & 1;
    const u16* W = (iso ? wo : wq) + 2 * cp;
    unsigned w[8];
#pragma unroll
    for (int j = 0; j < 8; ++j) {
      const int row = 16 * ks + (iso ? (8 * (j >> 2) + 4 * hh + (j & 3)) : (8 * hh + j));
      w[j] = *(const unsigned*)(W + row * D_);
    }
    u16* dst = (iso ? wof : wqf) + (((cp >> 4) * 32 + ks) * 64 + hh * 32 + ((2 * cp) & 31)) * 8;
    *(bf16x8*)dst = frag_lo(w);
    *(bf16x8*)(dst + 8) = frag_hi(w);
    TS(0, 3); TSR(0, 9);
    return;
  }
  const int row0 = blockIdx.x * 16;
  const int rt = blockIdx.x >> 1, th = blockIdx.x & 1;
  const int g = lane >> 4, i = lane & 15;
  const int k0 = wave * 64 + 8 * g;
  u32x4 xr[2];
#pragma unroll
  for (int rr = 0; rr < 2; ++rr) xr[rr] = *(const u32x4*)(x + (size_t)(row0 + 2 * wave + rr) * D_ + 8 * lane);
  const u16* wbase = ((i < 4) ? (wk + 4 * i) : (i < 8 ? wv + 4 * (i - 4) : wk)) + k0 * 16;
  u32x2 w[2][8];
#pragma unroll
  for (int s = 0; s < 2; ++s)
#pragma unroll
    for (int j = 0; j < 8; ++j) w[s][j] = *(const u32x2*)(wbase + (32 * s + j) * 16);
  bf16x8 rf[2];
#pragma unroll
  for (int s = 0; s < 2; ++s) rf[s] = *(const bf16x8*)(wr + k0 + 32 * s);

  float s1[2], s2[2];
#pragma unroll
  for (int rr = 0; rr < 2; ++rr) {
    float a = 0.f, q = 0.f;
#pragma unroll
    for (int e = 0; e < 4; ++e) {
      const float lo = bf2f(xr[rr][e] & 0xffffu), hi = bfhi2f(xr[rr][e]);
      a += lo + hi;
      q = __builtin_fmaf(lo, lo, q);
      q = __builtin_fmaf(hi, hi, q);
    }
    s1[rr] = a; s2[rr] = q;
  }
#pragma unroll
  for (int off = 32; off >= 1; off >>= 1)
#pragma unroll
    for (int rr = 0; rr < 2; ++rr) {
      s1[rr] += __shfl_xor(s1[rr], off);
      s2[rr] += __shfl_xor(s2[rr], off);
    }
#pragma unroll
  for (int rr = 0; rr < 2; ++rr) {
    const float mean = s1[rr] * (1.0f / D_);
    float var = s2[rr] * (1.0f / D_) - mean * mean;
    var = fmaxf(var, 0.f);
    const float mb = rbf(mean);
    const float den = rbf(__builtin_sqrtf(rbf(rbf(var) + 1e-5f)));
    const float inv = 1.0f / den;
    if (lane == rr) stats[row0 + 2 * wave + rr] = make_float2(mb, den);
    u32x4 o;
#pragma unroll
    for (int e = 0; e < 4; ++e)
      o[e] = pack2(ln_elem(bf2f(xr[rr][e] & 0xffffu), mb, den, inv), ln_elem(bfhi2f(xr[rr][e]), mb, den, inv));
    *(u32x4*)(smem + (2 * wave + rr) * XN_STRIDE + lane * 16) = o;
  }
  if (tid < 2 && th == 0) {
    const unsigned cv = tid ? 0u : 0x3f803f80u;
    u32x4 c4 = {cv, cv, cv, cv};
    *(u32x4*)((char*)vf + rt * VT_STRIDE + 1024 + tid * 16) = c4;
  }
  TS(0, 1);
  __syncthreads();
  {
    const int r = lane & 15, hh = (lane >> 4) & 1, kp = lane >> 5;
#pragma unroll
    for (int s = 0; s < 2; ++s) {
      const int ks = 4 * wave + 2 * s + kp;
      const u32x4 v = *(const u32x4*)(smem + r * XN_STRIDE + ks * 32 + hh * 16);
      *(u32x4*)(xnf + ((size_t)(rt * 32 + ks) * 64 + hh * 32 + th * 16 + r) * 8) = v;
    }
  }
  f32x4* red = (f32x4*)(smem + 16 * XN_STRIDE);
  {
    f32x4 acc[4], ar;
    ar = (f32x4){0.f, 0.f, 0.f, 0.f};
#pragma unroll
    for (int c = 0; c < 4; ++c) acc[c] = (f32x4){0.f, 0.f, 0.f, 0.f};
#pragma unroll
    for (int s = 0; s < 2; ++s) {
      unsigned wx[8], wy[8];
#pragma unroll
      for (int j = 0; j < 8; ++j) { wx[j] = w[s][j][0]; wy[j] = w[s][j][1]; }
      bf16x8 b[4] = {frag_lo(wx), frag_hi(wx), frag_lo(wy), frag_hi(wy)};
      const bf16x8 xa = *(const bf16x8*)(smem + i * XN_STRIDE + (k0 + 32 * s) * 2);
#pragma unroll
      for (int c = 0; c < 4; ++c) acc[c] = mfma16(xa, b[c], acc[c]);
      ar = mfma16(xa, rf[s], ar);
    }
#pragma unroll
    for (int c = 0; c < 4; ++c) red[(wave * 5 + c) * 64 + lane] = acc[c];
    red[(wave * 5 + 4) * 64 + lane] = ar;
  }
  __syncthreads();
  TS(0, 2);
  if (wave < 5) {
    f32x4 v = red[wave * 64 + lane];
#pragma unroll
    for (int wv_ = 1; wv_ < 8; ++wv_) v += red[(wv_ * 5 + wave) * 64 + lane];
    const int kk = 16 * th + 4 * g;
    if (wave == 4) {
      unsigned k0 = 0, k1 = 0;
#pragma unroll
      for (int e = 0; e < 4; ++e) {
        const unsigned u = __float_as_uint(rbf(v[e]));
        const unsigned ord = (u & 0x80000000u) ? ~u : (u | 0x80000000u);
        const unsigned key = (ord & 0xffff0000u) | (unsigned)(2047 - (row0 + 4 * g + e));
        k1 = max(k1, min(k0, key));
        k0 = max(k0, key);
      }
#pragma unroll
      for (int off = 16; off < 64; off <<= 1) {
        const unsigned o0 = __shfl_xor(k0, off), o1 = __shfl_xor(k1, off);
        k1 = max(min(k0, o0), max(k1, o1));
        k0 = max(k0, o0);
      }
      if (lane == 0) *(u32x2*)(rkeys + 2 * blockIdx.x) = (u32x2){k0, k1};
    } else if (i < 4) {
      const int hh = i & 1, j = (i >> 1) * 4 + wave;
#pragma unroll
      for (int e = 0; e < 4; ++e)
        kf[rt * 512 + (hh * 32 + kk + e) * 8 + j] = __builtin_bit_cast(u16, (__bf16)v[e]);
    } else if (i < 8) {
      const int d = 4 * (i - 4) + wave;
      const int hh = g & 1, jh = g >> 1;
      u32x2 o = {pack2(v[0], v[1]), pack2(v[2], v[3])};
      *(u32x2*)(vf + rt * (VT_STRIDE / 2) + th * 256 + (hh * 16 + d) * 8 + jh * 4) = o;
    }
  }
  TS(0, 3); TSR(0, 9);
}

static constexpr int LDS_K = 0;
static constexpr int LDS_V = 65536;
static constexpr int LDS_H = 65536 + VF_BYTES + 2048;
static constexpr int LDS_TOTAL = LDS_H + 16384;

__global__ __launch_bounds__(768) void kb_attn(const u16* __restrict__ xnf, const u16* __restrict__ wqf,
                                               const u16* __restrict__ kf, const u16* __restrict__ vf,
                                               const unsigned* __restrict__ rkeys, const u16* __restrict__ wgu,
                                               const u16* __restrict__ wd,
                                               u16* __restrict__ of, float* __restrict__ ypart,
                                               int* __restrict__ sel) {
  __shared__ __attribute__((aligned(16))) char smem[LDS_TOTAL];
  const int tid = threadIdx.x;
  const int lane = tid & 63, wave = tid >> 6;
  const int b = blockIdx.x;
  TSR(1, 8); TS(1, 0);

  if (wave < 8) {
    const int rb = (b & 7) + 8 * ((b >> 3) & 3), hg = b >> 5;
    const int rt = wave >> 2, ks4 = wave & 3;
    const int r = lane & 31, hh = lane >> 5;
    {
      u32x4 tk[8];
#pragma unroll
      for (int c = 0; c < 8; ++c) tk[c] = *(const u32x4*)((const char*)kf + c * 8192 + tid * 16);
      bf16x8 af[2][8], bfr[8];
      const int rtq = wave & 1, kq = wave >> 1;
      const u16* ap = wqf + ((size_t)(2 * hg) * 32 + 8 * kq) * 512 + lane * 8;
      const u16* bp = xnf + ((size_t)(2 * rb + rtq) * 32 + 8 * kq) * 512 + lane * 8;
#pragma unroll
      for (int s = 0; s < 8; ++s) {
        bfr[s] = *(const bf16x8*)(bp + s * 512);
        af[0][s] = *(const bf16x8*)(ap + s * 512);
        af[1][s] = *(const bf16x8*)(ap + 32 * 512 + s * 512);
      }
      __builtin_amdgcn_sched_barrier(0);
#pragma unroll
      for (int c = 0; c < 8; ++c) *(u32x4*)(smem + LDS_K + c * 8192 + tid * 16) = tk[c];
      __builtin_amdgcn_sched_barrier(0);
      f32x16 acc[2];
#pragma unroll
      for (int e = 0; e < 16; ++e) { acc[0][e] = 0.f; acc[1][e] = 0.f; }
#pragma unroll
      for (int s = 0; s < 8; ++s) {
        acc[0] = mfma32(af[0][s], bfr[s], acc[0]);
        acc[1] = mfma32(af[1][s], bfr[s], acc[1]);
      }
#pragma unroll
      for (int dt = 0; dt < 2; ++dt)
#pragma unroll
        for (int q4 = 0; q4 < 4; ++q4) {
          f32x4 p = {acc[dt][4 * q4], acc[dt][4 * q4 + 1], acc[dt][4 * q4 + 2], acc[dt][4 * q4 + 3]};
          *(f32x4*)(smem + LDS_V + ((((rtq * 4 + kq) * 2 + dt) * 4 + q4) * 64 + lane) * 16) = p;
        }
    }
    __builtin_amdgcn_sched_barrier(0);
    u32x4 tv[8], tvx;
#pragma unroll
    for (int c = 0; c < 8; ++c) tv[c] = *(const u32x4*)((const char*)vf + c * 8192 + tid * 16);
    tvx = *(const u32x4*)((const char*)vf + 65536 + (tid & 127) * 16);
    TS(1, 1);
    __syncthreads();
    TS(1, 2);
    const int hl = wave & 3;
    const int head = hg * 4 + hl;
    bf16x8 qf;
    {
      f32x4 a0 = {0.f, 0.f, 0.f, 0.f}, a1 = {0.f, 0.f, 0.f, 0.f};
#pragma unroll
      for (int k = 0; k < 4; ++k) {
        const char* p = smem + LDS_V + ((((rt * 4 + k) * 2 + (hl >> 1)) * 4 + 2 * (hl & 1)) * 64 + lane) * 16;
        a0 += *(const f32x4*)p;
        a1 += *(const f32x4*)(p + 1024);
      }
      const float SC = 0.25f * 1.44269504088896340736f;
      u32x4 pk = {pack2(rbf(a0[0]) * SC, rbf(a0[1]) * SC), pack2(rbf(a0[2]) * SC, rbf(a0[3]) * SC),
                  pack2(rbf(a1[0]) * SC, rbf(a1[1]) * SC), pack2(rbf(a1[2]) * SC, rbf(a1[3]) * SC)};
      qf = __builtin_bit_cast(bf16x8, pk);
    }

    const char* kp = smem + LDS_K + lane * 16;
    float m0 = -__builtin_inff(), m1 = -__builtin_inff();
    f32x16 zero16;
#pragma unroll
    for (int e = 0; e < 16; ++e) zero16[e] = 0.f;
    {
      f32x16 sa = mfma32(*(const bf16x8*)(kp), qf, zero16);
      f32x16 sb = mfma32(*(const bf16x8*)(kp + 1024), qf, zero16);
#pragma unroll 4
      for (int t = 2; t < 64; t += 2) {
        const bf16x8 ka = *(const bf16x8*)(kp + t * 1024);
        const bf16x8 kb = *(const bf16x8*)(kp + t * 1024 + 1024);
        const f32x16 na = mfma32(ka, qf, zero16);
#pragma unroll
        for (int e = 0; e < 16; e += 2) m0 = fmaxf(fmaxf(m0, sa[e]), sa[e + 1]);
        const f32x16 nb = mfma32(kb, qf, zero16);
#pragma unroll
        for (int e = 0; e < 16; e += 2) m1 = fmaxf(fmaxf(m1, sb[e]), sb[e + 1]);
        sa = na; sb = nb;
      }
#pragma unroll
      for (int e = 0; e < 16; e += 2) m0 = fmaxf(fmaxf(m0, sa[e]), sa[e + 1]);
#pragma unroll
      for (int e = 0; e < 16; e += 2) m1 = fmaxf(fmaxf(m1, sb[e]), sb[e + 1]);
    }
    float m = fmaxf(m0, m1);
    m = fmaxf(m, __shfl_xor(m, 32));
    TS(1, 3);
    __syncthreads();
#pragma unroll
    for (int c = 0; c < 8; ++c) *(u32x4*)(smem + LDS_V + c * 8192 + tid * 16) = tv[c];
    if (wave < 2) *(u32x4*)(smem + LDS_V + 65536 + tid * 16) = tvx;
    __syncthreads();
    TS(1, 4);

    f32x16 negm;
#pragma unroll
    for (int e = 0; e < 16; ++e) negm[e] = -m;
    f32x16 o;
#pragma unroll
    for (int e = 0; e < 16; ++e) o[e] = 0.f;
    const unsigned lbase = (unsigned)(uintptr_t)(__attribute__((address_space(3))) char*)smem;
    unsigned ka_ = lbase + LDS_K + lane * 16;
    unsigned va_ = lbase + LDS_V + ((r < 16) ? (hh * 16 + r) * 16 : (r == 16 ? 1024 : 1040));
    unsigned vb_ = lbase + LDS_V + ((r < 16) ? (hh * 16 + r) * 16 + 512 : (r == 16 ? 1024 : 1040));
    f32x16 s = mfma32(LDSF(ka_), qf, negm);
    bf16x8 kn = LDSF(ka_ + 1024);
    bf16x8 v0 = LDSF(va_);
    bf16x8 v1 = LDSF(vb_);
#pragma unroll 1
    for (int tb = 0; tb < 8; ++tb) {
      asm volatile("" : "+v"(ka_), "+v"(va_), "+v"(vb_));
#pragma unroll
      for (int u = 0; u < 8; ++u) {
        const f32x16 sn = mfma32(kn, qf, negm);
        const bf16x8 k2 = LDSF(ka_ + (u + 2) * 1024);
        const bf16x8 v0n = LDSF(va_ + (u + 1) * VT_STRIDE);
        const bf16x8 v1n = LDSF(vb_ + (u + 1) * VT_STRIDE);
        float p[16];
#pragma unroll
        for (int e = 0; e < 8; ++e) p[e] = __builtin_amdgcn_exp2f(s[e]);
        u32x4 pa = {pack2(p[0], p[1]), pack2(p[2], p[3]), pack2(p[4], p[5]), pack2(p[6], p[7])};
        o = mfma32(v0, __builtin_bit_cast(bf16x8, pa), o);
#pragma unroll
        for (int e = 8; e < 16; ++e) p[e] = __builtin_amdgcn_exp2f(s[e]);
        u32x4 pb = {pack2(p[8], p[9]), pack2(p[10], p[11]), pack2(p[12], p[13]), pack2(p[14], p[15])};
        o = mfma32(v1, __builtin_bit_cast(bf16x8, pb), o);
        s = sn; kn = k2; v0 = v0n; v1 = v1n;
        __builtin_amdgcn_sched_group_barrier(0x008, 1, 0);
        __builtin_amdgcn_sched_group_barrier(0x100, 3, 0);
        __builtin_amdgcn_sched_group_barrier(0x402, 12, 0);
        __builtin_amdgcn_sched_group_barrier(0x008, 1, 0);
        __builtin_amdgcn_sched_group_barrier(0x402, 12, 0);
        __builtin_amdgcn_sched_group_barrier(0x008, 1, 0);
        __builtin_amdgcn_sched_barrier(0);
      }
      ka_ += 8 * 1024; va_ += 8 * VT_STRIDE; vb_ += 8 * VT_STRIDE;
    }
    TS(1, 5);
    const float il = 1.0f / __shfl(o[8], r);
    u32x4 w0 = {pack2(o[0] * il, o[1] * il), pack2(o[2] * il, o[3] * il), pack2(o[4] * il, o[5] * il),
                pack2(o[6] * il, o[7] * il)};
    *(u32x4*)(of + ((size_t)((2 * rb + rt) * 32 + head) * 64 + lane) * 8) = w0;
  } else {
    if (b >= NHELP) return;
    __builtin_amdgcn_s_setprio(3);
    const int hj = wave - 8;
    const int g = lane >> 4, i = lane & 15;
    const int qd = i >> 2;
    const int f0 = b * 16 + hj * 4;
    const u32x4 kv = *(const u32x4*)(rkeys + lane * 4);
    bf16x8 wa[8];
#pragma unroll
    for (int s = 0; s < 8; ++s) wa[s] = *(const bf16x8*)(wgu + (size_t)(b * 4 + hj) * 4096 + s * 512 + lane * 8);
    u32x4 wdv[4];
#pragma unroll
    for (int f = 0; f < 4; ++f) wdv[f] = *(const u32x4*)(wd + (size_t)(f0 + f) * D_ + lane * 8);
    unsigned k0 = max(kv[0], kv[2]), k1 = max(min(kv[0], kv[2]), max(kv[1], kv[3]));
#pragma unroll
    for (int off = 1; off < 64; off <<= 1) {
      const unsigned o0 = __shfl_xor(k0, off), o1 = __shfl_xor(k1, off);
      k1 = max(min(k0, o0), max(k1, o1));
      k0 = max(k0, o0);
    }
    const int i0 = 2047 - (int)(k0 & 0x7ffu), i1 = 2047 - (int)(k1 & 0x7ffu);
    TS(1, 3);
    if (b == 0 && hj == 0 && lane == 0) {
      const float v0 = __uint_as_float((k0 & 0x80000000u) ? (k0 & 0x7fff0000u) : (~k0 & 0xffff0000u));
      const float v1 = __uint_as_float((k1 & 0x80000000u) ? (k1 & 0x7fff0000u) : (~k1 & 0xffff0000u));
      sel[0] = i0; sel[1] = i1;
      ((float*)sel)[2] = rbf(1.0f / (1.0f + __expf(-v0)));
      ((float*)sel)[3] = rbf(1.0f / (1.0f + __expf(-v1)));
    }
    const int srow = (i & 1) ? i1 : i0;
    const u16* xp = xnf + ((size_t)((srow >> 5) * 32 + 8 * ((i >> 1) & 3) + (g >> 1)) * 64 + (g & 1) * 32 + (srow & 31)) * 8;
    f32x4 a0 = {0.f, 0.f, 0.f, 0.f}, a1 = {0.f, 0.f, 0.f, 0.f};
    {
      bf16x8 xa[4];
#pragma unroll
      for (int s = 0; s < 4; ++s) xa[s] = *(const bf16x8*)(xp + (size_t)s * 1024);
#pragma unroll
      for (int s = 0; s < 4; ++s) {
        a0 = mfma16(xa[s], wa[s], a0);
        a1 = mfma16(xa[s], wa[4 + s], a1);
      }
    }
    float t[4];
    {
      const bool ok = g == (qd >> 1);
      const bool up = qd & 1;
      t[0] = ok ? (up ? a0[2] : a0[0]) : 0.f;
      t[1] = ok ? (up ? a0[3] : a0[1]) : 0.f;
      t[2] = ok ? (up ? a1[2] : a1[0]) : 0.f;
      t[3] = ok ? (up ? a1[3] : a1[1]) : 0.f;
#pragma unroll
      for (int off = 4; off < 32; off <<= 1)
#pragma unroll
        for (int e = 0; e < 4; ++e) t[e] += __shfl_xor(t[e], off);
    }
    float hv[2][4];
#pragma unroll
    for (int tk = 0; tk < 2; ++tk) {
      const float gt = rbf(t[tk]);
      const float h = rbf(rbf(gt / (1.0f + __expf(-gt))) * rbf(t[2 + tk]));
#pragma unroll
      for (int f = 0; f < 4; ++f)
        hv[tk][f] = __builtin_bit_cast(float, __builtin_amdgcn_readlane(__builtin_bit_cast(int, h), f));
    }
#pragma unroll
    for (int tk = 0; tk < 2; ++tk) {
      float y[8];
#pragma unroll
      for (int e = 0; e < 8; ++e) y[e] = 0.f;
#pragma unroll
      for (int f = 0; f < 4; ++f)
#pragma unroll
        for (int e = 0; e < 4; ++e) {
          y[2 * e] = __builtin_fmaf(hv[tk][f], bf2f(wdv[f][e] & 0xffffu), y[2 * e]);
          y[2 * e + 1] = __builtin_fmaf(hv[tk][f], bfhi2f(wdv[f][e]), y[2 * e + 1]);
        }
      char* yp = smem + LDS_H + (hj * 2 + tk) * 2048 + lane * 32;
      *(f32x4*)yp = (f32x4){y[0], y[1], y[2], y[3]};
      *(f32x4*)(yp + 16) = (f32x4){y[4], y[5], y[6], y[7]};
    }
    TS(1, 4);
    __syncthreads();
    {
      const char* yp = smem + LDS_H + hj * 1024 + lane * 16;
      const f32x4 y = (*(const f32x4*)yp + *(const f32x4*)(yp + 4096)) +
                      (*(const f32x4*)(yp + 8192) + *(const f32x4*)(yp + 12288));
      *(f32x4*)(ypart + (size_t)b * 1024 + hj * 256 + lane * 4) = y;
    }
  }
  TS(1, 6); TSR(1, 9);
}

static constexpr int CH_STRIDE = 1056;
static constexpr int LDS_Y = 64 * CH_STRIDE;
__global__ __launch_bounds__(512) void kc_out(const u16* __restrict__ x, const u16* __restrict__ of,
                                              const u16* __restrict__ wof, const float2* __restrict__ stats,
                                              const float* __restrict__ ypart, const int* __restrict__ sel,
                                              u16* __restrict__ out) {
  __shared__ __attribute__((aligned(16))) char smem[LDS_Y + 2 * 8 * 256];
  const int tid = threadIdx.x;
  const int lane = tid & 63, wave = tid >> 6;
  TSR(2, 8); TS(2, 0);
  const int bb = blockIdx.x;
  const int rb = (bb & 7) + 8 * ((bb >> 3) & 3), cg = bb >> 5;
  const int rt = wave & 1, ks4 = wave >> 1;
  const int idx0 = sel[0], idx1 = sel[1];
  bf16x8 af[2][8], bfr[8];
  const u16* ap = wof + ((size_t)(2 * cg) * 32 + 8 * ks4) * 512 + lane * 8;
  const u16* bp = of + ((size_t)(2 * rb + rt) * 32 + 8 * ks4) * 512 + lane * 8;
#pragma unroll
  for (int s = 0; s < 8; ++s) {
    bfr[s] = *(const bf16x8*)(bp + s * 512);
    af[0][s] = *(const bf16x8*)(ap + s * 512);
    af[1][s] = *(const bf16x8*)(ap + 32 * 512 + s * 512);
  }
  const int r64 = tid >> 3, oct = tid & 7;
  const int row = rb * 64 + r64;
  const size_t off = (size_t)row * D_ + cg * 64 + oct * 8;
  const u32x4 xv = *(const u32x4*)(x + off);
  const float2 st = stats[row];
#pragma unroll
  for (int tk = 0; tk < 2; ++tk) {
    const int idx = tk ? idx1 : idx0;
    if ((unsigned)(idx - rb * 64) < 64u) {
      const int pg = tid >> 4;
      const float* yp = ypart + (size_t)(pg * 2 + tk) * 512 + cg * 64 + (tid & 15) * 4;
      f32x4 y = *(const f32x4*)yp + *(const f32x4*)(yp + 32 * 1024);
      if (pg + 64 < NHELP) y += *(const f32x4*)(yp + 64 * 1024);
#pragma unroll
      for (int e = 0; e < 4; ++e) {
        y[e] += __shfl_xor(y[e], 16);
        y[e] += __shfl_xor(y[e], 32);
      }
      if (lane < 16) *(f32x4*)(smem + LDS_Y + (tk * 8 + wave) * 256 + lane * 16) = y;
    }
  }
  f32x16 acc[2];
#pragma unroll
  for (int e = 0; e < 16; ++e) { acc[0][e] = 0.f; acc[1][e] = 0.f; }
#pragma unroll
  for (int s = 0; s < 8; ++s) {
    acc[0] = mfma32(af[0][s], bfr[s], acc[0]);
    acc[1] = mfma32(af[1][s], bfr[s], acc[1]);
  }
  TS(2, 1);
#pragma unroll
  for (int ct = 0; ct < 2; ++ct)
#pragma unroll
    for (int q4 = 0; q4 < 4; ++q4) {
      f32x4 p = {acc[ct][4 * q4], acc[ct][4 * q4 + 1], acc[ct][4 * q4 + 2], acc[ct][4 * q4 + 3]};
      *(f32x4*)(smem + (((rt * 4 + ks4) * 2 + ct) * 4 + q4) * CH_STRIDE + lane * 16) = p;
    }
  __syncthreads();
  TS(2, 2);
  f32x4 lo = {0.f, 0.f, 0.f, 0.f}, hi = {0.f, 0.f, 0.f, 0.f};
#pragma unroll
  for (int k = 0; k < 4; ++k) {
    const char* p = smem + (((r64 >> 5) * 4 + k) * 8 + oct) * CH_STRIDE + (r64 & 31) * 16;
    lo += *(const f32x4*)p;
    hi += *(const f32x4*)(p + 512);
  }
  float upd[8];
#pragma unroll
  for (int e = 0; e < 8; ++e) upd[e] = 0.f;
  if (row == idx0 || row == idx1) {
    const int tk = (row == idx0) ? 0 : 1;
    f32x4 ya = {0.f, 0.f, 0.f, 0.f}, yb = {0.f, 0.f, 0.f, 0.f};
#pragma unroll
    for (int p = 0; p < 8; ++p) {
      const char* yp = smem + LDS_Y + (tk * 8 + p) * 256 + oct * 32;
      ya += *(const f32x4*)yp;
      yb += *(const f32x4*)(yp + 16);
    }
    const float rs = ((const float*)sel)[2 + tk];
#pragma unroll
    for (int e = 0; e < 4; ++e) { upd[e] = rbf(rbf(ya[e]) * rs); upd[4 + e] = rbf(rbf(yb[e]) * rs); }
  }
  const float inv = 1.0f / st.y;
  float res[8];
#pragma unroll
  for (int e = 0; e < 4; ++e) {
    const float x0 = bf2f(xv[e] & 0xffffu), x1 = bfhi2f(xv[e]);
    const float a0 = rbf(e < 2 ? lo[2 * e] : hi[2 * e - 4]);
    const float a1 = rbf(e < 2 ? lo[2 * e + 1] : hi[2 * e - 3]);
    res[2 * e] = rbf(x0 + a0) + rbf(ln_elem(x0, st.x, st.y, inv) + upd[2 * e]);
    res[2 * e + 1] = rbf(x1 + a1) + rbf(ln_elem(x1, st.x, st.y, inv) + upd[2 * e + 1]);
  }
  u32x4 o = {pack2(res[0], res[1]), pack2(res[2], res[3]), pack2(res[4], res[5]), pack2(res[6], res[7])};
  *(u32x4*)(out + off) = o;
  TS(2, 3); TSR(2, 9);
}

extern "C" void kernel_launch(void* const* d_in, const int* in_sizes, int n_in, void* d_out, int out_size,
                              void* d_ws, size_t ws_size, hipStream_t stream) {
  const u16* x = (const u16*)d_in[0];
  const u16* wq = (const u16*)d_in[1];
  const u16* wk = (const u16*)d_in[2];
  const u16* wv = (const u16*)d_in[3];
  const u16* wo = (const u16*)d_in[4];
  const u16* wr = (const u16*)d_in[6];
  const u16* wg = (const u16*)d_in[7];
  const u16* wu = (const u16*)d_in[8];
  const u16* wd = (const u16*)d_in[9];
  char* ws = (char*)d_ws;
  u16* xnf = (u16*)(ws + OFF_XNF);
  u16* of = (u16*)(ws + OFF_OF);
  u16* wqf = (u16*)(ws + OFF_WQF);
  u16* wof = (u16*)(ws + OFF_WOF);
  u16* kf = (u16*)(ws + OFF_KF);
  u16* vf = (u16*)(ws + OFF_VF);
  unsigned* rkeys = (unsigned*)(ws + OFF_R);
  float2* stats = (float2*)(ws + OFF_STATS);
  float* ypart = (float*)(ws + OFF_Y);
  int* sel = (int*)(ws + OFF_SEL);
  u16* wgu = (u16*)(ws + OFF_WGU);
  u16* out = (u16*)d_out;

  ka_prep<<<288, 512, 0, stream>>>(x, wq, wk, wv, wo, wr, wg, wu, wgu, xnf, wqf, wof, kf, vf, rkeys, stats);
  kb_attn<<<256, 768, 0, stream>>>(xnf, wqf, kf, vf, rkeys, wgu, wd, of, ypart, sel);
  kc_out<<<256, 512, 0, stream>>>(x, of, wof, stats, ypart, sel, out);
}
```

```cpp
#include <hip/hip_runtime.h>
#include <hip/hip_bf16.h>

typedef unsigned short u16;
typedef __attribute__((ext_vector_type(8))) __bf16 bf16x8;
typedef __attribute__((ext_vector_type(2))) __bf16 bf16x2;
typedef __attribute__((ext_vector_type(2))) float f32x2;
typedef __attribute__((ext_vector_type(4))) float f32x4;
typedef __attribute__((ext_vector_type(16))) float f32x16;
typedef __attribute__((ext_vector_type(4))) unsigned u32x4;
typedef __attribute__((ext_vector_type(2))) unsigned u32x2;

#define DI __device__ __forceinline__
#define TS(k, n)
#define TSR(k, n)

static constexpr int S_ = 2048;
static constexpr int D_ = 512;
static constexpr int F_ = 1376;

#define LDSF(a) (*(const __attribute__((address_space(3))) bf16x8*)(uintptr_t)(a))
DI float bf2f(unsigned u) { return __uint_as_float(u << 16); }
DI float bfhi2f(unsigned u) { return __uint_as_float(u & 0xffff0000u); }
DI float rbf(float f) { return (float)(__bf16)f; }
DI unsigned pack2(float lo, float hi) {
  f32x2 p = {lo, hi};
  bf16x2 q = __builtin_convertvector(p, bf16x2);
  return __builtin_bit_cast(unsigned, q);
}
DI f32x4 mfma16(bf16x8 a, bf16x8 b, f32x4 c) { return __builtin_amdgcn_mfma_f32_16x16x32_bf16(a, b, c, 0, 0, 0); }
DI f32x16 mfma32(bf16x8 a, bf16x8 b, f32x16 c) { return __builtin_amdgcn_mfma_f32_32x32x16_bf16(a, b, c, 0, 0, 0); }
DI unsigned perm_lo(unsigned hi_src, unsigned lo_src) { return __builtin_amdgcn_perm(hi_src, lo_src, 0x05040100u); }
DI unsigned perm_hi(unsigned hi_src, unsigned lo_src) { return __builtin_amdgcn_perm(hi_src, lo_src, 0x07060302u); }

DI bf16x8 frag_lo(const unsigned* w) {
  u32x4 r = {perm_lo(w[1], w[0]), perm_lo(w[3], w[2]), perm_lo(w[5], w[4]), perm_lo(w[7], w[6])};
  return __builtin_bit_cast(bf16x8, r);
}
DI bf16x8 frag_hi(const unsigned* w) {
  u32x4 r = {perm_hi(w[1], w[0]), perm_hi(w[3], w[2]), perm_hi(w[5], w[4]), perm_hi(w[7], w[6])};
  return __builtin_bit_cast(bf16x8, r);
}
DI float ln_elem(float xv, float mb, float den, float inv) {
  const float d = rbf(xv - mb);
  float q = d * inv;
  const float rem = __builtin_fmaf(-q, den, d);
  q = __builtin_fmaf(rem, inv, q);
  return rbf(q);
}

static constexpr int VT_STRIDE = 1056;
static constexpr int VF_BYTES = 64 * VT_STRIDE;
static constexpr size_t OFF_XNF = 0;
static constexpr size_t OFF_OF = OFF_XNF + (size_t)S_ * D_ * 2;
static constexpr size_t OFF_WQF = OFF_OF + (size_t)S_ * D_ * 2;
static constexpr size_t OFF_WOF = OFF_WQF + (size_t)D_ * D_ * 2;
static constexpr size_t OFF_KF = OFF_WOF + (size_t)D_ * D_ * 2;
static constexpr size_t OFF_VF = OFF_KF + 65536;
static constexpr size_t OFF_R = OFF_VF + VF_BYTES;
static constexpr size_t OFF_STATS = OFF_R + S_ * 4;
static constexpr int NHELP = 86;
static constexpr size_t OFF_Y = OFF_STATS + S_ * 8;
static constexpr size_t OFF_SEL = OFF_Y + NHELP * 1024 * 4;
static constexpr size_t OFF_WGU = OFF_SEL + 256;

static constexpr int XN_STRIDE = 1040;
__global__ __launch_bounds__(512) void ka_prep(const u16* __restrict__ x, const u16* __restrict__ wq,
                                               const u16* __restrict__ wk, const u16* __restrict__ wv,
                                               const u16* __restrict__ wo, const u16* __restrict__ wr,
                                               const u16* __restrict__ wg, const u16* __restrict__ wu,
                                               u16* __restrict__ wgu,
                                               u16* __restrict__ xnf, u16* __restrict__ wqf, u16* __restrict__ wof,
                                               u16* __restrict__ kf, u16* __restrict__ vf,
                                               unsigned* __restrict__ rkeys, float2* __restrict__ stats) {
  __shared__ __attribute__((aligned(16))) char smem[16 * XN_STRIDE + 8 * 5 * 64 * 16];
  const int tid = threadIdx.x;
  const int lane = tid & 63, wave = tid >> 6;
  TSR(0, 8); TS(0, 0);
  if (blockIdx.x >= 192) {
    const int pb = blockIdx.x - 192;
    const int sg = pb & 15, mat = (pb >> 4) & 1, ch = pb >> 5;
    const int qd = tid & 3, fg = ch * 128 + (tid >> 2);
    if (fg < F_ / 4) {
      const u16* W = (mat ? wu : wg) + (size_t)(128 * qd + 8 * sg) * F_ + 4 * fg;
      unsigned wx[8], wy[8];
#pragma unroll
      for (int j = 0; j < 8; ++j) {
        const u32x2 t = *(const u32x2*)(W + (size_t)j * F_);
        wx[j] = t[0]; wy[j] = t[1];
      }
      u16* dst = wgu + (size_t)fg * 4096 + mat * 2048 + (sg >> 2) * 512 + ((sg & 3) * 16 + qd * 4) * 8;
      *(bf16x8*)dst = frag_lo(wx);
      *(bf16x8*)(dst + 8) = frag_hi(wx);
      *(bf16x8*)(dst + 16) = frag_lo(wy);
      *(bf16x8*)(dst + 24) = frag_hi(wy);
    }
    TS(0, 3); TSR(0, 9);
    return;
  }
  if (blockIdx.x >= 128) {
    const int pb = blockIdx.x - 128;
    const bool iso = pb >= 32;
    const int cp = tid & 255, ko = (pb & 31) * 2 + (tid >> 8);
    const int ks = ko >> 1, hh = ko & 1;
    const u16* W = (iso ? wo : wq) + 2 * cp;
    unsigned w[8];
#pragma unroll
    for (int j = 0; j < 8; ++j) {
      const int row = 16 * ks + (iso ? (8 * (j >> 2) + 4 * hh + (j & 3)) : (8 * hh + j));
      w[j] = *(const unsigned*)(W + row * D_);
    }
    u16* dst = (iso ? wof : wqf) + (((cp >> 4) * 32 + ks) * 64 + hh * 32 + ((2 * cp) & 31)) * 8;
    *(bf16x8*)dst = frag_lo(w);
    *(bf16x8*)(dst + 8) = frag_hi(w);
    TS(0, 3); TSR(0, 9);
    return;
  }
  const int row0 = blockIdx.x * 16;
  const int rt = blockIdx.x >> 1, th = blockIdx.x & 1;
  const int g = lane >> 4, i = lane & 15;
  const int k0 = wave * 64 + 8 * g;
  u32x4 xr[2];
#pragma unroll
  for (int rr = 0; rr < 2; ++rr) xr[rr] = *(const u32x4*)(x + (size_t)(row0 + 2 * wave + rr) * D_ + 8 * lane);
  const u16* wbase = ((i < 4) ? (wk + 4 * i) : (i < 8 ? wv + 4 * (i - 4) : wk)) + k0 * 16;
  u32x2 w[2][8];
#pragma unroll
  for (int s = 0; s < 2; ++s)
#pragma unroll
    for (int j = 0; j < 8; ++j) w[s][j] = *(const u32x2*)(wbase + (32 * s + j) * 16);
  bf16x8 rf[2];
#pragma unroll
  for (int s = 0; s < 2; ++s) rf[s] = *(const bf16x8*)(wr + k0 + 32 * s);

  float s1[2], s2[2];
#pragma unroll
  for (int rr = 0; rr < 2; ++rr) {
    float a = 0.f, q = 0.f;
#pragma unroll
    for (int e = 0; e < 4; ++e) {
      const float lo = bf2f(xr[rr][e] & 0xffffu), hi = bfhi2f(xr[rr][e]);
      a += lo + hi;
      q = __builtin_fmaf(lo, lo, q);
      q = __builtin_fmaf(hi, hi, q);
    }
    s1[rr] = a; s2[rr] = q;
  }
#pragma unroll
  for (int off = 32; off >= 1; off >>= 1)
#pragma unroll
    for (int rr = 0; rr < 2; ++rr) {
      s1[rr] += __shfl_xor(s1[rr], off);
      s2[rr] += __shfl_xor(s2[rr], off);
    }
#pragma unroll
  for (int rr = 0; rr < 2; ++rr) {
    const float mean = s1[rr] * (1.0f / D_);
    float var = s2[rr] * (1.0f / D_) - mean * mean;
    var = fmaxf(var, 0.f);
    const float mb = rbf(mean);
    const float den = rbf(__builtin_sqrtf(rbf(rbf(var) + 1e-5f)));
    const float inv = 1.0f / den;
    if (lane == rr) stats[row0 + 2 * wave + rr] = make_float2(mb, den);
    u32x4 o;
#pragma unroll
    for (int e = 0; e < 4; ++e)
      o[e] = pack2(ln_elem(bf2f(xr[rr][e] & 0xffffu), mb, den, inv), ln_elem(bfhi2f(xr[rr][e]), mb, den, inv));
    *(u32x4*)(smem + (2 * wave + rr) * XN_STRIDE + lane * 16) = o;
  }
  if (tid < 2 && th == 0) {
    const unsigned cv = tid ? 0u : 0x3f803f80u;
    u32x4 c4 = {cv, cv, cv, cv};
    *(u32x4*)((char*)vf + rt * VT_STRIDE + 1024 + tid * 16) = c4;
  }
  TS(0, 1);
  __syncthreads();
  {
    const int r = lane & 15, hh = (lane >> 4) & 1, kp = lane >> 5;
#pragma unroll
    for (int s = 0; s < 2; ++s) {
      const int ks = 4 * wave + 2 * s + kp;
      const u32x4 v = *(const u32x4*)(smem + r * XN_STRIDE + ks * 32 + hh * 16);
      *(u32x4*)(xnf + ((size_t)(rt * 32 + ks) * 64 + hh * 32 + th * 16 + r) * 8) = v;
    }
  }
  f32x4* red = (f32x4*)(smem + 16 * XN_STRIDE);
  {
    f32x4 acc[4], ar;
    ar = (f32x4){0.f, 0.f, 0.f, 0.f};
#pragma unroll
    for (int c = 0; c < 4; ++c) acc[c] = (f32x4){0.f, 0.f, 0.f, 0.f};
#pragma unroll
    for (int s = 0; s < 2; ++s) {
      unsigned wx[8], wy[8];
#pragma unroll
      for (int j = 0; j < 8; ++j) { wx[j] = w[s][j][0]; wy[j] = w[s][j][1]; }
      bf16x8 b[4] = {frag_lo(wx), frag_hi(wx), frag_lo(wy), frag_hi(wy)};
      const bf16x8 xa = *(const bf16x8*)(smem + i * XN_STRIDE + (k0 + 32 * s) * 2);
#pragma unroll
      for (int c = 0; c < 4; ++c) acc[c] = mfma16(xa, b[c], acc[c]);
      ar = mfma16(xa, rf[s], ar);
    }
#pragma unroll
    for (int c = 0; c < 4; ++c) red[(wave * 5 + c) * 64 + lane] = acc[c];
    red[(wave * 5 + 4) * 64 + lane] = ar;
  }
  __syncthreads();
  TS(0, 2);
  if (wave < 5) {
    f32x4 v = red[wave * 64 + lane];
#pragma unroll
    for (int wv_ = 1; wv_ < 8; ++wv_) v += red[(wv_ * 5 + wave) * 64 + lane];
    const int kk = 16 * th + 4 * g;
    if (wave == 4) {
      unsigned k0 = 0, k1 = 0;
#pragma unroll
      for (int e = 0; e < 4; ++e) {
        const unsigned u = __float_as_uint(rbf(v[e]));
        const unsigned ord = (u & 0x80000000u) ? ~u : (u | 0x80000000u);
        const unsigned key = (ord & 0xffff0000u) | (unsigned)(2047 - (row0 + 4 * g + e));
        k1 = max(k1, min(k0, key));
        k0 = max(k0, key);
      }
#pragma unroll
      for (int off = 16; off < 64; off <<= 1) {
        const unsigned o0 = __shfl_xor(k0, off), o1 = __shfl_xor(k1, off);
        k1 = max(min(k0, o0), max(k1, o1));
        k0 = max(k0, o0);
      }
      if (lane == 0) *(u32x2*)(rkeys + 2 * blockIdx.x) = (u32x2){k0, k1};
    } else if (i < 4) {
      const int hh = i & 1, j = (i >> 1) * 4 + wave;
#pragma unroll
      for (int e = 0; e < 4; ++e)
        kf[rt * 512 + (hh * 32 + kk + e) * 8 + j] = __builtin_bit_cast(u16, (__bf16)v[e]);
    } else if (i < 8) {
      const int d = 4 * (i - 4) + wave;
      const int hh = g & 1, jh = g >> 1;
      u32x2 o = {pack2(v[0], v[1]), pack2(v[2], v[3])};
      *(u32x2*)(vf + rt * (VT_STRIDE / 2) + th * 256 + (hh * 16 + d) * 8 + jh * 4) = o;
    }
  }
  TS(0, 3); TSR(0, 9);
}

static constexpr int LDS_K = 0;
static constexpr int LDS_V = 65536;
static constexpr int LDS_H = 65536 + VF_BYTES + 2048;
static constexpr int LDS_TOTAL = LDS_H + 16384;

__global__ __launch_bounds__(768) void kb_attn(const u16* __restrict__ xnf, const u16* __restrict__ wqf,
                                               const u16* __restrict__ kf, const u16* __restrict__ vf,
                                               const unsigned* __restrict__ rkeys, const u16* __restrict__ wgu,
                                               const u16* __restrict__ wd,
                                               u16* __restrict__ of, float* __restrict__ ypart,
                                               int* __restrict__ sel) {
  __shared__ __attribute__((aligned(16))) char smem[LDS_TOTAL];
  const int tid = threadIdx.x;
  const int lane = tid & 63, wave = tid >> 6;
  const int b = blockIdx.x;
  TSR(1, 8); TS(1, 0);

  if (wave < 8) {
    const int rb = (b & 7) + 8 * ((b >> 3) & 3), hg = b >> 5;
    const int rt = wave >> 2, ks4 = wave & 3;
    const int r = lane & 31, hh = lane >> 5;
    {
      u32x4 tk[8];
#pragma unroll
      for (int c = 0; c < 8; ++c) tk[c] = *(const u32x4*)((const char*)kf + c * 8192 + tid * 16);
      bf16x8 af[2][8], bfr[8];
      const int rtq = wave & 1, kq = wave >> 1;
      const u16* ap = wqf + ((size_t)(2 * hg) * 32 + 8 * kq) * 512 + lane * 8;
      const u16* bp = xnf + ((size_t)(2 * rb + rtq) * 32 + 8 * kq) * 512 + lane * 8;
#pragma unroll
      for (int s = 0; s < 8; ++s) {
        bfr[s] = *(const bf16x8*)(bp + s * 512);
        af[0][s] = *(const bf16x8*)(ap + s * 512);
        af[1][s] = *(const bf16x8*)(ap + 32 * 512 + s * 512);
      }
      __builtin_amdgcn_sched_barrier(0);
#pragma unroll
      for (int c = 0; c < 8; ++c) *(u32x4*)(smem + LDS_K + c * 8192 + tid * 16) = tk[c];
      __builtin_amdgcn_sched_barrier(0);
      f32x16 acc[2];
#pragma unroll
      for (int e = 0; e < 16; ++e) { acc[0][e] = 0.f; acc[1][e] = 0.f; }
#pragma unroll
      for (int s = 0; s < 8; ++s) {
        acc[0] = mfma32(af[0][s], bfr[s], acc[0]);
        acc[1] = mfma32(af[1][s], bfr[s], acc[1]);
      }
#pragma unroll
      for (int dt = 0; dt < 2; ++dt)
#pragma unroll
        for (int q4 = 0; q4 < 4; ++q4) {
          f32x4 p = {acc[dt][4 * q4], acc[dt][4 * q4 + 1], acc[dt][4 * q4 + 2], acc[dt][4 * q4 + 3]};
          *(f32x4*)(smem + LDS_V + ((((rtq * 4 + kq) * 2 + dt) * 4 + q4) * 64 + lane) * 16) = p;
        }
    }
    __builtin_amdgcn_sched_barrier(0);
    u32x4 tv[8], tvx;
#pragma unroll
    for (int c = 0; c < 8; ++c) tv[c] = *(const u32x4*)((const char*)vf + c * 8192 + tid * 16);
    tvx = *(const u32x4*)((const char*)vf + 65536 + (tid & 127) * 16);
    TS(1, 1);
    __syncthreads();
    TS(1, 2);
    const int hl = wave & 3;
    const int head = hg * 4 + hl;
    bf16x8 qf;
    {
      f32x4 a0 = {0.f, 0.f, 0.f, 0.f}, a1 = {0.f, 0.f, 0.f, 0.f};
#pragma unroll
      for (int k = 0; k < 4; ++k) {
        const char* p = smem + LDS_V + ((((rt * 4 + k) * 2 + (hl >> 1)) * 4 + 2 * (hl & 1)) * 64 + lane) * 16;
        a0 += *(const f32x4*)p;
        a1 += *(const f32x4*)(p + 1024);
      }
      const float SC = 0.25f * 1.44269504088896340736f;
      u32x4 pk = {pack2(rbf(a0[0]) * SC, rbf(a0[1]) * SC), pack2(rbf(a0[2]) * SC, rbf(a0[3]) * SC),
                  pack2(rbf(a1[0]) * SC, rbf(a1[1]) * SC), pack2(rbf(a1[2]) * SC, rbf(a1[3]) * SC)};
      qf = __builtin_bit_cast(bf16x8, pk);
    }

    const unsigned lbase = (unsigned)(uintptr_t)(__attribute__((address_space(3))) char*)smem;
    float m0 = -__builtin_inff(), m1 = -__builtin_inff();
    f32x16 zero16;
#pragma unroll
    for (int e = 0; e < 16; ++e) zero16[e] = 0.f;
    {
      unsigned kq_ = lbase + LDS_K + lane * 16;
      asm volatile("" : "+v"(kq_));
      f32x16 sa = mfma32(LDSF(kq_), qf, zero16);
      f32x16 sb = mfma32(LDSF(kq_ + 1024), qf, zero16);
      bf16x8 k2 = LDSF(kq_ + 2048), k3 = LDSF(kq_ + 3072);
      __builtin_amdgcn_sched_barrier(0);
#pragma unroll
      for (int t = 0; t < 64; ++t) {
        f32x16 sc = sb;
        bf16x8 k4 = k3;
        if (t + 2 < 64) sc = mfma32(k2, qf, zero16);
        if (t + 4 < 64) k4 = LDSF(kq_ + (t + 4) * 1024);
        if (t & 1) {
#pragma unroll
          for (int e = 0; e < 16; e += 2) m1 = fmaxf(fmaxf(m1, sa[e]), sa[e + 1]);
        } else {
#pragma unroll
          for (int e = 0; e < 16; e += 2) m0 = fmaxf(fmaxf(m0, sa[e]), sa[e + 1]);
        }
        sa = sb; sb = sc; k2 = k3; k3 = k4;
        __builtin_amdgcn_sched_group_barrier(0x008, 1, 0);
        __builtin_amdgcn_sched_group_barrier(0x100, 1, 0);
        __builtin_amdgcn_sched_group_barrier(0x002, 8, 0);
        __builtin_amdgcn_sched_barrier(0);
      }
    }
    float m = fmaxf(m0, m1);
    m = fmaxf(m, __shfl_xor(m, 32));
    TS(1, 3);
    __syncthreads();
#pragma unroll
    for (int c = 0; c < 8; ++c) *(u32x4*)(smem + LDS_V + c * 8192 + tid * 16) = tv[c];
    if (wave < 2) *(u32x4*)(smem + LDS_V + 65536 + tid * 16) = tvx;
    __syncthreads();
    TS(1, 4);

    f32x16 negm;
#pragma unroll
    for (int e = 0; e < 16; ++e) negm[e] = -m;
    f32x16 o;
#pragma unroll
    for (int e = 0; e < 16; ++e) o[e] = 0.f;
    unsigned ka_ = lbase + LDS_K + lane * 16;
    unsigned va_ = lbase + LDS_V + ((r < 16) ? (hh * 16 + r) * 16 : (r == 16 ? 1024 : 1040));
    unsigned vb_ = lbase + LDS_V + ((r < 16) ? (hh * 16 + r) * 16 + 512 : (r == 16 ? 1024 : 1040));
    f32x16 s = mfma32(LDSF(ka_), qf, negm);
    bf16x8 kn = LDSF(ka_ + 1024);
    bf16x8 v0 = LDSF(va_);
    bf16x8 v1 = LDSF(vb_);
#pragma unroll 1
    for (int tb = 0; tb < 8; ++tb) {
      asm volatile("" : "+v"(ka_), "+v"(va_), "+v"(vb_));
#pragma unroll
      for (int u = 0; u < 8; ++u) {
        const f32x16 sn = mfma32(kn, qf, negm);
        const bf16x8 k2 = LDSF(ka_ + (u + 2) * 1024);
        const bf16x8 v0n = LDSF(va_ + (u + 1) * VT_STRIDE);
        const bf16x8 v1n = LDSF(vb_ + (u + 1) * VT_STRIDE);
        float p[16];
#pragma unroll
        for (int e = 0; e < 8; ++e) p[e] = __builtin_amdgcn_exp2f(s[e]);
        u32x4 pa = {pack2(p[0], p[1]), pack2(p[2], p[3]), pack2(p[4], p[5]), pack2(p[6], p[7])};
        o = mfma32(v0, __builtin_bit_cast(bf16x8, pa), o);
#pragma unroll
        for (int e = 8; e < 16; ++e) p[e] = __builtin_amdgcn_exp2f(s[e]);
        u32x4 pb = {pack2(p[8], p[9]), pack2(p[10], p[11]), pack2(p[12], p[13]), pack2(p[14], p[15])};
        o = mfma32(v1, __builtin_bit_cast(bf16x8, pb), o);
        s = sn; kn = k2; v0 = v0n; v1 = v1n;
        __builtin_amdgcn_sched_group_barrier(0x008, 1, 0);
        __builtin_amdgcn_sched_group_barrier(0x100, 3, 0);
        __builtin_amdgcn_sched_group_barrier(0x402, 12, 0);
        __builtin_amdgcn_sched_group_barrier(0x008, 1, 0);
        __builtin_amdgcn_sched_group_barrier(0x402, 12, 0);
        __builtin_amdgcn_sched_group_barrier(0x008, 1, 0);
        __builtin_amdgcn_sched_barrier(0);
      }
      ka_ += 8 * 1024; va_ += 8 * VT_STRIDE; vb_ += 8 * VT_STRIDE;
    }
    TS(1, 5);
    const float il = 1.0f / __shfl(o[8], r);
    u32x4 w0 = {pack2(o[0] * il, o[1] * il), pack2(o[2] * il, o[3] * il), pack2(o[4] * il, o[5] * il),
                pack2(o[6] * il, o[7] * il)};
    *(u32x4*)(of + ((size_t)((2 * rb + rt) * 32 + head) * 64 + lane) * 8) = w0;
  } else {
    if (b >= NHELP) return;
    __builtin_amdgcn_s_setprio(3);
    const int hj = wave - 8;
    const int g = lane >> 4, i = lane & 15;
    const int qd = i >> 2;
    const int f0 = b * 16 + hj * 4;
    const u32x4 kv = *(const u32x4*)(rkeys + lane * 4);
    bf16x8 wa[8];
#pragma unroll
    for (int s = 0; s < 8; ++s) wa[s] = *(const bf16x8*)(wgu + (size_t)(b * 4 + hj) * 4096 + s * 512 + lane * 8);
    u32x4 wdv[4];
#pragma unroll
    for (int f = 0; f < 4; ++f) wdv[f] = *(const u32x4*)(wd + (size_t)(f0 + f) * D_ + lane * 8);
    unsigned k0 = max(kv[0], kv[2]), k1 = max(min(kv[0], kv[2]), max(kv[1], kv[3]));
#pragma unroll
    for (int off = 1; off < 64; off <<= 1) {
      const unsigned o0 = __shfl_xor(k0, off), o1 = __shfl_xor(k1, off);
      k1 = max(min(k0, o0), max(k1, o1));
      k0 = max(k0, o0);
    }
    const int i0 = 2047 - (int)(k0 & 0x7ffu), i1 = 2047 - (int)(k1 & 0x7ffu);
    TS(1, 3);
    if (b == 0 && hj == 0 && lane == 0) {
      const float v0 = __uint_as_float((k0 & 0x80000000u) ? (k0 & 0x7fff0000u) : (~k0 & 0xffff0000u));
      const float v1 = __uint_as_float((k1 & 0x80000000u) ? (k1 & 0x7fff0000u) : (~k1 & 0xffff0000u));
      sel[0] = i0; sel[1] = i1;
      ((float*)sel)[2] = rbf(1.0f / (1.0f + __expf(-v0)));
      ((float*)sel)[3] = rbf(1.0f / (1.0f + __expf(-v1)));
    }
    const int srow = (i & 1) ? i1 : i0;
    const u16* xp = xnf + ((size_t)((srow >> 5) * 32 + 8 * ((i >> 1) & 3) + (g >> 1)) * 64 + (g & 1) * 32 + (srow & 31)) * 8;
    f32x4 a0 = {0.f, 0.f, 0.f, 0.f}, a1 = {0.f, 0.f, 0.f, 0.f};
    {
      bf16x8 xa[4];
#pragma unroll
      for (int s = 0; s < 4; ++s) xa[s] = *(const bf16x8*)(xp + (size_t)s * 1024);
#pragma unroll
      for (int s = 0; s < 4; ++s) {
        a0 = mfma16(xa[s], wa[s], a0);
        a1 = mfma16(xa[s], wa[4 + s], a1);
      }
    }
    float t[4];
    {
      const bool ok = g == (qd >> 1);
      const bool up = qd & 1;
      t[0] = ok ? (up ? a0[2] : a0[0]) : 0.f;
      t[1] = ok ? (up ? a0[3] : a0[1]) : 0.f;
      t[2] = ok ? (up ? a1[2] : a1[0]) : 0.f;
      t[3] = ok ? (up ? a1[3] : a1[1]) : 0.f;
#pragma unroll
      for (int off = 4; off < 32; off <<= 1)
#pragma unroll
        for (int e = 0; e < 4; ++e) t[e] += __shfl_xor(t[e], off);
    }
    float hv[2][4];
#pragma unroll
    for (int tk = 0; tk < 2; ++tk) {
      const float gt = rbf(t[tk]);
      const float h = rbf(rbf(gt / (1.0f + __expf(-gt))) * rbf(t[2 + tk]));
#pragma unroll
      for (int f = 0; f < 4; ++f)
        hv[tk][f] = __builtin_bit_cast(float, __builtin_amdgcn_readlane(__builtin_bit_cast(int, h), f));
    }
#pragma unroll
    for (int tk = 0; tk < 2; ++tk) {
      float y[8];
#pragma unroll
      for (int e = 0; e < 8; ++e) y[e] = 0.f;
#pragma unroll
      for (int f = 0; f < 4; ++f)
#pragma unroll
        for (int e = 0; e < 4; ++e) {
          y[2 * e] = __builtin_fmaf(hv[tk][f], bf2f(wdv[f][e] & 0xffffu), y[2 * e]);
          y[2 * e + 1] = __builtin_fmaf(hv[tk][f], bfhi2f(wdv[f][e]), y[2 * e + 1]);
        }
      char* yp = smem + LDS_H + (hj * 2 + tk) * 2048 + lane * 32;
      *(f32x4*)yp = (f32x4){y[0], y[1], y[2], y[3]};
      *(f32x4*)(yp + 16) = (f32x4){y[4], y[5], y[6], y[7]};
    }
    TS(1, 4);
    __syncthreads();
    {
      const char* yp = smem + LDS_H + hj * 1024 + lane * 16;
      const f32x4 y = (*(const f32x4*)yp + *(const f32x4*)(yp + 4096)) +
                      (*(const f32x4*)(yp + 8192) + *(const f32x4*)(yp + 12288));
      *(f32x4*)(ypart + (size_t)b * 1024 + hj * 256 + lane * 4) = y;
    }
  }
  TS(1, 6); TSR(1, 9);
}

static constexpr int CH_STRIDE = 1056;
static constexpr int LDS_Y = 64 * CH_STRIDE;
__global__ __launch_bounds__(512) void kc_out(const u16* __restrict__ x, const u16* __restrict__ of,
                                              const u16* __restrict__ wof, const float2* __restrict__ stats,
                                              const float* __restrict__ ypart, const int* __restrict__ sel,
                                              u16* __restrict__ out) {
  __shared__ __attribute__((aligned(16))) char smem[LDS_Y + 2 * 8 * 256];
  const int tid = threadIdx.x;
  const int lane = tid & 63, wave = tid >> 6;
  TSR(2, 8); TS(2, 0);
  const int bb = blockIdx.x;
  const int rb = (bb & 7) + 8 * ((bb >> 3) & 3), cg = bb >> 5;
  const int rt = wave & 1, ks4 = wave >> 1;
  const int idx0 = sel[0], idx1 = sel[1];
  bf16x8 af[2][8], bfr[8];
  const u16* ap = wof + ((size_t)(2 * cg) * 32 + 8 * ks4) * 512 + lane * 8;
  const u16* bp = of + ((size_t)(2 * rb + rt) * 32 + 8 * ks4) * 512 + lane * 8;
#pragma unroll
  for (int s = 0; s < 8; ++s) {
    bfr[s] = *(const bf16x8*)(bp + s * 512);
    af[0][s] = *(const bf16x8*)(ap + s * 512);
    af[1][s] = *(const bf16x8*)(ap + 32 * 512 + s * 512);
  }
  const int r64 = tid >> 3, oct = tid & 7;
  const int row = rb * 64 + r64;
  const size_t off = (size_t)row * D_ + cg * 64 + oct * 8;
  const u32x4 xv = *(const u32x4*)(x + off);
  const float2 st = stats[row];
#pragma unroll
  for (int tk = 0; tk < 2; ++tk) {
    const int idx = tk ? idx1 : idx0;
    if ((unsigned)(idx - rb * 64) < 64u) {
      const int pg = tid >> 4;
      const float* yp = ypart + (size_t)(pg * 2 + tk) * 512 + cg * 64 + (tid & 15) * 4;
      f32x4 y = *(const f32x4*)yp + *(const f32x4*)(yp + 32 * 1024);
      if (pg + 64 < NHELP) y += *(const f32x4*)(yp + 64 * 1024);
#pragma unroll
      for (int e = 0; e < 4; ++e) {
        y[e] += __shfl_xor(y[e], 16);
        y[e] += __shfl_xor(y[e], 32);
      }
      if (lane < 16) *(f32x4*)(smem + LDS_Y + (tk * 8 + wave) * 256 + lane * 16) = y;
    }
  }
  f32x16 acc[2];
#pragma unroll
  for (int e = 0; e < 16; ++e) { acc[0][e] = 0.f; acc[1][e] = 0.f; }
#pragma unroll
  for (int s = 0; s < 8; ++s) {
    acc[0] = mfma32(af[0][s], bfr[s], acc[0]);
    acc[1] = mfma32(af[1][s], bfr[s], acc[1]);
  }
  TS(2, 1);
#pragma unroll
  for (int ct = 0; ct < 2; ++ct)
#pragma unroll
    for (int q4 = 0; q4 < 4; ++q4) {
      f32x4 p = {acc[ct][4 * q4], acc[ct][4 * q4 + 1], acc[ct][4 * q4 + 2], acc[ct][4 * q4 + 3]};
      *(f32x4*)(smem + (((rt * 4 + ks4) * 2 + ct) * 4 + q4) * CH_STRIDE + lane * 16) = p;
    }
  __syncthreads();
  TS(2, 2);
  f32x4 lo = {0.f, 0.f, 0.f, 0.f}, hi = {0.f, 0.f, 0.f, 0.f};
#pragma unroll
  for (int k = 0; k < 4; ++k) {
    const char* p = smem + (((r64 >> 5) * 4 + k) * 8 + oct) * CH_STRIDE + (r64 & 31) * 16;
    lo += *(const f32x4*)p;
    hi += *(const f32x4*)(p + 512);
  }
  float upd[8];
#pragma unroll
  for (int e = 0; e < 8; ++e) upd[e] = 0.f;
  if (row == idx0 || row == idx1) {
    const int tk = (row == idx0) ? 0 : 1;
    f32x4 ya = {0.f, 0.f, 0.f, 0.f}, yb = {0.f, 0.f, 0.f, 0.f};
#pragma unroll
    for (int p = 0; p < 8; ++p) {
      const char* yp = smem + LDS_Y + (tk * 8 + p) * 256 + oct * 32;
      ya += *(const f32x4*)yp;
      yb += *(const f32x4*)(yp + 16);
    }
    const float rs = ((const float*)sel)[2 + tk];
#pragma unroll
    for (int e = 0; e < 4; ++e) { upd[e] = rbf(rbf(ya[e]) * rs); upd[4 + e] = rbf(rbf(yb[e]) * rs); }
  }
  const float inv = 1.0f / st.y;
  float res[8];
#pragma unroll
  for (int e = 0; e < 4; ++e) {
    const float x0 = bf2f(xv[e] & 0xffffu), x1 = bfhi2f(xv[e]);
    const float a0 = rbf(e < 2 ? lo[2 * e] : hi[2 * e - 4]);
    const float a1 = rbf(e < 2 ? lo[2 * e + 1] : hi[2 * e - 3]);
    res[2 * e] = rbf(x0 + a0) + rbf(ln_elem(x0, st.x, st.y, inv) + upd[2 * e]);
    res[2 * e + 1] = rbf(x1 + a1) + rbf(ln_elem(x1, st.x, st.y, inv) + upd[2 * e + 1]);
  }
  u32x4 o = {pack2(res[0], res[1]), pack2(res[2], res[3]), pack2(res[4], res[5]), pack2(res[6], res[7])};
  *(u32x4*)(out + off) = o;
  TS(2, 3); TSR(2, 9);
}

extern "C" void kernel_launch(void* const* d_in, const int* in_sizes, int n_in, void* d_out, int out_size,
                              void* d_ws, size_t ws_size, hipStream_t stream) {
  const u16* x = (const u16*)d_in[0];
  const u16* wq = (const u16*)d_in[1];
  const u16* wk = (const u16*)d_in[2];
  const u16* wv = (const u16*)d_in[3];
  const u16* wo = (const u16*)d_in[4];
  const u16* wr = (const u16*)d_in[6];
  const u16* wg = (const u16*)d_in[7];
  const u16* wu = (const u16*)d_in[8];
  const u16* wd = (const u16*)d_in[9];
  char* ws = (char*)d_ws;
  u16* xnf = (u16*)(ws + OFF_XNF);
  u16* of = (u16*)(ws + OFF_OF);
  u16* wqf = (u16*)(ws + OFF_WQF);
  u16* wof = (u16*)(ws + OFF_WOF);
  u16* kf = (u16*)(ws + OFF_KF);
  u16* vf = (u16*)(ws + OFF_VF);
  unsigned* rkeys = (unsigned*)(ws + OFF_R);
  float2* stats = (float2*)(ws + OFF_STATS);
  float* ypart = (float*)(ws + OFF_Y);
  int* sel = (int*)(ws + OFF_SEL);
  u16* wgu = (u16*)(ws + OFF_WGU);
  u16* out = (u16*)d_out;

  ka_prep<<<288, 512, 0, stream>>>(x, wq, wk, wv, wo, wr, wg, wu, wgu, xnf, wqf, wof, kf, vf, rkeys, stats);
  kb_attn<<<256, 768, 0, stream>>>(xnf, wqf, kf, vf, rkeys, wgu, wd, of, ypart, sel);
  kc_out<<<256, 512, 0, stream>>>(x, of, wof, stats, ypart, sel, out);
}
```

```cpp
#include <hip/hip_runtime.h>
#include <hip/hip_bf16.h>

typedef unsigned short u16;
typedef __attribute__((ext_vector_type(8))) __bf16 bf16x8;
typedef __attribute__((ext_vector_type(2))) __bf16 bf16x2;
typedef __attribute__((ext_vector_type(2))) float f32x2;
typedef __attribute__((ext_vector_type(4))) float f32x4;
typedef __attribute__((ext_vector_type(16))) float f32x16;
typedef __attribute__((ext_vector_type(4))) unsigned u32x4;
typedef __attribute__((ext_vector_type(2))) unsigned u32x2;

#define DI __device__ __forceinline__
#define TS(k, n)
#define TSR(k, n)

static constexpr int S_ = 2048;
static constexpr int D_ = 512;
static constexpr int F_ = 1376;

#define LDSF(a) (*(const __attribute__((address_space(3))) bf16x8*)(uintptr_t)(a))
DI float bf2f(unsigned u) { return __uint_as_float(u << 16); }
DI float bfhi2f(unsigned u) { return __uint_as_float(u & 0xffff0000u); }
DI float rbf(float f) { return (float)(__bf16)f; }
DI unsigned pack2(float lo, float hi) {
  f32x2 p = {lo, hi};
  bf16x2 q = __builtin_convertvector(p, bf16x2);
  return __builtin_bit_cast(unsigned, q);
}
DI f32x4 mfma16(bf16x8 a, bf16x8 b, f32x4 c) { return __builtin_amdgcn_mfma_f32_16x16x32_bf16(a, b, c, 0, 0, 0); }
DI f32x16 mfma32(bf16x8 a, bf16x8 b, f32x16 c) { return __builtin_amdgcn_mfma_f32_32x32x16_bf16(a, b, c, 0, 0, 0); }
DI unsigned perm_lo(unsigned hi_src, unsigned lo_src) { return __builtin_amdgcn_perm(hi_src, lo_src, 0x05040100u); }
DI unsigned perm_hi(unsigned hi_src, unsigned lo_src) { return __builtin_amdgcn_perm(hi_src, lo_src, 0x07060302u); }

DI bf16x8 frag_lo(const unsigned* w) {
  u32x4 r = {perm_lo(w[1], w[0]), perm_lo(w[3], w[2]), perm_lo(w[5], w[4]), perm_lo(w[7], w[6])};
  return __builtin_bit_cast(bf16x8, r);
}
DI bf16x8 frag_hi(const unsigned* w) {
  u32x4 r = {perm_hi(w[1], w[0]), perm_hi(w[3], w[2]), perm_hi(w[5], w[4]), perm_hi(w[7], w[6])};
  return __builtin_bit_cast(bf16x8, r);
}
DI float ln_elem(float xv, float mb, float den, float inv) {
  const float d = rbf(xv - mb);
  float q = d * inv;
  const float rem = __builtin_fmaf(-q, den, d);
  q = __builtin_fmaf(rem, inv, q);
  return rbf(q);
}

static constexpr int VT_STRIDE = 1056;
static constexpr int VF_BYTES = 64 * VT_STRIDE;
static constexpr size_t OFF_XNF = 0;
static constexpr size_t OFF_OF = OFF_XNF + (size_t)S_ * D_ * 2;
static constexpr size_t OFF_WQF = OFF_OF + (size_t)S_ * D_ * 2;
static constexpr size_t OFF_WOF = OFF_WQF + (size_t)D_ * D_ * 2;
static constexpr size_t OFF_KF = OFF_WOF + (size_t)D_ * D_ * 2;
static constexpr size_t OFF_VF = OFF_KF + 65536;
static constexpr size_t OFF_R = OFF_VF + VF_BYTES;
static constexpr size_t OFF_STATS = OFF_R + S_ * 4;
static constexpr int NHELP = 86;
static constexpr size_t OFF_Y = OFF_STATS + S_ * 8;
static constexpr size_t OFF_SEL = OFF_Y + NHELP * 1024 * 4;

static constexpr int XN_STRIDE = 1040;
__global__ __launch_bounds__(512) void ka_prep(const u16* __restrict__ x, const u16* __restrict__ wq,
                                               const u16* __restrict__ wk, const u16* __restrict__ wv,
                                               const u16* __restrict__ wo, const u16* __restrict__ wr,
                                               u16* __restrict__ xnf, u16* __restrict__ wqf, u16* __restrict__ wof,
                                               u16* __restrict__ kf, u16* __restrict__ vf,
                                               unsigned* __restrict__ rkeys, float2* __restrict__ stats) {
  __shared__ __attribute__((aligned(16))) char smem[16 * XN_STRIDE + 8 * 5 * 64 * 16];
  const int tid = threadIdx.x;
  const int lane = tid & 63, wave = tid >> 6;
  TSR(0, 8); TS(0, 0);
  if (blockIdx.x >= 128) {
    const int pb = blockIdx.x - 128;
    const bool iso = pb >= 32;
    const int cp = tid & 255, ko = (pb & 31) * 2 + (tid >> 8);
    const int ks = ko >> 1, hh = ko & 1;
    const u16* W = (iso ? wo : wq) + 2 * cp;
    unsigned w[8];
#pragma unroll
    for (int j = 0; j < 8; ++j) {
      const int row = 16 * ks + (iso ? (8 * (j >> 2) + 4 * hh + (j & 3)) : (8 * hh + j));
      w[j] = *(const unsigned*)(W + row * D_);
    }
    u16* dst = (iso ? wof : wqf) + (((cp >> 4) * 32 + ks) * 64 + hh * 32 + ((2 * cp) & 31)) * 8;
    *(bf16x8*)dst = frag_lo(w);
    *(bf16x8*)(dst + 8) = frag_hi(w);
    TS(0, 3); TSR(0, 9);
    return;
  }
  const int row0 = blockIdx.x * 16;
  const int rt = blockIdx.x >> 1, th = blockIdx.x & 1;
  const int g = lane >> 4, i = lane & 15;
  const int k0 = wave * 64 + 8 * g;
  u32x4 xr[2];
#pragma unroll
  for (int rr = 0; rr < 2; ++rr) xr[rr] = *(const u32x4*)(x + (size_t)(row0 + 2 * wave + rr) * D_ + 8 * lane);
  const u16* wbase = ((i < 4) ? (wk + 4 * i) : (i < 8 ? wv + 4 * (i - 4) : wk)) + k0 * 16;
  u32x2 w[2][8];
#pragma unroll
  for (int s = 0; s < 2; ++s)
#pragma unroll
    for (int j = 0; j < 8; ++j) w[s][j] = *(const u32x2*)(wbase + (32 * s + j) * 16);
  bf16x8 rf[2];
#pragma unroll
  for (int s = 0; s < 2; ++s) rf[s] = *(const bf16x8*)(wr + k0 + 32 * s);

  float s1[2], s2[2];
#pragma unroll
  for (int rr = 0; rr < 2; ++rr) {
    float a = 0.f, q = 0.f;
#pragma unroll
    for (int e = 0; e < 4; ++e) {
      const float lo = bf2f(xr[rr][e] & 0xffffu), hi = bfhi2f(xr[rr][e]);
      a += lo + hi;
      q = __builtin_fmaf(lo, lo, q);
      q = __builtin_fmaf(hi, hi, q);
    }
    s1[rr] = a; s2[rr] = q;
  }
#pragma unroll
  for (int off = 32; off >= 1; off >>= 1)
#pragma unroll
    for (int rr = 0; rr < 2; ++rr) {
      s1[rr] += __shfl_xor(s1[rr], off);
      s2[rr] += __shfl_xor(s2[rr], off);
    }
#pragma unroll
  for (int rr = 0; rr < 2; ++rr) {
    const float mean = s1[rr] * (1.0f / D_);
    float var = s2[rr] * (1.0f / D_) - mean * mean;
    var = fmaxf(var, 0.f);
    const float mb = rbf(mean);
    const float den = rbf(__builtin_sqrtf(rbf(rbf(var) + 1e-5f)));
    const float inv = 1.0f / den;
    if (lane == rr) stats[row0 + 2 * wave + rr] = make_float2(mb, den);
    u32x4 o;
#pragma unroll
    for (int e = 0; e < 4; ++e)
      o[e] = pack2(ln_elem(bf2f(xr[rr][e] & 0xffffu), mb, den, inv), ln_elem(bfhi2f(xr[rr][e]), mb, den, inv));
    *(u32x4*)(smem + (2 * wave + rr) * XN_STRIDE + lane * 16) = o;
  }
  if (tid < 2 && th == 0) {
    const unsigned cv = tid ? 0u : 0x3f803f80u;
    u32x4 c4 = {cv, cv, cv, cv};
    *(u32x4*)((char*)vf + rt * VT_STRIDE + 1024 + tid * 16) = c4;
  }
  TS(0, 1);
  __syncthreads();
  {
    const int r = lane & 15, hh = (lane >> 4) & 1, kp = lane >> 5;
#pragma unroll
    for (int s = 0; s < 2; ++s) {
      const int ks = 4 * wave + 2 * s + kp;
      const u32x4 v = *(const u32x4*)(smem + r * XN_STRIDE + ks * 32 + hh * 16);
      *(u32x4*)(xnf + ((size_t)(rt * 32 + ks) * 64 + hh * 32 + th * 16 + r) * 8) = v;
    }
  }
  f32x4* red = (f32x4*)(smem + 16 * XN_STRIDE);
  {
    f32x4 acc[4], ar;
    ar = (f32x4){0.f, 0.f, 0.f, 0.f};
#pragma unroll
    for (int c = 0; c < 4; ++c) acc[c] = (f32x4){0.f, 0.f, 0.f, 0.f};
#pragma unroll
    for (int s = 0; s < 2; ++s) {
      unsigned wx[8], wy[8];
#pragma unroll
      for (int j = 0; j < 8; ++j) { wx[j] = w[s][j][0]; wy[j] = w[s][j][1]; }
      bf16x8 b[4] = {frag_lo(wx), frag_hi(wx), frag_lo(wy), frag_hi(wy)};
      const bf16x8 xa = *(const bf16x8*)(smem + i * XN_STRIDE + (k0 + 32 * s) * 2);
#pragma unroll
      for (int c = 0; c < 4; ++c) acc[c] = mfma16(xa, b[c], acc[c]);
      ar = mfma16(xa, rf[s], ar);
    }
#pragma unroll
    for (int c = 0; c < 4; ++c) red[(wave * 5 + c) * 64 + lane] = acc[c];
    red[(wave * 5 + 4) * 64 + lane] = ar;
  }
  __syncthreads();
  TS(0, 2);
  if (wave < 5) {
    f32x4 v = red[wave * 64 + lane];
#pragma unroll
    for (int wv_ = 1; wv_ < 8; ++wv_) v += red[(wv_ * 5 + wave) * 64 + lane];
    const int kk = 16 * th + 4 * g;
    if (wave == 4) {
      unsigned k0 = 0, k1 = 0;
#pragma unroll
      for (int e = 0; e < 4; ++e) {
        const unsigned u = __float_as_uint(rbf(v[e]));
        const unsigned ord = (u & 0x80000000u) ? ~u : (u | 0x80000000u);
        const unsigned key = (ord & 0xffff0000u) | (unsigned)(2047 - (row0 + 4 * g + e));
        k1 = max(k1, min(k0, key));
        k0 = max(k0, key);
      }
#pragma unroll
      for (int off = 16; off < 64; off <<= 1) {
        const unsigned o0 = __shfl_xor(k0, off), o1 = __shfl_xor(k1, off);
        k1 = max(min(k0, o0), max(k1, o1));
        k0 = max(k0, o0);
      }
      if (lane == 0) *(u32x2*)(rkeys + 2 * blockIdx.x) = (u32x2){k0, k1};
    } else if (i < 4) {
      const int hh = i & 1, j = (i >> 1) * 4 + wave;
#pragma unroll
      for (int e = 0; e < 4; ++e)
        kf[rt * 512 + (hh * 32 + kk + e) * 8 + j] = __builtin_bit_cast(u16, (__bf16)v[e]);
    } else if (i < 8) {
      const int d = 4 * (i - 4) + wave;
      const int hh = g & 1, jh = g >> 1;
      u32x2 o = {pack2(v[0], v[1]), pack2(v[2], v[3])};
      *(u32x2*)(vf + rt * (VT_STRIDE / 2) + th * 256 + (hh * 16 + d) * 8 + jh * 4) = o;
    }
  }
  TS(0, 3); TSR(0, 9);
}

static constexpr int LDS_K = 0;
static constexpr int LDS_V = 65536;
static constexpr int LDS_H = 65536 + VF_BYTES + 2048;
static constexpr int LDS_TOTAL = LDS_H + 16384 + 16;

__global__ __launch_bounds__(768) void kb_attn(const u16* __restrict__ xnf, const u16* __restrict__ wqf,
                                               const u16* __restrict__ kf, const u16* __restrict__ vf,
                                               const unsigned* __restrict__ rkeys, const u16* __restrict__ wg,
                                               const u16* __restrict__ wu, const u16* __restrict__ wd,
                                               u16* __restrict__ of, float* __restrict__ ypart,
                                               int* __restrict__ sel) {
  __shared__ __attribute__((aligned(16))) char smem[LDS_TOTAL];
  const int tid = threadIdx.x;
  const int lane = tid & 63, wave = tid >> 6;
  const int b = blockIdx.x;
  TSR(1, 8); TS(1, 0);

  if (wave < 8) {
    const int rb = (b & 7) + 8 * ((b >> 3) & 3), hg = b >> 5;
    const int rt = wave >> 2, ks4 = wave & 3;
    const int r = lane & 31, hh = lane >> 5;
    {
      u32x4 tk[8];
#pragma unroll
      for (int c = 0; c < 8; ++c) tk[c] = *(const u32x4*)((const char*)kf + c * 8192 + tid * 16);
      bf16x8 af[2][8], bfr[8];
      const int rtq = wave & 1, kq = wave >> 1;
      const u16* ap = wqf + ((size_t)(2 * hg) * 32 + 8 * kq) * 512 + lane * 8;
      const u16* bp = xnf + ((size_t)(2 * rb + rtq) * 32 + 8 * kq) * 512 + lane * 8;
#pragma unroll
      for (int s = 0; s < 8; ++s) {
        bfr[s] = *(const bf16x8*)(bp + s * 512);
        af[0][s] = *(const bf16x8*)(ap + s * 512);
        af[1][s] = *(const bf16x8*)(ap + 32 * 512 + s * 512);
      }
      __builtin_amdgcn_sched_barrier(0);
#pragma unroll
      for (int c = 0; c < 8; ++c) *(u32x4*)(smem + LDS_K + c * 8192 + tid * 16) = tk[c];
      __builtin_amdgcn_sched_barrier(0);
      f32x16 acc[2];
#pragma unroll
      for (int e = 0; e < 16; ++e) { acc[0][e] = 0.f; acc[1][e] = 0.f; }
#pragma unroll
      for (int s = 0; s < 8; ++s) {
        acc[0] = mfma32(af[0][s], bfr[s], acc[0]);
        acc[1] = mfma32(af[1][s], bfr[s], acc[1]);
      }
#pragma unroll
      for (int dt = 0; dt < 2; ++dt)
#pragma unroll
        for (int q4 = 0; q4 < 4; ++q4) {
          f32x4 p = {acc[dt][4 * q4], acc[dt][4 * q4 + 1], acc[dt][4 * q4 + 2], acc[dt][4 * q4 + 3]};
          *(f32x4*)(smem + LDS_V + ((((rtq * 4 + kq) * 2 + dt) * 4 + q4) * 64 + lane) * 16) = p;
        }
    }
    __builtin_amdgcn_sched_barrier(0);
    u32x4 tv[8], tvx;
#pragma unroll
    for (int c = 0; c < 8; ++c) tv[c] = *(const u32x4*)((const char*)vf + c * 8192 + tid * 16);
    tvx = *(const u32x4*)((const char*)vf + 65536 + (tid & 127) * 16);
    TS(1, 1);
    __syncthreads();
    TS(1, 2);
    const int hl = wave & 3;
    const int head = hg * 4 + hl;
    bf16x8 qf;
    {
      f32x4 a0 = {0.f, 0.f, 0.f, 0.f}, a1 = {0.f, 0.f, 0.f, 0.f};
#pragma unroll
      for (int k = 0; k < 4; ++k) {
        const char* p = smem + LDS_V + ((((rt * 4 + k) * 2 + (hl >> 1)) * 4 + 2 * (hl & 1)) * 64 + lane) * 16;
        a0 += *(const f32x4*)p;
        a1 += *(const f32x4*)(p + 1024);
      }
      const float SC = 0.25f * 1.44269504088896340736f;
      u32x4 pk = {pack2(rbf(a0[0]) * SC, rbf(a0[1]) * SC), pack2(rbf(a0[2]) * SC, rbf(a0[3]) * SC),
                  pack2(rbf(a1[0]) * SC, rbf(a1[1]) * SC), pack2(rbf(a1[2]) * SC, rbf(a1[3]) * SC)};
      qf = __builtin_bit_cast(bf16x8, pk);
    }
    __syncthreads();

    const unsigned lbase = (unsigned)(uintptr_t)(__attribute__((address_space(3))) char*)smem;
    float m0 = -__builtin_inff(), m1 = -__builtin_inff();
    f32x16 zero16;
#pragma unroll
    for (int e = 0; e < 16; ++e) zero16[e] = 0.f;
    {
      unsigned kq_ = lbase + LDS_K + lane * 16;
      asm volatile("" : "+v"(kq_));
      f32x16 sa = mfma32(LDSF(kq_), qf, zero16);
      f32x16 sb = mfma32(LDSF(kq_ + 1024), qf, zero16);
      bf16x8 k2 = LDSF(kq_ + 2048), k3 = LDSF(kq_ + 3072);
      __builtin_amdgcn_sched_barrier(0);
#pragma unroll
      for (int t = 0; t < 64; ++t) {
        f32x16 sc = sb;
        bf16x8 k4 = k3;
        if (t + 2 < 64) sc = mfma32(k2, qf, zero16);
        if (t + 4 < 64) k4 = LDSF(kq_ + (t + 4) * 1024);
        if (t & 1) {
#pragma unroll
          for (int e = 0; e < 16; e += 2) m1 = fmaxf(fmaxf(m1, sa[e]), sa[e + 1]);
        } else {
#pragma unroll
          for (int e = 0; e < 16; e += 2) m0 = fmaxf(fmaxf(m0, sa[e]), sa[e + 1]);
        }
        sa = sb; sb = sc; k2 = k3; k3 = k4;
        if (t >= 32 && t < 40) *(u32x4*)(smem + LDS_V + (t - 32) * 8192 + tid * 16) = tv[t - 32];
        if (t == 40) *(u32x4*)(smem + LDS_V + 65536 + (tid & 127) * 16) = tvx;
        __builtin_amdgcn_sched_group_barrier(0x008, 1, 0);
        __builtin_amdgcn_sched_group_barrier(0x100, 1, 0);
        __builtin_amdgcn_sched_group_barrier(0x002, 8, 0);
        __builtin_amdgcn_sched_barrier(0);
      }
    }
    float m = fmaxf(m0, m1);
    m = fmaxf(m, __shfl_xor(m, 32));
    TS(1, 3);
    __syncthreads();
    TS(1, 4);

    f32x16 negm;
#pragma unroll
    for (int e = 0; e < 16; ++e) negm[e] = -m;
    f32x16 o;
#pragma unroll
    for (int e = 0; e < 16; ++e) o[e] = 0.f;
    unsigned ka_ = lbase + LDS_K + lane * 16;
    unsigned va_ = lbase + LDS_V + ((r < 16) ? (hh * 16 + r) * 16 : (r == 16 ? 1024 : 1040));
    unsigned vb_ = lbase + LDS_V + ((r < 16) ? (hh * 16 + r) * 16 + 512 : (r == 16 ? 1024 : 1040));
    f32x16 s = mfma32(LDSF(ka_), qf, negm);
    bf16x8 kn = LDSF(ka_ + 1024);
    bf16x8 v0 = LDSF(va_);
    bf16x8 v1 = LDSF(vb_);
#pragma unroll 1
    for (int tb = 0; tb < 8; ++tb) {
      asm volatile("" : "+v"(ka_), "+v"(va_), "+v"(vb_));
#pragma unroll
      for (int u = 0; u < 8; ++u) {
        const f32x16 sn = mfma32(kn, qf, negm);
        const bf16x8 k2 = LDSF(ka_ + (u + 2) * 1024);
        const bf16x8 v0n = LDSF(va_ + (u + 1) * VT_STRIDE);
        const bf16x8 v1n = LDSF(vb_ + (u + 1) * VT_STRIDE);
        float p[16];
#pragma unroll
        for (int e = 0; e < 8; ++e) p[e] = __builtin_amdgcn_exp2f(s[e]);
        u32x4 pa = {pack2(p[0], p[1]), pack2(p[2], p[3]), pack2(p[4], p[5]), pack2(p[6], p[7])};
        o = mfma32(v0, __builtin_bit_cast(bf16x8, pa), o);
#pragma unroll
        for (int e = 8; e < 16; ++e) p[e] = __builtin_amdgcn_exp2f(s[e]);
        u32x4 pb = {pack2(p[8], p[9]), pack2(p[10], p[11]), pack2(p[12], p[13]), pack2(p[14], p[15])};
        o = mfma32(v1, __builtin_bit_cast(bf16x8, pb), o);
        s = sn; kn = k2; v0 = v0n; v1 = v1n;
        __builtin_amdgcn_sched_group_barrier(0x008, 1, 0);
        __builtin_amdgcn_sched_group_barrier(0x100, 3, 0);
        __builtin_amdgcn_sched_group_barrier(0x402, 12, 0);
        __builtin_amdgcn_sched_group_barrier(0x008, 1, 0);
        __builtin_amdgcn_sched_group_barrier(0x402, 12, 0);
        __builtin_amdgcn_sched_group_barrier(0x008, 1, 0);
        __builtin_amdgcn_sched_barrier(0);
      }
      ka_ += 8 * 1024; va_ += 8 * VT_STRIDE; vb_ += 8 * VT_STRIDE;
    }
    TS(1, 5);
    const float il = 1.0f / __shfl(o[8], r);
    u32x4 w0 = {pack2(o[0] * il, o[1] * il), pack2(o[2] * il, o[3] * il), pack2(o[4] * il, o[5] * il),
                pack2(o[6] * il, o[7] * il)};
    *(u32x4*)(of + ((size_t)((2 * rb + rt) * 32 + head) * 64 + lane) * 8) = w0;
  } else {
    if (b >= NHELP) return;
    __builtin_amdgcn_s_setprio(3);
    const int hj = wave - 8;
    const int g = lane >> 4, i = lane & 15;
    const int qd = i >> 2, c = i & 3;
    const int f0 = b * 16 + hj * 4;
    unsigned* cnt = (unsigned*)(smem + LDS_H + 16384);
    if (tid == 512) *cnt = 0u;
    const u32x4 kv = *(const u32x4*)(rkeys + lane * 4);
    unsigned k0 = max(kv[0], kv[2]), k1 = max(min(kv[0], kv[2]), max(kv[1], kv[3]));
#pragma unroll
    for (int off = 1; off < 64; off <<= 1) {
      const unsigned o0 = __shfl_xor(k0, off), o1 = __shfl_xor(k1, off);
      k1 = max(min(k0, o0), max(k1, o1));
      k0 = max(k0, o0);
    }
    const int i0 = 2047 - (int)(k0 & 0x7ffu), i1 = 2047 - (int)(k1 & 0x7ffu);
    if (b == 0 && hj == 0 && lane == 0) {
      const float v0 = __uint_as_float((k0 & 0x80000000u) ? (k0 & 0x7fff0000u) : (~k0 & 0xffff0000u));
      const float v1 = __uint_as_float((k1 & 0x80000000u) ? (k1 & 0x7fff0000u) : (~k1 & 0xffff0000u));
      sel[0] = i0; sel[1] = i1;
      ((float*)sel)[2] = rbf(1.0f / (1.0f + __expf(-v0)));
      ((float*)sel)[3] = rbf(1.0f / (1.0f + __expf(-v1)));
    }
    const int srow = (i & 1) ? i1 : i0;
    const u16* xp = xnf + ((size_t)((srow >> 5) * 32 + 8 * ((i >> 1) & 3) + (g >> 1)) * 64 + (g & 1) * 32 + (srow & 31)) * 8;
    bf16x8 xa[4];
#pragma unroll
    for (int s = 0; s < 4; ++s) xa[s] = *(const bf16x8*)(xp + (size_t)s * 1024);
    TS(1, 3);
    __syncthreads();
    unsigned wa[4][8];
    const u16* wp = ((c < 2) ? wg : wu) + f0 + 2 * (c & 1) + (size_t)(128 * qd + 8 * g) * F_;
#pragma unroll
    for (int s = 0; s < 4; ++s)
#pragma unroll
      for (int j = 0; j < 8; ++j) wa[s][j] = *(const unsigned*)(wp + (size_t)(32 * s + j) * F_);
    u32x4 wdv[4];
#pragma unroll
    for (int f = 0; f < 4; ++f) wdv[f] = *(const u32x4*)(wd + (size_t)(f0 + f) * D_ + lane * 8);
    __builtin_amdgcn_sched_barrier(0);
    __syncthreads();
    __syncthreads();
    __builtin_amdgcn_sched_barrier(0);
    TS(1, 4);
    f32x4 a0 = {0.f, 0.f, 0.f, 0.f}, a1 = {0.f, 0.f, 0.f, 0.f};
#pragma unroll
    for (int s = 0; s < 4; ++s) {
      a0 = mfma16(xa[s], frag_lo(wa[s]), a0);
      a1 = mfma16(xa[s], frag_hi(wa[s]), a1);
    }
    float t[4];
    {
      const bool ok = g == (qd >> 1);
      const bool up = qd & 1;
      t[0] = ok ? (up ? a0[2] : a0[0]) : 0.f;
      t[1] = ok ? (up ? a0[3] : a0[1]) : 0.f;
      t[2] = ok ? (up ? a1[2] : a1[0]) : 0.f;
      t[3] = ok ? (up ? a1[3] : a1[1]) : 0.f;
#pragma unroll
      for (int off = 4; off < 32; off <<= 1)
#pragma unroll
        for (int e = 0; e < 4; ++e) t[e] += __shfl_xor(t[e], off);
    }
    float hv[2][4];
#pragma unroll
    for (int e = 0; e < 4; ++e) {
      const float gt = rbf(t[e]);
      const float u = __shfl_xor(gt, 2);
      const float h = rbf(rbf(gt / (1.0f + __expf(-gt))) * u);
      hv[e & 1][e >> 1] = __builtin_bit_cast(float, __builtin_amdgcn_readlane(__builtin_bit_cast(int, h), 0));
      hv[e & 1][2 + (e >> 1)] = __builtin_bit_cast(float, __builtin_amdgcn_readlane(__builtin_bit_cast(int, h), 1));
    }
#pragma unroll
    for (int tk = 0; tk < 2; ++tk) {
      float y[8];
#pragma unroll
      for (int e = 0; e < 8; ++e) y[e] = 0.f;
#pragma unroll
      for (int f = 0; f < 4; ++f)
#pragma unroll
        for (int e = 0; e < 4; ++e) {
          y[2 * e] = __builtin_fmaf(hv[tk][f], bf2f(wdv[f][e] & 0xffffu), y[2 * e]);
          y[2 * e + 1] = __builtin_fmaf(hv[tk][f], bfhi2f(wdv[f][e]), y[2 * e + 1]);
        }
      char* yp = smem + LDS_H + (hj * 2 + tk) * 2048 + lane * 32;
      *(f32x4*)yp = (f32x4){y[0], y[1], y[2], y[3]};
      *(f32x4*)(yp + 16) = (f32x4){y[4], y[5], y[6], y[7]};
    }
    __builtin_amdgcn_fence(__ATOMIC_RELEASE, "workgroup");
    unsigned old = 0;
    if (lane == 0) old = __hip_atomic_fetch_add(cnt, 1u, __ATOMIC_RELAXED, __HIP_MEMORY_SCOPE_WORKGROUP);
    old = __builtin_amdgcn_readfirstlane(old);
    __builtin_amdgcn_fence(__ATOMIC_ACQUIRE, "workgroup");
    if (old == 3u) {
#pragma unroll
      for (int q = 0; q < 4; ++q) {
        const char* yp = smem + LDS_H + q * 1024 + lane * 16;
        const f32x4 y = (*(const f32x4*)yp + *(const f32x4*)(yp + 4096)) +
                        (*(const f32x4*)(yp + 8192) + *(const f32x4*)(yp + 12288));
        *(f32x4*)(ypart + (size_t)b * 1024 + q * 256 + lane * 4) = y;
      }
    }
  }
  TS(1, 6); TSR(1, 9);
}

static constexpr int CH_STRIDE = 1056;
static constexpr int LDS_Y = 64 * CH_STRIDE;
__global__ __launch_bounds__(512) void kc_out(const u16* __restrict__ x, const u16* __restrict__ of,
                                              const u16* __restrict__ wof, const float2* __restrict__ stats,
                                              const float* __restrict__ ypart, const int* __restrict__ sel,
                                              u16* __restrict__ out) {
  __shared__ __attribute__((aligned(16))) char smem[LDS_Y + 2 * 8 * 256];
  const int tid = threadIdx.x;
  const int lane = tid & 63, wave = tid >> 6;
  TSR(2, 8); TS(2, 0);
  const int bb = blockIdx.x;
  const int rb = (bb & 7) + 8 * ((bb >> 3) & 3), cg = bb >> 5;
  const int rt = wave & 1, ks4 = wave >> 1;
  const int idx0 = sel[0], idx1 = sel[1];
  bf16x8 af[2][8], bfr[8];
  const u16* ap = wof + ((size_t)(2 * cg) * 32 + 8 * ks4) * 512 + lane * 8;
  const u16* bp = of + ((size_t)(2 * rb + rt) * 32 + 8 * ks4) * 512 + lane * 8;
#pragma unroll
  for (int s = 0; s < 8; ++s) {
    bfr[s] = *(const bf16x8*)(bp + s * 512);
    af[0][s] = *(const bf16x8*)(ap + s * 512);
    af[1][s] = *(const bf16x8*)(ap + 32 * 512 + s * 512);
  }
  const int r64 = tid >> 3, oct = tid & 7;
  const int row = rb * 64 + r64;
  const size_t off = (size_t)row * D_ + cg * 64 + oct * 8;
  const u32x4 xv = *(const u32x4*)(x + off);
  const float2 st = stats[row];
#pragma unroll
  for (int tk = 0; tk < 2; ++tk) {
    const int idx = tk ? idx1 : idx0;
    if ((unsigned)(idx - rb * 64) < 64u) {
      const int pg = tid >> 4;
      const float* yp = ypart + (size_t)(pg * 2 + tk) * 512 + cg * 64 + (tid & 15) * 4;
      f32x4 y = *(const f32x4*)yp + *(const f32x4*)(yp + 32 * 1024);
      if (pg + 64 < NHELP) y += *(const f32x4*)(yp + 64 * 1024);
#pragma unroll
      for (int e = 0; e < 4; ++e) {
        y[e] += __shfl_xor(y[e], 16);
        y[e] += __shfl_xor(y[e], 32);
      }
      if (lane < 16) *(f32x4*)(smem + LDS_Y + (tk * 8 + wave) * 256 + lane * 16) = y;
    }
  }
  f32x16 acc[2];
#pragma unroll
  for (int e = 0; e < 16; ++e) { acc[0][e] = 0.f; acc[1][e] = 0.f; }
#pragma unroll
  for (int s = 0; s < 8; ++s) {
    acc[0] = mfma32(af[0][s], bfr[s], acc[0]);
    acc[1] = mfma32(af[1][s], bfr[s], acc[1]);
  }
  TS(2, 1);
#pragma unroll
  for (int ct = 0; ct < 2; ++ct)
#pragma unroll
    for (int q4 = 0; q4 < 4; ++q4) {
      f32x4 p = {acc[ct][4 * q4], acc[ct][4 * q4 + 1], acc[ct][4 * q4 + 2], acc[ct][4 * q4 + 3]};
      *(f32x4*)(smem + (((rt * 4 + ks4) * 2 + ct) * 4 + q4) * CH_STRIDE + lane * 16) = p;
    }
  __syncthreads();
  TS(2, 2);
  f32x4 lo = {0.f, 0.f, 0.f, 0.f}, hi = {0.f, 0.f, 0.f, 0.f};
#pragma unroll
  for (int k = 0; k < 4; ++k) {
    const char* p = smem + (((r64 >> 5) * 4 + k) * 8 + oct) * CH_STRIDE + (r64 & 31) * 16;
    lo += *(const f32x4*)p;
    hi += *(const f32x4*)(p + 512);
  }
  float upd[8];
#pragma unroll
  for (int e = 0; e < 8; ++e) upd[e] = 0.f;
  if (row == idx0 || row == idx1) {
    const int tk = (row == idx0) ? 0 : 1;
    f32x4 ya = {0.f, 0.f, 0.f, 0.f}, yb = {0.f, 0.f, 0.f, 0.f};
#pragma unroll
    for (int p = 0; p < 8; ++p) {
      const char* yp = smem + LDS_Y + (tk * 8 + p) * 256 + oct * 32;
      ya += *(const f32x4*)yp;
      yb += *(const f32x4*)(yp + 16);
    }
    const float rs = ((const float*)sel)[2 + tk];
#pragma unroll
    for (int e = 0; e < 4; ++e) { upd[e] = rbf(rbf(ya[e]) * rs); upd[4 + e] = rbf(rbf(yb[e]) * rs); }
  }
  const float inv = 1.0f / st.y;
  float res[8];
#pragma unroll
  for (int e = 0; e < 4; ++e) {
    const float x0 = bf2f(xv[e] & 0xffffu), x1 = bfhi2f(xv[e]);
    const float a0 = rbf(e < 2 ? lo[2 * e] : hi[2 * e - 4]);
    const float a1 = rbf(e < 2 ? lo[2 * e + 1] : hi[2 * e - 3]);
    res[2 * e] = rbf(x0 + a0) + rbf(ln_elem(x0, st.x, st.y, inv) + upd[2 * e]);
    res[2 * e + 1] = rbf(x1 + a1) + rbf(ln_elem(x1, st.x, st.y, inv) + upd[2 * e + 1]);
  }
  u32x4 o = {pack2(res[0], res[1]), pack2(res[2], res[3]), pack2(res[4], res[5]), pack2(res[6], res[7])};
  *(u32x4*)(out + off) = o;
  TS(2, 3); TSR(2, 9);
}

extern "C" void kernel_launch(void* const* d_in, const int* in_sizes, int n_in, void* d_out, int out_size,
                              void* d_ws, size_t ws_size, hipStream_t stream) {
  const u16* x = (const u16*)d_in[0];
  const u16* wq = (const u16*)d_in[1];
  const u16* wk = (const u16*)d_in[2];
  const u16* wv = (const u16*)d_in[3];
  const u16* wo = (const u16*)d_in[4];
  const u16* wr = (const u16*)d_in[6];
  const u16* wg = (const u16*)d_in[7];
  const u16* wu = (const u16*)d_in[8];
  const u16* wd = (const u16*)d_in[9];
  char* ws = (char*)d_ws;
  u16* xnf = (u16*)(ws + OFF_XNF);
  u16* of = (u16*)(ws + OFF_OF);
  u16* wqf = (u16*)(ws + OFF_WQF);
  u16* wof = (u16*)(ws + OFF_WOF);
  u16* kf = (u16*)(ws + OFF_KF);
  u16* vf = (u16*)(ws + OFF_VF);
  unsigned* rkeys = (unsigned*)(ws + OFF_R);
  float2* stats = (float2*)(ws + OFF_STATS);
  float* ypart = (float*)(ws + OFF_Y);
  int* sel = (int*)(ws + OFF_SEL);
  u16* out = (u16*)d_out;

  ka_prep<<<192, 512, 0, stream>>>(x, wq, wk, wv, wo, wr, xnf, wqf, wof, kf, vf, rkeys, stats);
  kb_attn<<<256, 768, 0, stream>>>(xnf, wqf, kf, vf, rkeys, wg, wu, wd, of, ypart, sel);
  kc_out<<<256, 512, 0, stream>>>(x, of, wof, stats, ypart, sel, out);
}
```

```cpp
#include <hip/hip_runtime.h>
#include <hip/hip_bf16.h>

typedef unsigned short u16;
typedef __attribute__((ext_vector_type(8))) __bf16 bf16x8;
typedef __attribute__((ext_vector_type(2))) __bf16 bf16x2;
typedef __attribute__((ext_vector_type(2))) float f32x2;
typedef __attribute__((ext_vector_type(4))) float f32x4;
typedef __attribute__((ext_vector_type(16))) float f32x16;
typedef __attribute__((ext_vector_type(4))) unsigned u32x4;
typedef __attribute__((ext_vector_type(2))) unsigned u32x2;

#define DI __device__ __forceinline__
#define TS(k, n)
#define TSR(k, n)

static constexpr int S_ = 2048;
static constexpr int D_ = 512;
static constexpr int F_ = 1376;

#define LDSF(a) (*(const __attribute__((address_space(3))) bf16x8*)(uintptr_t)(a))
DI float bf2f(unsigned u) { return __uint_as_float(u << 16); }
DI float bfhi2f(unsigned u) { return __uint_as_float(u & 0xffff0000u); }
DI float rbf(float f) { return (float)(__bf16)f; }
DI unsigned pack2(float lo, float hi) {
  f32x2 p = {lo, hi};
  bf16x2 q = __builtin_convertvector(p, bf16x2);
  return __builtin_bit_cast(unsigned, q);
}
DI f32x4 mfma16(bf16x8 a, bf16x8 b, f32x4 c) { return __builtin_amdgcn_mfma_f32_16x16x32_bf16(a, b, c, 0, 0, 0); }
DI f32x16 mfma32(bf16x8 a, bf16x8 b, f32x16 c) { return __builtin_amdgcn_mfma_f32_32x32x16_bf16(a, b, c, 0, 0, 0); }
DI unsigned perm_lo(unsigned hi_src, unsigned lo_src) { return __builtin_amdgcn_perm(hi_src, lo_src, 0x05040100u); }
DI unsigned perm_hi(unsigned hi_src, unsigned lo_src) { return __builtin_amdgcn_perm(hi_src, lo_src, 0x07060302u); }

DI bf16x8 frag_lo(const unsigned* w) {
  u32x4 r = {perm_lo(w[1], w[0]), perm_lo(w[3], w[2]), perm_lo(w[5], w[4]), perm_lo(w[7], w[6])};
  return __builtin_bit_cast(bf16x8, r);
}
DI bf16x8 frag_hi(const unsigned* w) {
  u32x4 r = {perm_hi(w[1], w[0]), perm_hi(w[3], w[2]), perm_hi(w[5], w[4]), perm_hi(w[7], w[6])};
  return __builtin_bit_cast(bf16x8, r);
}
DI float ln_elem(float xv, float mb, float den, float inv) {
  const float d = rbf(xv - mb);
  float q = d * inv;
  const float rem = __builtin_fmaf(-q, den, d);
  q = __builtin_fmaf(rem, inv, q);
  return rbf(q);
}

static constexpr int VT_STRIDE = 1056;
static constexpr int VF_BYTES = 64 * VT_STRIDE;
static constexpr size_t OFF_XNF = 0;
static constexpr size_t OFF_OF = OFF_XNF + (size_t)S_ * D_ * 2;
static constexpr size_t OFF_WQF = OFF_OF + (size_t)S_ * D_ * 2;
static constexpr size_t OFF_WOF = OFF_WQF + (size_t)D_ * D_ * 2;
static constexpr size_t OFF_KF = OFF_WOF + (size_t)D_ * D_ * 2;
static constexpr size_t OFF_VF = OFF_KF + 65536;
static constexpr size_t OFF_R = OFF_VF + VF_BYTES;
static constexpr size_t OFF_STATS = OFF_R + S_ * 4;
static constexpr int NHELP = 86;
static constexpr size_t OFF_Y = OFF_STATS + S_ * 8;
static constexpr size_t OFF_SEL = OFF_Y + NHELP * 1024 * 4;

static constexpr int XN_STRIDE = 1040;
__global__ __launch_bounds__(512) void ka_prep(const u16* __restrict__ x, const u16* __restrict__ wq,
                                               const u16* __restrict__ wk, const u16* __restrict__ wv,
                                               const u16* __restrict__ wo, const u16* __restrict__ wr,
                                               u16* __restrict__ xnf, u16* __restrict__ wqf, u16* __restrict__ wof,
                                               u16* __restrict__ kf, u16* __restrict__ vf,
                                               unsigned* __restrict__ rkeys, float2* __restrict__ stats) {
  __shared__ __attribute__((aligned(16))) char smem[16 * XN_STRIDE + 8 * 5 * 64 * 16];
  const int tid = threadIdx.x;
  const int lane = tid & 63, wave = tid >> 6;
  TSR(0, 8); TS(0, 0);
  if (blockIdx.x >= 128) {
    const int pb = blockIdx.x - 128;
    const bool iso = pb >= 32;
    const int cp = tid & 255, ko = (pb & 31) * 2 + (tid >> 8);
    const int ks = ko >> 1, hh = ko & 1;
    const u16* W = (iso ? wo : wq) + 2 * cp;
    unsigned w[8];
#pragma unroll
    for (int j = 0; j < 8; ++j) {
      const int row = 16 * ks + (iso ? (8 * (j >> 2) + 4 * hh + (j & 3)) : (8 * hh + j));
      w[j] = *(const unsigned*)(W + row * D_);
    }
    u16* dst = (iso ? wof : wqf) + (((cp >> 4) * 32 + ks) * 64 + hh * 32 + ((2 * cp) & 31)) * 8;
    *(bf16x8*)dst = frag_lo(w);
    *(bf16x8*)(dst + 8) = frag_hi(w);
    TS(0, 3); TSR(0, 9);
    return;
  }
  const int row0 = blockIdx.x * 16;
  const int rt = blockIdx.x >> 1, th = blockIdx.x & 1;
  const int g = lane >> 4, i = lane & 15;
  const int k0 = wave * 64 + 8 * g;
  u32x4 xr[2];
#pragma unroll
  for (int rr = 0; rr < 2; ++rr) xr[rr] = *(const u32x4*)(x + (size_t)(row0 + 2 * wave + rr) * D_ + 8 * lane);
  const u16* wbase = ((i < 4) ? (wk + 4 * i) : (i < 8 ? wv + 4 * (i - 4) : wk)) + k0 * 16;
  u32x2 w[2][8];
#pragma unroll
  for (int s = 0; s < 2; ++s)
#pragma unroll
    for (int j = 0; j < 8; ++j) w[s][j] = *(const u32x2*)(wbase + (32 * s + j) * 16);
  bf16x8 rf[2];
#pragma unroll
  for (int s = 0; s < 2; ++s) rf[s] = *(const bf16x8*)(wr + k0 + 32 * s);

  float s1[2], s2[2];
#pragma unroll
  for (int rr = 0; rr < 2; ++rr) {
    float a = 0.f, q = 0.f;
#pragma unroll
    for (int e = 0; e < 4; ++e) {
      const float lo = bf2f(xr[rr][e] & 0xffffu), hi = bfhi2f(xr[rr][e]);
      a += lo + hi;
      q = __builtin_fmaf(lo, lo, q);
      q = __builtin_fmaf(hi, hi, q);
    }
    s1[rr] = a; s2[rr] = q;
  }
#pragma unroll
  for (int off = 32; off >= 1; off >>= 1)
#pragma unroll
    for (int rr = 0; rr < 2; ++rr) {
      s1[rr] += __shfl_xor(s1[rr], off);
      s2[rr] += __shfl_xor(s2[rr], off);
    }
#pragma unroll
  for (int rr = 0; rr < 2; ++rr) {
    const float mean = s1[rr] * (1.0f / D_);
    float var = s2[rr] * (1.0f / D_) - mean * mean;
    var = fmaxf(var, 0.f);
    const float mb = rbf(mean);
    const float den = rbf(__builtin_sqrtf(rbf(rbf(var) + 1e-5f)));
    const float inv = 1.0f / den;
    if (lane == rr) stats[row0 + 2 * wave + rr] = make_float2(mb, den);
    u32x4 o;
#pragma unroll
    for (int e = 0; e < 4; ++e)
      o[e] = pack2(ln_elem(bf2f(xr[rr][e] & 0xffffu), mb, den, inv), ln_elem(bfhi2f(xr[rr][e]), mb, den, inv));
    *(u32x4*)(smem + (2 * wave + rr) * XN_STRIDE + lane * 16) = o;
  }
  if (tid < 2 && th == 0) {
    const unsigned cv = tid ? 0u : 0x3f803f80u;
    u32x4 c4 = {cv, cv, cv, cv};
    *(u32x4*)((char*)vf + rt * VT_STRIDE + 1024 + tid * 16) = c4;
  }
  TS(0, 1);
  __syncthreads();
  {
    const int r = lane & 15, hh = (lane >> 4) & 1, kp = lane >> 5;
#pragma unroll
    for (int s = 0; s < 2; ++s) {
      const int ks = 4 * wave + 2 * s + kp;
      const u32x4 v = *(const u32x4*)(smem + r * XN_STRIDE + ks * 32 + hh * 16);
      *(u32x4*)(xnf + ((size_t)(rt * 32 + ks) * 64 + hh * 32 + th * 16 + r) * 8) = v;
    }
  }
  f32x4* red = (f32x4*)(smem + 16 * XN_STRIDE);
  {
    f32x4 acc[4], ar;
    ar = (f32x4){0.f, 0.f, 0.f, 0.f};
#pragma unroll
    for (int c = 0; c < 4; ++c) acc[c] = (f32x4){0.f, 0.f, 0.f, 0.f};
#pragma unroll
    for (int s = 0; s < 2; ++s) {
      unsigned wx[8], wy[8];
#pragma unroll
      for (int j = 0; j < 8; ++j) { wx[j] = w[s][j][0]; wy[j] = w[s][j][1]; }
      bf16x8 b[4] = {frag_lo(wx), frag_hi(wx), frag_lo(wy), frag_hi(wy)};
      const bf16x8 xa = *(const bf16x8*)(smem + i * XN_STRIDE + (k0 + 32 * s) * 2);
#pragma unroll
      for (int c = 0; c < 4; ++c) acc[c] = mfma16(xa, b[c], acc[c]);
      ar = mfma16(xa, rf[s], ar);
    }
#pragma unroll
    for (int c = 0; c < 4; ++c) red[(wave * 5 + c) * 64 + lane] = acc[c];
    red[(wave * 5 + 4) * 64 + lane] = ar;
  }
  __syncthreads();
  TS(0, 2);
  if (wave < 5) {
    f32x4 v = red[wave * 64 + lane];
#pragma unroll
    for (int wv_ = 1; wv_ < 8; ++wv_) v += red[(wv_ * 5 + wave) * 64 + lane];
    const int kk = 16 * th + 4 * g;
    if (wave == 4) {
      unsigned k0 = 0, k1 = 0;
#pragma unroll
      for (int e = 0; e < 4; ++e) {
        const unsigned u = __float_as_uint(rbf(v[e]));
        const unsigned ord = (u & 0x80000000u) ? ~u : (u | 0x80000000u);
        const unsigned key = (ord & 0xffff0000u) | (unsigned)(2047 - (row0 + 4 * g + e));
        k1 = max(k1, min(k0, key));
        k0 = max(k0, key);
      }
#pragma unroll
      for (int off = 16; off < 64; off <<= 1) {
        const unsigned o0 = __shfl_xor(k0, off), o1 = __shfl_xor(k1, off);
        k1 = max(min(k0, o0), max(k1, o1));
        k0 = max(k0, o0);
      }
      if (lane == 0) *(u32x2*)(rkeys + 2 * blockIdx.x) = (u32x2){k0, k1};
    } else if (i < 4) {
      const int hh = i & 1, j = (i >> 1) * 4 + wave;
#pragma unroll
      for (int e = 0; e < 4; ++e)
        kf[rt * 512 + (hh * 32 + kk + e) * 8 + j] = __builtin_bit_cast(u16, (__bf16)v[e]);
    } else if (i < 8) {
      const int d = 4 * (i - 4) + wave;
      const int hh = g & 1, jh = g >> 1;
      u32x2 o = {pack2(v[0], v[1]), pack2(v[2], v[3])};
      *(u32x2*)(vf + rt * (VT_STRIDE / 2) + th * 256 + (hh * 16 + d) * 8 + jh * 4) = o;
    }
  }
  TS(0, 3); TSR(0, 9);
}

static constexpr int LDS_K = 0;
static constexpr int LDS_V = 65536;
static constexpr int LDS_H = 65536 + VF_BYTES + 2048;
static constexpr int LDS_TOTAL = LDS_H + 1024;

__global__ __launch_bounds__(768) void kb_attn(const u16* __restrict__ xnf, const u16* __restrict__ wqf,
                                               const u16* __restrict__ kf, const u16* __restrict__ vf,
                                               const unsigned* __restrict__ rkeys, const u16* __restrict__ wg,
                                               const u16* __restrict__ wu, const u16* __restrict__ wd,
                                               u16* __restrict__ of, float* __restrict__ ypart,
                                               int* __restrict__ sel) {
  __shared__ __attribute__((aligned(16))) char smem[LDS_TOTAL];
  const int tid = threadIdx.x;
  const int lane = tid & 63, wave = tid >> 6;
  const int b = blockIdx.x;
  TSR(1, 8); TS(1, 0);

  if (wave < 8) {
    const int rb = (b & 7) + 8 * ((b >> 3) & 3), hg = b >> 5;
    const int rt = wave >> 2, ks4 = wave & 3;
    const int r = lane & 31, hh = lane >> 5;
    {
      u32x4 tk[8];
#pragma unroll
      for (int c = 0; c < 8; ++c) tk[c] = *(const u32x4*)((const char*)kf + c * 8192 + tid * 16);
      bf16x8 af[2][8], bfr[8];
      const int rtq = wave & 1, kq = wave >> 1;
      const u16* ap = wqf + ((size_t)(2 * hg) * 32 + 8 * kq) * 512 + lane * 8;
      const u16* bp = xnf + ((size_t)(2 * rb + rtq) * 32 + 8 * kq) * 512 + lane * 8;
#pragma unroll
      for (int s = 0; s < 8; ++s) {
        bfr[s] = *(const bf16x8*)(bp + s * 512);
        af[0][s] = *(const bf16x8*)(ap + s * 512);
        af[1][s] = *(const bf16x8*)(ap + 32 * 512 + s * 512);
      }
      __builtin_amdgcn_sched_barrier(0);
#pragma unroll
      for (int c = 0; c < 8; ++c) *(u32x4*)(smem + LDS_K + c * 8192 + tid * 16) = tk[c];
      __builtin_amdgcn_sched_barrier(0);
      f32x16 acc[2];
#pragma unroll
      for (int e = 0; e < 16; ++e) { acc[0][e] = 0.f; acc[1][e] = 0.f; }
#pragma unroll
      for (int s = 0; s < 8; ++s) {
        acc[0] = mfma32(af[0][s], bfr[s], acc[0]);
        acc[1] = mfma32(af[1][s], bfr[s], acc[1]);
      }
#pragma unroll
      for (int dt = 0; dt < 2; ++dt)
#pragma unroll
        for (int q4 = 0; q4 < 4; ++q4) {
          f32x4 p = {acc[dt][4 * q4], acc[dt][4 * q4 + 1], acc[dt][4 * q4 + 2], acc[dt][4 * q4 + 3]};
          *(f32x4*)(smem + LDS_V + ((((rtq * 4 + kq) * 2 + dt) * 4 + q4) * 64 + lane) * 16) = p;
        }
    }
    __builtin_amdgcn_sched_barrier(0);
    u32x4 tv[8], tvx;
#pragma unroll
    for (int c = 0; c < 8; ++c) tv[c] = *(const u32x4*)((const char*)vf + c * 8192 + tid * 16);
    tvx = *(const u32x4*)((const char*)vf + 65536 + (tid & 127) * 16);
    TS(1, 1);
    __syncthreads();
    TS(1, 2);
    const int hl = wave & 3;
    const int head = hg * 4 + hl;
    bf16x8 qf;
    {
      f32x4 a0 = {0.f, 0.f, 0.f, 0.f}, a1 = {0.f, 0.f, 0.f, 0.f};
#pragma unroll
      for (int k = 0; k < 4; ++k) {
        const char* p = smem + LDS_V + ((((rt * 4 + k) * 2 + (hl >> 1)) * 4 + 2 * (hl & 1)) * 64 + lane) * 16;
        a0 += *(const f32x4*)p;
        a1 += *(const f32x4*)(p + 1024);
      }
      const float SC = 0.25f * 1.44269504088896340736f;
      u32x4 pk = {pack2(rbf(a0[0]) * SC, rbf(a0[1]) * SC), pack2(rbf(a0[2]) * SC, rbf(a0[3]) * SC),
                  pack2(rbf(a1[0]) * SC, rbf(a1[1]) * SC), pack2(rbf(a1[2]) * SC, rbf(a1[3]) * SC)};
      qf = __builtin_bit_cast(bf16x8, pk);
    }
    __syncthreads();

    const unsigned lbase = (unsigned)(uintptr_t)(__attribute__((address_space(3))) char*)smem;
    float m0 = -__builtin_inff(), m1 = -__builtin_inff();
    f32x16 zero16;
#pragma unroll
    for (int e = 0; e < 16; ++e) zero16[e] = 0.f;
    {
      unsigned kq_ = lbase + LDS_K + lane * 16;
      asm volatile("" : "+v"(kq_));
      f32x16 sa = mfma32(LDSF(kq_), qf, zero16);
      f32x16 sb = mfma32(LDSF(kq_ + 1024), qf, zero16);
      bf16x8 k2 = LDSF(kq_ + 2048), k3 = LDSF(kq_ + 3072);
      __builtin_amdgcn_sched_barrier(0);
#pragma unroll
      for (int t = 0; t < 64; ++t) {
        f32x16 sc = sb;
        bf16x8 k4 = k3;
        if (t + 2 < 64) sc = mfma32(k2, qf, zero16);
        if (t + 4 < 64) k4 = LDSF(kq_ + (t + 4) * 1024);
        if (t & 1) {
#pragma unroll
          for (int e = 0; e < 16; e += 2) m1 = fmaxf(fmaxf(m1, sa[e]), sa[e + 1]);
        } else {
#pragma unroll
          for (int e = 0; e < 16; e += 2) m0 = fmaxf(fmaxf(m0, sa[e]), sa[e + 1]);
        }
        sa = sb; sb = sc; k2 = k3; k3 = k4;
        if (t >= 32 && t < 40) *(u32x4*)(smem + LDS_V + (t - 32) * 8192 + tid * 16) = tv[t - 32];
        if (t == 40) *(u32x4*)(smem + LDS_V + 65536 + (tid & 127) * 16) = tvx;
        __builtin_amdgcn_sched_group_barrier(0x008, 1, 0);
        __builtin_amdgcn_sched_group_barrier(0x100, 1, 0);
        __builtin_amdgcn_sched_group_barrier(0x002, 8, 0);
        __builtin_amdgcn_sched_barrier(0);
      }
    }
    float m = fmaxf(m0, m1);
    m = fmaxf(m, __shfl_xor(m, 32));
    TS(1, 3);
    __syncthreads();
    TS(1, 4);

    f32x16 negm;
#pragma unroll
    for (int e = 0; e < 16; ++e) negm[e] = -m;
    f32x16 o;
#pragma unroll
    for (int e = 0; e < 16; ++e) o[e] = 0.f;
    unsigned ka_ = lbase + LDS_K + lane * 16;
    unsigned va_ = lbase + LDS_V + ((r < 16) ? (hh * 16 + r) * 16 : (r == 16 ? 1024 : 1040));
    unsigned vb_ = lbase + LDS_V + ((r < 16) ? (hh * 16 + r) * 16 + 512 : (r == 16 ? 1024 : 1040));
    f32x16 s = mfma32(LDSF(ka_), qf, negm);
    bf16x8 kn = LDSF(ka_ + 1024);
    bf16x8 v0 = LDSF(va_);
    bf16x8 v1 = LDSF(vb_);
#pragma unroll 1
    for (int tb = 0; tb < 8; ++tb) {
      asm volatile("" : "+v"(ka_), "+v"(va_), "+v"(vb_));
#pragma unroll
      for (int u = 0; u < 8; ++u) {
        const f32x16 sn = mfma32(kn, qf, negm);
        const bf16x8 k2 = LDSF(ka_ + (u + 2) * 1024);
        const bf16x8 v0n = LDSF(va_ + (u + 1) * VT_STRIDE);
        const bf16x8 v1n = LDSF(vb_ + (u + 1) * VT_STRIDE);
        float p[16];
#pragma unroll
        for (int e = 0; e < 8; ++e) p[e] = __builtin_amdgcn_exp2f(s[e]);
        u32x4 pa = {pack2(p[0], p[1]), pack2(p[2], p[3]), pack2(p[4], p[5]), pack2(p[6], p[7])};
        o = mfma32(v0, __builtin_bit_cast(bf16x8, pa), o);
#pragma unroll
        for (int e = 8; e < 16; ++e) p[e] = __builtin_amdgcn_exp2f(s[e]);
        u32x4 pb = {pack2(p[8], p[9]), pack2(p[10], p[11]), pack2(p[12], p[13]), pack2(p[14], p[15])};
        o = mfma32(v1, __builtin_bit_cast(bf16x8, pb), o);
        s = sn; kn = k2; v0 = v0n; v1 = v1n;
        __builtin_amdgcn_sched_group_barrier(0x008, 1, 0);
        __builtin_amdgcn_sched_group_barrier(0x100, 3, 0);
        __builtin_amdgcn_sched_group_barrier(0x402, 12, 0);
        __builtin_amdgcn_sched_group_barrier(0x008, 1, 0);
        __builtin_amdgcn_sched_group_barrier(0x402, 12, 0);
        __builtin_amdgcn_sched_group_barrier(0x008, 1, 0);
        __builtin_amdgcn_sched_barrier(0);
      }
      ka_ += 8 * 1024; va_ += 8 * VT_STRIDE; vb_ += 8 * VT_STRIDE;
    }
    TS(1, 5);
    const float il = 1.0f / __shfl(o[8], r);
    u32x4 w0 = {pack2(o[0] * il, o[1] * il), pack2(o[2] * il, o[3] * il), pack2(o[4] * il, o[5] * il),
                pack2(o[6] * il, o[7] * il)};
    *(u32x4*)(of + ((size_t)((2 * rb + rt) * 32 + head) * 64 + lane) * 8) = w0;
  } else {
    if (b >= NHELP) return;
    __builtin_amdgcn_s_setprio(3);
    const int hj = wave - 8;
    const int g = lane >> 4, i = lane & 15;
    const int f0 = b * 16;
    const u32x4 kv = *(const u32x4*)(rkeys + lane * 4);
    unsigned k0 = max(kv[0], kv[2]), k1 = max(min(kv[0], kv[2]), max(kv[1], kv[3]));
#pragma unroll
    for (int off = 1; off < 64; off <<= 1) {
      const unsigned o0 = __shfl_xor(k0, off), o1 = __shfl_xor(k1, off);
      k1 = max(min(k0, o0), max(k1, o1));
      k0 = max(k0, o0);
    }
    const int i0 = 2047 - (int)(k0 & 0x7ffu), i1 = 2047 - (int)(k1 & 0x7ffu);
    if (b == 0 && hj == 0 && lane == 0) {
      const float v0 = __uint_as_float((k0 & 0x80000000u) ? (k0 & 0x7fff0000u) : (~k0 & 0xffff0000u));
      const float v1 = __uint_as_float((k1 & 0x80000000u) ? (k1 & 0x7fff0000u) : (~k1 & 0xffff0000u));
      sel[0] = i0; sel[1] = i1;
      ((float*)sel)[2] = rbf(1.0f / (1.0f + __expf(-v0)));
      ((float*)sel)[3] = rbf(1.0f / (1.0f + __expf(-v1)));
    }
    const int srow = (i == 0) ? i0 : i1;
    const u16* xp = xnf + ((size_t)((srow >> 5) * 32 + 8 * hj + (g >> 1)) * 64 + (g & 1) * 32 + (srow & 31)) * 8;
    bf16x8 xa[4];
#pragma unroll
    for (int s = 0; s < 4; ++s) xa[s] = *(const bf16x8*)(xp + (size_t)s * 1024);
    TS(1, 3);
    __syncthreads();
    __syncthreads();
    __builtin_amdgcn_sched_barrier(0);
    unsigned wa[4][8];
    const u16* wp = ((i < 8) ? wg : wu) + f0 + 2 * (i & 7) + (size_t)(128 * hj + 8 * g) * F_;
#pragma unroll
    for (int s = 0; s < 4; ++s)
#pragma unroll
      for (int j = 0; j < 8; ++j) wa[s][j] = *(const unsigned*)(wp + (size_t)(32 * s + j) * F_);
    unsigned wdv[16];
#pragma unroll
    for (int f = 0; f < 16; ++f) wdv[f] = *(const unsigned*)(wd + (size_t)(f0 + f) * D_ + hj * 128 + 2 * lane);
    f32x4 a0 = {0.f, 0.f, 0.f, 0.f}, a1 = {0.f, 0.f, 0.f, 0.f};
#pragma unroll
    for (int s = 0; s < 4; ++s) {
      a0 = mfma16(xa[s], frag_lo(wa[s]), a0);
      a1 = mfma16(xa[s], frag_hi(wa[s]), a1);
    }
    if (lane < 16) *(f32x4*)(smem + LDS_H + (hj * 16 + i) * 16) = (f32x4){a0[0], a0[1], a1[0], a1[1]};
    TS(1, 4);
    __syncthreads();
    __builtin_amdgcn_sched_barrier(0);
    const char* pp = smem + LDS_H + i * 16;
    const f32x4 t = (*(const f32x4*)pp + *(const f32x4*)(pp + 256)) + (*(const f32x4*)(pp + 512) + *(const f32x4*)(pp + 768));
    float y[2][2] = {{0.f, 0.f}, {0.f, 0.f}};
    int hb[4];
#pragma unroll
    for (int e = 0; e < 4; ++e) {
      const float gt = rbf(t[e]);
      const float u = __shfl_xor(gt, 8);
      hb[e] = __builtin_bit_cast(int, rbf(rbf(gt / (1.0f + __expf(-gt))) * u));
    }
#pragma unroll
    for (int f = 0; f < 16; ++f) {
      const float w0 = bf2f(wdv[f] & 0xffffu), w1 = bfhi2f(wdv[f]);
#pragma unroll
      for (int tk = 0; tk < 2; ++tk) {
        const float h = __builtin_bit_cast(float, __builtin_amdgcn_readlane(hb[(f & 1) * 2 + tk], f >> 1));
        y[tk][0] = __builtin_fmaf(h, w0, y[tk][0]);
        y[tk][1] = __builtin_fmaf(h, w1, y[tk][1]);
      }
    }
#pragma unroll
    for (int tk = 0; tk < 2; ++tk)
      *(float2*)(ypart + (size_t)b * 1024 + tk * 512 + hj * 128 + 2 * lane) = make_float2(y[tk][0], y[tk][1]);
  }
  TS(1, 6); TSR(1, 9);
}

static constexpr int CH_STRIDE = 1056;
static constexpr int LDS_Y = 64 * CH_STRIDE;
__global__ __launch_bounds__(512) void kc_out(const u16* __restrict__ x, const u16* __restrict__ of,
                                              const u16* __restrict__ wof, const float2* __restrict__ stats,
                                              const float* __restrict__ ypart, const int* __restrict__ sel,
                                              u16* __restrict__ out) {
  __shared__ __attribute__((aligned(16))) char smem[LDS_Y + 2 * 8 * 256];
  const int tid = threadIdx.x;
  const int lane = tid & 63, wave = tid >> 6;
  TSR(2, 8); TS(2, 0);
  const int bb = blockIdx.x;
  const int rb = (bb & 7) + 8 * ((bb >> 3) & 3), cg = bb >> 5;
  const int rt = wave & 1, ks4 = wave >> 1;
  const int idx0 = sel[0], idx1 = sel[1];
  bf16x8 af[2][8], bfr[8];
  const u16* ap = wof + ((size_t)(2 * cg) * 32 + 8 * ks4) * 512 + lane * 8;
  const u16* bp = of + ((size_t)(2 * rb + rt) * 32 + 8 * ks4) * 512 + lane * 8;
#pragma unroll
  for (int s = 0; s < 8; ++s) {
    bfr[s] = *(const bf16x8*)(bp + s * 512);
    af[0][s] = *(const bf16x8*)(ap + s * 512);
    af[1][s] = *(const bf16x8*)(ap + 32 * 512 + s * 512);
  }
  const int r64 = tid >> 3, oct = tid & 7;
  const int row = rb * 64 + r64;
  const size_t off = (size_t)row * D_ + cg * 64 + oct * 8;
  const u32x4 xv = *(const u32x4*)(x + off);
  const float2 st = stats[row];
#pragma unroll
  for (int tk = 0; tk < 2; ++tk) {
    const int idx = tk ? idx1 : idx0;
    if ((unsigned)(idx - rb * 64) < 64u) {
      const int pg = tid >> 4;
      const float* yp = ypart + (size_t)(pg * 2 + tk) * 512 + cg * 64 + (tid & 15) * 4;
      f32x4 y = *(const f32x4*)yp + *(const f32x4*)(yp + 32 * 1024);
      if (pg + 64 < NHELP) y += *(const f32x4*)(yp + 64 * 1024);
#pragma unroll
      for (int e = 0; e < 4; ++e) {
        y[e] += __shfl_xor(y[e], 16);
        y[e] += __shfl_xor(y[e], 32);
      }
      if (lane < 16) *(f32x4*)(smem + LDS_Y + (tk * 8 + wave) * 256 + lane * 16) = y;
    }
  }
  f32x16 acc[2];
#pragma unroll
  for (int e = 0; e < 16; ++e) { acc[0][e] = 0.f; acc[1][e] = 0.f; }
#pragma unroll
  for (int s = 0; s < 8; ++s) {
    acc[0] = mfma32(af[0][s], bfr[s], acc[0]);
    acc[1] = mfma32(af[1][s], bfr[s], acc[1]);
  }
  TS(2, 1);
#pragma unroll
  for (int ct = 0; ct < 2; ++ct)
#pragma unroll
    for (int q4 = 0; q4 < 4; ++q4) {
      f32x4 p = {acc[ct][4 * q4], acc[ct][4 * q4 + 1], acc[ct][4 * q4 + 2], acc[ct][4 * q4 + 3]};
      *(f32x4*)(smem + (((rt * 4 + ks4) * 2 + ct) * 4 + q4) * CH_STRIDE + lane * 16) = p;
    }
  __syncthreads();
  TS(2, 2);
  f32x4 lo = {0.f, 0.f, 0.f, 0.f}, hi = {0.f, 0.f, 0.f, 0.f};
#pragma unroll
  for (int k = 0; k < 4; ++k) {
    const char* p = smem + (((r64 >> 5) * 4 + k) * 8 + oct) * CH_STRIDE + (r64 & 31) * 16;
    lo += *(const f32x4*)p;
    hi += *(const f32x4*)(p + 512);
  }
  float upd[8];
#pragma unroll
  for (int e = 0; e < 8; ++e) upd[e] = 0.f;
  if (row == idx0 || row == idx1) {
    const int tk = (row == idx0) ? 0 : 1;
    f32x4 ya = {0.f, 0.f, 0.f, 0.f}, yb = {0.f, 0.f, 0.f, 0.f};
#pragma unroll
    for (int p = 0; p < 8; ++p) {
      const char* yp = smem + LDS_Y + (tk * 8 + p) * 256 + oct * 32;
      ya += *(const f32x4*)yp;
      yb += *(const f32x4*)(yp + 16);
    }
    const float rs = ((const float*)sel)[2 + tk];
#pragma unroll
    for (int e = 0; e < 4; ++e) { upd[e] = rbf(rbf(ya[e]) * rs); upd[4 + e] = rbf(rbf(yb[e]) * rs); }
  }
  const float inv = 1.0f / st.y;
  float res[8];
#pragma unroll
  for (int e = 0; e < 4; ++e) {
    const float x0 = bf2f(xv[e] & 0xffffu), x1 = bfhi2f(xv[e]);
    const float a0 = rbf(e < 2 ? lo[2 * e] : hi[2 * e - 4]);
    const float a1 = rbf(e < 2 ? lo[2 * e + 1] : hi[2 * e - 3]);
    res[2 * e] = rbf(x0 + a0) + rbf(ln_elem(x0, st.x, st.y, inv) + upd[2 * e]);
    res[2 * e + 1] = rbf(x1 + a1) + rbf(ln_elem(x1, st.x, st.y, inv) + upd[2 * e + 1]);
  }
  u32x4 o = {pack2(res[0], res[1]), pack2(res[2], res[3]), pack2(res[4], res[5]), pack2(res[6], res[7])};
  *(u32x4*)(out + off) = o;
  TS(2, 3); TSR(2, 9);
}

extern "C" void kernel_launch(void* const* d_in, const int* in_sizes, int n_in, void* d_out, int out_size,
                              void* d_ws, size_t ws_size, hipStream_t stream) {
  const u16* x = (const u16*)d_in[0];
  const u16* wq = (const u16*)d_in[1];
  const u16* wk = (const u16*)d_in[2];
  const u16* wv = (const u16*)d_in[3];
  const u16* wo = (const u16*)d_in[4];
  const u16* wr = (const u16*)d_in[6];
  const u16* wg = (const u16*)d_in[7];
  const u16* wu = (const u16*)d_in[8];
  const u16* wd = (const u16*)d_in[9];
  char* ws = (char*)d_ws;
  u16* xnf = (u16*)(ws + OFF_XNF);
  u16* of = (u16*)(ws + OFF_OF);
  u16* wqf = (u16*)(ws + OFF_WQF);
  u16* wof = (u16*)(ws + OFF_WOF);
  u16* kf = (u16*)(ws + OFF_KF);
  u16* vf = (u16*)(ws + OFF_VF);
  unsigned* rkeys = (unsigned*)(ws + OFF_R);
  float2* stats = (float2*)(ws + OFF_STATS);
  float* ypart = (float*)(ws + OFF_Y);
  int* sel = (int*)(ws + OFF_SEL);
  u16* out = (u16*)d_out;

  ka_prep<<<192, 512, 0, stream>>>(x, wq, wk, wv, wo, wr, xnf, wqf, wof, kf, vf, rkeys, stats);
  kb_attn<<<256, 768, 0, stream>>>(xnf, wqf, kf, vf, rkeys, wg, wu, wd, of, ypart, sel);
  kc_out<<<256, 512, 0, stream>>>(x, of, wof, stats, ypart, sel, out);
}
```

```cpp
#include <hip/hip_runtime.h>
#include <hip/hip_bf16.h>

typedef unsigned short u16;
typedef __attribute__((ext_vector_type(8))) __bf16 bf16x8;
typedef __attribute__((ext_vector_type(2))) __bf16 bf16x2;
typedef __attribute__((ext_vector_type(2))) float f32x2;
typedef __attribute__((ext_vector_type(4))) float f32x4;
typedef __attribute__((ext_vector_type(16))) float f32x16;
typedef __attribute__((ext_vector_type(4))) unsigned u32x4;
typedef __attribute__((ext_vector_type(2))) unsigned u32x2;

#define DI __device__ __forceinline__
#define TS(k, n)
#define TSR(k, n)

static constexpr int S_ = 2048;
static constexpr int D_ = 512;
static constexpr int F_ = 1376;

#define LDSF(a) (*(const __attribute__((address_space(3))) bf16x8*)(uintptr_t)(a))
DI float bf2f(unsigned u) { return __uint_as_float(u << 16); }
DI float bfhi2f(unsigned u) { return __uint_as_float(u & 0xffff0000u); }
DI float rbf(float f) { return (float)(__bf16)f; }
DI unsigned pack2(float lo, float hi) {
  f32x2 p = {lo, hi};
  bf16x2 q = __builtin_convertvector(p, bf16x2);
  return __builtin_bit_cast(unsigned, q);
}
DI f32x4 mfma16(bf16x8 a, bf16x8 b, f32x4 c) { return __builtin_amdgcn_mfma_f32_16x16x32_bf16(a, b, c, 0, 0, 0); }
DI f32x16 mfma32(bf16x8 a, bf16x8 b, f32x16 c) { return __builtin_amdgcn_mfma_f32_32x32x16_bf16(a, b, c, 0, 0, 0); }
DI unsigned perm_lo(unsigned hi_src, unsigned lo_src) { return __builtin_amdgcn_perm(hi_src, lo_src, 0x05040100u); }
DI unsigned perm_hi(unsigned hi_src, unsigned lo_src) { return __builtin_amdgcn_perm(hi_src, lo_src, 0x07060302u); }

DI bf16x8 frag_lo(const unsigned* w) {
  u32x4 r = {perm_lo(w[1], w[0]), perm_lo(w[3], w[2]), perm_lo(w[5], w[4]), perm_lo(w[7], w[6])};
  return __builtin_bit_cast(bf16x8, r);
}
DI bf16x8 frag_hi(const unsigned* w) {
  u32x4 r = {perm_hi(w[1], w[0]), perm_hi(w[3], w[2]), perm_hi(w[5], w[4]), perm_hi(w[7], w[6])};
  return __builtin_bit_cast(bf16x8, r);
}
DI float ln_elem(float xv, float mb, float den, float inv) {
  const float d = rbf(xv - mb);
  float q = d * inv;
  const float rem = __builtin_fmaf(-q, den, d);
  q = __builtin_fmaf(rem, inv, q);
  return rbf(q);
}

static constexpr int VT_STRIDE = 1056;
static constexpr int VF_BYTES = 64 * VT_STRIDE;
static constexpr size_t OFF_XNF = 0;
static constexpr size_t OFF_OF = OFF_XNF + (size_t)S_ * D_ * 2;
static constexpr size_t OFF_WQF = OFF_OF + (size_t)S_ * D_ * 2;
static constexpr size_t OFF_WOF = OFF_WQF + (size_t)D_ * D_ * 2;
static constexpr size_t OFF_KF = OFF_WOF + (size_t)D_ * D_ * 2;
static constexpr size_t OFF_VF = OFF_KF + 65536;
static constexpr size_t OFF_R = OFF_VF + VF_BYTES;
static constexpr size_t OFF_STATS = OFF_R + S_ * 4;
static constexpr int NHELP = 86;
static constexpr size_t OFF_Y = OFF_STATS + S_ * 8;
static constexpr size_t OFF_SEL = OFF_Y + NHELP * 1024 * 4;

static constexpr int XN_STRIDE = 1040;
__global__ __launch_bounds__(512) void ka_prep(const u16* __restrict__ x, const u16* __restrict__ wq,
                                               const u16* __restrict__ wk, const u16* __restrict__ wv,
                                               const u16* __restrict__ wo, const u16* __restrict__ wr,
                                               u16* __restrict__ xnf, u16* __restrict__ wqf, u16* __restrict__ wof,
                                               u16* __restrict__ kf, u16* __restrict__ vf,
                                               unsigned* __restrict__ rkeys, float2* __restrict__ stats) {
  __shared__ __attribute__((aligned(16))) char smem[16 * XN_STRIDE + 8 * 5 * 64 * 16];
  const int tid = threadIdx.x;
  const int lane = tid & 63, wave = tid >> 6;
  TSR(0, 8); TS(0, 0);
  if (blockIdx.x >= 128) {
    const int pb = blockIdx.x - 128;
    const bool iso = pb >= 32;
    const int cp = tid & 255, ko = (pb & 31) * 2 + (tid >> 8);
    const int ks = ko >> 1, hh = ko & 1;
    const u16* W = (iso ? wo : wq) + 2 * cp;
    unsigned w[8];
#pragma unroll
    for (int j = 0; j < 8; ++j) {
      const int row = 16 * ks + (iso ? (8 * (j >> 2) + 4 * hh + (j & 3)) : (8 * hh + j));
      w[j] = *(const unsigned*)(W + row * D_);
    }
    u16* dst = (iso ? wof : wqf) + (((cp >> 4) * 32 + ks) * 64 + hh * 32 + ((2 * cp) & 31)) * 8;
    *(bf16x8*)dst = frag_lo(w);
    *(bf16x8*)(dst + 8) = frag_hi(w);
    TS(0, 3); TSR(0, 9);
    return;
  }
  const int row0 = blockIdx.x * 16;
  const int rt = blockIdx.x >> 1, th = blockIdx.x & 1;
  const int g = lane >> 4, i = lane & 15;
  const int k0 = wave * 64 + 8 * g;
  u32x4 xr[2];
#pragma unroll
  for (int rr = 0; rr < 2; ++rr) xr[rr] = *(const u32x4*)(x + (size_t)(row0 + 2 * wave + rr) * D_ + 8 * lane);
  const u16* wbase = ((i < 4) ? (wk + 4 * i) : (i < 8 ? wv + 4 * (i - 4) : wk)) + k0 * 16;
  u32x2 w[2][8];
#pragma unroll
  for (int s = 0; s < 2; ++s)
#pragma unroll
    for (int j = 0; j < 8; ++j) w[s][j] = *(const u32x2*)(wbase + (32 * s + j) * 16);
  bf16x8 rf[2];
#pragma unroll
  for (int s = 0; s < 2; ++s) rf[s] = *(const bf16x8*)(wr + k0 + 32 * s);

  float s1[2], s2[2];
#pragma unroll
  for (int rr = 0; rr < 2; ++rr) {
    float a = 0.f, q = 0.f;
#pragma unroll
    for (int e = 0; e < 4; ++e) {
      const float lo = bf2f(xr[rr][e] & 0xffffu), hi = bfhi2f(xr[rr][e]);
      a += lo + hi;
      q = __builtin_fmaf(lo, lo, q);
      q = __builtin_fmaf(hi, hi, q);
    }
    s1[rr] = a; s2[rr] = q;
  }
#pragma unroll
  for (int off = 32; off >= 1; off >>= 1)
#pragma unroll
    for (int rr = 0; rr < 2; ++rr) {
      s1[rr] += __shfl_xor(s1[rr], off);
      s2[rr] += __shfl_xor(s2[rr], off);
    }
#pragma unroll
  for (int rr = 0; rr < 2; ++rr) {
    const float mean = s1[rr] * (1.0f / D_);
    float var = s2[rr] * (1.0f / D_) - mean * mean;
    var = fmaxf(var, 0.f);
    const float mb = rbf(mean);
    const float den = rbf(__builtin_sqrtf(rbf(rbf(var) + 1e-5f)));
    const float inv = 1.0f / den;
    if (lane == rr) stats[row0 + 2 * wave + rr] = make_float2(mb, den);
    u32x4 o;
#pragma unroll
    for (int e = 0; e < 4; ++e)
      o[e] = pack2(ln_elem(bf2f(xr[rr][e] & 0xffffu), mb, den, inv), ln_elem(bfhi2f(xr[rr][e]), mb, den, inv));
    *(u32x4*)(smem + (2 * wave + rr) * XN_STRIDE + lane * 16) = o;
  }
  if (tid < 2 && th == 0) {
    const unsigned cv = tid ? 0u : 0x3f803f80u;
    u32x4 c4 = {cv, cv, cv, cv};
    *(u32x4*)((char*)vf + rt * VT_STRIDE + 1024 + tid * 16) = c4;
  }
  TS(0, 1);
  __syncthreads();
  {
    const int r = lane & 15, hh = (lane >> 4) & 1, kp = lane >> 5;
#pragma unroll
    for (int s = 0; s < 2; ++s) {
      const int ks = 4 * wave + 2 * s + kp;
      const u32x4 v = *(const u32x4*)(smem + r * XN_STRIDE + ks * 32 + hh * 16);
      *(u32x4*)(xnf + ((size_t)(rt * 32 + ks) * 64 + hh * 32 + th * 16 + r) * 8) = v;
    }
  }
  f32x4* red = (f32x4*)(smem + 16 * XN_STRIDE);
  {
    f32x4 acc[4], ar;
    ar = (f32x4){0.f, 0.f, 0.f, 0.f};
#pragma unroll
    for (int c = 0; c < 4; ++c) acc[c] = (f32x4){0.f, 0.f, 0.f, 0.f};
#pragma unroll
    for (int s = 0; s < 2; ++s) {
      unsigned wx[8], wy[8];
#pragma unroll
      for (int j = 0; j < 8; ++j) { wx[j] = w[s][j][0]; wy[j] = w[s][j][1]; }
      bf16x8 b[4] = {frag_lo(wx), frag_hi(wx), frag_lo(wy), frag_hi(wy)};
      const bf16x8 xa = *(const bf16x8*)(smem + i * XN_STRIDE + (k0 + 32 * s) * 2);
#pragma unroll
      for (int c = 0; c < 4; ++c) acc[c] = mfma16(xa, b[c], acc[c]);
      ar = mfma16(xa, rf[s], ar);
    }
#pragma unroll
    for (int c = 0; c < 4; ++c) red[(wave * 5 + c) * 64 + lane] = acc[c];
    red[(wave * 5 + 4) * 64 + lane] = ar;
  }
  __syncthreads();
  TS(0, 2);
  if (wave < 5) {
    f32x4 v = red[wave * 64 + lane];
#pragma unroll
    for (int wv_ = 1; wv_ < 8; ++wv_) v += red[(wv_ * 5 + wave) * 64 + lane];
    const int kk = 16 * th + 4 * g;
    if (wave == 4) {
      unsigned k0 = 0, k1 = 0;
#pragma unroll
      for (int e = 0; e < 4; ++e) {
        const unsigned u = __float_as_uint(rbf(v[e]));
        const unsigned ord = (u & 0x80000000u) ? ~u : (u | 0x80000000u);
        const unsigned key = (ord & 0xffff0000u) | (unsigned)(2047 - (row0 + 4 * g + e));
        k1 = max(k1, min(k0, key));
        k0 = max(k0, key);
      }
#pragma unroll
      for (int off = 16; off < 64; off <<= 1) {
        const unsigned o0 = __shfl_xor(k0, off), o1 = __shfl_xor(k1, off);
        k1 = max(min(k0, o0), max(k1, o1));
        k0 = max(k0, o0);
      }
      if (lane == 0) *(u32x2*)(rkeys + 2 * blockIdx.x) = (u32x2){k0, k1};
    } else if (i < 4) {
      const int hh = i & 1, j = (i >> 1) * 4 + wave;
#pragma unroll
      for (int e = 0; e < 4; ++e)
        kf[rt * 512 + (hh * 32 + kk + e) * 8 + j] = __builtin_bit_cast(u16, (__bf16)v[e]);
    } else if (i < 8) {
      const int d = 4 * (i - 4) + wave;
      const int hh = g & 1, jh = g >> 1;
      u32x2 o = {pack2(v[0], v[1]), pack2(v[2], v[3])};
      *(u32x2*)(vf + rt * (VT_STRIDE / 2) + th * 256 + (hh * 16 + d) * 8 + jh * 4) = o;
    }
  }
  TS(0, 3); TSR(0, 9);
}

static constexpr int LDS_K = 0;
static constexpr int LDS_V = 65536;
static constexpr int LDS_H = 65536 + VF_BYTES + 2048;
static constexpr int LDS_TOTAL = LDS_H + 1024;

__global__ __launch_bounds__(768) void kb_attn(const u16* __restrict__ xnf, const u16* __restrict__ wqf,
                                               const u16* __restrict__ kf, const u16* __restrict__ vf,
                                               const unsigned* __restrict__ rkeys, const u16* __restrict__ wg,
                                               const u16* __restrict__ wu, const u16* __restrict__ wd,
                                               u16* __restrict__ of, float* __restrict__ ypart,
                                               int* __restrict__ sel) {
  __shared__ __attribute__((aligned(16))) char smem[LDS_TOTAL];
  const int tid = threadIdx.x;
  const int lane = tid & 63, wave = tid >> 6;
  const int b = blockIdx.x;
  TSR(1, 8); TS(1, 0);

  if (wave < 8) {
    const int rb = (b & 7) + 8 * ((b >> 3) & 3), hg = b >> 5;
    const int rt = wave >> 2, ks4 = wave & 3;
    const int r = lane & 31, hh = lane >> 5;
    {
      u32x4 tk[8];
#pragma unroll
      for (int c = 0; c < 8; ++c) tk[c] = *(const u32x4*)((const char*)kf + c * 8192 + tid * 16);
      bf16x8 af[2][8], bfr[8];
      const int rtq = wave & 1, kq = wave >> 1;
      const u16* ap = wqf + ((size_t)(2 * hg) * 32 + 8 * kq) * 512 + lane * 8;
      const u16* bp = xnf + ((size_t)(2 * rb + rtq) * 32 + 8 * kq) * 512 + lane * 8;
#pragma unroll
      for (int s = 0; s < 8; ++s) {
        bfr[s] = *(const bf16x8*)(bp + s * 512);
        af[0][s] = *(const bf16x8*)(ap + s * 512);
        af[1][s] = *(const bf16x8*)(ap + 32 * 512 + s * 512);
      }
      __builtin_amdgcn_sched_barrier(0);
#pragma unroll
      for (int c = 0; c < 8; ++c) *(u32x4*)(smem + LDS_K + c * 8192 + tid * 16) = tk[c];
      __builtin_amdgcn_sched_barrier(0);
      f32x16 acc[2];
#pragma unroll
      for (int e = 0; e < 16; ++e) { acc[0][e] = 0.f; acc[1][e] = 0.f; }
#pragma unroll
      for (int s = 0; s < 8; ++s) {
        acc[0] = mfma32(af[0][s], bfr[s], acc[0]);
        acc[1] = mfma32(af[1][s], bfr[s], acc[1]);
      }
#pragma unroll
      for (int dt = 0; dt < 2; ++dt)
#pragma unroll
        for (int q4 = 0; q4 < 4; ++q4) {
          f32x4 p = {acc[dt][4 * q4], acc[dt][4 * q4 + 1], acc[dt][4 * q4 + 2], acc[dt][4 * q4 + 3]};
          *(f32x4*)(smem + LDS_V + ((((rtq * 4 + kq) * 2 + dt) * 4 + q4) * 64 + lane) * 16) = p;
        }
    }
    __builtin_amdgcn_sched_barrier(0);
    u32x4 tv[8], tvx;
#pragma unroll
    for (int c = 0; c < 8; ++c) tv[c] = *(const u32x4*)((const char*)vf + c * 8192 + tid * 16);
    tvx = *(const u32x4*)((const char*)vf + 65536 + (tid & 127) * 16);
    TS(1, 1);
    __syncthreads();
    TS(1, 2);
    const int hl = wave & 3;
    const int head = hg * 4 + hl;
    bf16x8 qf;
    {
      f32x4 a0 = {0.f, 0.f, 0.f, 0.f}, a1 = {0.f, 0.f, 0.f, 0.f};
#pragma unroll
      for (int k = 0; k < 4; ++k) {
        const char* p = smem + LDS_V + ((((rt * 4 + k) * 2 + (hl >> 1)) * 4 + 2 * (hl & 1)) * 64 + lane) * 16;
        a0 += *(const f32x4*)p;
        a1 += *(const f32x4*)(p + 1024);
      }
      const float SC = 0.25f * 1.44269504088896340736f;
      u32x4 pk = {pack2(rbf(a0[0]) * SC, rbf(a0[1]) * SC), pack2(rbf(a0[2]) * SC, rbf(a0[3]) * SC),
                  pack2(rbf(a1[0]) * SC, rbf(a1[1]) * SC), pack2(rbf(a1[2]) * SC, rbf(a1[3]) * SC)};
      qf = __builtin_bit_cast(bf16x8, pk);
    }
    __syncthreads();

    const unsigned lbase = (unsigned)(uintptr_t)(__attribute__((address_space(3))) char*)smem;
    float m0 = -__builtin_inff(), m1 = -__builtin_inff();
    f32x16 zero16;
#pragma unroll
    for (int e = 0; e < 16; ++e) zero16[e] = 0.f;
    {
      unsigned kq_ = lbase + LDS_K + lane * 16;
      asm volatile("" : "+v"(kq_));
      f32x16 sa = mfma32(LDSF(kq_), qf, zero16);
      f32x16 sb = mfma32(LDSF(kq_ + 1024), qf, zero16);
      bf16x8 k2 = LDSF(kq_ + 2048), k3 = LDSF(kq_ + 3072);
      __builtin_amdgcn_sched_barrier(0);
#pragma unroll
      for (int t = 0; t < 64; ++t) {
        f32x16 sc = sb;
        bf16x8 k4 = k3;
        if (t + 2 < 64) sc = mfma32(k2, qf, zero16);
        if (t + 4 < 64) k4 = LDSF(kq_ + (t + 4) * 1024);
        if (t & 1) {
#pragma unroll
          for (int e = 0; e < 16; e += 2) m1 = fmaxf(fmaxf(m1, sa[e]), sa[e + 1]);
        } else {
#pragma unroll
          for (int e = 0; e < 16; e += 2) m0 = fmaxf(fmaxf(m0, sa[e]), sa[e + 1]);
        }
        sa = sb; sb = sc; k2 = k3; k3 = k4;
        if (t >= 32 && t < 40) *(u32x4*)(smem + LDS_V + (t - 32) * 8192 + tid * 16) = tv[t - 32];
        if (t == 40) *(u32x4*)(smem + LDS_V + 65536 + (tid & 127) * 16) = tvx;
        __builtin_amdgcn_sched_group_barrier(0x008, 1, 0);
        __builtin_amdgcn_sched_group_barrier(0x100, 1, 0);
        __builtin_amdgcn_sched_group_barrier(0x002, 8, 0);
        __builtin_amdgcn_sched_barrier(0);
      }
    }
    float m = fmaxf(m0, m1);
    m = fmaxf(m, __shfl_xor(m, 32));
    TS(1, 3);
    __syncthreads();
    TS(1, 4);

    f32x16 negm;
#pragma unroll
    for (int e = 0; e < 16; ++e) negm[e] = -m;
    f32x16 o;
#pragma unroll
    for (int e = 0; e < 16; ++e) o[e] = 0.f;
    unsigned ka_ = lbase + LDS_K + lane * 16;
    unsigned va_ = lbase + LDS_V + ((r < 16) ? (hh * 16 + r) * 16 : (r == 16 ? 1024 : 1040));
    unsigned vb_ = lbase + LDS_V + ((r < 16) ? (hh * 16 + r) * 16 + 512 : (r == 16 ? 1024 : 1040));
    f32x16 s = mfma32(LDSF(ka_), qf, negm);
    bf16x8 kn = LDSF(ka_ + 1024);
    bf16x8 v0 = LDSF(va_);
    bf16x8 v1 = LDSF(vb_);
#pragma unroll 1
    for (int tb = 0; tb < 8; ++tb) {
      asm volatile("" : "+v"(ka_), "+v"(va_), "+v"(vb_));
#pragma unroll
      for (int u = 0; u < 8; ++u) {
        const f32x16 sn = mfma32(kn, qf, negm);
        const bf16x8 k2 = LDSF(ka_ + (u + 2) * 1024);
        const bf16x8 v0n = LDSF(va_ + (u + 1) * VT_STRIDE);
        const bf16x8 v1n = LDSF(vb_ + (u + 1) * VT_STRIDE);
        float p[16];
#pragma unroll
        for (int e = 0; e < 8; ++e) p[e] = __builtin_amdgcn_exp2f(s[e]);
        u32x4 pa = {pack2(p[0], p[1]), pack2(p[2], p[3]), pack2(p[4], p[5]), pack2(p[6], p[7])};
        o = mfma32(v0, __builtin_bit_cast(bf16x8, pa), o);
#pragma unroll
        for (int e = 8; e < 16; ++e) p[e] = __builtin_amdgcn_exp2f(s[e]);
        u32x4 pb = {pack2(p[8], p[9]), pack2(p[10], p[11]), pack2(p[12], p[13]), pack2(p[14], p[15])};
        o = mfma32(v1, __builtin_bit_cast(bf16x8, pb), o);
        s = sn; kn = k2; v0 = v0n; v1 = v1n;
        __builtin_amdgcn_sched_group_barrier(0x008, 1, 0);
        __builtin_amdgcn_sched_group_barrier(0x100, 3, 0);
        __builtin_amdgcn_sched_group_barrier(0x402, 12, 0);
        __builtin_amdgcn_sched_group_barrier(0x008, 1, 0);
        __builtin_amdgcn_sched_group_barrier(0x402, 12, 0);
        __builtin_amdgcn_sched_group_barrier(0x008, 1, 0);
        __builtin_amdgcn_sched_barrier(0);
      }
      ka_ += 8 * 1024; va_ += 8 * VT_STRIDE; vb_ += 8 * VT_STRIDE;
    }
    TS(1, 5);
    const float il = 1.0f / __shfl(o[8], r);
    u32x4 w0 = {pack2(o[0] * il, o[1] * il), pack2(o[2] * il, o[3] * il), pack2(o[4] * il, o[5] * il),
                pack2(o[6] * il, o[7] * il)};
    *(u32x4*)(of + ((size_t)((2 * rb + rt) * 32 + head) * 64 + lane) * 8) = w0;
  } else {
    if (b >= NHELP) return;
    __builtin_amdgcn_s_setprio(3);
    const int hj = wave - 8;
    const int g = lane >> 4, i = lane & 15;
    const int f0 = b * 16;
    const u32x4 kv = *(const u32x4*)(rkeys + lane * 4);
    unsigned k0 = max(kv[0], kv[2]), k1 = max(min(kv[0], kv[2]), max(kv[1], kv[3]));
#pragma unroll
    for (int off = 1; off < 64; off <<= 1) {
      const unsigned o0 = __shfl_xor(k0, off), o1 = __shfl_xor(k1, off);
      k1 = max(min(k0, o0), max(k1, o1));
      k0 = max(k0, o0);
    }
    const int i0 = 2047 - (int)(k0 & 0x7ffu), i1 = 2047 - (int)(k1 & 0x7ffu);
    if (b == 0 && hj == 0 && lane == 0) {
      const float v0 = __uint_as_float((k0 & 0x80000000u) ? (k0 & 0x7fff0000u) : (~k0 & 0xffff0000u));
      const float v1 = __uint_as_float((k1 & 0x80000000u) ? (k1 & 0x7fff0000u) : (~k1 & 0xffff0000u));
      sel[0] = i0; sel[1] = i1;
      ((float*)sel)[2] = rbf(1.0f / (1.0f + __expf(-v0)));
      ((float*)sel)[3] = rbf(1.0f / (1.0f + __expf(-v1)));
    }
    const int srow = (i == 0) ? i0 : i1;
    const u16* xp = xnf + ((size_t)((srow >> 5) * 32 + 8 * hj + (g >> 1)) * 64 + (g & 1) * 32 + (srow & 31)) * 8;
    bf16x8 xa[4];
#pragma unroll
    for (int s = 0; s < 4; ++s) xa[s] = *(const bf16x8*)(xp + (size_t)s * 1024);
    TS(1, 3);
    __syncthreads();
    __syncthreads();
    __builtin_amdgcn_sched_barrier(0);
    unsigned wa[4][8];
    const u16* wp = ((i < 8) ? wg : wu) + f0 + 2 * (i & 7) + (size_t)(128 * hj + 8 * g) * F_;
#pragma unroll
    for (int s = 0; s < 4; ++s)
#pragma unroll
      for (int j = 0; j < 8; ++j) wa[s][j] = *(const unsigned*)(wp + (size_t)(32 * s + j) * F_);
    unsigned wdv[16];
#pragma unroll
    for (int f = 0; f < 16; ++f) wdv[f] = *(const unsigned*)(wd + (size_t)(f0 + f) * D_ + hj * 128 + 2 * lane);
    f32x4 a0 = {0.f, 0.f, 0.f, 0.f}, a1 = {0.f, 0.f, 0.f, 0.f};
#pragma unroll
    for (int s = 0; s < 4; ++s) {
      a0 = mfma16(xa[s], frag_lo(wa[s]), a0);
      a1 = mfma16(xa[s], frag_hi(wa[s]), a1);
    }
    if (lane < 16) *(f32x4*)(smem + LDS_H + (hj * 16 + i) * 16) = (f32x4){a0[0], a0[1], a1[0], a1[1]};
    TS(1, 4);
    __syncthreads();
    __builtin_amdgcn_sched_barrier(0);
    const char* pp = smem + LDS_H + i * 16;
    const f32x4 t = (*(const f32x4*)pp + *(const f32x4*)(pp + 256)) + (*(const f32x4*)(pp + 512) + *(const f32x4*)(pp + 768));
    float y[2][2] = {{0.f, 0.f}, {0.f, 0.f}};
    int hb[4];
#pragma unroll
    for (int e = 0; e < 4; ++e) {
      const float gt = rbf(t[e]);
      const float u = __shfl_xor(gt, 8);
      hb[e] = __builtin_bit_cast(int, rbf(rbf(gt / (1.0f + __expf(-gt))) * u));
    }
#pragma unroll
    for (int f = 0; f < 16; ++f) {
      const float w0 = bf2f(wdv[f] & 0xffffu), w1 = bfhi2f(wdv[f]);
#pragma unroll
      for (int tk = 0; tk < 2; ++tk) {
        const float h = __builtin_bit_cast(float, __builtin_amdgcn_readlane(hb[(f & 1) * 2 + tk], f >> 1));
        y[tk][0] = __builtin_fmaf(h, w0, y[tk][0]);
        y[tk][1] = __builtin_fmaf(h, w1, y[tk][1]);
      }
    }
#pragma unroll
    for (int tk = 0; tk < 2; ++tk)
      *(float2*)(ypart + (size_t)b * 1024 + tk * 512 + hj * 128 + 2 * lane) = make_float2(y[tk][0], y[tk][1]);
  }
  TS(1, 6); TSR(1, 9);
}

static constexpr int CH_STRIDE = 1056;
static constexpr int LDS_Y = 64 * CH_STRIDE;
__global__ __launch_bounds__(512) void kc_out(const u16* __restrict__ x, const u16* __restrict__ of,
                                              const u16* __restrict__ wof, const float2* __restrict__ stats,
                                              const float* __restrict__ ypart, const int* __restrict__ sel,
                                              u16* __restrict__ out) {
  __shared__ __attribute__((aligned(16))) char smem[LDS_Y + 2 * 8 * 256];
  const int tid = threadIdx.x;
  const int lane = tid & 63, wave = tid >> 6;
  TSR(2, 8); TS(2, 0);
  const int bb = blockIdx.x;
  const int rb = (bb & 7) + 8 * ((bb >> 3) & 3), cg = bb >> 5;
  const int rt = wave & 1, ks4 = wave >> 1;
  const int idx0 = sel[0], idx1 = sel[1];
  const float rs0 = ((const float*)sel)[2], rs1 = ((const float*)sel)[3];
  bf16x8 af[2][8], bfr[8];
  const u16* ap = wof + ((size_t)(2 * cg) * 32 + 8 * ks4) * 512 + lane * 8;
  const u16* bp = of + ((size_t)(2 * rb + rt) * 32 + 8 * ks4) * 512 + lane * 8;
#pragma unroll
  for (int s = 0; s < 8; ++s) {
    bfr[s] = *(const bf16x8*)(bp + s * 512);
    af[0][s] = *(const bf16x8*)(ap + s * 512);
    af[1][s] = *(const bf16x8*)(ap + 32 * 512 + s * 512);
  }
  const int r64 = tid >> 3, oct = tid & 7;
  const int row = rb * 64 + r64;
  const size_t off = (size_t)row * D_ + cg * 64 + oct * 8;
  const u32x4 xv = *(const u32x4*)(x + off);
  const float2 st = stats[row];
  f32x4 yv[2][3];
#pragma unroll
  for (int tk = 0; tk < 2; ++tk) {
    const int idx = tk ? idx1 : idx0;
#pragma unroll
    for (int q = 0; q < 3; ++q) yv[tk][q] = (f32x4){0.f, 0.f, 0.f, 0.f};
    if ((unsigned)(idx - rb * 64) < 64u) {
      const int pg = tid >> 4;
      const float* yp = ypart + (size_t)(pg * 2 + tk) * 512 + cg * 64 + (tid & 15) * 4;
      yv[tk][0] = *(const f32x4*)yp;
      yv[tk][1] = *(const f32x4*)(yp + 32 * 1024);
      yv[tk][2] = *(const f32x4*)(yp + (pg + 64 < NHELP ? 64 * 1024 : 0));
    }
  }
  f32x16 acc[2];
#pragma unroll
  for (int e = 0; e < 16; ++e) { acc[0][e] = 0.f; acc[1][e] = 0.f; }
#pragma unroll
  for (int s = 0; s < 8; ++s) {
    acc[0] = mfma32(af[0][s], bfr[s], acc[0]);
    acc[1] = mfma32(af[1][s], bfr[s], acc[1]);
  }
  TS(2, 1);
#pragma unroll
  for (int ct = 0; ct < 2; ++ct)
#pragma unroll
    for (int q4 = 0; q4 < 4; ++q4) {
      f32x4 p = {acc[ct][4 * q4], acc[ct][4 * q4 + 1], acc[ct][4 * q4 + 2], acc[ct][4 * q4 + 3]};
      *(f32x4*)(smem + (((rt * 4 + ks4) * 2 + ct) * 4 + q4) * CH_STRIDE + lane * 16) = p;
    }
#pragma unroll
  for (int tk = 0; tk < 2; ++tk) {
    const int idx = tk ? idx1 : idx0;
    if ((unsigned)(idx - rb * 64) < 64u) {
      f32x4 y = yv[tk][0] + yv[tk][1];
      if ((tid >> 4) + 64 < NHELP) y += yv[tk][2];
#pragma unroll
      for (int e = 0; e < 4; ++e) {
        y[e] += __shfl_xor(y[e], 16);
        y[e] += __shfl_xor(y[e], 32);
      }
      if (lane < 16) *(f32x4*)(smem + LDS_Y + (tk * 8 + wave) * 256 + lane * 16) = y;
    }
  }
  __syncthreads();
  TS(2, 2);
  f32x4 lo = {0.f, 0.f, 0.f, 0.f}, hi = {0.f, 0.f, 0.f, 0.f};
#pragma unroll
  for (int k = 0; k < 4; ++k) {
    const char* p = smem + (((r64 >> 5) * 4 + k) * 8 + oct) * CH_STRIDE + (r64 & 31) * 16;
    lo += *(const f32x4*)p;
    hi += *(const f32x4*)(p + 512);
  }
  float upd[8];
#pragma unroll
  for (int e = 0; e < 8; ++e) upd[e] = 0.f;
  if (row == idx0 || row == idx1) {
    const int tk = (row == idx0) ? 0 : 1;
    f32x4 ya = {0.f, 0.f, 0.f, 0.f}, yb = {0.f, 0.f, 0.f, 0.f};
#pragma unroll
    for (int p = 0; p < 8; ++p) {
      const char* yp = smem + LDS_Y + (tk * 8 + p) * 256 + oct * 32;
      ya += *(const f32x4*)yp;
      yb += *(const f32x4*)(yp + 16);
    }
    const float rs = tk ? rs1 : rs0;
#pragma unroll
    for (int e = 0; e < 4; ++e) { upd[e] = rbf(rbf(ya[e]) * rs); upd[4 + e] = rbf(rbf(yb[e]) * rs); }
  }
  const float inv = 1.0f / st.y;
  float res[8];
#pragma unroll
  for (int e = 0; e < 4; ++e) {
    const float x0 = bf2f(xv[e] & 0xffffu), x1 = bfhi2f(xv[e]);
    const float a0 = rbf(e < 2 ? lo[2 * e] : hi[2 * e - 4]);
    const float a1 = rbf(e < 2 ? lo[2 * e + 1] : hi[2 * e - 3]);
    res[2 * e] = rbf(x0 + a0) + rbf(ln_elem(x0, st.x, st.y, inv) + upd[2 * e]);
    res[2 * e + 1] = rbf(x1 + a1) + rbf(ln_elem(x1, st.x, st.y, inv) + upd[2 * e + 1]);
  }
  u32x4 o = {pack2(res[0], res[1]), pack2(res[2], res[3]), pack2(res[4], res[5]), pack2(res[6], res[7])};
  *(u32x4*)(out + off) = o;
  TS(2, 3); TSR(2, 9);
}

extern "C" void kernel_launch(void* const* d_in, const int* in_sizes, int n_in, void* d_out, int out_size,
                              void* d_ws, size_t ws_size, hipStream_t stream) {
  const u16* x = (const u16*)d_in[0];
  const u16* wq = (const u16*)d_in[1];
  const u16* wk = (const u16*)d_in[2];
  const u16* wv = (const u16*)d_in[3];
  const u16* wo = (const u16*)d_in[4];
  const u16* wr = (const u16*)d_in[6];
  const u16* wg = (const u16*)d_in[7];
  const u16* wu = (const u16*)d_in[8];
  const u16* wd = (const u16*)d_in[9];
  char* ws = (char*)d_ws;
  u16* xnf = (u16*)(ws + OFF_XNF);
  u16* of = (u16*)(ws + OFF_OF);
  u16* wqf = (u16*)(ws + OFF_WQF);
  u16* wof = (u16*)(ws + OFF_WOF);
  u16* kf = (u16*)(ws + OFF_KF);
  u16* vf = (u16*)(ws + OFF_VF);
  unsigned* rkeys = (unsigned*)(ws + OFF_R);
  float2* stats = (float2*)(ws + OFF_STATS);
  float* ypart = (float*)(ws + OFF_Y);
  int* sel = (int*)(ws + OFF_SEL);
  u16* out = (u16*)d_out;

  ka_prep<<<192, 512, 0, stream>>>(x, wq, wk, wv, wo, wr, xnf, wqf, wof, kf, vf, rkeys, stats);
  kb_attn<<<256, 768, 0, stream>>>(xnf, wqf, kf, vf, rkeys, wg, wu, wd, of, ypart, sel);
  kc_out<<<256, 512, 0, stream>>>(x, of, wof, stats, ypart, sel, out);
}
```

```cpp
#include <hip/hip_runtime.h>
#include <hip/hip_bf16.h>

typedef unsigned short u16;
typedef __attribute__((ext_vector_type(8))) __bf16 bf16x8;
typedef __attribute__((ext_vector_type(2))) __bf16 bf16x2;
typedef __attribute__((ext_vector_type(2))) float f32x2;
typedef __attribute__((ext_vector_type(4))) float f32x4;
typedef __attribute__((ext_vector_type(16))) float f32x16;
typedef __attribute__((ext_vector_type(4))) unsigned u32x4;
typedef __attribute__((ext_vector_type(2))) unsigned u32x2;

#define DI __device__ __forceinline__
#define TS(k, n)
#define TSR(k, n)

static constexpr int S_ = 2048;
static constexpr int D_ = 512;
static constexpr int F_ = 1376;

#define LDSF(a) (*(const __attribute__((address_space(3))) bf16x8*)(uintptr_t)(a))
DI float bf2f(unsigned u) { return __uint_as_float(u << 16); }
DI float bfhi2f(unsigned u) { return __uint_as_float(u & 0xffff0000u); }
DI float rbf(float f) { return (float)(__bf16)f; }
DI unsigned pack2(float lo, float hi) {
  f32x2 p = {lo, hi};
  bf16x2 q = __builtin_convertvector(p, bf16x2);
  return __builtin_bit_cast(unsigned, q);
}
DI f32x4 mfma16(bf16x8 a, bf16x8 b, f32x4 c) { return __builtin_amdgcn_mfma_f32_16x16x32_bf16(a, b, c, 0, 0, 0); }
DI f32x16 mfma32(bf16x8 a, bf16x8 b, f32x16 c) { return __builtin_amdgcn_mfma_f32_32x32x16_bf16(a, b, c, 0, 0, 0); }
DI unsigned perm_lo(unsigned hi_src, unsigned lo_src) { return __builtin_amdgcn_perm(hi_src, lo_src, 0x05040100u); }
DI unsigned perm_hi(unsigned hi_src, unsigned lo_src) { return __builtin_amdgcn_perm(hi_src, lo_src, 0x07060302u); }

DI bf16x8 frag_lo(const unsigned* w) {
  u32x4 r = {perm_lo(w[1], w[0]), perm_lo(w[3], w[2]), perm_lo(w[5], w[4]), perm_lo(w[7], w[6])};
  return __builtin_bit_cast(bf16x8, r);
}
DI bf16x8 frag_hi(const unsigned* w) {
  u32x4 r = {perm_hi(w[1], w[0]), perm_hi(w[3], w[2]), perm_hi(w[5], w[4]), perm_hi(w[7], w[6])};
  return __builtin_bit_cast(bf16x8, r);
}
DI float ln_elem(float xv, float mb, float den, float inv) {
  const float d = rbf(xv - mb);
  float q = d * inv;
  const float rem = __builtin_fmaf(-q, den, d);
  q = __builtin_fmaf(rem, inv, q);
  return rbf(q);
}

static constexpr int VT_STRIDE = 1056;
static constexpr int VF_BYTES = 64 * VT_STRIDE;
static constexpr size_t OFF_XNF = 0;
static constexpr size_t OFF_OF = OFF_XNF + (size_t)S_ * D_ * 2;
static constexpr size_t OFF_WQF = OFF_OF + (size_t)S_ * D_ * 2;
static constexpr size_t OFF_WOF = OFF_WQF + (size_t)D_ * D_ * 2;
static constexpr size_t OFF_KF = OFF_WOF + (size_t)D_ * D_ * 2;
static constexpr size_t OFF_VF = OFF_KF + 65536;
static constexpr size_t OFF_R = OFF_VF + VF_BYTES;
static constexpr size_t OFF_STATS = OFF_R + S_ * 4;
static constexpr int NHELP = 172;
static constexpr size_t OFF_Y = OFF_STATS + S_ * 8;
static constexpr size_t OFF_SEL = OFF_Y + NHELP * 1024 * 4;

static constexpr int XN_STRIDE = 1040;
__global__ __launch_bounds__(512) void ka_prep(const u16* __restrict__ x, const u16* __restrict__ wq,
                                               const u16* __restrict__ wk, const u16* __restrict__ wv,
                                               const u16* __restrict__ wo, const u16* __restrict__ wr,
                                               u16* __restrict__ xnf, u16* __restrict__ wqf, u16* __restrict__ wof,
                                               u16* __restrict__ kf, u16* __restrict__ vf,
                                               unsigned* __restrict__ rkeys, float2* __restrict__ stats) {
  __shared__ __attribute__((aligned(16))) char smem[16 * XN_STRIDE + 8 * 5 * 64 * 16];
  const int tid = threadIdx.x;
  const int lane = tid & 63, wave = tid >> 6;
  TSR(0, 8); TS(0, 0);
  if (blockIdx.x >= 128) {
    const int pb = blockIdx.x - 128;
    const bool iso = pb >= 32;
    const int cp = tid & 255, ko = (pb & 31) * 2 + (tid >> 8);
    const int ks = ko >> 1, hh = ko & 1;
    const u16* W = (iso ? wo : wq) + 2 * cp;
    unsigned w[8];
#pragma unroll
    for (int j = 0; j < 8; ++j) {
      const int row = 16 * ks + (iso ? (8 * (j >> 2) + 4 * hh + (j & 3)) : (8 * hh + j));
      w[j] = *(const unsigned*)(W + row * D_);
    }
    u16* dst = (iso ? wof : wqf) + (((cp >> 4) * 32 + ks) * 64 + hh * 32 + ((2 * cp) & 31)) * 8;
    *(bf16x8*)dst = frag_lo(w);
    *(bf16x8*)(dst + 8) = frag_hi(w);
    TS(0, 3); TSR(0, 9);
    return;
  }
  const int row0 = blockIdx.x * 16;
  const int rt = blockIdx.x >> 1, th = blockIdx.x & 1;
  const int g = lane >> 4, i = lane & 15;
  const int k0 = wave * 64 + 8 * g;
  u32x4 xr[2];
#pragma unroll
  for (int rr = 0; rr < 2; ++rr) xr[rr] = *(const u32x4*)(x + (size_t)(row0 + 2 * wave + rr) * D_ + 8 * lane);
  const u16* wbase = ((i < 4) ? (wk + 4 * i) : (i < 8 ? wv + 4 * (i - 4) : wk)) + k0 * 16;
  u32x2 w[2][8];
#pragma unroll
  for (int s = 0; s < 2; ++s)
#pragma unroll
    for (int j = 0; j < 8; ++j) w[s][j] = *(const u32x2*)(wbase + (32 * s + j) * 16);
  bf16x8 rf[2];
#pragma unroll
  for (int s = 0; s < 2; ++s) rf[s] = *(const bf16x8*)(wr + k0 + 32 * s);

  float s1[2], s2[2];
#pragma unroll
  for (int rr = 0; rr < 2; ++rr) {
    float a = 0.f, q = 0.f;
#pragma unroll
    for (int e = 0; e < 4; ++e) {
      const float lo = bf2f(xr[rr][e] & 0xffffu), hi = bfhi2f(xr[rr][e]);
      a += lo + hi;
      q = __builtin_fmaf(lo, lo, q);
      q = __builtin_fmaf(hi, hi, q);
    }
    s1[rr] = a; s2[rr] = q;
  }
#pragma unroll
  for (int off = 32; off >= 1; off >>= 1)
#pragma unroll
    for (int rr = 0; rr < 2; ++rr) {
      s1[rr] += __shfl_xor(s1[rr], off);
      s2[rr] += __shfl_xor(s2[rr], off);
    }
#pragma unroll
  for (int rr = 0; rr < 2; ++rr) {
    const float mean = s1[rr] * (1.0f / D_);
    float var = s2[rr] * (1.0f / D_) - mean * mean;
    var = fmaxf(var, 0.f);
    const float mb = rbf(mean);
    const float den = rbf(__builtin_sqrtf(rbf(rbf(var) + 1e-5f)));
    const float inv = 1.0f / den;
    if (lane == rr) stats[row0 + 2 * wave + rr] = make_float2(mb, den);
    u32x4 o;
#pragma unroll
    for (int e = 0; e < 4; ++e)
      o[e] = pack2(ln_elem(bf2f(xr[rr][e] & 0xffffu), mb, den, inv), ln_elem(bfhi2f(xr[rr][e]), mb, den, inv));
    *(u32x4*)(smem + (2 * wave + rr) * XN_STRIDE + lane * 16) = o;
  }
  if (tid < 2 && th == 0) {
    const unsigned cv = tid ? 0u : 0x3f803f80u;
    u32x4 c4 = {cv, cv, cv, cv};
    *(u32x4*)((char*)vf + rt * VT_STRIDE + 1024 + tid * 16) = c4;
  }
  TS(0, 1);
  __syncthreads();
  {
    const int r = lane & 15, hh = (lane >> 4) & 1, kp = lane >> 5;
#pragma unroll
    for (int s = 0; s < 2; ++s) {
      const int ks = 4 * wave + 2 * s + kp;
      const u32x4 v = *(const u32x4*)(smem + r * XN_STRIDE + ks * 32 + hh * 16);
      *(u32x4*)(xnf + ((size_t)(rt * 32 + ks) * 64 + hh * 32 + th * 16 + r) * 8) = v;
    }
  }
  f32x4* red = (f32x4*)(smem + 16 * XN_STRIDE);
  {
    f32x4 acc[4], ar;
    ar = (f32x4){0.f, 0.f, 0.f, 0.f};
#pragma unroll
    for (int c = 0; c < 4; ++c) acc[c] = (f32x4){0.f, 0.f, 0.f, 0.f};
#pragma unroll
    for (int s = 0; s < 2; ++s) {
      unsigned wx[8], wy[8];
#pragma unroll
      for (int j = 0; j < 8; ++j) { wx[j] = w[s][j][0]; wy[j] = w[s][j][1]; }
      bf16x8 b[4] = {frag_lo(wx), frag_hi(wx), frag_lo(wy), frag_hi(wy)};
      const bf16x8 xa = *(const bf16x8*)(smem + i * XN_STRIDE + (k0 + 32 * s) * 2);
#pragma unroll
      for (int c = 0; c < 4; ++c) acc[c] = mfma16(xa, b[c], acc[c]);
      ar = mfma16(xa, rf[s], ar);
    }
#pragma unroll
    for (int c = 0; c < 4; ++c) red[(wave * 5 + c) * 64 + lane] = acc[c];
    red[(wave * 5 + 4) * 64 + lane] = ar;
  }
  __syncthreads();
  TS(0, 2);
  if (wave < 5) {
    f32x4 v = red[wave * 64 + lane];
#pragma unroll
    for (int wv_ = 1; wv_ < 8; ++wv_) v += red[(wv_ * 5 + wave) * 64 + lane];
    const int kk = 16 * th + 4 * g;
    if (wave == 4) {
      unsigned k0 = 0, k1 = 0;
#pragma unroll
      for (int e = 0; e < 4; ++e) {
        const unsigned u = __float_as_uint(rbf(v[e]));
        const unsigned ord = (u & 0x80000000u) ? ~u : (u | 0x80000000u);
        const unsigned key = (ord & 0xffff0000u) | (unsigned)(2047 - (row0 + 4 * g + e));
        k1 = max(k1, min(k0, key));
        k0 = max(k0, key);
      }
#pragma unroll
      for (int off = 16; off < 64; off <<= 1) {
        const unsigned o0 = __shfl_xor(k0, off), o1 = __shfl_xor(k1, off);
        k1 = max(min(k0, o0), max(k1, o1));
        k0 = max(k0, o0);
      }
      if (lane == 0) *(u32x2*)(rkeys + 2 * blockIdx.x) = (u32x2){k0, k1};
    } else if (i < 4) {
      const int hh = i & 1, j = (i >> 1) * 4 + wave;
#pragma unroll
      for (int e = 0; e < 4; ++e)
        kf[rt * 512 + (hh * 32 + kk + e) * 8 + j] = __builtin_bit_cast(u16, (__bf16)v[e]);
    } else if (i < 8) {
      const int d = 4 * (i - 4) + wave;
      const int hh = g & 1, jh = g >> 1;
      u32x2 o = {pack2(v[0], v[1]), pack2(v[2], v[3])};
      *(u32x2*)(vf + rt * (VT_STRIDE / 2) + th * 256 + (hh * 16 + d) * 8 + jh * 4) = o;
    }
  }
  TS(0, 3); TSR(0, 9);
}

static constexpr int LDS_K = 0;
static constexpr int LDS_V = 65536;
static constexpr int LDS_H = 65536 + VF_BYTES + 2048;
static constexpr int LDS_TOTAL = LDS_H + 1024;

__global__ __launch_bounds__(768) void kb_attn(const u16* __restrict__ xnf, const u16* __restrict__ wqf,
                                               const u16* __restrict__ kf, const u16* __restrict__ vf,
                                               const unsigned* __restrict__ rkeys, const u16* __restrict__ wg,
                                               const u16* __restrict__ wu, const u16* __restrict__ wd,
                                               u16* __restrict__ of, float* __restrict__ ypart,
                                               int* __restrict__ sel) {
  __shared__ __attribute__((aligned(16))) char smem[LDS_TOTAL];
  const int tid = threadIdx.x;
  const int lane = tid & 63, wave = tid >> 6;
  const int b = blockIdx.x;
  TSR(1, 8); TS(1, 0);

  if (wave < 8) {
    const int rb = (b & 7) + 8 * ((b >> 3) & 3), hg = b >> 5;
    const int rt = wave >> 2, ks4 = wave & 3;
    const int r = lane & 31, hh = lane >> 5;
    {
      u32x4 tk[8];
#pragma unroll
      for (int c = 0; c < 8; ++c) tk[c] = *(const u32x4*)((const char*)kf + c * 8192 + tid * 16);
      bf16x8 af[2][8], bfr[8];
      const int rtq = wave & 1, kq = wave >> 1;
      const u16* ap = wqf + ((size_t)(2 * hg) * 32 + 8 * kq) * 512 + lane * 8;
      const u16* bp = xnf + ((size_t)(2 * rb + rtq) * 32 + 8 * kq) * 512 + lane * 8;
#pragma unroll
      for (int s = 0; s < 8; ++s) {
        bfr[s] = *(const bf16x8*)(bp + s * 512);
        af[0][s] = *(const bf16x8*)(ap + s * 512);
        af[1][s] = *(const bf16x8*)(ap + 32 * 512 + s * 512);
      }
      __builtin_amdgcn_sched_barrier(0);
#pragma unroll
      for (int c = 0; c < 8; ++c) *(u32x4*)(smem + LDS_K + c * 8192 + tid * 16) = tk[c];
      __builtin_amdgcn_sched_barrier(0);
      f32x16 acc[2];
#pragma unroll
      for (int e = 0; e < 16; ++e) { acc[0][e] = 0.f; acc[1][e] = 0.f; }
#pragma unroll
      for (int s = 0; s < 8; ++s) {
        acc[0] = mfma32(af[0][s], bfr[s], acc[0]);
        acc[1] = mfma32(af[1][s], bfr[s], acc[1]);
      }
#pragma unroll
      for (int dt = 0; dt < 2; ++dt)
#pragma unroll
        for (int q4 = 0; q4 < 4; ++q4) {
          f32x4 p = {acc[dt][4 * q4], acc[dt][4 * q4 + 1], acc[dt][4 * q4 + 2], acc[dt][4 * q4 + 3]};
          *(f32x4*)(smem + LDS_V + ((((rtq * 4 + kq) * 2 + dt) * 4 + q4) * 64 + lane) * 16) = p;
        }
    }
    __builtin_amdgcn_sched_barrier(0);
    u32x4 tv[8], tvx;
#pragma unroll
    for (int c = 0; c < 8; ++c) tv[c] = *(const u32x4*)((const char*)vf + c * 8192 + tid * 16);
    tvx = *(const u32x4*)((const char*)vf + 65536 + (tid & 127) * 16);
    TS(1, 1);
    __syncthreads();
    TS(1, 2);
    const int hl = wave & 3;
    const int head = hg * 4 + hl;
    bf16x8 qf;
    {
      f32x4 a0 = {0.f, 0.f, 0.f, 0.f}, a1 = {0.f, 0.f, 0.f, 0.f};
#pragma unroll
      for (int k = 0; k < 4; ++k) {
        const char* p = smem + LDS_V + ((((rt * 4 + k) * 2 + (hl >> 1)) * 4 + 2 * (hl & 1)) * 64 + lane) * 16;
        a0 += *(const f32x4*)p;
        a1 += *(const f32x4*)(p + 1024);
      }
      const float SC = 0.25f * 1.44269504088896340736f;
      u32x4 pk = {pack2(rbf(a0[0]) * SC, rbf(a0[1]) * SC), pack2(rbf(a0[2]) * SC, rbf(a0[3]) * SC),
                  pack2(rbf(a1[0]) * SC, rbf(a1[1]) * SC), pack2(rbf(a1[2]) * SC, rbf(a1[3]) * SC)};
      qf = __builtin_bit_cast(bf16x8, pk);
    }
    __syncthreads();

    const unsigned lbase = (unsigned)(uintptr_t)(__attribute__((address_space(3))) char*)smem;
    float m0 = -__builtin_inff(), m1 = -__builtin_inff();
    f32x16 zero16;
#pragma unroll
    for (int e = 0; e < 16; ++e) zero16[e] = 0.f;
    {
      unsigned kq_ = lbase + LDS_K + lane * 16;
      asm volatile("" : "+v"(kq_));
      f32x16 sa = mfma32(LDSF(kq_), qf, zero16);
      f32x16 sb = mfma32(LDSF(kq_ + 1024), qf, zero16);
      bf16x8 k2 = LDSF(kq_ + 2048), k3 = LDSF(kq_ + 3072);
      __builtin_amdgcn_sched_barrier(0);
#pragma unroll
      for (int t = 0; t < 64; ++t) {
        f32x16 sc = sb;
        bf16x8 k4 = k3;
        if (t + 2 < 64) sc = mfma32(k2, qf, zero16);
        if (t + 4 < 64) k4 = LDSF(kq_ + (t + 4) * 1024);
        if (t & 1) {
#pragma unroll
          for (int e = 0; e < 16; e += 2) m1 = fmaxf(fmaxf(m1, sa[e]), sa[e + 1]);
        } else {
#pragma unroll
          for (int e = 0; e < 16; e += 2) m0 = fmaxf(fmaxf(m0, sa[e]), sa[e + 1]);
        }
        sa = sb; sb = sc; k2 = k3; k3 = k4;
        if (t >= 32 && t < 40) *(u32x4*)(smem + LDS_V + (t - 32) * 8192 + tid * 16) = tv[t - 32];
        if (t == 40) *(u32x4*)(smem + LDS_V + 65536 + (tid & 127) * 16) = tvx;
        __builtin_amdgcn_sched_group_barrier(0x008, 1, 0);
        __builtin_amdgcn_sched_group_barrier(0x100, 1, 0);
        __builtin_amdgcn_sched_group_barrier(0x002, 8, 0);
        __builtin_amdgcn_sched_barrier(0);
      }
    }
    float m = fmaxf(m0, m1);
    m = fmaxf(m, __shfl_xor(m, 32));
    TS(1, 3);
    __syncthreads();
    TS(1, 4);

    f32x16 negm;
#pragma unroll
    for (int e = 0; e < 16; ++e) negm[e] = -m;
    f32x16 o;
#pragma unroll
    for (int e = 0; e < 16; ++e) o[e] = 0.f;
    unsigned ka_ = lbase + LDS_K + lane * 16;
    unsigned va_ = lbase + LDS_V + ((r < 16) ? (hh * 16 + r) * 16 : (r == 16 ? 1024 : 1040));
    unsigned vb_ = lbase + LDS_V + ((r < 16) ? (hh * 16 + r) * 16 + 512 : (r == 16 ? 1024 : 1040));
    f32x16 s = mfma32(LDSF(ka_), qf, negm);
    bf16x8 kn = LDSF(ka_ + 1024);
    bf16x8 v0 = LDSF(va_);
    bf16x8 v1 = LDSF(vb_);
#pragma unroll 1
    for (int tb = 0; tb < 8; ++tb) {
      asm volatile("" : "+v"(ka_), "+v"(va_), "+v"(vb_));
#pragma unroll
      for (int u = 0; u < 8; ++u) {
        const f32x16 sn = mfma32(kn, qf, negm);
        const bf16x8 k2 = LDSF(ka_ + (u + 2) * 1024);
        const bf16x8 v0n = LDSF(va_ + (u + 1) * VT_STRIDE);
        const bf16x8 v1n = LDSF(vb_ + (u + 1) * VT_STRIDE);
        float p[16];
#pragma unroll
        for (int e = 0; e < 8; ++e) p[e] = __builtin_amdgcn_exp2f(s[e]);
        u32x4 pa = {pack2(p[0], p[1]), pack2(p[2], p[3]), pack2(p[4], p[5]), pack2(p[6], p[7])};
        o = mfma32(v0, __builtin_bit_cast(bf16x8, pa), o);
#pragma unroll
        for (int e = 8; e < 16; ++e) p[e] = __builtin_amdgcn_exp2f(s[e]);
        u32x4 pb = {pack2(p[8], p[9]), pack2(p[10], p[11]), pack2(p[12], p[13]), pack2(p[14], p[15])};
        o = mfma32(v1, __builtin_bit_cast(bf16x8, pb), o);
        s = sn; kn = k2; v0 = v0n; v1 = v1n;
        __builtin_amdgcn_sched_group_barrier(0x008, 1, 0);
        __builtin_amdgcn_sched_group_barrier(0x100, 3, 0);
        __builtin_amdgcn_sched_group_barrier(0x402, 12, 0);
        __builtin_amdgcn_sched_group_barrier(0x008, 1, 0);
        __builtin_amdgcn_sched_group_barrier(0x402, 12, 0);
        __builtin_amdgcn_sched_group_barrier(0x008, 1, 0);
        __builtin_amdgcn_sched_barrier(0);
      }
      ka_ += 8 * 1024; va_ += 8 * VT_STRIDE; vb_ += 8 * VT_STRIDE;
    }
    TS(1, 5);
    const float il = 1.0f / __shfl(o[8], r);
    u32x4 w0 = {pack2(o[0] * il, o[1] * il), pack2(o[2] * il, o[3] * il), pack2(o[4] * il, o[5] * il),
                pack2(o[6] * il, o[7] * il)};
    *(u32x4*)(of + ((size_t)((2 * rb + rt) * 32 + head) * 64 + lane) * 8) = w0;
  } else {
    if (b >= NHELP) return;
    __builtin_amdgcn_s_setprio(3);
    const int hj = wave - 8;
    const int g = lane >> 4, i = lane & 15;
    const int f0 = b * 8;
    const u32x4 kv = *(const u32x4*)(rkeys + lane * 4);
    unsigned k0 = max(kv[0], kv[2]), k1 = max(min(kv[0], kv[2]), max(kv[1], kv[3]));
#pragma unroll
    for (int off = 1; off < 64; off <<= 1) {
      const unsigned o0 = __shfl_xor(k0, off), o1 = __shfl_xor(k1, off);
      k1 = max(min(k0, o0), max(k1, o1));
      k0 = max(k0, o0);
    }
    const int i0 = 2047 - (int)(k0 & 0x7ffu), i1 = 2047 - (int)(k1 & 0x7ffu);
    if (b == 0 && hj == 0 && lane == 0) {
      const float v0 = __uint_as_float((k0 & 0x80000000u) ? (k0 & 0x7fff0000u) : (~k0 & 0xffff0000u));
      const float v1 = __uint_as_float((k1 & 0x80000000u) ? (k1 & 0x7fff0000u) : (~k1 & 0xffff0000u));
      sel[0] = i0; sel[1] = i1;
      ((float*)sel)[2] = rbf(1.0f / (1.0f + __expf(-v0)));
      ((float*)sel)[3] = rbf(1.0f / (1.0f + __expf(-v1)));
    }
    const int srow = (i & 1) ? i1 : i0;
    const u16* xp = xnf + ((size_t)((srow >> 5) * 32 + 8 * hj + 4 * ((i >> 1) & 1) + (g >> 1)) * 64 + (g & 1) * 32 + (srow & 31)) * 8;
    bf16x8 xa[2];
#pragma unroll
    for (int s = 0; s < 2; ++s) xa[s] = *(const bf16x8*)(xp + (size_t)s * 1024);
    TS(1, 3);
    __syncthreads();
    __syncthreads();
    __builtin_amdgcn_sched_barrier(0);
    unsigned wa[2][8];
    const u16* wp = ((i & 4) ? wu : wg) + f0 + 2 * (i & 3) + (size_t)(128 * hj + 64 * (i >> 3) + 8 * g) * F_;
#pragma unroll
    for (int s = 0; s < 2; ++s)
#pragma unroll
      for (int j = 0; j < 8; ++j) wa[s][j] = *(const unsigned*)(wp + (size_t)(32 * s + j) * F_);
    unsigned wdv[8];
#pragma unroll
    for (int f = 0; f < 8; ++f) wdv[f] = *(const unsigned*)(wd + (size_t)(f0 + f) * D_ + hj * 128 + 2 * lane);
    f32x4 a0 = {0.f, 0.f, 0.f, 0.f}, a1 = {0.f, 0.f, 0.f, 0.f};
#pragma unroll
    for (int s = 0; s < 2; ++s) {
      a0 = mfma16(xa[s], frag_lo(wa[s]), a0);
      a1 = mfma16(xa[s], frag_hi(wa[s]), a1);
    }
    if (lane < 16) {
      const f32x4 pv = (i & 8) ? (f32x4){a0[2], a0[3], a1[2], a1[3]} : (f32x4){a0[0], a0[1], a1[0], a1[1]};
      *(f32x4*)(smem + LDS_H + (hj * 16 + i) * 16) = pv;
    }
    TS(1, 4);
    __syncthreads();
    __builtin_amdgcn_sched_barrier(0);
    const char* pp = smem + LDS_H + (i & 7) * 16;
    f32x4 t = *(const f32x4*)pp;
#pragma unroll
    for (int q = 1; q < 8; ++q) t += *(const f32x4*)(pp + q * 128);
    float y[2][2] = {{0.f, 0.f}, {0.f, 0.f}};
    int hb[4];
#pragma unroll
    for (int e = 0; e < 4; ++e) {
      const float gt = rbf(t[e]);
      const float u = __shfl_xor(gt, 4);
      hb[e] = __builtin_bit_cast(int, rbf(rbf(gt / (1.0f + __expf(-gt))) * u));
    }
#pragma unroll
    for (int f = 0; f < 8; ++f) {
      const float w0 = bf2f(wdv[f] & 0xffffu), w1 = bfhi2f(wdv[f]);
#pragma unroll
      for (int tk = 0; tk < 2; ++tk) {
        const float h = __builtin_bit_cast(float, __builtin_amdgcn_readlane(hb[(f & 1) * 2 + tk], f >> 1));
        y[tk][0] = __builtin_fmaf(h, w0, y[tk][0]);
        y[tk][1] = __builtin_fmaf(h, w1, y[tk][1]);
      }
    }
#pragma unroll
    for (int tk = 0; tk < 2; ++tk)
      *(float2*)(ypart + (size_t)b * 1024 + tk * 512 + hj * 128 + 2 * lane) = make_float2(y[tk][0], y[tk][1]);
  }
  TS(1, 6); TSR(1, 9);
}

static constexpr int CH_STRIDE = 1056;
static constexpr int LDS_Y = 64 * CH_STRIDE;
__global__ __launch_bounds__(512) void kc_out(const u16* __restrict__ x, const u16* __restrict__ of,
                                              const u16* __restrict__ wof, const float2* __restrict__ stats,
                                              const float* __restrict__ ypart, const int* __restrict__ sel,
                                              u16* __restrict__ out) {
  __shared__ __attribute__((aligned(16))) char smem[LDS_Y + 2 * 8 * 256];
  const int tid = threadIdx.x;
  const int lane = tid & 63, wave = tid >> 6;
  TSR(2, 8); TS(2, 0);
  const int bb = blockIdx.x;
  const int rb = (bb & 7) + 8 * ((bb >> 3) & 3), cg = bb >> 5;
  const int rt = wave & 1, ks4 = wave >> 1;
  const int idx0 = sel[0], idx1 = sel[1];
  const float rs0 = ((const float*)sel)[2], rs1 = ((const float*)sel)[3];
  bf16x8 af[2][8], bfr[8];
  const u16* ap = wof + ((size_t)(2 * cg) * 32 + 8 * ks4) * 512 + lane * 8;
  const u16* bp = of + ((size_t)(2 * rb + rt) * 32 + 8 * ks4) * 512 + lane * 8;
#pragma unroll
  for (int s = 0; s < 8; ++s) {
    bfr[s] = *(const bf16x8*)(bp + s * 512);
    af[0][s] = *(const bf16x8*)(ap + s * 512);
    af[1][s] = *(const bf16x8*)(ap + 32 * 512 + s * 512);
  }
  const int r64 = tid >> 3, oct = tid & 7;
  const int row = rb * 64 + r64;
  const size_t off = (size_t)row * D_ + cg * 64 + oct * 8;
  const u32x4 xv = *(const u32x4*)(x + off);
  const float2 st = stats[row];
  f32x4 yv[2][6];
#pragma unroll
  for (int tk = 0; tk < 2; ++tk) {
    const int idx = tk ? idx1 : idx0;
#pragma unroll
    for (int q = 0; q < 6; ++q) yv[tk][q] = (f32x4){0.f, 0.f, 0.f, 0.f};
    if ((unsigned)(idx - rb * 64) < 64u) {
      const int pg = tid >> 4;
      const float* yp = ypart + (size_t)(pg * 2 + tk) * 512 + cg * 64 + (tid & 15) * 4;
#pragma unroll
      for (int q = 0; q < 5; ++q) yv[tk][q] = *(const f32x4*)(yp + q * 32 * 1024);
      yv[tk][5] = *(const f32x4*)(yp + (pg + 160 < NHELP ? 160 * 1024 : 0));
    }
  }
  f32x16 acc[2];
#pragma unroll
  for (int e = 0; e < 16; ++e) { acc[0][e] = 0.f; acc[1][e] = 0.f; }
#pragma unroll
  for (int s = 0; s < 8; ++s) {
    acc[0] = mfma32(af[0][s], bfr[s], acc[0]);
    acc[1] = mfma32(af[1][s], bfr[s], acc[1]);
  }
  TS(2, 1);
#pragma unroll
  for (int ct = 0; ct < 2; ++ct)
#pragma unroll
    for (int q4 = 0; q4 < 4; ++q4) {
      f32x4 p = {acc[ct][4 * q4], acc[ct][4 * q4 + 1], acc[ct][4 * q4 + 2], acc[ct][4 * q4 + 3]};
      *(f32x4*)(smem + (((rt * 4 + ks4) * 2 + ct) * 4 + q4) * CH_STRIDE + lane * 16) = p;
    }
#pragma unroll
  for (int tk = 0; tk < 2; ++tk) {
    const int idx = tk ? idx1 : idx0;
    if ((unsigned)(idx - rb * 64) < 64u) {
      f32x4 y = ((yv[tk][0] + yv[tk][1]) + (yv[tk][2] + yv[tk][3])) + yv[tk][4];
      if ((tid >> 4) + 160 < NHELP) y += yv[tk][5];
#pragma unroll
      for (int e = 0; e < 4; ++e) {
        y[e] += __shfl_xor(y[e], 16);
        y[e] += __shfl_xor(y[e], 32);
      }
      if (lane < 16) *(f32x4*)(smem + LDS_Y + (tk * 8 + wave) * 256 + lane * 16) = y;
    }
  }
  __syncthreads();
  TS(2, 2);
  f32x4 lo = {0.f, 0.f, 0.f, 0.f}, hi = {0.f, 0.f, 0.f, 0.f};
#pragma unroll
  for (int k = 0; k < 4; ++k) {
    const char* p = smem + (((r64 >> 5) * 4 + k) * 8 + oct) * CH_STRIDE + (r64 & 31) * 16;
    lo += *(const f32x4*)p;
    hi += *(const f32x4*)(p + 512);
  }
  float upd[8];
#pragma unroll
  for (int e = 0; e < 8; ++e) upd[e] = 0.f;
  if (row == idx0 || row == idx1) {
    const int tk = (row == idx0) ? 0 : 1;
    f32x4 ya = {0.f, 0.f, 0.f, 0.f}, yb = {0.f, 0.f, 0.f, 0.f};
#pragma unroll
    for (int p = 0; p < 8; ++p) {
      const char* yp = smem + LDS_Y + (tk * 8 + p) * 256 + oct * 32;
      ya += *(const f32x4*)yp;
      yb += *(const f32x4*)(yp + 16);
    }
    const float rs = tk ? rs1 : rs0;
#pragma unroll
    for (int e = 0; e < 4; ++e) { upd[e] = rbf(rbf(ya[e]) * rs); upd[4 + e] = rbf(rbf(yb[e]) * rs); }
  }
  const float inv = 1.0f / st.y;
  float res[8];
#pragma unroll
  for (int e = 0; e < 4; ++e) {
    const float x0 = bf2f(xv[e] & 0xffffu), x1 = bfhi2f(xv[e]);
    const float a0 = rbf(e < 2 ? lo[2 * e] : hi[2 * e - 4]);
    const float a1 = rbf(e < 2 ? lo[2 * e + 1] : hi[2 * e - 3]);
    res[2 * e] = rbf(x0 + a0) + rbf(ln_elem(x0, st.x, st.y, inv) + upd[2 * e]);
    res[2 * e + 1] = rbf(x1 + a1) + rbf(ln_elem(x1, st.x, st.y, inv) + upd[2 * e + 1]);
  }
  u32x4 o = {pack2(res[0], res[1]), pack2(res[2], res[3]), pack2(res[4], res[5]), pack2(res[6], res[7])};
  *(u32x4*)(out + off) = o;
  TS(2, 3); TSR(2, 9);
}

extern "C" void kernel_launch(void* const* d_in, const int* in_sizes, int n_in, void* d_out, int out_size,
                              void* d_ws, size_t ws_size, hipStream_t stream) {
  const u16* x = (const u16*)d_in[0];
  const u16* wq = (const u16*)d_in[1];
  const u16* wk = (const u16*)d_in[2];
  const u16* wv = (const u16*)d_in[3];
  const u16* wo = (const u16*)d_in[4];
  const u16* wr = (const u16*)d_in[6];
  const u16* wg = (const u16*)d_in[7];
  const u16* wu = (const u16*)d_in[8];
  const u16* wd = (const u16*)d_in[9];
  char* ws = (char*)d_ws;
  u16* xnf = (u16*)(ws + OFF_XNF);
  u16* of = (u16*)(ws + OFF_OF);
  u16* wqf = (u16*)(ws + OFF_WQF);
  u16* wof = (u16*)(ws + OFF_WOF);
  u16* kf = (u16*)(ws + OFF_KF);
  u16* vf = (u16*)(ws + OFF_VF);
  unsigned* rkeys = (unsigned*)(ws + OFF_R);
  float2* stats = (float2*)(ws + OFF_STATS);
  float* ypart = (float*)(ws + OFF_Y);
  int* sel = (int*)(ws + OFF_SEL);
  u16* out = (u16*)d_out;

  ka_prep<<<192, 512, 0, stream>>>(x, wq, wk, wv, wo, wr, xnf, wqf, wof, kf, vf, rkeys, stats);
  kb_attn<<<256, 768, 0, stream>>>(xnf, wqf, kf, vf, rkeys, wg, wu, wd, of, ypart, sel);
  kc_out<<<256, 512, 0, stream>>>(x, of, wof, stats, ypart, sel, out);
}
```

```cpp
#include <hip/hip_runtime.h>
#include <hip/hip_bf16.h>

typedef unsigned short u16;
typedef __attribute__((ext_vector_type(8))) __bf16 bf16x8;
typedef __attribute__((ext_vector_type(2))) __bf16 bf16x2;
typedef __attribute__((ext_vector_type(2))) float f32x2;
typedef __attribute__((ext_vector_type(4))) float f32x4;
typedef __attribute__((ext_vector_type(16))) float f32x16;
typedef __attribute__((ext_vector_type(4))) unsigned u32x4;
typedef __attribute__((ext_vector_type(2))) unsigned u32x2;

#define DI __device__ __forceinline__
#define TS(k, n)
#define TSR(k, n)

static constexpr int S_ = 2048;
static constexpr int D_ = 512;
static constexpr int F_ = 1376;

#define LDSF(a) (*(const __attribute__((address_space(3))) bf16x8*)(uintptr_t)(a))
DI float bf2f(unsigned u) { return __uint_as_float(u << 16); }
DI float bfhi2f(unsigned u) { return __uint_as_float(u & 0xffff0000u); }
DI float rbf(float f) { return (float)(__bf16)f; }
DI unsigned pack2(float lo, float hi) {
  f32x2 p = {lo, hi};
  bf16x2 q = __builtin_convertvector(p, bf16x2);
  return __builtin_bit_cast(unsigned, q);
}
DI f32x4 mfma16(bf16x8 a, bf16x8 b, f32x4 c) { return __builtin_amdgcn_mfma_f32_16x16x32_bf16(a, b, c, 0, 0, 0); }
DI f32x16 mfma32(bf16x8 a, bf16x8 b, f32x16 c) { return __builtin_amdgcn_mfma_f32_32x32x16_bf16(a, b, c, 0, 0, 0); }
DI unsigned perm_lo(unsigned hi_src, unsigned lo_src) { return __builtin_amdgcn_perm(hi_src, lo_src, 0x05040100u); }
DI unsigned perm_hi(unsigned hi_src, unsigned lo_src) { return __builtin_amdgcn_perm(hi_src, lo_src, 0x07060302u); }

DI bf16x8 frag_lo(const unsigned* w) {
  u32x4 r = {perm_lo(w[1], w[0]), perm_lo(w[3], w[2]), perm_lo(w[5], w[4]), perm_lo(w[7], w[6])};
  return __builtin_bit_cast(bf16x8, r);
}
DI bf16x8 frag_hi(const unsigned* w) {
  u32x4 r = {perm_hi(w[1], w[0]), perm_hi(w[3], w[2]), perm_hi(w[5], w[4]), perm_hi(w[7], w[6])};
  return __builtin_bit_cast(bf16x8, r);
}
DI float ln_elem(float xv, float mb, float den, float inv) {
  const float d = rbf(xv - mb);
  float q = d * inv;
  const float rem = __builtin_fmaf(-q, den, d);
  q = __builtin_fmaf(rem, inv, q);
  return rbf(q);
}

static constexpr int VT_STRIDE = 1056;
static constexpr int VF_BYTES = 64 * VT_STRIDE;
static constexpr size_t OFF_XNF = 0;
static constexpr size_t OFF_OF = OFF_XNF + (size_t)S_ * D_ * 2;
static constexpr size_t OFF_WQF = OFF_OF + (size_t)S_ * D_ * 2;
static constexpr size_t OFF_WOF = OFF_WQF + (size_t)D_ * D_ * 2;
static constexpr size_t OFF_KF = OFF_WOF + (size_t)D_ * D_ * 2;
static constexpr size_t OFF_VF = OFF_KF + 65536;
static constexpr size_t OFF_R = OFF_VF + VF_BYTES;
static constexpr size_t OFF_STATS = OFF_R + S_ * 4;
static constexpr int NHELP = 86;
static constexpr size_t OFF_Y = OFF_STATS + S_ * 8;
static constexpr size_t OFF_SEL = OFF_Y + NHELP * 1024 * 4;

static constexpr int XN_STRIDE = 1040;
__global__ __launch_bounds__(512) void ka_prep(const u16* __restrict__ x, const u16* __restrict__ wq,
                                               const u16* __restrict__ wk, const u16* __restrict__ wv,
                                               const u16* __restrict__ wo, const u16* __restrict__ wr,
                                               u16* __restrict__ xnf, u16* __restrict__ wqf, u16* __restrict__ wof,
                                               u16* __restrict__ kf, u16* __restrict__ vf,
                                               unsigned* __restrict__ rkeys, float2* __restrict__ stats) {
  __shared__ __attribute__((aligned(16))) char smem[16 * XN_STRIDE + 8 * 5 * 64 * 16];
  const int tid = threadIdx.x;
  const int lane = tid & 63, wave = tid >> 6;
  TSR(0, 8); TS(0, 0);
  if (blockIdx.x >= 128) {
    const int pb = blockIdx.x - 128;
    const bool iso = pb >= 32;
    const int cp = tid & 255, ko = (pb & 31) * 2 + (tid >> 8);
    const int ks = ko >> 1, hh = ko & 1;
    const u16* W = (iso ? wo : wq) + 2 * cp;
    unsigned w[8];
#pragma unroll
    for (int j = 0; j < 8; ++j) {
      const int row = 16 * ks + (iso ? (8 * (j >> 2) + 4 * hh + (j & 3)) : (8 * hh + j));
      w[j] = *(const unsigned*)(W + row * D_);
    }
    u16* dst = (iso ? wof : wqf) + (((cp >> 4) * 32 + ks) * 64 + hh * 32 + ((2 * cp) & 31)) * 8;
    *(bf16x8*)dst = frag_lo(w);
    *(bf16x8*)(dst + 8) = frag_hi(w);
    TS(0, 3); TSR(0, 9);
    return;
  }
  const int row0 = blockIdx.x * 16;
  const int rt = blockIdx.x >> 1, th = blockIdx.x & 1;
  const int g = lane >> 4, i = lane & 15;
  const int k0 = wave * 64 + 8 * g;
  u32x4 xr[2];
#pragma unroll
  for (int rr = 0; rr < 2; ++rr) xr[rr] = *(const u32x4*)(x + (size_t)(row0 + 2 * wave + rr) * D_ + 8 * lane);
  const u16* wbase = ((i < 4) ? (wk + 4 * i) : (i < 8 ? wv + 4 * (i - 4) : wk)) + k0 * 16;
  u32x2 w[2][8];
#pragma unroll
  for (int s = 0; s < 2; ++s)
#pragma unroll
    for (int j = 0; j < 8; ++j) w[s][j] = *(const u32x2*)(wbase + (32 * s + j) * 16);
  bf16x8 rf[2];
#pragma unroll
  for (int s = 0; s < 2; ++s) rf[s] = *(const bf16x8*)(wr + k0 + 32 * s);

  float s1[2], s2[2];
#pragma unroll
  for (int rr = 0; rr < 2; ++rr) {
    float a = 0.f, q = 0.f;
#pragma unroll
    for (int e = 0; e < 4; ++e) {
      const float lo = bf2f(xr[rr][e] & 0xffffu), hi = bfhi2f(xr[rr][e]);
      a += lo + hi;
      q = __builtin_fmaf(lo, lo, q);
      q = __builtin_fmaf(hi, hi, q);
    }
    s1[rr] = a; s2[rr] = q;
  }
#pragma unroll
  for (int off = 32; off >= 1; off >>= 1)
#pragma unroll
    for (int rr = 0; rr < 2; ++rr) {
      s1[rr] += __shfl_xor(s1[rr], off);
      s2[rr] += __shfl_xor(s2[rr], off);
    }
#pragma unroll
  for (int rr = 0; rr < 2; ++rr) {
    const float mean = s1[rr] * (1.0f / D_);
    float var = s2[rr] * (1.0f / D_) - mean * mean;
    var = fmaxf(var, 0.f);
    const float mb = rbf(mean);
    const float den = rbf(__builtin_sqrtf(rbf(rbf(var) + 1e-5f)));
    const float inv = 1.0f / den;
    if (lane == rr) stats[row0 + 2 * wave + rr] = make_float2(mb, den);
    u32x4 o;
#pragma unroll
    for (int e = 0; e < 4; ++e)
      o[e] = pack2(ln_elem(bf2f(xr[rr][e] & 0xffffu), mb, den, inv), ln_elem(bfhi2f(xr[rr][e]), mb, den, inv));
    *(u32x4*)(smem + (2 * wave + rr) * XN_STRIDE + lane * 16) = o;
  }
  if (tid < 2 && th == 0) {
    const unsigned cv = tid ? 0u : 0x3f803f80u;
    u32x4 c4 = {cv, cv, cv, cv};
    *(u32x4*)((char*)vf + rt * VT_STRIDE + 1024 + tid * 16) = c4;
  }
  TS(0, 1);
  __syncthreads();
  {
    const int r = lane & 15, hh = (lane >> 4) & 1, kp = lane >> 5;
#pragma unroll
    for (int s = 0; s < 2; ++s) {
      const int ks = 4 * wave + 2 * s + kp;
      const u32x4 v = *(const u32x4*)(smem + r * XN_STRIDE + ks * 32 + hh * 16);
      *(u32x4*)(xnf + ((size_t)(rt * 32 + ks) * 64 + hh * 32 + th * 16 + r) * 8) = v;
    }
  }
  f32x4* red = (f32x4*)(smem + 16 * XN_STRIDE);
  {
    f32x4 acc[4], ar;
    ar = (f32x4){0.f, 0.f, 0.f, 0.f};
#pragma unroll
    for (int c = 0; c < 4; ++c) acc[c] = (f32x4){0.f, 0.f, 0.f, 0.f};
#pragma unroll
    for (int s = 0; s < 2; ++s) {
      unsigned wx[8], wy[8];
#pragma unroll
      for (int j = 0; j < 8; ++j) { wx[j] = w[s][j][0]; wy[j] = w[s][j][1]; }
      bf16x8 b[4] = {frag_lo(wx), frag_hi(wx), frag_lo(wy), frag_hi(wy)};
      const bf16x8 xa = *(const bf16x8*)(smem + i * XN_STRIDE + (k0 + 32 * s) * 2);
#pragma unroll
      for (int c = 0; c < 4; ++c) acc[c] = mfma16(xa, b[c], acc[c]);
      ar = mfma16(xa, rf[s], ar);
    }
#pragma unroll
    for (int c = 0; c < 4; ++c) red[(wave * 5 + c) * 64 + lane] = acc[c];
    red[(wave * 5 + 4) * 64 + lane] = ar;
  }
  __syncthreads();
  TS(0, 2);
  if (wave < 5) {
    f32x4 v = red[wave * 64 + lane];
#pragma unroll
    for (int wv_ = 1; wv_ < 8; ++wv_) v += red[(wv_ * 5 + wave) * 64 + lane];
    const int kk = 16 * th + 4 * g;
    if (wave == 4) {
      unsigned k0 = 0, k1 = 0;
#pragma unroll
      for (int e = 0; e < 4; ++e) {
        const unsigned u = __float_as_uint(rbf(v[e]));
        const unsigned ord = (u & 0x80000000u) ? ~u : (u | 0x80000000u);
        const unsigned key = (ord & 0xffff0000u) | (unsigned)(2047 - (row0 + 4 * g + e));
        k1 = max(k1, min(k0, key));
        k0 = max(k0, key);
      }
#pragma unroll
      for (int off = 16; off < 64; off <<= 1) {
        const unsigned o0 = __shfl_xor(k0, off), o1 = __shfl_xor(k1, off);
        k1 = max(min(k0, o0), max(k1, o1));
        k0 = max(k0, o0);
      }
      if (lane == 0) *(u32x2*)(rkeys + 2 * blockIdx.x) = (u32x2){k0, k1};
    } else if (i < 4) {
      const int hh = i & 1, j = (i >> 1) * 4 + wave;
#pragma unroll
      for (int e = 0; e < 4; ++e)
        kf[rt * 512 + (hh * 32 + kk + e) * 8 + j] = __builtin_bit_cast(u16, (__bf16)v[e]);
    } else if (i < 8) {
      const int d = 4 * (i - 4) + wave;
      const int hh = g & 1, jh = g >> 1;
      u32x2 o = {pack2(v[0], v[1]), pack2(v[2], v[3])};
      *(u32x2*)(vf + rt * (VT_STRIDE / 2) + th * 256 + (hh * 16 + d) * 8 + jh * 4) = o;
    }
  }
  TS(0, 3); TSR(0, 9);
}

static constexpr int LDS_K = 0;
static constexpr int LDS_V = 65536;
static constexpr int LDS_H = 65536 + VF_BYTES + 2048;
static constexpr int LDS_TOTAL = LDS_H + 1024;

__global__ __launch_bounds__(768) void kb_attn(const u16* __restrict__ xnf, const u16* __restrict__ wqf,
                                               const u16* __restrict__ kf, const u16* __restrict__ vf,
                                               const unsigned* __restrict__ rkeys, const u16* __restrict__ wg,
                                               const u16* __restrict__ wu, const u16* __restrict__ wd,
                                               u16* __restrict__ of, float* __restrict__ ypart,
                                               int* __restrict__ sel) {
  __shared__ __attribute__((aligned(16))) char smem[LDS_TOTAL];
  const int tid = threadIdx.x;
  const int lane = tid & 63, wave = tid >> 6;
  const int b = blockIdx.x;
  TSR(1, 8); TS(1, 0);

  if (wave < 8) {
    const int rb = (b & 7) + 8 * ((b >> 3) & 3), hg = b >> 5;
    const int rt = wave >> 2, ks4 = wave & 3;
    const int r = lane & 31, hh = lane >> 5;
    {
      u32x4 tk[8];
#pragma unroll
      for (int c = 0; c < 8; ++c) tk[c] = *(const u32x4*)((const char*)kf + c * 8192 + tid * 16);
      bf16x8 af[2][8], bfr[8];
      const int rtq = wave & 1, kq = wave >> 1;
      const u16* ap = wqf + ((size_t)(2 * hg) * 32 + 8 * kq) * 512 + lane * 8;
      const u16* bp = xnf + ((size_t)(2 * rb + rtq) * 32 + 8 * kq) * 512 + lane * 8;
#pragma unroll
      for (int s = 0; s < 8; ++s) {
        bfr[s] = *(const bf16x8*)(bp + s * 512);
        af[0][s] = *(const bf16x8*)(ap + s * 512);
        af[1][s] = *(const bf16x8*)(ap + 32 * 512 + s * 512);
      }
      __builtin_amdgcn_sched_barrier(0);
#pragma unroll
      for (int c = 0; c < 8; ++c) *(u32x4*)(smem + LDS_K + c * 8192 + tid * 16) = tk[c];
      __builtin_amdgcn_sched_barrier(0);
      f32x16 acc[2];
#pragma unroll
      for (int e = 0; e < 16; ++e) { acc[0][e] = 0.f; acc[1][e] = 0.f; }
#pragma unroll
      for (int s = 0; s < 8; ++s) {
        acc[0] = mfma32(af[0][s], bfr[s], acc[0]);
        acc[1] = mfma32(af[1][s], bfr[s], acc[1]);
      }
#pragma unroll
      for (int dt = 0; dt < 2; ++dt)
#pragma unroll
        for (int q4 = 0; q4 < 4; ++q4) {
          f32x4 p = {acc[dt][4 * q4], acc[dt][4 * q4 + 1], acc[dt][4 * q4 + 2], acc[dt][4 * q4 + 3]};
          *(f32x4*)(smem + LDS_V + ((((rtq * 4 + kq) * 2 + dt) * 4 + q4) * 64 + lane) * 16) = p;
        }
    }
    __builtin_amdgcn_sched_barrier(0);
    u32x4 tv[8], tvx;
#pragma unroll
    for (int c = 0; c < 8; ++c) tv[c] = *(const u32x4*)((const char*)vf + c * 8192 + tid * 16);
    tvx = *(const u32x4*)((const char*)vf + 65536 + (tid & 127) * 16);
    TS(1, 1);
    __syncthreads();
    TS(1, 2);
    const int hl = wave & 3;
    const int head = hg * 4 + hl;
    bf16x8 qf;
    {
      f32x4 a0 = {0.f, 0.f, 0.f, 0.f}, a1 = {0.f, 0.f, 0.f, 0.f};
#pragma unroll
      for (int k = 0; k < 4; ++k) {
        const char* p = smem + LDS_V + ((((rt * 4 + k) * 2 + (hl >> 1)) * 4 + 2 * (hl & 1)) * 64 + lane) * 16;
        a0 += *(const f32x4*)p;
        a1 += *(const f32x4*)(p + 1024);
      }
      const float SC = 0.25f * 1.44269504088896340736f;
      u32x4 pk = {pack2(rbf(a0[0]) * SC, rbf(a0[1]) * SC), pack2(rbf(a0[2]) * SC, rbf(a0[3]) * SC),
                  pack2(rbf(a1[0]) * SC, rbf(a1[1]) * SC), pack2(rbf(a1[2]) * SC, rbf(a1[3]) * SC)};
      qf = __builtin_bit_cast(bf16x8, pk);
    }
    __syncthreads();

    const unsigned lbase = (unsigned)(uintptr_t)(__attribute__((address_space(3))) char*)smem;
    float m0 = -__builtin_inff(), m1 = -__builtin_inff();
    f32x16 zero16;
#pragma unroll
    for (int e = 0; e < 16; ++e) zero16[e] = 0.f;
    {
      unsigned kq_ = lbase + LDS_K + lane * 16;
      asm volatile("" : "+v"(kq_));
      f32x16 sa = mfma32(LDSF(kq_), qf, zero16);
      f32x16 sb = mfma32(LDSF(kq_ + 1024), qf, zero16);
      bf16x8 k2 = LDSF(kq_ + 2048), k3 = LDSF(kq_ + 3072);
      __builtin_amdgcn_sched_barrier(0);
#pragma unroll
      for (int t = 0; t < 64; ++t) {
        f32x16 sc = sb;
        bf16x8 k4 = k3;
        if (t + 2 < 64) sc = mfma32(k2, qf, zero16);
        if (t + 4 < 64) k4 = LDSF(kq_ + (t + 4) * 1024);
        if (t & 1) {
#pragma unroll
          for (int e = 0; e < 16; e += 2) m1 = fmaxf(fmaxf(m1, sa[e]), sa[e + 1]);
        } else {
#pragma unroll
          for (int e = 0; e < 16; e += 2) m0 = fmaxf(fmaxf(m0, sa[e]), sa[e + 1]);
        }
        sa = sb; sb = sc; k2 = k3; k3 = k4;
        if (t >= 32 && t < 40) *(u32x4*)(smem + LDS_V + (t - 32) * 8192 + tid * 16) = tv[t - 32];
        if (t == 40) *(u32x4*)(smem + LDS_V + 65536 + (tid & 127) * 16) = tvx;
        __builtin_amdgcn_sched_group_barrier(0x008, 1, 0);
        __builtin_amdgcn_sched_group_barrier(0x100, 1, 0);
        __builtin_amdgcn_sched_group_barrier(0x002, 8, 0);
        __builtin_amdgcn_sched_barrier(0);
      }
    }
    float m = fmaxf(m0, m1);
    m = fmaxf(m, __shfl_xor(m, 32));
    TS(1, 3);
    __syncthreads();
    TS(1, 4);

    f32x16 negm;
#pragma unroll
    for (int e = 0; e < 16; ++e) negm[e] = -m;
    f32x16 o;
#pragma unroll
    for (int e = 0; e < 16; ++e) o[e] = 0.f;
    unsigned ka_ = lbase + LDS_K + lane * 16;
    unsigned va_ = lbase + LDS_V + ((r < 16) ? (hh * 16 + r) * 16 : (r == 16 ? 1024 : 1040));
    unsigned vb_ = lbase + LDS_V + ((r < 16) ? (hh * 16 + r) * 16 + 512 : (r == 16 ? 1024 : 1040));
    f32x16 s = mfma32(LDSF(ka_), qf, negm);
    bf16x8 kn = LDSF(ka_ + 1024);
    bf16x8 v0 = LDSF(va_);
    bf16x8 v1 = LDSF(vb_);
    bf16x8 v1p = v1;
    u32x4 pbp = {0u, 0u, 0u, 0u};
#pragma unroll 1
    for (int tb = 0; tb < 8; ++tb) {
      asm volatile("" : "+v"(ka_), "+v"(va_), "+v"(vb_));
#pragma unroll
      for (int u = 0; u < 8; ++u) {
        const f32x16 sn = mfma32(kn, qf, negm);
        const bf16x8 k2 = LDSF(ka_ + (u + 2) * 1024);
        const bf16x8 v0n = LDSF(va_ + (u + 1) * VT_STRIDE);
        const bf16x8 v1n = LDSF(vb_ + (u + 1) * VT_STRIDE);
        float p[16];
#pragma unroll
        for (int e = 0; e < 6; ++e) p[e] = __builtin_amdgcn_exp2f(s[e]);
        o = mfma32(v1p, __builtin_bit_cast(bf16x8, pbp), o);
#pragma unroll
        for (int e = 6; e < 10; ++e) p[e] = __builtin_amdgcn_exp2f(s[e]);
        u32x4 pa = {pack2(p[0], p[1]), pack2(p[2], p[3]), pack2(p[4], p[5]), pack2(p[6], p[7])};
        o = mfma32(v0, __builtin_bit_cast(bf16x8, pa), o);
#pragma unroll
        for (int e = 10; e < 16; ++e) p[e] = __builtin_amdgcn_exp2f(s[e]);
        pbp = (u32x4){pack2(p[8], p[9]), pack2(p[10], p[11]), pack2(p[12], p[13]), pack2(p[14], p[15])};
        v1p = v1;
        s = sn; kn = k2; v0 = v0n; v1 = v1n;
        __builtin_amdgcn_sched_group_barrier(0x008, 1, 0);
        __builtin_amdgcn_sched_group_barrier(0x100, 3, 0);
        __builtin_amdgcn_sched_group_barrier(0x402, 6, 0);
        __builtin_amdgcn_sched_group_barrier(0x008, 1, 0);
        __builtin_amdgcn_sched_group_barrier(0x402, 8, 0);
        __builtin_amdgcn_sched_group_barrier(0x008, 1, 0);
        __builtin_amdgcn_sched_group_barrier(0x402, 10, 0);
        __builtin_amdgcn_sched_barrier(0);
      }
      ka_ += 8 * 1024; va_ += 8 * VT_STRIDE; vb_ += 8 * VT_STRIDE;
    }
    o = mfma32(v1p, __builtin_bit_cast(bf16x8, pbp), o);
    TS(1, 5);
    const float il = 1.0f / __shfl(o[8], r);
    u32x4 w0 = {pack2(o[0] * il, o[1] * il), pack2(o[2] * il, o[3] * il), pack2(o[4] * il, o[5] * il),
                pack2(o[6] * il, o[7] * il)};
    *(u32x4*)(of + ((size_t)((2 * rb + rt) * 32 + head) * 64 + lane) * 8) = w0;
  } else {
    if (b >= NHELP) return;
    __builtin_amdgcn_s_setprio(3);
    const int hj = wave - 8;
    const int g = lane >> 4, i = lane & 15;
    const int f0 = b * 16;
    const u32x4 kv = *(const u32x4*)(rkeys + lane * 4);
    unsigned k0 = max(kv[0], kv[2]), k1 = max(min(kv[0], kv[2]), max(kv[1], kv[3]));
#pragma unroll
    for (int off = 1; off < 64; off <<= 1) {
      const unsigned o0 = __shfl_xor(k0, off), o1 = __shfl_xor(k1, off);
      k1 = max(min(k0, o0), max(k1, o1));
      k0 = max(k0, o0);
    }
    const int i0 = 2047 - (int)(k0 & 0x7ffu), i1 = 2047 - (int)(k1 & 0x7ffu);
    if (b == 0 && hj == 0 && lane == 0) {
      const float v0 = __uint_as_float((k0 & 0x80000000u) ? (k0 & 0x7fff0000u) : (~k0 & 0xffff0000u));
      const float v1 = __uint_as_float((k1 & 0x80000000u) ? (k1 & 0x7fff0000u) : (~k1 & 0xffff0000u));
      sel[0] = i0; sel[1] = i1;
      ((float*)sel)[2] = rbf(1.0f / (1.0f + __expf(-v0)));
      ((float*)sel)[3] = rbf(1.0f / (1.0f + __expf(-v1)));
    }
    const int srow = (i == 0) ? i0 : i1;
    const u16* xp = xnf + ((size_t)((srow >> 5) * 32 + 8 * hj + (g >> 1)) * 64 + (g & 1) * 32 + (srow & 31)) * 8;
    bf16x8 xa[4];
#pragma unroll
    for (int s = 0; s < 4; ++s) xa[s] = *(const bf16x8*)(xp + (size_t)s * 1024);
    TS(1, 3);
    __syncthreads();
    __syncthreads();
    __builtin_amdgcn_sched_barrier(0);
    unsigned wa[4][8];
    const u16* wp = ((i < 8) ? wg : wu) + f0 + 2 * (i & 7) + (size_t)(128 * hj + 8 * g) * F_;
#pragma unroll
    for (int s = 0; s < 4; ++s)
#pragma unroll
      for (int j = 0; j < 8; ++j) wa[s][j] = *(const unsigned*)(wp + (size_t)(32 * s + j) * F_);
    unsigned wdv[16];
#pragma unroll
    for (int f = 0; f < 16; ++f) wdv[f] = *(const unsigned*)(wd + (size_t)(f0 + f) * D_ + hj * 128 + 2 * lane);
    f32x4 a0 = {0.f, 0.f, 0.f, 0.f}, a1 = {0.f, 0.f, 0.f, 0.f};
#pragma unroll
    for (int s = 0; s < 4; ++s) {
      a0 = mfma16(xa[s], frag_lo(wa[s]), a0);
      a1 = mfma16(xa[s], frag_hi(wa[s]), a1);
    }
    if (lane < 16) *(f32x4*)(smem + LDS_H + (hj * 16 + i) * 16) = (f32x4){a0[0], a0[1], a1[0], a1[1]};
    TS(1, 4);
    __syncthreads();
    __builtin_amdgcn_sched_barrier(0);
    const char* pp = smem + LDS_H + i * 16;
    const f32x4 t = (*(const f32x4*)pp + *(const f32x4*)(pp + 256)) + (*(const f32x4*)(pp + 512) + *(const f32x4*)(pp + 768));
    float y[2][2] = {{0.f, 0.f}, {0.f, 0.f}};
    int hb[4];
#pragma unroll
    for (int e = 0; e < 4; ++e) {
      const float gt = rbf(t[e]);
      const float u = __shfl_xor(gt, 8);
      hb[e] = __builtin_bit_cast(int, rbf(rbf(gt / (1.0f + __expf(-gt))) * u));
    }
#pragma unroll
    for (int f = 0; f < 16; ++f) {
      const float w0 = bf2f(wdv[f] & 0xffffu), w1 = bfhi2f(wdv[f]);
#pragma unroll
      for (int tk = 0; tk < 2; ++tk) {
        const float h = __builtin_bit_cast(float, __builtin_amdgcn_readlane(hb[(f & 1) * 2 + tk], f >> 1));
        y[tk][0] = __builtin_fmaf(h, w0, y[tk][0]);
        y[tk][1] = __builtin_fmaf(h, w1, y[tk][1]);
      }
    }
#pragma unroll
    for (int tk = 0; tk < 2; ++tk)
      *(float2*)(ypart + (size_t)b * 1024 + tk * 512 + hj * 128 + 2 * lane) = make_float2(y[tk][0], y[tk][1]);
  }
  TS(1, 6); TSR(1, 9);
}

static constexpr int CH_STRIDE = 1056;
static constexpr int LDS_Y = 64 * CH_STRIDE;
__global__ __launch_bounds__(512) void kc_out(const u16* __restrict__ x, const u16* __restrict__ of,
                                              const u16* __restrict__ wof, const float2* __restrict__ stats,
                                              const float* __restrict__ ypart, const int* __restrict__ sel,
                                              u16* __restrict__ out) {
  __shared__ __attribute__((aligned(16))) char smem[LDS_Y + 2 * 8 * 256];
  const int tid = threadIdx.x;
  const int lane = tid & 63, wave = tid >> 6;
  TSR(2, 8); TS(2, 0);
  const int bb = blockIdx.x;
  const int rb = (bb & 7) + 8 * ((bb >> 3) & 3), cg = bb >> 5;
  const int rt = wave & 1, ks4 = wave >> 1;
  const int idx0 = sel[0], idx1 = sel[1];
  const float rs0 = ((const float*)sel)[2], rs1 = ((const float*)sel)[3];
  bf16x8 af[2][8], bfr[8];
  const u16* ap = wof + ((size_t)(2 * cg) * 32 + 8 * ks4) * 512 + lane * 8;
  const u16* bp = of + ((size_t)(2 * rb + rt) * 32 + 8 * ks4) * 512 + lane * 8;
#pragma unroll
  for (int s = 0; s < 8; ++s) {
    bfr[s] = *(const bf16x8*)(bp + s * 512);
    af[0][s] = *(const bf16x8*)(ap + s * 512);
    af[1][s] = *(const bf16x8*)(ap + 32 * 512 + s * 512);
  }
  const int r64 = tid >> 3, oct = tid & 7;
  const int row = rb * 64 + r64;
  const size_t off = (size_t)row * D_ + cg * 64 + oct * 8;
  const u32x4 xv = *(const u32x4*)(x + off);
  const float2 st = stats[row];
  f32x4 yv[2][3];
#pragma unroll
  for (int tk = 0; tk < 2; ++tk) {
    const int idx = tk ? idx1 : idx0;
#pragma unroll
    for (int q = 0; q < 3; ++q) yv[tk][q] = (f32x4){0.f, 0.f, 0.f, 0.f};
    if ((unsigned)(idx - rb * 64) < 64u) {
      const int pg = tid >> 4;
      const float* yp = ypart + (size_t)(pg * 2 + tk) * 512 + cg * 64 + (tid & 15) * 4;
      yv[tk][0] = *(const f32x4*)yp;
      yv[tk][1] = *(const f32x4*)(yp + 32 * 1024);
      yv[tk][2] = *(const f32x4*)(yp + (pg + 64 < NHELP ? 64 * 1024 : 0));
    }
  }
  f32x16 acc[2];
#pragma unroll
  for (int e = 0; e < 16; ++e) { acc[0][e] = 0.f; acc[1][e] = 0.f; }
#pragma unroll
  for (int s = 0; s < 8; ++s) {
    acc[0] = mfma32(af[0][s], bfr[s], acc[0]);
    acc[1] = mfma32(af[1][s], bfr[s], acc[1]);
  }
  TS(2, 1);
#pragma unroll
  for (int ct = 0; ct < 2; ++ct)
#pragma unroll
    for (int q4 = 0; q4 < 4; ++q4) {
      f32x4 p = {acc[ct][4 * q4], acc[ct][4 * q4 + 1], acc[ct][4 * q4 + 2], acc[ct][4 * q4 + 3]};
      *(f32x4*)(smem + (((rt * 4 + ks4) * 2 + ct) * 4 + q4) * CH_STRIDE + lane * 16) = p;
    }
#pragma unroll
  for (int tk = 0; tk < 2; ++tk) {
    const int idx = tk ? idx1 : idx0;
    if ((unsigned)(idx - rb * 64) < 64u) {
      f32x4 y = yv[tk][0] + yv[tk][1];
      if ((tid >> 4) + 64 < NHELP) y += yv[tk][2];
#pragma unroll
      for (int e = 0; e < 4; ++e) {
        y[e] += __shfl_xor(y[e], 16);
        y[e] += __shfl_xor(y[e], 32);
      }
      if (lane < 16) *(f32x4*)(smem + LDS_Y + (tk * 8 + wave) * 256 + lane * 16) = y;
    }
  }
  __syncthreads();
  TS(2, 2);
  f32x4 lo = {0.f, 0.f, 0.f, 0.f}, hi = {0.f, 0.f, 0.f, 0.f};
#pragma unroll
  for (int k = 0; k < 4; ++k) {
    const char* p = smem + (((r64 >> 5) * 4 + k) * 8 + oct) * CH_STRIDE + (r64 & 31) * 16;
    lo += *(const f32x4*)p;
    hi += *(const f32x4*)(p + 512);
  }
  float upd[8];
#pragma unroll
  for (int e = 0; e < 8; ++e) upd[e] = 0.f;
  if (row == idx0 || row == idx1) {
    const int tk = (row == idx0) ? 0 : 1;
    f32x4 ya = {0.f, 0.f, 0.f, 0.f}, yb = {0.f, 0.f, 0.f, 0.f};
#pragma unroll
    for (int p = 0; p < 8; ++p) {
      const char* yp = smem + LDS_Y + (tk * 8 + p) * 256 + oct * 32;
      ya += *(const f32x4*)yp;
      yb += *(const f32x4*)(yp + 16);
    }
    const float rs = tk ? rs1 : rs0;
#pragma unroll
    for (int e = 0; e < 4; ++e) { upd[e] = rbf(rbf(ya[e]) * rs); upd[4 + e] = rbf(rbf(yb[e]) * rs); }
  }
  const float inv = 1.0f / st.y;
  float res[8];
#pragma unroll
  for (int e = 0; e < 4; ++e) {
    const float x0 = bf2f(xv[e] & 0xffffu), x1 = bfhi2f(xv[e]);
    const float a0 = rbf(e < 2 ? lo[2 * e] : hi[2 * e - 4]);
    const float a1 = rbf(e < 2 ? lo[2 * e + 1] : hi[2 * e - 3]);
    res[2 * e] = rbf(x0 + a0) + rbf(ln_elem(x0, st.x, st.y, inv) + upd[2 * e]);
    res[2 * e + 1] = rbf(x1 + a1) + rbf(ln_elem(x1, st.x, st.y, inv) + upd[2 * e + 1]);
  }
  u32x4 o = {pack2(res[0], res[1]), pack2(res[2], res[3]), pack2(res[4], res[5]), pack2(res[6], res[7])};
  *(u32x4*)(out + off) = o;
  TS(2, 3); TSR(2, 9);
}

extern "C" void kernel_launch(void* const* d_in, const int* in_sizes, int n_in, void* d_out, int out_size,
                              void* d_ws, size_t ws_size, hipStream_t stream) {
  const u16* x = (const u16*)d_in[0];
  const u16* wq = (const u16*)d_in[1];
  const u16* wk = (const u16*)d_in[2];
  const u16* wv = (const u16*)d_in[3];
  const u16* wo = (const u16*)d_in[4];
  const u16* wr = (const u16*)d_in[6];
  const u16* wg = (const u16*)d_in[7];
  const u16* wu = (const u16*)d_in[8];
  const u16* wd = (const u16*)d_in[9];
  char* ws = (char*)d_ws;
  u16* xnf = (u16*)(ws + OFF_XNF);
  u16* of = (u16*)(ws + OFF_OF);
  u16* wqf = (u16*)(ws + OFF_WQF);
  u16* wof = (u16*)(ws + OFF_WOF);
  u16* kf = (u16*)(ws + OFF_KF);
  u16* vf = (u16*)(ws + OFF_VF);
  unsigned* rkeys = (unsigned*)(ws + OFF_R);
  float2* stats = (float2*)(ws + OFF_STATS);
  float* ypart = (float*)(ws + OFF_Y);
  int* sel = (int*)(ws + OFF_SEL);
  u16* out = (u16*)d_out;

  ka_prep<<<192, 512, 0, stream>>>(x, wq, wk, wv, wo, wr, xnf, wqf, wof, kf, vf, rkeys, stats);
  kb_attn<<<256, 768, 0, stream>>>(xnf, wqf, kf, vf, rkeys, wg, wu, wd, of, ypart, sel);
  kc_out<<<256, 512, 0, stream>>>(x, of, wof, stats, ypart, sel, out);
}
```

```cpp
#include <hip/hip_runtime.h>
#include <hip/hip_bf16.h>

typedef unsigned short u16;
typedef __attribute__((ext_vector_type(8))) __bf16 bf16x8;
typedef __attribute__((ext_vector_type(2))) __bf16 bf16x2;
typedef __attribute__((ext_vector_type(2))) float f32x2;
typedef __attribute__((ext_vector_type(4))) float f32x4;
typedef __attribute__((ext_vector_type(16))) float f32x16;
typedef __attribute__((ext_vector_type(4))) unsigned u32x4;
typedef __attribute__((ext_vector_type(2))) unsigned u32x2;

#define DI __device__ __forceinline__
#define TS(k, n)
#define TSR(k, n)

static constexpr int S_ = 2048;
static constexpr int D_ = 512;
static constexpr int F_ = 1376;

#define LDSF(a) (*(const __attribute__((address_space(3))) bf16x8*)(uintptr_t)(a))
DI float bf2f(unsigned u) { return __uint_as_float(u << 16); }
DI float bfhi2f(unsigned u) { return __uint_as_float(u & 0xffff0000u); }
DI float rbf(float f) { return (float)(__bf16)f; }
DI unsigned pack2(float lo, float hi) {
  f32x2 p = {lo, hi};
  bf16x2 q = __builtin_convertvector(p, bf16x2);
  return __builtin_bit_cast(unsigned, q);
}
DI f32x4 mfma16(bf16x8 a, bf16x8 b, f32x4 c) { return __builtin_amdgcn_mfma_f32_16x16x32_bf16(a, b, c, 0, 0, 0); }
DI f32x16 mfma32(bf16x8 a, bf16x8 b, f32x16 c) { return __builtin_amdgcn_mfma_f32_32x32x16_bf16(a, b, c, 0, 0, 0); }
DI unsigned perm_lo(unsigned hi_src, unsigned lo_src) { return __builtin_amdgcn_perm(hi_src, lo_src, 0x05040100u); }
DI unsigned perm_hi(unsigned hi_src, unsigned lo_src) { return __builtin_amdgcn_perm(hi_src, lo_src, 0x07060302u); }

DI bf16x8 frag_lo(const unsigned* w) {
  u32x4 r = {perm_lo(w[1], w[0]), perm_lo(w[3], w[2]), perm_lo(w[5], w[4]), perm_lo(w[7], w[6])};
  return __builtin_bit_cast(bf16x8, r);
}
DI bf16x8 frag_hi(const unsigned* w) {
  u32x4 r = {perm_hi(w[1], w[0]), perm_hi(w[3], w[2]), perm_hi(w[5], w[4]), perm_hi(w[7], w[6])};
  return __builtin_bit_cast(bf16x8, r);
}
DI float ln_elem(float xv, float mb, float den, float inv) {
  const float d = rbf(xv - mb);
  float q = d * inv;
  const float rem = __builtin_fmaf(-q, den, d);
  q = __builtin_fmaf(rem, inv, q);
  return rbf(q);
}

static constexpr int VT_STRIDE = 1056;
static constexpr int VF_BYTES = 64 * VT_STRIDE;
static constexpr size_t OFF_XNF = 0;
static constexpr size_t OFF_OF = OFF_XNF + (size_t)S_ * D_ * 2;
static constexpr size_t OFF_WQF = OFF_OF + (size_t)S_ * D_ * 2;
static constexpr size_t OFF_WOF = OFF_WQF + (size_t)D_ * D_ * 2;
static constexpr size_t OFF_KF = OFF_WOF + (size_t)D_ * D_ * 2;
static constexpr size_t OFF_VF = OFF_KF + 65536;
static constexpr size_t OFF_R = OFF_VF + VF_BYTES;
static constexpr size_t OFF_STATS = OFF_R + S_ * 4;
static constexpr int NHELP = 86;
static constexpr size_t OFF_Y = OFF_STATS + S_ * 8;
static constexpr size_t OFF_SEL = OFF_Y + NHELP * 1024 * 4;

static constexpr int XN_STRIDE = 1040;
__global__ __launch_bounds__(512) void ka_prep(const u16* __restrict__ x, const u16* __restrict__ wq,
                                               const u16* __restrict__ wk, const u16* __restrict__ wv,
                                               const u16* __restrict__ wo, const u16* __restrict__ wr,
                                               u16* __restrict__ xnf, u16* __restrict__ wqf, u16* __restrict__ wof,
                                               u16* __restrict__ kf, u16* __restrict__ vf,
                                               unsigned* __restrict__ rkeys, float2* __restrict__ stats) {
  __shared__ __attribute__((aligned(16))) char smem[16 * XN_STRIDE + 8 * 5 * 64 * 16];
  const int tid = threadIdx.x;
  const int lane = tid & 63, wave = tid >> 6;
  TSR(0, 8); TS(0, 0);
  if (blockIdx.x >= 128) {
    const int pb = blockIdx.x - 128;
    const bool iso = pb >= 32;
    const int cp = tid & 255, ko = (pb & 31) * 2 + (tid >> 8);
    const int ks = ko >> 1, hh = ko & 1;
    const u16* W = (iso ? wo : wq) + 2 * cp;
    unsigned w[8];
#pragma unroll
    for (int j = 0; j < 8; ++j) {
      const int row = 16 * ks + (iso ? (8 * (j >> 2) + 4 * hh + (j & 3)) : (8 * hh + j));
      w[j] = *(const unsigned*)(W + row * D_);
    }
    u16* dst = (iso ? wof : wqf) + (((cp >> 4) * 32 + ks) * 64 + hh * 32 + ((2 * cp) & 31)) * 8;
    *(bf16x8*)dst = frag_lo(w);
    *(bf16x8*)(dst + 8) = frag_hi(w);
    TS(0, 3); TSR(0, 9);
    return;
  }
  const int row0 = blockIdx.x * 16;
  const int rt = blockIdx.x >> 1, th = blockIdx.x & 1;
  const int g = lane >> 4, i = lane & 15;
  const int k0 = wave * 64 + 8 * g;
  u32x4 xr[2];
#pragma unroll
  for (int rr = 0; rr < 2; ++rr) xr[rr] = *(const u32x4*)(x + (size_t)(row0 + 2 * wave + rr) * D_ + 8 * lane);
  const u16* wbase = ((i < 4) ? (wk + 4 * i) : (i < 8 ? wv + 4 * (i - 4) : wk)) + k0 * 16;
  u32x2 w[2][8];
#pragma unroll
  for (int s = 0; s < 2; ++s)
#pragma unroll
    for (int j = 0; j < 8; ++j) w[s][j] = *(const u32x2*)(wbase + (32 * s + j) * 16);
  bf16x8 rf[2];
#pragma unroll
  for (int s = 0; s < 2; ++s) rf[s] = *(const bf16x8*)(wr + k0 + 32 * s);

  float s1[2], s2[2];
#pragma unroll
  for (int rr = 0; rr < 2; ++rr) {
    float a = 0.f, q = 0.f;
#pragma unroll
    for (int e = 0; e < 4; ++e) {
      const float lo = bf2f(xr[rr][e] & 0xffffu), hi = bfhi2f(xr[rr][e]);
      a += lo + hi;
      q = __builtin_fmaf(lo, lo, q);
      q = __builtin_fmaf(hi, hi, q);
    }
    s1[rr] = a; s2[rr] = q;
  }
#pragma unroll
  for (int off = 32; off >= 1; off >>= 1)
#pragma unroll
    for (int rr = 0; rr < 2; ++rr) {
      s1[rr] += __shfl_xor(s1[rr], off);
      s2[rr] += __shfl_xor(s2[rr], off);
    }
#pragma unroll
  for (int rr = 0; rr < 2; ++rr) {
    const float mean = s1[rr] * (1.0f / D_);
    float var = s2[rr] * (1.0f / D_) - mean * mean;
    var = fmaxf(var, 0.f);
    const float mb = rbf(mean);
    const float den = rbf(__builtin_sqrtf(rbf(rbf(var) + 1e-5f)));
    const float inv = 1.0f / den;
    if (lane == rr) stats[row0 + 2 * wave + rr] = make_float2(mb, den);
    u32x4 o;
#pragma unroll
    for (int e = 0; e < 4; ++e)
      o[e] = pack2(ln_elem(bf2f(xr[rr][e] & 0xffffu), mb, den, inv), ln_elem(bfhi2f(xr[rr][e]), mb, den, inv));
    *(u32x4*)(smem + (2 * wave + rr) * XN_STRIDE + lane * 16) = o;
  }
  if (tid < 2 && th == 0) {
    const unsigned cv = tid ? 0u : 0x3f803f80u;
    u32x4 c4 = {cv, cv, cv, cv};
    *(u32x4*)((char*)vf + rt * VT_STRIDE + 1024 + tid * 16) = c4;
  }
  TS(0, 1);
  __syncthreads();
  {
    const int r = lane & 15, hh = (lane >> 4) & 1, kp = lane >> 5;
#pragma unroll
    for (int s = 0; s < 2; ++s) {
      const int ks = 4 * wave + 2 * s + kp;
      const u32x4 v = *(const u32x4*)(smem + r * XN_STRIDE + ks * 32 + hh * 16);
      *(u32x4*)(xnf + ((size_t)(rt * 32 + ks) * 64 + hh * 32 + th * 16 + r) * 8) = v;
    }
  }
  f32x4* red = (f32x4*)(smem + 16 * XN_STRIDE);
  {
    f32x4 acc[4], ar;
    ar = (f32x4){0.f, 0.f, 0.f, 0.f};
#pragma unroll
    for (int c = 0; c < 4; ++c) acc[c] = (f32x4){0.f, 0.f, 0.f, 0.f};
#pragma unroll
    for (int s = 0; s < 2; ++s) {
      unsigned wx[8], wy[8];
#pragma unroll
      for (int j = 0; j < 8; ++j) { wx[j] = w[s][j][0]; wy[j] = w[s][j][1]; }
      bf16x8 b[4] = {frag_lo(wx), frag_hi(wx), frag_lo(wy), frag_hi(wy)};
      const bf16x8 xa = *(const bf16x8*)(smem + i * XN_STRIDE + (k0 + 32 * s) * 2);
#pragma unroll
      for (int c = 0; c < 4; ++c) acc[c] = mfma16(xa, b[c], acc[c]);
      ar = mfma16(xa, rf[s], ar);
    }
#pragma unroll
    for (int c = 0; c < 4; ++c) red[(wave * 5 + c) * 64 + lane] = acc[c];
    red[(wave * 5 + 4) * 64 + lane] = ar;
  }
  __syncthreads();
  TS(0, 2);
  if (wave < 5) {
    f32x4 v = red[wave * 64 + lane];
#pragma unroll
    for (int wv_ = 1; wv_ < 8; ++wv_) v += red[(wv_ * 5 + wave) * 64 + lane];
    const int kk = 16 * th + 4 * g;
    if (wave == 4) {
      unsigned k0 = 0, k1 = 0;
#pragma unroll
      for (int e = 0; e < 4; ++e) {
        const unsigned u = __float_as_uint(rbf(v[e]));
        const unsigned ord = (u & 0x80000000u) ? ~u : (u | 0x80000000u);
        const unsigned key = (ord & 0xffff0000u) | (unsigned)(2047 - (row0 + 4 * g + e));
        k1 = max(k1, min(k0, key));
        k0 = max(k0, key);
      }
#pragma unroll
      for (int off = 16; off < 64; off <<= 1) {
        const unsigned o0 = __shfl_xor(k0, off), o1 = __shfl_xor(k1, off);
        k1 = max(min(k0, o0), max(k1, o1));
        k0 = max(k0, o0);
      }
      if (lane == 0) *(u32x2*)(rkeys + 2 * blockIdx.x) = (u32x2){k0, k1};
    } else if (i < 4) {
      const int hh = i & 1, j = (i >> 1) * 4 + wave;
#pragma unroll
      for (int e = 0; e < 4; ++e)
        kf[rt * 512 + (hh * 32 + kk + e) * 8 + j] = __builtin_bit_cast(u16, (__bf16)v[e]);
    } else if (i < 8) {
      const int d = 4 * (i - 4) + wave;
      const int hh = g & 1, jh = g >> 1;
      u32x2 o = {pack2(v[0], v[1]), pack2(v[2], v[3])};
      *(u32x2*)(vf + rt * (VT_STRIDE / 2) + th * 256 + (hh * 16 + d) * 8 + jh * 4) = o;
    }
  }
  TS(0, 3); TSR(0, 9);
}

static constexpr int LDS_K = 0;
static constexpr int LDS_V = 65536;
static constexpr int LDS_H = 65536 + VF_BYTES + 2048;
static constexpr int LDS_TOTAL = LDS_H + 1024;

__global__ __launch_bounds__(768) void kb_attn(const u16* __restrict__ xnf, const u16* __restrict__ wqf,
                                               const u16* __restrict__ kf, const u16* __restrict__ vf,
                                               const unsigned* __restrict__ rkeys, const u16* __restrict__ wg,
                                               const u16* __restrict__ wu, const u16* __restrict__ wd,
                                               u16* __restrict__ of, float* __restrict__ ypart,
                                               int* __restrict__ sel) {
  __shared__ __attribute__((aligned(16))) char smem[LDS_TOTAL];
  const int tid = threadIdx.x;
  const int lane = tid & 63, wave = tid >> 6;
  const int b = blockIdx.x;
  TSR(1, 8); TS(1, 0);

  if (wave < 8) {
    const int rb = (b & 7) + 8 * ((b >> 3) & 3), hg = b >> 5;
    const int rt = wave >> 2, ks4 = wave & 3;
    const int r = lane & 31, hh = lane >> 5;
    {
      u32x4 tk[8];
#pragma unroll
      for (int c = 0; c < 8; ++c) tk[c] = *(const u32x4*)((const char*)kf + c * 8192 + tid * 16);
      bf16x8 af[2][8], bfr[8];
      const int rtq = wave & 1, kq = wave >> 1;
      const u16* ap = wqf + ((size_t)(2 * hg) * 32 + 8 * kq) * 512 + lane * 8;
      const u16* bp = xnf + ((size_t)(2 * rb + rtq) * 32 + 8 * kq) * 512 + lane * 8;
#pragma unroll
      for (int s = 0; s < 8; ++s) {
        bfr[s] = *(const bf16x8*)(bp + s * 512);
        af[0][s] = *(const bf16x8*)(ap + s * 512);
        af[1][s] = *(const bf16x8*)(ap + 32 * 512 + s * 512);
      }
      __builtin_amdgcn_sched_barrier(0);
#pragma unroll
      for (int c = 0; c < 8; ++c) *(u32x4*)(smem + LDS_K + c * 8192 + tid * 16) = tk[c];
      __builtin_amdgcn_sched_barrier(0);
      f32x16 acc[2];
#pragma unroll
      for (int e = 0; e < 16; ++e) { acc[0][e] = 0.f; acc[1][e] = 0.f; }
#pragma unroll
      for (int s = 0; s < 8; ++s) {
        acc[0] = mfma32(af[0][s], bfr[s], acc[0]);
        acc[1] = mfma32(af[1][s], bfr[s], acc[1]);
      }
#pragma unroll
      for (int dt = 0; dt < 2; ++dt)
#pragma unroll
        for (int q4 = 0; q4 < 4; ++q4) {
          f32x4 p = {acc[dt][4 * q4], acc[dt][4 * q4 + 1], acc[dt][4 * q4 + 2], acc[dt][4 * q4 + 3]};
          *(f32x4*)(smem + LDS_V + ((((rtq * 4 + kq) * 2 + dt) * 4 + q4) * 64 + lane) * 16) = p;
        }
    }
    __builtin_amdgcn_sched_barrier(0);
    u32x4 tv[8], tvx;
#pragma unroll
    for (int c = 0; c < 8; ++c) tv[c] = *(const u32x4*)((const char*)vf + c * 8192 + tid * 16);
    tvx = *(const u32x4*)((const char*)vf + 65536 + (tid & 127) * 16);
    TS(1, 1);
    __syncthreads();
    TS(1, 2);
    const int hl = wave & 3;
    const int head = hg * 4 + hl;
    bf16x8 qf;
    {
      f32x4 a0 = {0.f, 0.f, 0.f, 0.f}, a1 = {0.f, 0.f, 0.f, 0.f};
#pragma unroll
      for (int k = 0; k < 4; ++k) {
        const char* p = smem + LDS_V + ((((rt * 4 + k) * 2 + (hl >> 1)) * 4 + 2 * (hl & 1)) * 64 + lane) * 16;
        a0 += *(const f32x4*)p;
        a1 += *(const f32x4*)(p + 1024);
      }
      const float SC = 0.25f * 1.44269504088896340736f;
      u32x4 pk = {pack2(rbf(a0[0]) * SC, rbf(a0[1]) * SC), pack2(rbf(a0[2]) * SC, rbf(a0[3]) * SC),
                  pack2(rbf(a1[0]) * SC, rbf(a1[1]) * SC), pack2(rbf(a1[2]) * SC, rbf(a1[3]) * SC)};
      qf = __builtin_bit_cast(bf16x8, pk);
    }
    __syncthreads();

    const unsigned lbase = (unsigned)(uintptr_t)(__attribute__((address_space(3))) char*)smem;
    float m0 = -__builtin_inff(), m1 = -__builtin_inff();
    f32x16 zero16;
#pragma unroll
    for (int e = 0; e < 16; ++e) zero16[e] = 0.f;
    {
      unsigned kq_ = lbase + LDS_K + lane * 16;
      asm volatile("" : "+v"(kq_));
      f32x16 sa = mfma32(LDSF(kq_), qf, zero16);
      f32x16 sb = mfma32(LDSF(kq_ + 1024), qf, zero16);
      bf16x8 k2 = LDSF(kq_ + 2048), k3 = LDSF(kq_ + 3072);
      __builtin_amdgcn_sched_barrier(0);
#pragma unroll
      for (int t = 0; t < 64; ++t) {
        if (t == 0) __builtin_amdgcn_s_setprio(2);
        if (t == 36) __builtin_amdgcn_s_setprio(1);
        if (t == 58) __builtin_amdgcn_s_setprio(0);
        f32x16 sc = sb;
        bf16x8 k4 = k3;
        if (t + 2 < 64) sc = mfma32(k2, qf, zero16);
        if (t + 4 < 64) k4 = LDSF(kq_ + (t + 4) * 1024);
        if (t & 1) {
#pragma unroll
          for (int e = 0; e < 16; e += 2) m1 = fmaxf(fmaxf(m1, sa[e]), sa[e + 1]);
        } else {
#pragma unroll
          for (int e = 0; e < 16; e += 2) m0 = fmaxf(fmaxf(m0, sa[e]), sa[e + 1]);
        }
        sa = sb; sb = sc; k2 = k3; k3 = k4;
        if (t >= 32 && t < 40) *(u32x4*)(smem + LDS_V + (t - 32) * 8192 + tid * 16) = tv[t - 32];
        if (t == 40) *(u32x4*)(smem + LDS_V + 65536 + (tid & 127) * 16) = tvx;
        __builtin_amdgcn_sched_group_barrier(0x008, 1, 0);
        __builtin_amdgcn_sched_group_barrier(0x100, 1, 0);
        __builtin_amdgcn_sched_group_barrier(0x002, 8, 0);
        __builtin_amdgcn_sched_barrier(0);
      }
    }
    float m = fmaxf(m0, m1);
    m = fmaxf(m, __shfl_xor(m, 32));
    TS(1, 3);
    __syncthreads();
    TS(1, 4);

    f32x16 negm;
#pragma unroll
    for (int e = 0; e < 16; ++e) negm[e] = -m;
    f32x16 o;
#pragma unroll
    for (int e = 0; e < 16; ++e) o[e] = 0.f;
    unsigned ka_ = lbase + LDS_K + lane * 16;
    unsigned va_ = lbase + LDS_V + ((r < 16) ? (hh * 16 + r) * 16 : (r == 16 ? 1024 : 1040));
    unsigned vb_ = lbase + LDS_V + ((r < 16) ? (hh * 16 + r) * 16 + 512 : (r == 16 ? 1024 : 1040));
    f32x16 s = mfma32(LDSF(ka_), qf, negm);
    bf16x8 kn = LDSF(ka_ + 1024);
    bf16x8 v0 = LDSF(va_);
    bf16x8 v1 = LDSF(vb_);
    bf16x8 v1p = v1;
    u32x4 pbp = {0u, 0u, 0u, 0u};
#pragma unroll 1
    for (int tb = 0; tb < 8; ++tb) {
      asm volatile("" : "+v"(ka_), "+v"(va_), "+v"(vb_));
      if (tb == 0) __builtin_amdgcn_s_setprio(2);
      if (tb == 4) __builtin_amdgcn_s_setprio(1);
      if (tb == 7) __builtin_amdgcn_s_setprio(0);
#pragma unroll
      for (int u = 0; u < 8; ++u) {
        const f32x16 sn = mfma32(kn, qf, negm);
        const bf16x8 k2 = LDSF(ka_ + (u + 2) * 1024);
        const bf16x8 v0n = LDSF(va_ + (u + 1) * VT_STRIDE);
        const bf16x8 v1n = LDSF(vb_ + (u + 1) * VT_STRIDE);
        float p[16];
#pragma unroll
        for (int e = 0; e < 6; ++e) p[e] = __builtin_amdgcn_exp2f(s[e]);
        o = mfma32(v1p, __builtin_bit_cast(bf16x8, pbp), o);
#pragma unroll
        for (int e = 6; e < 10; ++e) p[e] = __builtin_amdgcn_exp2f(s[e]);
        u32x4 pa = {pack2(p[0], p[1]), pack2(p[2], p[3]), pack2(p[4], p[5]), pack2(p[6], p[7])};
        o = mfma32(v0, __builtin_bit_cast(bf16x8, pa), o);
#pragma unroll
        for (int e = 10; e < 16; ++e) p[e] = __builtin_amdgcn_exp2f(s[e]);
        pbp = (u32x4){pack2(p[8], p[9]), pack2(p[10], p[11]), pack2(p[12], p[13]), pack2(p[14], p[15])};
        v1p = v1;
        s = sn; kn = k2; v0 = v0n; v1 = v1n;
        __builtin_amdgcn_sched_group_barrier(0x008, 1, 0);
        __builtin_amdgcn_sched_group_barrier(0x100, 3, 0);
        __builtin_amdgcn_sched_group_barrier(0x402, 6, 0);
        __builtin_amdgcn_sched_group_barrier(0x008, 1, 0);
        __builtin_amdgcn_sched_group_barrier(0x402, 8, 0);
        __builtin_amdgcn_sched_group_barrier(0x008, 1, 0);
        __builtin_amdgcn_sched_group_barrier(0x402, 10, 0);
        __builtin_amdgcn_sched_barrier(0);
      }
      ka_ += 8 * 1024; va_ += 8 * VT_STRIDE; vb_ += 8 * VT_STRIDE;
    }
    o = mfma32(v1p, __builtin_bit_cast(bf16x8, pbp), o);
    TS(1, 5);
    const float il = 1.0f / __shfl(o[8], r);
    u32x4 w0 = {pack2(o[0] * il, o[1] * il), pack2(o[2] * il, o[3] * il), pack2(o[4] * il, o[5] * il),
                pack2(o[6] * il, o[7] * il)};
    *(u32x4*)(of + ((size_t)((2 * rb + rt) * 32 + head) * 64 + lane) * 8) = w0;
  } else {
    if (b >= NHELP) return;
    __builtin_amdgcn_s_setprio(3);
    const int hj = wave - 8;
    const int g = lane >> 4, i = lane & 15;
    const int f0 = b * 16;
    const u32x4 kv = *(const u32x4*)(rkeys + lane * 4);
    unsigned k0 = max(kv[0], kv[2]), k1 = max(min(kv[0], kv[2]), max(kv[1], kv[3]));
#pragma unroll
    for (int off = 1; off < 64; off <<= 1) {
      const unsigned o0 = __shfl_xor(k0, off), o1 = __shfl_xor(k1, off);
      k1 = max(min(k0, o0), max(k1, o1));
      k0 = max(k0, o0);
    }
    const int i0 = 2047 - (int)(k0 & 0x7ffu), i1 = 2047 - (int)(k1 & 0x7ffu);
    if (b == 0 && hj == 0 && lane == 0) {
      const float v0 = __uint_as_float((k0 & 0x80000000u) ? (k0 & 0x7fff0000u) : (~k0 & 0xffff0000u));
      const float v1 = __uint_as_float((k1 & 0x80000000u) ? (k1 & 0x7fff0000u) : (~k1 & 0xffff0000u));
      sel[0] = i0; sel[1] = i1;
      ((float*)sel)[2] = rbf(1.0f / (1.0f + __expf(-v0)));
      ((float*)sel)[3] = rbf(1.0f / (1.0f + __expf(-v1)));
    }
    const int srow = (i == 0) ? i0 : i1;
    const u16* xp = xnf + ((size_t)((srow >> 5) * 32 + 8 * hj + (g >> 1)) * 64 + (g & 1) * 32 + (srow & 31)) * 8;
    bf16x8 xa[4];
#pragma unroll
    for (int s = 0; s < 4; ++s) xa[s] = *(const bf16x8*)(xp + (size_t)s * 1024);
    TS(1, 3);
    __syncthreads();
    __syncthreads();
    __builtin_amdgcn_sched_barrier(0);
    unsigned wa[4][8];
    const u16* wp = ((i < 8) ? wg : wu) + f0 + 2 * (i & 7) + (size_t)(128 * hj + 8 * g) * F_;
#pragma unroll
    for (int s = 0; s < 4; ++s)
#pragma unroll
      for (int j = 0; j < 8; ++j) wa[s][j] = *(const unsigned*)(wp + (size_t)(32 * s + j) * F_);
    unsigned wdv[16];
#pragma unroll
    for (int f = 0; f < 16; ++f) wdv[f] = *(const unsigned*)(wd + (size_t)(f0 + f) * D_ + hj * 128 + 2 * lane);
    f32x4 a0 = {0.f, 0.f, 0.f, 0.f}, a1 = {0.f, 0.f, 0.f, 0.f};
#pragma unroll
    for (int s = 0; s < 4; ++s) {
      a0 = mfma16(xa[s], frag_lo(wa[s]), a0);
      a1 = mfma16(xa[s], frag_hi(wa[s]), a1);
    }
    if (lane < 16) *(f32x4*)(smem + LDS_H + (hj * 16 + i) * 16) = (f32x4){a0[0], a0[1], a1[0], a1[1]};
    TS(1, 4);
    __syncthreads();
    __builtin_amdgcn_sched_barrier(0);
    const char* pp = smem + LDS_H + i * 16;
    const f32x4 t = (*(const f32x4*)pp + *(const f32x4*)(pp + 256)) + (*(const f32x4*)(pp + 512) + *(const f32x4*)(pp + 768));
    float y[2][2] = {{0.f, 0.f}, {0.f, 0.f}};
    int hb[4];
#pragma unroll
    for (int e = 0; e < 4; ++e) {
      const float gt = rbf(t[e]);
      const float u = __shfl_xor(gt, 8);
      hb[e] = __builtin_bit_cast(int, rbf(rbf(gt / (1.0f + __expf(-gt))) * u));
    }
#pragma unroll
    for (int f = 0; f < 16; ++f) {
      const float w0 = bf2f(wdv[f] & 0xffffu), w1 = bfhi2f(wdv[f]);
#pragma unroll
      for (int tk = 0; tk < 2; ++tk) {
        const float h = __builtin_bit_cast(float, __builtin_amdgcn_readlane(hb[(f & 1) * 2 + tk], f >> 1));
        y[tk][0] = __builtin_fmaf(h, w0, y[tk][0]);
        y[tk][1] = __builtin_fmaf(h, w1, y[tk][1]);
      }
    }
#pragma unroll
    for (int tk = 0; tk < 2; ++tk)
      *(float2*)(ypart + (size_t)b * 1024 + tk * 512 + hj * 128 + 2 * lane) = make_float2(y[tk][0], y[tk][1]);
  }
  TS(1, 6); TSR(1, 9);
}

static constexpr int CH_STRIDE = 1056;
static constexpr int LDS_Y = 64 * CH_STRIDE;
__global__ __launch_bounds__(512) void kc_out(const u16* __restrict__ x, const u16* __restrict__ of,
                                              const u16* __restrict__ wof, const float2* __restrict__ stats,
                                              const float* __restrict__ ypart, const int* __restrict__ sel,
                                              u16* __restrict__ out) {
  __shared__ __attribute__((aligned(16))) char smem[LDS_Y + 2 * 8 * 256];
  const int tid = threadIdx.x;
  const int lane = tid & 63, wave = tid >> 6;
  TSR(2, 8); TS(2, 0);
  const int bb = blockIdx.x;
  const int rb = (bb & 7) + 8 * ((bb >> 3) & 3), cg = bb >> 5;
  const int rt = wave & 1, ks4 = wave >> 1;
  const int idx0 = sel[0], idx1 = sel[1];
  const float rs0 = ((const float*)sel)[2], rs1 = ((const float*)sel)[3];
  bf16x8 af[2][8], bfr[8];
  const u16* ap = wof + ((size_t)(2 * cg) * 32 + 8 * ks4) * 512 + lane * 8;
  const u16* bp = of + ((size_t)(2 * rb + rt) * 32 + 8 * ks4) * 512 + lane * 8;
#pragma unroll
  for (int s = 0; s < 8; ++s) {
    bfr[s] = *(const bf16x8*)(bp + s * 512);
    af[0][s] = *(const bf16x8*)(ap + s * 512);
    af[1][s] = *(const bf16x8*)(ap + 32 * 512 + s * 512);
  }
  const int r64 = tid >> 3, oct = tid & 7;
  const int row = rb * 64 + r64;
  const size_t off = (size_t)row * D_ + cg * 64 + oct * 8;
  const u32x4 xv = *(const u32x4*)(x + off);
  const float2 st = stats[row];
  f32x4 yv[2][3];
#pragma unroll
  for (int tk = 0; tk < 2; ++tk) {
    const int idx = tk ? idx1 : idx0;
#pragma unroll
    for (int q = 0; q < 3; ++q) yv[tk][q] = (f32x4){0.f, 0.f, 0.f, 0.f};
    if ((unsigned)(idx - rb * 64) < 64u) {
      const int pg = tid >> 4;
      const float* yp = ypart + (size_t)(pg * 2 + tk) * 512 + cg * 64 + (tid & 15) * 4;
      yv[tk][0] = *(const f32x4*)yp;
      yv[tk][1] = *(const f32x4*)(yp + 32 * 1024);
      yv[tk][2] = *(const f32x4*)(yp + (pg + 64 < NHELP ? 64 * 1024 : 0));
    }
  }
  f32x16 acc[2];
#pragma unroll
  for (int e = 0; e < 16; ++e) { acc[0][e] = 0.f; acc[1][e] = 0.f; }
#pragma unroll
  for (int s = 0; s < 8; ++s) {
    acc[0] = mfma32(af[0][s], bfr[s], acc[0]);
    acc[1] = mfma32(af[1][s], bfr[s], acc[1]);
  }
  TS(2, 1);
#pragma unroll
  for (int ct = 0; ct < 2; ++ct)
#pragma unroll
    for (int q4 = 0; q4 < 4; ++q4) {
      f32x4 p = {acc[ct][4 * q4], acc[ct][4 * q4 + 1], acc[ct][4 * q4 + 2], acc[ct][4 * q4 + 3]};
      *(f32x4*)(smem + (((rt * 4 + ks4) * 2 + ct) * 4 + q4) * CH_STRIDE + lane * 16) = p;
    }
#pragma unroll
  for (int tk = 0; tk < 2; ++tk) {
    const int idx = tk ? idx1 : idx0;
    if ((unsigned)(idx - rb * 64) < 64u) {
      f32x4 y = yv[tk][0] + yv[tk][1];
      if ((tid >> 4) + 64 < NHELP) y += yv[tk][2];
#pragma unroll
      for (int e = 0; e < 4; ++e) {
        y[e] += __shfl_xor(y[e], 16);
        y[e] += __shfl_xor(y[e], 32);
      }
      if (lane < 16) *(f32x4*)(smem + LDS_Y + (tk * 8 + wave) * 256 + lane * 16) = y;
    }
  }
  __syncthreads();
  TS(2, 2);
  f32x4 lo = {0.f, 0.f, 0.f, 0.f}, hi = {0.f, 0.f, 0.f, 0.f};
#pragma unroll
  for (int k = 0; k < 4; ++k) {
    const char* p = smem + (((r64 >> 5) * 4 + k) * 8 + oct) * CH_STRIDE + (r64 & 31) * 16;
    lo += *(const f32x4*)p;
    hi += *(const f32x4*)(p + 512);
  }
  float upd[8];
#pragma unroll
  for (int e = 0; e < 8; ++e) upd[e] = 0.f;
  if (row == idx0 || row == idx1) {
    const int tk = (row == idx0) ? 0 : 1;
    f32x4 ya = {0.f, 0.f, 0.f, 0.f}, yb = {0.f, 0.f, 0.f, 0.f};
#pragma unroll
    for (int p = 0; p < 8; ++p) {
      const char* yp = smem + LDS_Y + (tk * 8 + p) * 256 + oct * 32;
      ya += *(const f32x4*)yp;
      yb += *(const f32x4*)(yp + 16);
    }
    const float rs = tk ? rs1 : rs0;
#pragma unroll
    for (int e = 0; e < 4; ++e) { upd[e] = rbf(rbf(ya[e]) * rs); upd[4 + e] = rbf(rbf(yb[e]) * rs); }
  }
  const float inv = 1.0f / st.y;
  float res[8];
#pragma unroll
  for (int e = 0; e < 4; ++e) {
    const float x0 = bf2f(xv[e] & 0xffffu), x1 = bfhi2f(xv[e]);
    const float a0 = rbf(e < 2 ? lo[2 * e] : hi[2 * e - 4]);
    const float a1 = rbf(e < 2 ? lo[2 * e + 1] : hi[2 * e - 3]);
    res[2 * e] = rbf(x0 + a0) + rbf(ln_elem(x0, st.x, st.y, inv) + upd[2 * e]);
    res[2 * e + 1] = rbf(x1 + a1) + rbf(ln_elem(x1, st.x, st.y, inv) + upd[2 * e + 1]);
  }
  u32x4 o = {pack2(res[0], res[1]), pack2(res[2], res[3]), pack2(res[4], res[5]), pack2(res[6], res[7])};
  *(u32x4*)(out + off) = o;
  TS(2, 3); TSR(2, 9);
}

extern "C" void kernel_launch(void* const* d_in, const int* in_sizes, int n_in, void* d_out, int out_size,
                              void* d_ws, size_t ws_size, hipStream_t stream) {
  const u16* x = (const u16*)d_in[0];
  const u16* wq = (const u16*)d_in[1];
  const u16* wk = (const u16*)d_in[2];
  const u16* wv = (const u16*)d_in[3];
  const u16* wo = (const u16*)d_in[4];
  const u16* wr = (const u16*)d_in[6];
  const u16* wg = (const u16*)d_in[7];
  const u16* wu = (const u16*)d_in[8];
  const u16* wd = (const u16*)d_in[9];
  char* ws = (char*)d_ws;
  u16* xnf = (u16*)(ws + OFF_XNF);
  u16* of = (u16*)(ws + OFF_OF);
  u16* wqf = (u16*)(ws + OFF_WQF);
  u16* wof = (u16*)(ws + OFF_WOF);
  u16* kf = (u16*)(ws + OFF_KF);
  u16* vf = (u16*)(ws + OFF_VF);
  unsigned* rkeys = (unsigned*)(ws + OFF_R);
  float2* stats = (float2*)(ws + OFF_STATS);
  float* ypart = (float*)(ws + OFF_Y);
  int* sel = (int*)(ws + OFF_SEL);
  u16* out = (u16*)d_out;

  ka_prep<<<192, 512, 0, stream>>>(x, wq, wk, wv, wo, wr, xnf, wqf, wof, kf, vf, rkeys, stats);
  kb_attn<<<256, 768, 0, stream>>>(xnf, wqf, kf, vf, rkeys, wg, wu, wd, of, ypart, sel);
  kc_out<<<256, 512, 0, stream>>>(x, of, wof, stats, ypart, sel, out);
}
```

```cpp
#include <hip/hip_runtime.h>
#include <hip/hip_bf16.h>

typedef unsigned short u16;
typedef __attribute__((ext_vector_type(8))) __bf16 bf16x8;
typedef __attribute__((ext_vector_type(2))) __bf16 bf16x2;
typedef __attribute__((ext_vector_type(2))) float f32x2;
typedef __attribute__((ext_vector_type(4))) float f32x4;
typedef __attribute__((ext_vector_type(16))) float f32x16;
typedef __attribute__((ext_vector_type(4))) unsigned u32x4;
typedef __attribute__((ext_vector_type(2))) unsigned u32x2;

#define DI __device__ __forceinline__
#define TS(k, n)
#define TSR(k, n)

static constexpr int S_ = 2048;
static constexpr int D_ = 512;
static constexpr int F_ = 1376;

#define LDSF(a) (*(const __attribute__((address_space(3))) bf16x8*)(uintptr_t)(a))
DI float bf2f(unsigned u) { return __uint_as_float(u << 16); }
DI float bfhi2f(unsigned u) { return __uint_as_float(u & 0xffff0000u); }
DI float rbf(float f) { return (float)(__bf16)f; }
DI unsigned pack2(float lo, float hi) {
  f32x2 p = {lo, hi};
  bf16x2 q = __builtin_convertvector(p, bf16x2);
  return __builtin_bit_cast(unsigned, q);
}
DI f32x4 mfma16(bf16x8 a, bf16x8 b, f32x4 c) { return __builtin_amdgcn_mfma_f32_16x16x32_bf16(a, b, c, 0, 0, 0); }
DI f32x16 mfma32(bf16x8 a, bf16x8 b, f32x16 c) { return __builtin_amdgcn_mfma_f32_32x32x16_bf16(a, b, c, 0, 0, 0); }
DI unsigned perm_lo(unsigned hi_src, unsigned lo_src) { return __builtin_amdgcn_perm(hi_src, lo_src, 0x05040100u); }
DI unsigned perm_hi(unsigned hi_src, unsigned lo_src) { return __builtin_amdgcn_perm(hi_src, lo_src, 0x07060302u); }

DI bf16x8 frag_lo(const unsigned* w) {
  u32x4 r = {perm_lo(w[1], w[0]), perm_lo(w[3], w[2]), perm_lo(w[5], w[4]), perm_lo(w[7], w[6])};
  return __builtin_bit_cast(bf16x8, r);
}
DI bf16x8 frag_hi(const unsigned* w) {
  u32x4 r = {perm_hi(w[1], w[0]), perm_hi(w[3], w[2]), perm_hi(w[5], w[4]), perm_hi(w[7], w[6])};
  return __builtin_bit_cast(bf16x8, r);
}
DI float ln_elem(float xv, float mb, float den, float inv) {
  const float d = rbf(xv - mb);
  float q = d * inv;
  const float rem = __builtin_fmaf(-q, den, d);
  q = __builtin_fmaf(rem, inv, q);
  return rbf(q);
}

static constexpr int VT_STRIDE = 1056;
static constexpr int VF_BYTES = 64 * VT_STRIDE;
static constexpr size_t OFF_XNF = 0;
static constexpr size_t OFF_OF = OFF_XNF + (size_t)S_ * D_ * 2;
static constexpr size_t OFF_WQF = OFF_OF + (size_t)S_ * D_ * 2;
static constexpr size_t OFF_WOF = OFF_WQF + (size_t)D_ * D_ * 2;
static constexpr size_t OFF_KF = OFF_WOF + (size_t)D_ * D_ * 2;
static constexpr size_t OFF_VF = OFF_KF + 65536;
static constexpr size_t OFF_R = OFF_VF + VF_BYTES;
static constexpr size_t OFF_STATS = OFF_R + S_ * 4;
static constexpr int NHELP = 86;
static constexpr size_t OFF_Y = OFF_STATS + S_ * 8;
static constexpr size_t OFF_SEL = OFF_Y + NHELP * 1024 * 4;

static constexpr int XN_STRIDE = 1040;
__global__ __launch_bounds__(512) void ka_prep(const u16* __restrict__ x, const u16* __restrict__ wq,
                                               const u16* __restrict__ wk, const u16* __restrict__ wv,
                                               const u16* __restrict__ wo, const u16* __restrict__ wr,
                                               u16* __restrict__ xnf, u16* __restrict__ wqf, u16* __restrict__ wof,
                                               u16* __restrict__ kf, u16* __restrict__ vf,
                                               unsigned* __restrict__ rkeys, float2* __restrict__ stats) {
  __shared__ __attribute__((aligned(16))) char smem[16 * XN_STRIDE + 8 * 5 * 64 * 16];
  const int tid = threadIdx.x;
  const int lane = tid & 63, wave = tid >> 6;
  TSR(0, 8); TS(0, 0);
  if (blockIdx.x >= 128) {
    const int pb = blockIdx.x - 128;
    const bool iso = pb >= 32;
    const int cp = tid & 255, ko = (pb & 31) * 2 + (tid >> 8);
    const int ks = ko >> 1, hh = ko & 1;
    const u16* W = (iso ? wo : wq) + 2 * cp;
    unsigned w[8];
#pragma unroll
    for (int j = 0; j < 8; ++j) {
      const int row = 16 * ks + (iso ? (8 * (j >> 2) + 4 * hh + (j & 3)) : (8 * hh + j));
      w[j] = *(const unsigned*)(W + row * D_);
    }
    u16* dst = (iso ? wof : wqf) + (((cp >> 4) * 32 + ks) * 64 + hh * 32 + ((2 * cp) & 31)) * 8;
    *(bf16x8*)dst = frag_lo(w);
    *(bf16x8*)(dst + 8) = frag_hi(w);
    TS(0, 3); TSR(0, 9);
    return;
  }
  const int row0 = blockIdx.x * 16;
  const int rt = blockIdx.x >> 1, th = blockIdx.x & 1;
  const int g = lane >> 4, i = lane & 15;
  const int k0 = wave * 64 + 8 * g;
  u32x4 xr[2];
#pragma unroll
  for (int rr = 0; rr < 2; ++rr) xr[rr] = *(const u32x4*)(x + (size_t)(row0 + 2 * wave + rr) * D_ + 8 * lane);
  const u16* wbase = ((i < 4) ? (wk + 4 * i) : (i < 8 ? wv + 4 * (i - 4) : wk)) + k0 * 16;
  u32x2 w[2][8];
#pragma unroll
  for (int s = 0; s < 2; ++s)
#pragma unroll
    for (int j = 0; j < 8; ++j) w[s][j] = *(const u32x2*)(wbase + (32 * s + j) * 16);
  bf16x8 rf[2];
#pragma unroll
  for (int s = 0; s < 2; ++s) rf[s] = *(const bf16x8*)(wr + k0 + 32 * s);

  float s1[2], s2[2];
#pragma unroll
  for (int rr = 0; rr < 2; ++rr) {
    float a = 0.f, q = 0.f;
#pragma unroll
    for (int e = 0; e < 4; ++e) {
      const float lo = bf2f(xr[rr][e] & 0xffffu), hi = bfhi2f(xr[rr][e]);
      a += lo + hi;
      q = __builtin_fmaf(lo, lo, q);
      q = __builtin_fmaf(hi, hi, q);
    }
    s1[rr] = a; s2[rr] = q;
  }
#pragma unroll
  for (int off = 32; off >= 1; off >>= 1)
#pragma unroll
    for (int rr = 0; rr < 2; ++rr) {
      s1[rr] += __shfl_xor(s1[rr], off);
      s2[rr] += __shfl_xor(s2[rr], off);
    }
#pragma unroll
  for (int rr = 0; rr < 2; ++rr) {
    const float mean = s1[rr] * (1.0f / D_);
    float var = s2[rr] * (1.0f / D_) - mean * mean;
    var = fmaxf(var, 0.f);
    const float mb = rbf(mean);
    const float den = rbf(__builtin_sqrtf(rbf(rbf(var) + 1e-5f)));
    const float inv = 1.0f / den;
    if (lane == rr) stats[row0 + 2 * wave + rr] = make_float2(mb, den);
    u32x4 o;
#pragma unroll
    for (int e = 0; e < 4; ++e)
      o[e] = pack2(ln_elem(bf2f(xr[rr][e] & 0xffffu), mb, den, inv), ln_elem(bfhi2f(xr[rr][e]), mb, den, inv));
    *(u32x4*)(smem + (2 * wave + rr) * XN_STRIDE + lane * 16) = o;
  }
  if (tid < 2 && th == 0) {
    const unsigned cv = tid ? 0u : 0x3f803f80u;
    u32x4 c4 = {cv, cv, cv, cv};
    *(u32x4*)((char*)vf + rt * VT_STRIDE + 1024 + tid * 16) = c4;
  }
  TS(0, 1);
  __syncthreads();
  {
    const int r = lane & 15, hh = (lane >> 4) & 1, kp = lane >> 5;
#pragma unroll
    for (int s = 0; s < 2; ++s) {
      const int ks = 4 * wave + 2 * s + kp;
      const u32x4 v = *(const u32x4*)(smem + r * XN_STRIDE + ks * 32 + hh * 16);
      *(u32x4*)(xnf + ((size_t)(rt * 32 + ks) * 64 + hh * 32 + th * 16 + r) * 8) = v;
    }
  }
  f32x4* red = (f32x4*)(smem + 16 * XN_STRIDE);
  {
    f32x4 acc[4], ar;
    ar = (f32x4){0.f, 0.f, 0.f, 0.f};
#pragma unroll
    for (int c = 0; c < 4; ++c) acc[c] = (f32x4){0.f, 0.f, 0.f, 0.f};
#pragma unroll
    for (int s = 0; s < 2; ++s) {
      unsigned wx[8], wy[8];
#pragma unroll
      for (int j = 0; j < 8; ++j) { wx[j] = w[s][j][0]; wy[j] = w[s][j][1]; }
      bf16x8 b[4] = {frag_lo(wx), frag_hi(wx), frag_lo(wy), frag_hi(wy)};
      const bf16x8 xa = *(const bf16x8*)(smem + i * XN_STRIDE + (k0 + 32 * s) * 2);
#pragma unroll
      for (int c = 0; c < 4; ++c) acc[c] = mfma16(xa, b[c], acc[c]);
      ar = mfma16(xa, rf[s], ar);
    }
#pragma unroll
    for (int c = 0; c < 4; ++c) red[(wave * 5 + c) * 64 + lane] = acc[c];
    red[(wave * 5 + 4) * 64 + lane] = ar;
  }
  __syncthreads();
  TS(0, 2);
  if (wave < 5) {
    f32x4 v = red[wave * 64 + lane];
#pragma unroll
    for (int wv_ = 1; wv_ < 8; ++wv_) v += red[(wv_ * 5 + wave) * 64 + lane];
    const int kk = 16 * th + 4 * g;
    if (wave == 4) {
      unsigned k0 = 0, k1 = 0;
#pragma unroll
      for (int e = 0; e < 4; ++e) {
        const unsigned u = __float_as_uint(rbf(v[e]));
        const unsigned ord = (u & 0x80000000u) ? ~u : (u | 0x80000000u);
        const unsigned key = (ord & 0xffff0000u) | (unsigned)(2047 - (row0 + 4 * g + e));
        k1 = max(k1, min(k0, key));
        k0 = max(k0, key);
      }
#pragma unroll
      for (int off = 16; off < 64; off <<= 1) {
        const unsigned o0 = __shfl_xor(k0, off), o1 = __shfl_xor(k1, off);
        k1 = max(min(k0, o0), max(k1, o1));
        k0 = max(k0, o0);
      }
      if (lane == 0) *(u32x2*)(rkeys + 2 * blockIdx.x) = (u32x2){k0, k1};
    } else if (i < 4) {
      const int hh = i & 1, j = (i >> 1) * 4 + wave;
#pragma unroll
      for (int e = 0; e < 4; ++e)
        kf[rt * 512 + (hh * 32 + kk + e) * 8 + j] = __builtin_bit_cast(u16, (__bf16)v[e]);
    } else if (i < 8) {
      const int d = 4 * (i - 4) + wave;
      const int hh = g & 1, jh = g >> 1;
      u32x2 o = {pack2(v[0], v[1]), pack2(v[2], v[3])};
      *(u32x2*)(vf + rt * (VT_STRIDE / 2) + th * 256 + (hh * 16 + d) * 8 + jh * 4) = o;
    }
  }
  TS(0, 3); TSR(0, 9);
}

#define P1A1 32
#define P1B1 52
#define P1A2 61
#define P2A1 5
#define P2B1 7
#define P2A2U 6
#define PRIO_IF(a, b, p) asm volatile("s_cmp_lg_u32 %0, %1\n\ts_cbranch_scc1 1f\n\ts_setprio " #p "\n1:" :: "s"(a), "s"(b) : "scc")
static constexpr int LDS_K = 0;
static constexpr int LDS_V = 65536;
static constexpr int LDS_H = 65536 + VF_BYTES + 2048;
static constexpr int LDS_F = LDS_H + 1024;
static constexpr int LDS_TOTAL = LDS_F + 64;

DI void glds16(const char* gsrc, unsigned lds_dst) {
  unsigned keep;
  asm volatile("s_mov_b32 %0, m0\n\ts_mov_b32 m0, %2\n\ts_nop 0\n\tglobal_load_lds_dwordx4 %1, off\n\ts_mov_b32 m0, %0"
               : "=&s"(keep) : "v"(gsrc), "s"(lds_dst) : "memory");
}
DI void stage_v(const char* vsrc, unsigned vdst, int hj) {
#pragma unroll
  for (int c = 0; c < 16; ++c) glds16(vsrc + c * 4096, vdst + c * 4096);
  if (hj < 2) glds16(vsrc + 65536, vdst + 65536);
}

__global__ __launch_bounds__(768) void kb_attn(const u16* __restrict__ xnf, const u16* __restrict__ wqf,
                                               const u16* __restrict__ kf, const u16* __restrict__ vf,
                                               const unsigned* __restrict__ rkeys, const u16* __restrict__ wg,
                                               const u16* __restrict__ wu, const u16* __restrict__ wd,
                                               u16* __restrict__ of, float* __restrict__ ypart,
                                               int* __restrict__ sel) {
  __shared__ __attribute__((aligned(16))) char smem[LDS_TOTAL];
  const int tid = threadIdx.x;
  const int lane = tid & 63, wave = tid >> 6;
  const int b = blockIdx.x;
  unsigned* flags = (unsigned*)(smem + LDS_F);
  TSR(1, 8); TS(1, 0);

  if (wave < 8) {
    const int rb = (b & 7) + 8 * ((b >> 3) & 3), hg = b >> 5;
    const int rt = wave >> 2, ks4 = wave & 3;
    const int r = lane & 31, hh = lane >> 5;
    {
      bf16x8 af[2][8], bfr[8];
      const int rtq = wave & 1, kq = wave >> 1;
      const u16* ap = wqf + ((size_t)(2 * hg) * 32 + 8 * kq) * 512 + lane * 8;
      const u16* bp = xnf + ((size_t)(2 * rb + rtq) * 32 + 8 * kq) * 512 + lane * 8;
#pragma unroll
      for (int s = 0; s < 8; ++s) {
        bfr[s] = *(const bf16x8*)(bp + s * 512);
        af[0][s] = *(const bf16x8*)(ap + s * 512);
        af[1][s] = *(const bf16x8*)(ap + 32 * 512 + s * 512);
      }
      f32x16 acc[2];
#pragma unroll
      for (int e = 0; e < 16; ++e) { acc[0][e] = 0.f; acc[1][e] = 0.f; }
#pragma unroll
      for (int s = 0; s < 8; ++s) {
        acc[0] = mfma32(af[0][s], bfr[s], acc[0]);
        acc[1] = mfma32(af[1][s], bfr[s], acc[1]);
      }
#pragma unroll
      for (int dt = 0; dt < 2; ++dt)
#pragma unroll
        for (int q4 = 0; q4 < 4; ++q4) {
          f32x4 p = {acc[dt][4 * q4], acc[dt][4 * q4 + 1], acc[dt][4 * q4 + 2], acc[dt][4 * q4 + 3]};
          *(f32x4*)(smem + LDS_V + ((((rtq * 4 + kq) * 2 + dt) * 4 + q4) * 64 + lane) * 16) = p;
        }
    }
    __builtin_amdgcn_sched_barrier(0);
    TS(1, 1);
    __syncthreads();
    TS(1, 2);
    const int hl = wave & 3;
    const int head = hg * 4 + hl;
    bf16x8 qf;
    {
      f32x4 a0 = {0.f, 0.f, 0.f, 0.f}, a1 = {0.f, 0.f, 0.f, 0.f};
#pragma unroll
      for (int k = 0; k < 4; ++k) {
        const char* p = smem + LDS_V + ((((rt * 4 + k) * 2 + (hl >> 1)) * 4 + 2 * (hl & 1)) * 64 + lane) * 16;
        a0 += *(const f32x4*)p;
        a1 += *(const f32x4*)(p + 1024);
      }
      const float SC = 0.25f * 1.44269504088896340736f;
      u32x4 pk = {pack2(rbf(a0[0]) * SC, rbf(a0[1]) * SC), pack2(rbf(a0[2]) * SC, rbf(a0[3]) * SC),
                  pack2(rbf(a1[0]) * SC, rbf(a1[1]) * SC), pack2(rbf(a1[2]) * SC, rbf(a1[3]) * SC)};
      qf = __builtin_bit_cast(bf16x8, pk);
    }
    __syncthreads();

    const int yng = __builtin_amdgcn_readfirstlane(wave >> 2);
    const unsigned lbase = (unsigned)(uintptr_t)(__attribute__((address_space(3))) char*)smem;
    float m0 = -__builtin_inff(), m1 = -__builtin_inff();
    f32x16 zero16;
#pragma unroll
    for (int e = 0; e < 16; ++e) zero16[e] = 0.f;
    {
      unsigned kq_ = lbase + LDS_K + lane * 16;
      asm volatile("" : "+v"(kq_));
      f32x16 sa = mfma32(LDSF(kq_), qf, zero16);
      f32x16 sb = mfma32(LDSF(kq_ + 1024), qf, zero16);
      bf16x8 k2 = LDSF(kq_ + 2048), k3 = LDSF(kq_ + 3072);
      __builtin_amdgcn_sched_barrier(0);
#pragma unroll
      for (int t = 0; t < 64; ++t) {
        if (t == 0) __builtin_amdgcn_s_setprio(2);
        if (t == P1A1) PRIO_IF(yng, 0, 1);
        if (t == P1B1) PRIO_IF(yng, 1, 1);
        if (t == P1A2) PRIO_IF(yng, 0, 0);
        f32x16 sc = sb;
        bf16x8 k4 = k3;
        if (t + 2 < 64) sc = mfma32(k2, qf, zero16);
        if (t + 4 < 64) k4 = LDSF(kq_ + (t + 4) * 1024);
        if (t & 1) {
#pragma unroll
          for (int e = 0; e < 16; e += 2) m1 = fmaxf(fmaxf(m1, sa[e]), sa[e + 1]);
        } else {
#pragma unroll
          for (int e = 0; e < 16; e += 2) m0 = fmaxf(fmaxf(m0, sa[e]), sa[e + 1]);
        }
        sa = sb; sb = sc; k2 = k3; k3 = k4;
        __builtin_amdgcn_sched_group_barrier(0x008, 1, 0);
        __builtin_amdgcn_sched_group_barrier(0x100, 1, 0);
        __builtin_amdgcn_sched_group_barrier(0x002, 8, 0);
        __builtin_amdgcn_sched_barrier(0);
      }
    }
    float m = fmaxf(m0, m1);
    m = fmaxf(m, __shfl_xor(m, 32));
    TS(1, 3);
    while (__hip_atomic_load(flags, __ATOMIC_ACQUIRE, __HIP_MEMORY_SCOPE_WORKGROUP) < 4) __builtin_amdgcn_s_sleep(1);
    asm volatile("" ::: "memory");
    TS(1, 4);

    f32x16 negm;
#pragma unroll
    for (int e = 0; e < 16; ++e) negm[e] = -m;
    f32x16 o;
#pragma unroll
    for (int e = 0; e < 16; ++e) o[e] = 0.f;
    unsigned ka_ = lbase + LDS_K + lane * 16;
    unsigned va_ = lbase + LDS_V + ((r < 16) ? (hh * 16 + r) * 16 : (r == 16 ? 1024 : 1040));
    unsigned vb_ = lbase + LDS_V + ((r < 16) ? (hh * 16 + r) * 16 + 512 : (r == 16 ? 1024 : 1040));
    f32x16 s = mfma32(LDSF(ka_), qf, negm);
    bf16x8 kn = LDSF(ka_ + 1024);
    bf16x8 v0 = LDSF(va_);
    bf16x8 v1 = LDSF(vb_);
    bf16x8 v1p = v1;
    u32x4 pbp = {0u, 0u, 0u, 0u};
    const int thr1 = P2A1 + (P2B1 - P2A1) * yng, thr2 = 7 + 92 * yng;
    __builtin_amdgcn_s_setprio(2);
#pragma unroll 1
    for (int tb = 0; tb < 8; ++tb) {
      asm volatile("" : "+v"(ka_), "+v"(va_), "+v"(vb_));
      PRIO_IF(tb, thr1, 1);
#pragma unroll
      for (int u = 0; u < 8; ++u) {
        if (u == P2A2U) PRIO_IF(tb, thr2, 0);
        const f32x16 sn = mfma32(kn, qf, negm);
        const bf16x8 k2 = LDSF(ka_ + (u + 2) * 1024);
        const bf16x8 v0n = LDSF(va_ + (u + 1) * VT_STRIDE);
        const bf16x8 v1n = LDSF(vb_ + (u + 1) * VT_STRIDE);
        float p[16];
#pragma unroll
        for (int e = 0; e < 6; ++e) p[e] = __builtin_amdgcn_exp2f(s[e]);
        o = mfma32(v1p, __builtin_bit_cast(bf16x8, pbp), o);
#pragma unroll
        for (int e = 6; e < 10; ++e) p[e] = __builtin_amdgcn_exp2f(s[e]);
        u32x4 pa = {pack2(p[0], p[1]), pack2(p[2], p[3]), pack2(p[4], p[5]), pack2(p[6], p[7])};
        o = mfma32(v0, __builtin_bit_cast(bf16x8, pa), o);
#pragma unroll
        for (int e = 10; e < 16; ++e) p[e] = __builtin_amdgcn_exp2f(s[e]);
        pbp = (u32x4){pack2(p[8], p[9]), pack2(p[10], p[11]), pack2(p[12], p[13]), pack2(p[14], p[15])};
        v1p = v1;
        s = sn; kn = k2; v0 = v0n; v1 = v1n;
        __builtin_amdgcn_sched_group_barrier(0x008, 1, 0);
        __builtin_amdgcn_sched_group_barrier(0x100, 3, 0);
        __builtin_amdgcn_sched_group_barrier(0x402, 6, 0);
        __builtin_amdgcn_sched_group_barrier(0x008, 1, 0);
        __builtin_amdgcn_sched_group_barrier(0x402, 8, 0);
        __builtin_amdgcn_sched_group_barrier(0x008, 1, 0);
        __builtin_amdgcn_sched_group_barrier(0x402, 10, 0);
        __builtin_amdgcn_sched_barrier(0);
      }
      ka_ += 8 * 1024; va_ += 8 * VT_STRIDE; vb_ += 8 * VT_STRIDE;
    }
    o = mfma32(v1p, __builtin_bit_cast(bf16x8, pbp), o);
    TS(1, 5);
    const float il = 1.0f / __shfl(o[8], r);
    u32x4 w0 = {pack2(o[0] * il, o[1] * il), pack2(o[2] * il, o[3] * il), pack2(o[4] * il, o[5] * il),
                pack2(o[6] * il, o[7] * il)};
    *(u32x4*)(of + ((size_t)((2 * rb + rt) * 32 + head) * 64 + lane) * 8) = w0;
  } else {
    __builtin_amdgcn_s_setprio(3);
    const int hj = __builtin_amdgcn_readfirstlane(wave - 8);
    const unsigned ldst = (unsigned)(uintptr_t)(__attribute__((address_space(3))) char*)smem + hj * 1024;
    {
      const char* ksrc = (const char*)kf + hj * 1024 + lane * 16;
#pragma unroll
      for (int c = 0; c < 16; ++c) glds16(ksrc + c * 4096, ldst + LDS_K + c * 4096);
    }
    if (hj == 0 && lane < 2) flags[lane] = 0u;
    const char* vsrc = (const char*)vf + hj * 1024 + lane * 16;
    if (b >= NHELP) {
      asm volatile("s_waitcnt vmcnt(0)" ::: "memory");
      __syncthreads();
      __syncthreads();
      stage_v(vsrc, ldst + LDS_V, hj);
      asm volatile("s_waitcnt vmcnt(0)" ::: "memory");
      if (lane == 0) __hip_atomic_fetch_add(flags, 1u, __ATOMIC_RELEASE, __HIP_MEMORY_SCOPE_WORKGROUP);
      return;
    }
    const int g = lane >> 4, i = lane & 15;
    const int f0 = b * 16;
    const u32x4 kv = *(const u32x4*)(rkeys + lane * 4);
    unsigned k0 = max(kv[0], kv[2]), k1 = max(min(kv[0], kv[2]), max(kv[1], kv[3]));
#pragma unroll
    for (int off = 1; off < 64; off <<= 1) {
      const unsigned o0 = __shfl_xor(k0, off), o1 = __shfl_xor(k1, off);
      k1 = max(min(k0, o0), max(k1, o1));
      k0 = max(k0, o0);
    }
    const int i0 = 2047 - (int)(k0 & 0x7ffu), i1 = 2047 - (int)(k1 & 0x7ffu);
    if (b == 0 && hj == 0 && lane == 0) {
      const float v0 = __uint_as_float((k0 & 0x80000000u) ? (k0 & 0x7fff0000u) : (~k0 & 0xffff0000u));
      const float v1 = __uint_as_float((k1 & 0x80000000u) ? (k1 & 0x7fff0000u) : (~k1 & 0xffff0000u));
      sel[0] = i0; sel[1] = i1;
      ((float*)sel)[2] = rbf(1.0f / (1.0f + __expf(-v0)));
      ((float*)sel)[3] = rbf(1.0f / (1.0f + __expf(-v1)));
    }
    const int srow = (i == 0) ? i0 : i1;
    const u16* xp = xnf + ((size_t)((srow >> 5) * 32 + 8 * hj + (g >> 1)) * 64 + (g & 1) * 32 + (srow & 31)) * 8;
    bf16x8 xa[4];
#pragma unroll
    for (int s = 0; s < 4; ++s) xa[s] = *(const bf16x8*)(xp + (size_t)s * 1024);
    TS(1, 3);
    asm volatile("s_waitcnt vmcnt(0)" ::: "memory");
    __syncthreads();
    __syncthreads();
    stage_v(vsrc, ldst + LDS_V, hj);
    asm volatile("s_waitcnt vmcnt(0)" ::: "memory");
    if (lane == 0) __hip_atomic_fetch_add(flags, 1u, __ATOMIC_RELEASE, __HIP_MEMORY_SCOPE_WORKGROUP);
    __builtin_amdgcn_sched_barrier(0);
    unsigned wa[4][8];
    const u16* wp = ((i < 8) ? wg : wu) + f0 + 2 * (i & 7) + (size_t)(128 * hj + 8 * g) * F_;
#pragma unroll
    for (int s = 0; s < 4; ++s)
#pragma unroll
      for (int j = 0; j < 8; ++j) wa[s][j] = *(const unsigned*)(wp + (size_t)(32 * s + j) * F_);
    unsigned wdv[16];
#pragma unroll
    for (int f = 0; f < 16; ++f) wdv[f] = *(const unsigned*)(wd + (size_t)(f0 + f) * D_ + hj * 128 + 2 * lane);
    f32x4 a0 = {0.f, 0.f, 0.f, 0.f}, a1 = {0.f, 0.f, 0.f, 0.f};
#pragma unroll
    for (int s = 0; s < 4; ++s) {
      a0 = mfma16(xa[s], frag_lo(wa[s]), a0);
      a1 = mfma16(xa[s], frag_hi(wa[s]), a1);
    }
    if (lane < 16) *(f32x4*)(smem + LDS_H + (hj * 16 + i) * 16) = (f32x4){a0[0], a0[1], a1[0], a1[1]};
    TS(1, 4);
    if (lane == 0) __hip_atomic_fetch_add(flags + 1, 1u, __ATOMIC_RELEASE, __HIP_MEMORY_SCOPE_WORKGROUP);
    while (__hip_atomic_load(flags + 1, __ATOMIC_ACQUIRE, __HIP_MEMORY_SCOPE_WORKGROUP) < 4) __builtin_amdgcn_s_sleep(2);
    asm volatile("" ::: "memory");
    __builtin_amdgcn_sched_barrier(0);
    const char* pp = smem + LDS_H + i * 16;
    const f32x4 t = (*(const f32x4*)pp + *(const f32x4*)(pp + 256)) + (*(const f32x4*)(pp + 512) + *(const f32x4*)(pp + 768));
    float y[2][2] = {{0.f, 0.f}, {0.f, 0.f}};
    int hb[4];
#pragma unroll
    for (int e = 0; e < 4; ++e) {
      const float gt = rbf(t[e]);
      const float u = __shfl_xor(gt, 8);
      hb[e] = __builtin_bit_cast(int, rbf(rbf(gt / (1.0f + __expf(-gt))) * u));
    }
#pragma unroll
    for (int f = 0; f < 16; ++f) {
      const float w0 = bf2f(wdv[f] & 0xffffu), w1 = bfhi2f(wdv[f]);
#pragma unroll
      for (int tk = 0; tk < 2; ++tk) {
        const float h = __builtin_bit_cast(float, __builtin_amdgcn_readlane(hb[(f & 1) * 2 + tk], f >> 1));
        y[tk][0] = __builtin_fmaf(h, w0, y[tk][0]);
        y[tk][1] = __builtin_fmaf(h, w1, y[tk][1]);
      }
    }
#pragma unroll
    for (int tk = 0; tk < 2; ++tk)
      *(float2*)(ypart + (size_t)b * 1024 + tk * 512 + hj * 128 + 2 * lane) = make_float2(y[tk][0], y[tk][1]);
  }
  TS(1, 6); TSR(1, 9);
}

static constexpr int CH_STRIDE = 1056;
static constexpr int LDS_Y = 64 * CH_STRIDE;
__global__ __launch_bounds__(512) void kc_out(const u16* __restrict__ x, const u16* __restrict__ of,
                                              const u16* __restrict__ wof, const float2* __restrict__ stats,
                                              const float* __restrict__ ypart, const int* __restrict__ sel,
                                              u16* __restrict__ out) {
  __shared__ __attribute__((aligned(16))) char smem[LDS_Y + 2 * 8 * 256];
  const int tid = threadIdx.x;
  const int lane = tid & 63, wave = tid >> 6;
  TSR(2, 8); TS(2, 0);
  const int bb = blockIdx.x;
  const int rb = (bb & 7) + 8 * ((bb >> 3) & 3), cg = bb >> 5;
  const int rt = wave & 1, ks4 = wave >> 1;
  const int idx0 = sel[0], idx1 = sel[1];
  const float rs0 = ((const float*)sel)[2], rs1 = ((const float*)sel)[3];
  bf16x8 af[2][8], bfr[8];
  const u16* ap = wof + ((size_t)(2 * cg) * 32 + 8 * ks4) * 512 + lane * 8;
  const u16* bp = of + ((size_t)(2 * rb + rt) * 32 + 8 * ks4) * 512 + lane * 8;
#pragma unroll
  for (int s = 0; s < 8; ++s) {
    bfr[s] = *(const bf16x8*)(bp + s * 512);
    af[0][s] = *(const bf16x8*)(ap + s * 512);
    af[1][s] = *(const bf16x8*)(ap + 32 * 512 + s * 512);
  }
  const int r64 = tid >> 3, oct = tid & 7;
  const int row = rb * 64 + r64;
  const size_t off = (size_t)row * D_ + cg * 64 + oct * 8;
  const u32x4 xv = *(const u32x4*)(x + off);
  const float2 st = stats[row];
  f32x4 yv[2][3];
#pragma unroll
  for (int tk = 0; tk < 2; ++tk) {
    const int idx = tk ? idx1 : idx0;
#pragma unroll
    for (int q = 0; q < 3; ++q) yv[tk][q] = (f32x4){0.f, 0.f, 0.f, 0.f};
    if ((unsigned)(idx - rb * 64) < 64u) {
      const int pg = tid >> 4;
      const float* yp = ypart + (size_t)(pg * 2 + tk) * 512 + cg * 64 + (tid & 15) * 4;
      yv[tk][0] = *(const f32x4*)yp;
      yv[tk][1] = *(const f32x4*)(yp + 32 * 1024);
      yv[tk][2] = *(const f32x4*)(yp + (pg + 64 < NHELP ? 64 * 1024 : 0));
    }
  }
  f32x16 acc[2];
#pragma unroll
  for (int e = 0; e < 16; ++e) { acc[0][e] = 0.f; acc[1][e] = 0.f; }
#pragma unroll
  for (int s = 0; s < 8; ++s) {
    acc[0] = mfma32(af[0][s], bfr[s], acc[0]);
    acc[1] = mfma32(af[1][s], bfr[s], acc[1]);
  }
  TS(2, 1);
#pragma unroll
  for (int ct = 0; ct < 2; ++ct)
#pragma unroll
    for (int q4 = 0; q4 < 4; ++q4) {
      f32x4 p = {acc[ct][4 * q4], acc[ct][4 * q4 + 1], acc[ct][4 * q4 + 2], acc[ct][4 * q4 + 3]};
      *(f32x4*)(smem + (((rt * 4 + ks4) * 2 + ct) * 4 + q4) * CH_STRIDE + lane * 16) = p;
    }
#pragma unroll
  for (int tk = 0; tk < 2; ++tk) {
    const int idx = tk ? idx1 : idx0;
    if ((unsigned)(idx - rb * 64) < 64u) {
      f32x4 y = yv[tk][0] + yv[tk][1];
      if ((tid >> 4) + 64 < NHELP) y += yv[tk][2];
#pragma unroll
      for (int e = 0; e < 4; ++e) {
        y[e] += __shfl_xor(y[e], 16);
        y[e] += __shfl_xor(y[e], 32);
      }
      if (lane < 16) *(f32x4*)(smem + LDS_Y + (tk * 8 + wave) * 256 + lane * 16) = y;
    }
  }
  __syncthreads();
  TS(2, 2);
  f32x4 lo = {0.f, 0.f, 0.f, 0.f}, hi = {0.f, 0.f, 0.f, 0.f};
#pragma unroll
  for (int k = 0; k < 4; ++k) {
    const char* p = smem + (((r64 >> 5) * 4 + k) * 8 + oct) * CH_STRIDE + (r64 & 31) * 16;
    lo += *(const f32x4*)p;
    hi += *(const f32x4*)(p + 512);
  }
  float upd[8];
#pragma unroll
  for (int e = 0; e < 8; ++e) upd[e] = 0.f;
  if (row == idx0 || row == idx1) {
    const int tk = (row == idx0) ? 0 : 1;
    f32x4 ya = {0.f, 0.f, 0.f, 0.f}, yb = {0.f, 0.f, 0.f, 0.f};
#pragma unroll
    for (int p = 0; p < 8; ++p) {
      const char* yp = smem + LDS_Y + (tk * 8 + p) * 256 + oct * 32;
      ya += *(const f32x4*)yp;
      yb += *(const f32x4*)(yp + 16);
    }
    const float rs = tk ? rs1 : rs0;
#pragma unroll
    for (int e = 0; e < 4; ++e) { upd[e] = rbf(rbf(ya[e]) * rs); upd[4 + e] = rbf(rbf(yb[e]) * rs); }
  }
  const float inv = 1.0f / st.y;
  float res[8];
#pragma unroll
  for (int e = 0; e < 4; ++e) {
    const float x0 = bf2f(xv[e] & 0xffffu), x1 = bfhi2f(xv[e]);
    const float a0 = rbf(e < 2 ? lo[2 * e] : hi[2 * e - 4]);
    const float a1 = rbf(e < 2 ? lo[2 * e + 1] : hi[2 * e - 3]);
    res[2 * e] = rbf(x0 + a0) + rbf(ln_elem(x0, st.x, st.y, inv) + upd[2 * e]);
    res[2 * e + 1] = rbf(x1 + a1) + rbf(ln_elem(x1, st.x, st.y, inv) + upd[2 * e + 1]);
  }
  u32x4 o = {pack2(res[0], res[1]), pack2(res[2], res[3]), pack2(res[4], res[5]), pack2(res[6], res[7])};
  *(u32x4*)(out + off) = o;
  TS(2, 3); TSR(2, 9);
}

extern "C" void kernel_launch(void* const* d_in, const int* in_sizes, int n_in, void* d_out, int out_size,
                              void* d_ws, size_t ws_size, hipStream_t stream) {
  const u16* x = (const u16*)d_in[0];
  const u16* wq = (const u16*)d_in[1];
  const u16* wk = (const u16*)d_in[2];
  const u16* wv = (const u16*)d_in[3];
  const u16* wo = (const u16*)d_in[4];
  const u16* wr = (const u16*)d_in[6];
  const u16* wg = (const u16*)d_in[7];
  const u16* wu = (const u16*)d_in[8];
  const u16* wd = (const u16*)d_in[9];
  char* ws = (char*)d_ws;
  u16* xnf = (u16*)(ws + OFF_XNF);
  u16* of = (u16*)(ws + OFF_OF);
  u16* wqf = (u16*)(ws + OFF_WQF);
  u16* wof = (u16*)(ws + OFF_WOF);
  u16* kf = (u16*)(ws + OFF_KF);
  u16* vf = (u16*)(ws + OFF_VF);
  unsigned* rkeys = (unsigned*)(ws + OFF_R);
  float2* stats = (float2*)(ws + OFF_STATS);
  float* ypart = (float*)(ws + OFF_Y);
  int* sel = (int*)(ws + OFF_SEL);
  u16* out = (u16*)d_out;

  ka_prep<<<192, 512, 0, stream>>>(x, wq, wk, wv, wo, wr, xnf, wqf, wof, kf, vf, rkeys, stats);
  kb_attn<<<256, 768, 0, stream>>>(xnf, wqf, kf, vf, rkeys, wg, wu, wd, of, ypart, sel);
  kc_out<<<256, 512, 0, stream>>>(x, of, wof, stats, ypart, sel, out);
}
```

```cpp
#include <hip/hip_runtime.h>
#include <hip/hip_bf16.h>

typedef unsigned short u16;
typedef __attribute__((ext_vector_type(8))) __bf16 bf16x8;
typedef __attribute__((ext_vector_type(2))) __bf16 bf16x2;
typedef __attribute__((ext_vector_type(2))) float f32x2;
typedef __attribute__((ext_vector_type(4))) float f32x4;
typedef __attribute__((ext_vector_type(16))) float f32x16;
typedef __attribute__((ext_vector_type(4))) unsigned u32x4;
typedef __attribute__((ext_vector_type(2))) unsigned u32x2;
typedef __attribute__((ext_vector_type(4))) short s16x4;
typedef const __attribute__((address_space(1))) char* gcp;

#define DI __device__ __forceinline__
#define TS(k, n)
#define TSR(k, n)

static constexpr int S_ = 2048;
static constexpr int D_ = 512;
static constexpr int F_ = 1376;

#define LDSF(a) (*(const __attribute__((address_space(3))) bf16x8*)(uintptr_t)(a))
DI float bf2f(unsigned u) { return __uint_as_float(u << 16); }
DI float bfhi2f(unsigned u) { return __uint_as_float(u & 0xffff0000u); }
DI float rbf(float f) { return (float)(__bf16)f; }
DI unsigned pack2(float lo, float hi) {
  f32x2 p = {lo, hi};
  bf16x2 q = __builtin_convertvector(p, bf16x2);
  return __builtin_bit_cast(unsigned, q);
}
DI f32x4 mfma16(bf16x8 a, bf16x8 b, f32x4 c) { return __builtin_amdgcn_mfma_f32_16x16x32_bf16(a, b, c, 0, 0, 0); }
DI f32x16 mfma32(bf16x8 a, bf16x8 b, f32x16 c) { return __builtin_amdgcn_mfma_f32_32x32x16_bf16(a, b, c, 0, 0, 0); }
DI unsigned perm_lo(unsigned hi_src, unsigned lo_src) { return __builtin_amdgcn_perm(hi_src, lo_src, 0x05040100u); }
DI unsigned perm_hi(unsigned hi_src, unsigned lo_src) { return __builtin_amdgcn_perm(hi_src, lo_src, 0x07060302u); }

DI bf16x8 frag_lo(const unsigned* w) {
  u32x4 r = {perm_lo(w[1], w[0]), perm_lo(w[3], w[2]), perm_lo(w[5], w[4]), perm_lo(w[7], w[6])};
  return __builtin_bit_cast(bf16x8, r);
}
DI bf16x8 frag_hi(const unsigned* w) {
  u32x4 r = {perm_hi(w[1], w[0]), perm_hi(w[3], w[2]), perm_hi(w[5], w[4]), perm_hi(w[7], w[6])};
  return __builtin_bit_cast(bf16x8, r);
}
DI float ln_elem(float xv, float mb, float den, float inv) {
  const float d = rbf(xv - mb);
  float q = d * inv;
  const float rem = __builtin_fmaf(-q, den, d);
  q = __builtin_fmaf(rem, inv, q);
  return rbf(q);
}

static constexpr int VT_STRIDE = 1056;
static constexpr int VF_BYTES = 64 * VT_STRIDE;
static constexpr size_t OFF_XNF = 0;
static constexpr size_t OFF_OF = OFF_XNF + (size_t)S_ * D_ * 2;
static constexpr size_t OFF_WQF = OFF_OF + (size_t)S_ * D_ * 2;
static constexpr size_t OFF_WOF = OFF_WQF + (size_t)D_ * D_ * 2;
static constexpr size_t OFF_KF = OFF_WOF + (size_t)D_ * D_ * 2;
static constexpr size_t OFF_VF = OFF_KF + 65536;
static constexpr size_t OFF_R = OFF_VF + VF_BYTES;
static constexpr size_t OFF_STATS = OFF_R + S_ * 4;
static constexpr int NHELP = 86;
static constexpr size_t OFF_Y = OFF_STATS + S_ * 8;
static constexpr size_t OFF_SEL = OFF_Y + NHELP * 1024 * 4;

static constexpr int XN_STRIDE = 1040;
__global__ __launch_bounds__(512) void ka_prep(const u16* __restrict__ x, const u16* __restrict__ wq,
                                               const u16* __restrict__ wk, const u16* __restrict__ wv,
                                               const u16* __restrict__ wo, const u16* __restrict__ wr,
                                               u16* __restrict__ xnf, u16* __restrict__ wqf, u16* __restrict__ wof,
                                               u16* __restrict__ kf, u16* __restrict__ vf,
                                               unsigned* __restrict__ rkeys, float2* __restrict__ stats) {
  __shared__ __attribute__((aligned(16))) char smem[16 * XN_STRIDE + 8 * 5 * 64 * 16];
  const int tid = threadIdx.x;
  const int lane = tid & 63, wave = tid >> 6;
  TSR(0, 8); TS(0, 0);
  if (blockIdx.x >= 128) {
    const int pb = blockIdx.x - 128;
    const bool iso = pb >= 32;
    const int cp = tid & 255, ko = (pb & 31) * 2 + (tid >> 8);
    const int ks = ko >> 1, hh = ko & 1;
    const u16* W = (iso ? wo : wq) + 2 * cp;
    unsigned w[8];
#pragma unroll
    for (int j = 0; j < 8; ++j) {
      const int row = 16 * ks + (iso ? (8 * (j >> 2) + 4 * hh + (j & 3)) : (8 * hh + j));
      w[j] = *(const unsigned*)(W + row * D_);
    }
    u16* dst = (iso ? wof : wqf) + (((cp >> 4) * 32 + ks) * 64 + hh * 32 + ((2 * cp) & 31)) * 8;
    *(bf16x8*)dst = frag_lo(w);
    *(bf16x8*)(dst + 8) = frag_hi(w);
    TS(0, 3); TSR(0, 9);
    return;
  }
  const int row0 = blockIdx.x * 16;
  const int rt = blockIdx.x >> 1, th = blockIdx.x & 1;
  const int g = lane >> 4, i = lane & 15;
  const int k0 = wave * 64 + 8 * g;
  u32x4 xr[2];
#pragma unroll
  for (int rr = 0; rr < 2; ++rr) xr[rr] = *(const u32x4*)(x + (size_t)(row0 + 2 * wave + rr) * D_ + 8 * lane);
  const u16* wbase = ((i < 4) ? (wk + 4 * i) : (i < 8 ? wv + 4 * (i - 4) : wk)) + k0 * 16;
  u32x2 w[2][8];
#pragma unroll
  for (int s = 0; s < 2; ++s)
#pragma unroll
    for (int j = 0; j < 8; ++j) w[s][j] = *(const u32x2*)(wbase + (32 * s + j) * 16);
  bf16x8 rf[2];
#pragma unroll
  for (int s = 0; s < 2; ++s) rf[s] = *(const bf16x8*)(wr + k0 + 32 * s);

  float s1[2], s2[2];
#pragma unroll
  for (int rr = 0; rr < 2; ++rr) {
    float a = 0.f, q = 0.f;
#pragma unroll
    for (int e = 0; e < 4; ++e) {
      const float lo = bf2f(xr[rr][e] & 0xffffu), hi = bfhi2f(xr[rr][e]);
      a += lo + hi;
      q = __builtin_fmaf(lo, lo, q);
      q = __builtin_fmaf(hi, hi, q);
    }
    s1[rr] = a; s2[rr] = q;
  }
#pragma unroll
  for (int off = 32; off >= 1; off >>= 1)
#pragma unroll
    for (int rr = 0; rr < 2; ++rr) {
      s1[rr] += __shfl_xor(s1[rr], off);
      s2[rr] += __shfl_xor(s2[rr], off);
    }
#pragma unroll
  for (int rr = 0; rr < 2; ++rr) {
    const float mean = s1[rr] * (1.0f / D_);
    float var = s2[rr] * (1.0f / D_) - mean * mean;
    var = fmaxf(var, 0.f);
    const float mb = rbf(mean);
    const float den = rbf(__builtin_sqrtf(rbf(rbf(var) + 1e-5f)));
    const float inv = 1.0f / den;
    if (lane == rr) stats[row0 + 2 * wave + rr] = make_float2(mb, den);
    u32x4 o;
#pragma unroll
    for (int e = 0; e < 4; ++e)
      o[e] = pack2(ln_elem(bf2f(xr[rr][e] & 0xffffu), mb, den, inv), ln_elem(bfhi2f(xr[rr][e]), mb, den, inv));
    *(u32x4*)(smem + (2 * wave + rr) * XN_STRIDE + lane * 16) = o;
  }
  if (tid < 2 && th == 0) {
    const unsigned cv = tid ? 0u : 0x3f803f80u;
    u32x4 c4 = {cv, cv, cv, cv};
    *(u32x4*)((char*)vf + rt * VT_STRIDE + 1024 + tid * 16) = c4;
  }
  TS(0, 1);
  __syncthreads();
  {
    const int r = lane & 15, hh = (lane >> 4) & 1, kp = lane >> 5;
#pragma unroll
    for (int s = 0; s < 2; ++s) {
      const int ks = 4 * wave + 2 * s + kp;
      const u32x4 v = *(const u32x4*)(smem + r * XN_STRIDE + ks * 32 + hh * 16);
      *(u32x4*)(xnf + ((size_t)(rt * 32 + ks) * 64 + hh * 32 + th * 16 + r) * 8) = v;
    }
  }
  f32x4* red = (f32x4*)(smem + 16 * XN_STRIDE);
  {
    f32x4 acc[4], ar;
    ar = (f32x4){0.f, 0.f, 0.f, 0.f};
#pragma unroll
    for (int c = 0; c < 4; ++c) acc[c] = (f32x4){0.f, 0.f, 0.f, 0.f};
#pragma unroll
    for (int s = 0; s < 2; ++s) {
      unsigned wx[8], wy[8];
#pragma unroll
      for (int j = 0; j < 8; ++j) { wx[j] = w[s][j][0]; wy[j] = w[s][j][1]; }
      bf16x8 b[4] = {frag_lo(wx), frag_hi(wx), frag_lo(wy), frag_hi(wy)};
      const bf16x8 xa = *(const bf16x8*)(smem + i * XN_STRIDE + (k0 + 32 * s) * 2);
#pragma unroll
      for (int c = 0; c < 4; ++c) acc[c] = mfma16(xa, b[c], acc[c]);
      ar = mfma16(xa, rf[s], ar);
    }
#pragma unroll
    for (int c = 0; c < 4; ++c) red[(wave * 5 + c) * 64 + lane] = acc[c];
    red[(wave * 5 + 4) * 64 + lane] = ar;
  }
  __syncthreads();
  TS(0, 2);
  if (wave < 5) {
    f32x4 v = red[wave * 64 + lane];
#pragma unroll
    for (int wv_ = 1; wv_ < 8; ++wv_) v += red[(wv_ * 5 + wave) * 64 + lane];
    const int kk = 16 * th + 4 * g;
    if (wave == 4) {
      unsigned k0 = 0, k1 = 0;
#pragma unroll
      for (int e = 0; e < 4; ++e) {
        const unsigned u = __float_as_uint(rbf(v[e]));
        const unsigned ord = (u & 0x80000000u) ? ~u : (u | 0x80000000u);
        const unsigned key = (ord & 0xffff0000u) | (unsigned)(2047 - (row0 + 4 * g + e));
        k1 = max(k1, min(k0, key));
        k0 = max(k0, key);
      }
#pragma unroll
      for (int off = 16; off < 64; off <<= 1) {
        const unsigned o0 = __shfl_xor(k0, off), o1 = __shfl_xor(k1, off);
        k1 = max(min(k0, o0), max(k1, o1));
        k0 = max(k0, o0);
      }
      if (lane == 0) *(u32x2*)(rkeys + 2 * blockIdx.x) = (u32x2){k0, k1};
    } else if (i < 4) {
      const int hh = i & 1, j = (i >> 1) * 4 + wave;
#pragma unroll
      for (int e = 0; e < 4; ++e)
        kf[rt * 512 + (hh * 32 + kk + e) * 8 + j] = __builtin_bit_cast(u16, (__bf16)v[e]);
    } else if (i < 8) {
      const int d = 4 * (i - 4) + wave;
      const int hh = g & 1, jh = g >> 1;
      u32x2 o = {pack2(v[0], v[1]), pack2(v[2], v[3])};
      *(u32x2*)(vf + rt * (VT_STRIDE / 2) + th * 256 + (hh * 16 + d) * 8 + jh * 4) = o;
    }
  }
  TS(0, 3); TSR(0, 9);
}

#define P1A1 32
#define P1B1 52
#define P1A2 61
#define P2A1 5
#define P2B1 7
#define P2A2U 6
#define PRIO_IF(a, b, p) asm volatile("s_cmp_lg_u32 %0, %1\n\ts_cbranch_scc1 1f\n\ts_setprio " #p "\n1:" :: "s"(a), "s"(b) : "scc")
static constexpr int LDS_K = 0;
static constexpr int LDS_V = 65536;
static constexpr int LDS_H = 65536 + VF_BYTES + 2048;
static constexpr int LDS_F = LDS_H + 1024;
static constexpr int LDS_HH = LDS_F + 64;
static constexpr int LDS_TOTAL = LDS_HH + 256;

DI void glds16(const char* gsrc, unsigned lds_dst) {
  unsigned keep;
  asm volatile("s_mov_b32 %0, m0\n\ts_mov_b32 m0, %2\n\ts_nop 0\n\tglobal_load_lds_dwordx4 %1, off\n\ts_mov_b32 m0, %0"
               : "=&s"(keep) : "v"(gsrc), "s"(lds_dst) : "memory");
}
DI void stage_v(const char* vsrc, unsigned vdst, int hj) {
#pragma unroll
  for (int c = 0; c < 16; ++c) glds16(vsrc + c * 4096, vdst + c * 4096);
  if (hj < 2) glds16(vsrc + 65536, vdst + 65536);
}

__global__ __launch_bounds__(768) void kb_attn(const u16* __restrict__ xnf, const u16* __restrict__ wqf,
                                               const u16* __restrict__ kf, const u16* __restrict__ vf,
                                               const unsigned* __restrict__ rkeys, const u16* __restrict__ wg,
                                               const u16* __restrict__ wu, const u16* __restrict__ wd,
                                               u16* __restrict__ of, float* __restrict__ ypart,
                                               int* __restrict__ sel) {
  __shared__ __attribute__((aligned(16))) char smem[LDS_TOTAL];
  const int tid = threadIdx.x;
  const int lane = tid & 63, wave = tid >> 6;
  const int b = blockIdx.x;
  unsigned* flags = (unsigned*)(smem + LDS_F);
  TSR(1, 8); TS(1, 0);

  if (wave < 8) {
    const int rb = (b & 7) + 8 * ((b >> 3) & 3), hg = b >> 5;
    const int rt = wave >> 2, ks4 = wave & 3;
    const int r = lane & 31, hh = lane >> 5;
    {
      bf16x8 af[2][8], bfr[8];
      const int rtq = wave & 1, kq = wave >> 1;
      const u16* ap = wqf + ((size_t)(2 * hg) * 32 + 8 * kq) * 512 + lane * 8;
      const u16* bp = xnf + ((size_t)(2 * rb + rtq) * 32 + 8 * kq) * 512 + lane * 8;
#pragma unroll
      for (int s = 0; s < 8; ++s) {
        bfr[s] = *(const bf16x8*)(bp + s * 512);
        af[0][s] = *(const bf16x8*)(ap + s * 512);
        af[1][s] = *(const bf16x8*)(ap + 32 * 512 + s * 512);
      }
      f32x16 acc[2];
#pragma unroll
      for (int e = 0; e < 16; ++e) { acc[0][e] = 0.f; acc[1][e] = 0.f; }
#pragma unroll
      for (int s = 0; s < 8; ++s) {
        acc[0] = mfma32(af[0][s], bfr[s], acc[0]);
        acc[1] = mfma32(af[1][s], bfr[s], acc[1]);
      }
#pragma unroll
      for (int dt = 0; dt < 2; ++dt)
#pragma unroll
        for (int q4 = 0; q4 < 4; ++q4) {
          f32x4 p = {acc[dt][4 * q4], acc[dt][4 * q4 + 1], acc[dt][4 * q4 + 2], acc[dt][4 * q4 + 3]};
          *(f32x4*)(smem + LDS_V + ((((rtq * 4 + kq) * 2 + dt) * 4 + q4) * 64 + lane) * 16) = p;
        }
    }
    __builtin_amdgcn_sched_barrier(0);
    TS(1, 1);
    __syncthreads();
    TS(1, 2);
    const int hl = wave & 3;
    const int head = hg * 4 + hl;
    bf16x8 qf;
    {
      f32x4 a0 = {0.f, 0.f, 0.f, 0.f}, a1 = {0.f, 0.f, 0.f, 0.f};
#pragma unroll
      for (int k = 0; k < 4; ++k) {
        const char* p = smem + LDS_V + ((((rt * 4 + k) * 2 + (hl >> 1)) * 4 + 2 * (hl & 1)) * 64 + lane) * 16;
        a0 += *(const f32x4*)p;
        a1 += *(const f32x4*)(p + 1024);
      }
      const float SC = 0.25f * 1.44269504088896340736f;
      u32x4 pk = {pack2(rbf(a0[0]) * SC, rbf(a0[1]) * SC), pack2(rbf(a0[2]) * SC, rbf(a0[3]) * SC),
                  pack2(rbf(a1[0]) * SC, rbf(a1[1]) * SC), pack2(rbf(a1[2]) * SC, rbf(a1[3]) * SC)};
      qf = __builtin_bit_cast(bf16x8, pk);
    }
    __syncthreads();

    const int yng = __builtin_amdgcn_readfirstlane(wave >> 2);
    const unsigned lbase = (unsigned)(uintptr_t)(__attribute__((address_space(3))) char*)smem;
    float m0 = -__builtin_inff(), m1 = -__builtin_inff();
    f32x16 zero16;
#pragma unroll
    for (int e = 0; e < 16; ++e) zero16[e] = 0.f;
    {
      unsigned kq_ = lbase + LDS_K + lane * 16;
      asm volatile("" : "+v"(kq_));
      f32x16 sa = mfma32(LDSF(kq_), qf, zero16);
      f32x16 sb = mfma32(LDSF(kq_ + 1024), qf, zero16);
      bf16x8 k2 = LDSF(kq_ + 2048), k3 = LDSF(kq_ + 3072);
      __builtin_amdgcn_sched_barrier(0);
#pragma unroll
      for (int t = 0; t < 64; ++t) {
        if (t == 0) __builtin_amdgcn_s_setprio(2);
        if (t == P1A1) PRIO_IF(yng, 0, 1);
        if (t == P1B1) PRIO_IF(yng, 1, 1);
        if (t == P1A2) PRIO_IF(yng, 0, 0);
        f32x16 sc = sb;
        bf16x8 k4 = k3;
        if (t + 2 < 64) sc = mfma32(k2, qf, zero16);
        if (t + 4 < 64) k4 = LDSF(kq_ + (t + 4) * 1024);
        if (t & 1) {
#pragma unroll
          for (int e = 0; e < 16; e += 2) m1 = fmaxf(fmaxf(m1, sa[e]), sa[e + 1]);
        } else {
#pragma unroll
          for (int e = 0; e < 16; e += 2) m0 = fmaxf(fmaxf(m0, sa[e]), sa[e + 1]);
        }
        sa = sb; sb = sc; k2 = k3; k3 = k4;
        __builtin_amdgcn_sched_group_barrier(0x008, 1, 0);
        __builtin_amdgcn_sched_group_barrier(0x100, 1, 0);
        __builtin_amdgcn_sched_group_barrier(0x002, 8, 0);
        __builtin_amdgcn_sched_barrier(0);
      }
    }
    float m = fmaxf(m0, m1);
    m = fmaxf(m, __shfl_xor(m, 32));
    TS(1, 3);
    while (__hip_atomic_load(flags, __ATOMIC_ACQUIRE, __HIP_MEMORY_SCOPE_WORKGROUP) < 4) __builtin_amdgcn_s_sleep(1);
    asm volatile("" ::: "memory");
    TS(1, 4);

    f32x16 negm;
#pragma unroll
    for (int e = 0; e < 16; ++e) negm[e] = -m;
    f32x16 o;
#pragma unroll
    for (int e = 0; e < 16; ++e) o[e] = 0.f;
    unsigned ka_ = lbase + LDS_K + lane * 16;
    unsigned va_ = lbase + LDS_V + ((r < 16) ? (hh * 16 + r) * 16 : (r == 16 ? 1024 : 1040));
    unsigned vb_ = lbase + LDS_V + ((r < 16) ? (hh * 16 + r) * 16 + 512 : (r == 16 ? 1024 : 1040));
    f32x16 s = mfma32(LDSF(ka_), qf, negm);
    bf16x8 kn = LDSF(ka_ + 1024);
    bf16x8 v0 = LDSF(va_);
    bf16x8 v1 = LDSF(vb_);
    bf16x8 v1p = v1;
    u32x4 pbp = {0u, 0u, 0u, 0u};
    const int thr1 = P2A1 + (P2B1 - P2A1) * yng, thr2 = 7 + 92 * yng;
    __builtin_amdgcn_s_setprio(2);
#pragma unroll 1
    for (int tb = 0; tb < 8; ++tb) {
      asm volatile("" : "+v"(ka_), "+v"(va_), "+v"(vb_));
      PRIO_IF(tb, thr1, 1);
#pragma unroll
      for (int u = 0; u < 8; ++u) {
        if (u == P2A2U) PRIO_IF(tb, thr2, 0);
        const f32x16 sn = mfma32(kn, qf, negm);
        const bf16x8 k2 = LDSF(ka_ + (u + 2) * 1024);
        const bf16x8 v0n = LDSF(va_ + (u + 1) * VT_STRIDE);
        const bf16x8 v1n = LDSF(vb_ + (u + 1) * VT_STRIDE);
        float p[16];
#pragma unroll
        for (int e = 0; e < 6; ++e) p[e] = __builtin_amdgcn_exp2f(s[e]);
        o = mfma32(v1p, __builtin_bit_cast(bf16x8, pbp), o);
#pragma unroll
        for (int e = 6; e < 10; ++e) p[e] = __builtin_amdgcn_exp2f(s[e]);
        u32x4 pa = {pack2(p[0], p[1]), pack2(p[2], p[3]), pack2(p[4], p[5]), pack2(p[6], p[7])};
        o = mfma32(v0, __builtin_bit_cast(bf16x8, pa), o);
#pragma unroll
        for (int e = 10; e < 16; ++e) p[e] = __builtin_amdgcn_exp2f(s[e]);
        pbp = (u32x4){pack2(p[8], p[9]), pack2(p[10], p[11]), pack2(p[12], p[13]), pack2(p[14], p[15])};
        v1p = v1;
        s = sn; kn = k2; v0 = v0n; v1 = v1n;
        __builtin_amdgcn_sched_group_barrier(0x008, 1, 0);
        __builtin_amdgcn_sched_group_barrier(0x100, 3, 0);
        __builtin_amdgcn_sched_group_barrier(0x402, 6, 0);
        __builtin_amdgcn_sched_group_barrier(0x008, 1, 0);
        __builtin_amdgcn_sched_group_barrier(0x402, 8, 0);
        __builtin_amdgcn_sched_group_barrier(0x008, 1, 0);
        __builtin_amdgcn_sched_group_barrier(0x402, 10, 0);
        __builtin_amdgcn_sched_barrier(0);
      }
      ka_ += 8 * 1024; va_ += 8 * VT_STRIDE; vb_ += 8 * VT_STRIDE;
    }
    o = mfma32(v1p, __builtin_bit_cast(bf16x8, pbp), o);
    TS(1, 5);
    const float il = 1.0f / __shfl(o[8], r);
    u32x4 w0 = {pack2(o[0] * il, o[1] * il), pack2(o[2] * il, o[3] * il), pack2(o[4] * il, o[5] * il),
                pack2(o[6] * il, o[7] * il)};
    *(u32x4*)(of + ((size_t)((2 * rb + rt) * 32 + head) * 64 + lane) * 8) = w0;
  } else {
    __builtin_amdgcn_s_setprio(3);
    const int hj = __builtin_amdgcn_readfirstlane(wave - 8);
    const unsigned ldst = (unsigned)(uintptr_t)(__attribute__((address_space(3))) char*)smem + hj * 1024;
    {
      const char* ksrc = (const char*)kf + hj * 1024 + lane * 16;
#pragma unroll
      for (int c = 0; c < 16; ++c) glds16(ksrc + c * 4096, ldst + LDS_K + c * 4096);
    }
    if (hj == 0 && lane < 2) flags[lane] = 0u;
    const char* vsrc = (const char*)vf + hj * 1024 + lane * 16;
    if (b >= NHELP) {
      asm volatile("s_waitcnt vmcnt(0)" ::: "memory");
      __syncthreads();
      __syncthreads();
      stage_v(vsrc, ldst + LDS_V, hj);
      asm volatile("s_waitcnt vmcnt(0)" ::: "memory");
      if (lane == 0) __hip_atomic_fetch_add(flags, 1u, __ATOMIC_RELEASE, __HIP_MEMORY_SCOPE_WORKGROUP);
      return;
    }
    const int g = lane >> 4, i = lane & 15;
    const int f0 = b * 16;
    const u32x4 kv = *(const u32x4*)(rkeys + lane * 4);
    unsigned k0 = max(kv[0], kv[2]), k1 = max(min(kv[0], kv[2]), max(kv[1], kv[3]));
#pragma unroll
    for (int off = 1; off < 64; off <<= 1) {
      const unsigned o0 = __shfl_xor(k0, off), o1 = __shfl_xor(k1, off);
      k1 = max(min(k0, o0), max(k1, o1));
      k0 = max(k0, o0);
    }
    const int i0 = 2047 - (int)(k0 & 0x7ffu), i1 = 2047 - (int)(k1 & 0x7ffu);
    if (b == 0 && hj == 0 && lane == 0) {
      const float v0 = __uint_as_float((k0 & 0x80000000u) ? (k0 & 0x7fff0000u) : (~k0 & 0xffff0000u));
      const float v1 = __uint_as_float((k1 & 0x80000000u) ? (k1 & 0x7fff0000u) : (~k1 & 0xffff0000u));
      sel[0] = i0; sel[1] = i1;
      ((float*)sel)[2] = rbf(1.0f / (1.0f + __expf(-v0)));
      ((float*)sel)[3] = rbf(1.0f / (1.0f + __expf(-v1)));
    }
    const int srow = (i & 1) ? i1 : i0;
    const u16* xp = xnf + ((size_t)((srow >> 5) * 32 + 16 * (hj >> 1) + 8 * ((i >> 1) & 1) + (g >> 1)) * 64 + (g & 1) * 32 + (srow & 31)) * 8;
    bf16x8 xa[4];
#pragma unroll
    for (int s = 0; s < 4; ++s) xa[s] = *(const bf16x8*)(xp + (size_t)s * 1024);
    TS(1, 3);
    asm volatile("s_waitcnt vmcnt(0)" ::: "memory");
    __syncthreads();
    __syncthreads();
    stage_v(vsrc, ldst + LDS_V, hj);
    asm volatile("s_waitcnt vmcnt(0)" ::: "memory");
    if (lane == 0) __hip_atomic_fetch_add(flags, 1u, __ATOMIC_RELEASE, __HIP_MEMORY_SCOPE_WORKGROUP);
    __builtin_amdgcn_sched_barrier(0);
    unsigned wa[4][8];
    const char* ub = (const char*)(((hj & 1) ? wu : wg) + (size_t)(256 * (hj >> 1)) * F_ + f0);
    const unsigned lo1 = (unsigned)((128 * (i >> 3) + 8 * g) * F_ + 2 * (i & 7)) * 2u;
#pragma unroll
    for (int s = 0; s < 4; ++s)
#pragma unroll
      for (int j = 0; j < 8; j += 2) {
        gcp rp = (gcp)(ub + (size_t)((32 * s + j) * F_ * 2));
        asm volatile("" : "+s"(rp));
        wa[s][j] = *(const __attribute__((address_space(1))) unsigned*)(rp + lo1);
        wa[s][j + 1] = *(const __attribute__((address_space(1))) unsigned*)(rp + F_ * 2 + lo1);
      }
    unsigned wdv[4][4];
    const char* db = (const char*)(wd + (size_t)f0 * D_ + hj * 128);
    const unsigned lo2 = (unsigned)(4 * g * D_ + 2 * i) * 2u;
#pragma unroll
    for (int q = 0; q < 4; ++q)
#pragma unroll
      for (int j = 0; j < 4; ++j) wdv[q][j] = *(const unsigned*)(db + (j * D_ + 32 * q) * 2 + lo2);
    f32x4 a0 = {0.f, 0.f, 0.f, 0.f}, a1 = {0.f, 0.f, 0.f, 0.f};
#pragma unroll
    for (int s = 0; s < 4; ++s) {
      a0 = mfma16(xa[s], frag_lo(wa[s]), a0);
      a1 = mfma16(xa[s], frag_hi(wa[s]), a1);
    }
    if (lane < 16)
      *(f32x4*)(smem + LDS_H + (hj * 16 + i) * 16) =
          (i < 8) ? (f32x4){a0[0], a0[1], a1[0], a1[1]} : (f32x4){a0[2], a0[3], a1[2], a1[3]};
    TS(1, 4);
    if (lane == 0) __hip_atomic_fetch_add(flags + 1, 1u, __ATOMIC_RELEASE, __HIP_MEMORY_SCOPE_WORKGROUP);
    while (__hip_atomic_load(flags + 1, __ATOMIC_ACQUIRE, __HIP_MEMORY_SCOPE_WORKGROUP) < 4) __builtin_amdgcn_s_sleep(2);
    asm volatile("" ::: "memory");
    __builtin_amdgcn_sched_barrier(0);
    const float* hp = (const float*)(smem + LDS_H) + ((i >> 3) * 16 + (i & 7)) * 4 + g;
    const float gt = rbf((hp[0] + hp[32]) + (hp[128] + hp[160]));
    const float up = __shfl_xor(gt, 8);
    const float hv = rbf(gt / (1.0f + __expf(-gt))) * up;
    if (i < 8) *(u16*)(smem + LDS_HH + hj * 64 + (g & 1) * 32 + (2 * i + (g >> 1)) * 2) = __builtin_bit_cast(u16, (__bf16)hv);
    const s16x4 ha = *(const s16x4*)(smem + LDS_HH + hj * 64 + (i & 1) * 32 + g * 8);
#pragma unroll
    for (int q = 0; q < 4; ++q) {
      const u32x2 be = {perm_lo(wdv[q][1], wdv[q][0]), perm_lo(wdv[q][3], wdv[q][2])};
      const u32x2 bo = {perm_hi(wdv[q][1], wdv[q][0]), perm_hi(wdv[q][3], wdv[q][2])};
      const f32x4 z = {0.f, 0.f, 0.f, 0.f};
      const f32x4 ye = __builtin_amdgcn_mfma_f32_16x16x16bf16_1k(ha, __builtin_bit_cast(s16x4, be), z, 0, 0, 0);
      const f32x4 yo = __builtin_amdgcn_mfma_f32_16x16x16bf16_1k(ha, __builtin_bit_cast(s16x4, bo), z, 0, 0, 0);
      if (lane < 16) {
        float* yp = ypart + (size_t)b * 1024 + hj * 128 + 32 * q + 2 * i;
        *(float2*)yp = make_float2(ye[0], yo[0]);
        *(float2*)(yp + 512) = make_float2(ye[1], yo[1]);
      }
    }
  }
  TS(1, 6); TSR(1, 9);
}

static constexpr int CH_STRIDE = 1056;
static constexpr int LDS_Y = 64 * CH_STRIDE;
__global__ __launch_bounds__(512) void kc_out(const u16* __restrict__ x, const u16* __restrict__ of,
                                              const u16* __restrict__ wof, const float2* __restrict__ stats,
                                              const float* __restrict__ ypart, const int* __restrict__ sel,
                                              u16* __restrict__ out) {
  __shared__ __attribute__((aligned(16))) char smem[LDS_Y + 2 * 8 * 256];
  const int tid = threadIdx.x;
  const int lane = tid & 63, wave = tid >> 6;
  TSR(2, 8); TS(2, 0);
  const int bb = blockIdx.x;
  const int rb = (bb & 7) + 8 * ((bb >> 3) & 3), cg = bb >> 5;
  const int rt = wave & 1, ks4 = wave >> 1;
  const int idx0 = sel[0], idx1 = sel[1];
  const float rs0 = ((const float*)sel)[2], rs1 = ((const float*)sel)[3];
  bf16x8 af[2][8], bfr[8];
  const u16* ap = wof + ((size_t)(2 * cg) * 32 + 8 * ks4) * 512 + lane * 8;
  const u16* bp = of + ((size_t)(2 * rb + rt) * 32 + 8 * ks4) * 512 + lane * 8;
#pragma unroll
  for (int s = 0; s < 8; ++s) {
    bfr[s] = *(const bf16x8*)(bp + s * 512);
    af[0][s] = *(const bf16x8*)(ap + s * 512);
    af[1][s] = *(const bf16x8*)(ap + 32 * 512 + s * 512);
  }
  const int r64 = tid >> 3, oct = tid & 7;
  const int row = rb * 64 + r64;
  const size_t off = (size_t)row * D_ + cg * 64 + oct * 8;
  const u32x4 xv = *(const u32x4*)(x + off);
  const float2 st = stats[row];
  f32x4 yv[2][3];
#pragma unroll
  for (int tk = 0; tk < 2; ++tk) {
    const int idx = tk ? idx1 : idx0;
#pragma unroll
    for (int q = 0; q < 3; ++q) yv[tk][q] = (f32x4){0.f, 0.f, 0.f, 0.f};
    if ((unsigned)(idx - rb * 64) < 64u) {
      const int pg = tid >> 4;
      const float* yp = ypart + (size_t)(pg * 2 + tk) * 512 + cg * 64 + (tid & 15) * 4;
      yv[tk][0] = *(const f32x4*)yp;
      yv[tk][1] = *(const f32x4*)(yp + 32 * 1024);
      yv[tk][2] = *(const f32x4*)(yp + (pg + 64 < NHELP ? 64 * 1024 : 0));
    }
  }
  f32x16 acc[2];
#pragma unroll
  for (int e = 0; e < 16; ++e) { acc[0][e] = 0.f; acc[1][e] = 0.f; }
#pragma unroll
  for (int s = 0; s < 8; ++s) {
    acc[0] = mfma32(af[0][s], bfr[s], acc[0]);
    acc[1] = mfma32(af[1][s], bfr[s], acc[1]);
  }
  TS(2, 1);
#pragma unroll
  for (int ct = 0; ct < 2; ++ct)
#pragma unroll
    for (int q4 = 0; q4 < 4; ++q4) {
      f32x4 p = {acc[ct][4 * q4], acc[ct][4 * q4 + 1], acc[ct][4 * q4 + 2], acc[ct][4 * q4 + 3]};
      *(f32x4*)(smem + (((rt * 4 + ks4) * 2 + ct) * 4 + q4) * CH_STRIDE + lane * 16) = p;
    }
#pragma unroll
  for (int tk = 0; tk < 2; ++tk) {
    const int idx = tk ? idx1 : idx0;
    if ((unsigned)(idx - rb * 64) < 64u) {
      f32x4 y = yv[tk][0] + yv[tk][1];
      if ((tid >> 4) + 64 < NHELP) y += yv[tk][2];
#pragma unroll
      for (int e = 0; e < 4; ++e) {
        y[e] += __shfl_xor(y[e], 16);
        y[e] += __shfl_xor(y[e], 32);
      }
      if (lane < 16) *(f32x4*)(smem + LDS_Y + (tk * 8 + wave) * 256 + lane * 16) = y;
    }
  }
  __syncthreads();
  TS(2, 2);
  f32x4 lo = {0.f, 0.f, 0.f, 0.f}, hi = {0.f, 0.f, 0.f, 0.f};
#pragma unroll
  for (int k = 0; k < 4; ++k) {
    const char* p = smem + (((r64 >> 5) * 4 + k) * 8 + oct) * CH_STRIDE + (r64 & 31) * 16;
    lo += *(const f32x4*)p;
    hi += *(const f32x4*)(p + 512);
  }
  float upd[8];
#pragma unroll
  for (int e = 0; e < 8; ++e) upd[e] = 0.f;
  if (row == idx0 || row == idx1) {
    const int tk = (row == idx0) ? 0 : 1;
    f32x4 ya = {0.f, 0.f, 0.f, 0.f}, yb = {0.f, 0.f, 0.f, 0.f};
#pragma unroll
    for (int p = 0; p < 8; ++p) {
      const char* yp = smem + LDS_Y + (tk * 8 + p) * 256 + oct * 32;
      ya += *(const f32x4*)yp;
      yb += *(const f32x4*)(yp + 16);
    }
    const float rs = tk ? rs1 : rs0;
#pragma unroll
    for (int e = 0; e < 4; ++e) { upd[e] = rbf(rbf(ya[e]) * rs); upd[4 + e] = rbf(rbf(yb[e]) * rs); }
  }
  const float inv = 1.0f / st.y;
  float res[8];
#pragma unroll
  for (int e = 0; e < 4; ++e) {
    const float x0 = bf2f(xv[e] & 0xffffu), x1 = bfhi2f(xv[e]);
    const float a0 = rbf(e < 2 ? lo[2 * e] : hi[2 * e - 4]);
    const float a1 = rbf(e < 2 ? lo[2 * e + 1] : hi[2 * e - 3]);
    res[2 * e] = rbf(x0 + a0) + rbf(ln_elem(x0, st.x, st.y, inv) + upd[2 * e]);
    res[2 * e + 1] = rbf(x1 + a1) + rbf(ln_elem(x1, st.x, st.y, inv) + upd[2 * e + 1]);
  }
  u32x4 o = {pack2(res[0], res[1]), pack2(res[2], res[3]), pack2(res[4], res[5]), pack2(res[6], res[7])};
  *(u32x4*)(out + off) = o;
  TS(2, 3); TSR(2, 9);
}

extern "C" void kernel_launch(void* const* d_in, const int* in_sizes, int n_in, void* d_out, int out_size,
                              void* d_ws, size_t ws_size, hipStream_t stream) {
  const u16* x = (const u16*)d_in[0];
  const u16* wq = (const u16*)d_in[1];
  const u16* wk = (const u16*)d_in[2];
  const u16* wv = (const u16*)d_in[3];
  const u16* wo = (const u16*)d_in[4];
  const u16* wr = (const u16*)d_in[6];
  const u16* wg = (const u16*)d_in[7];
  const u16* wu = (const u16*)d_in[8];
  const u16* wd = (const u16*)d_in[9];
  char* ws = (char*)d_ws;
  u16* xnf = (u16*)(ws + OFF_XNF);
  u16* of = (u16*)(ws + OFF_OF);
  u16* wqf = (u16*)(ws + OFF_WQF);
  u16* wof = (u16*)(ws + OFF_WOF);
  u16* kf = (u16*)(ws + OFF_KF);
  u16* vf = (u16*)(ws + OFF_VF);
  unsigned* rkeys = (unsigned*)(ws + OFF_R);
  float2* stats = (float2*)(ws + OFF_STATS);
  float* ypart = (float*)(ws + OFF_Y);
  int* sel = (int*)(ws + OFF_SEL);
  u16* out = (u16*)d_out;

  ka_prep<<<192, 512, 0, stream>>>(x, wq, wk, wv, wo, wr, xnf, wqf, wof, kf, vf, rkeys, stats);
  kb_attn<<<256, 768, 0, stream>>>(xnf, wqf, kf, vf, rkeys, wg, wu, wd, of, ypart, sel);
  kc_out<<<256, 512, 0, stream>>>(x, of, wof, stats, ypart, sel, out);
}
```

```cpp
#include <hip/hip_runtime.h>
#include <hip/hip_bf16.h>

typedef unsigned short u16;
typedef __attribute__((ext_vector_type(8))) __bf16 bf16x8;
typedef __attribute__((ext_vector_type(16))) __bf16 bf16x16;
typedef __attribute__((ext_vector_type(2))) __bf16 bf16x2;
typedef __attribute__((ext_vector_type(8))) float f32x8;
typedef __attribute__((ext_vector_type(2))) float f32x2;
typedef __attribute__((ext_vector_type(4))) float f32x4;
typedef __attribute__((ext_vector_type(16))) float f32x16;
typedef __attribute__((ext_vector_type(4))) unsigned u32x4;
typedef __attribute__((ext_vector_type(2))) unsigned u32x2;
typedef __attribute__((ext_vector_type(4))) short s16x4;
typedef const __attribute__((address_space(1))) char* gcp;

#define DI __device__ __forceinline__
#define TS(k, n)
#define TSR(k, n)

static constexpr int S_ = 2048;
static constexpr int D_ = 512;
static constexpr int F_ = 1376;

#define LDSF(a) (*(const __attribute__((address_space(3))) bf16x8*)(uintptr_t)(a))
DI float bf2f(unsigned u) { return __uint_as_float(u << 16); }
DI float bfhi2f(unsigned u) { return __uint_as_float(u & 0xffff0000u); }
DI float rbf(float f) { return (float)(__bf16)f; }
DI unsigned pack2(float lo, float hi) {
  f32x2 p = {lo, hi};
  bf16x2 q = __builtin_convertvector(p, bf16x2);
  return __builtin_bit_cast(unsigned, q);
}
DI f32x4 mfma16(bf16x8 a, bf16x8 b, f32x4 c) { return __builtin_amdgcn_mfma_f32_16x16x32_bf16(a, b, c, 0, 0, 0); }
DI f32x16 mfma32(bf16x8 a, bf16x8 b, f32x16 c) { return __builtin_amdgcn_mfma_f32_32x32x16_bf16(a, b, c, 0, 0, 0); }
DI f32x16 smfma32(bf16x8 a, f32x8 p, f32x16 c) {
  return __builtin_amdgcn_smfmac_f32_32x32x32_bf16(a, __builtin_bit_cast(bf16x16, p), c, 0xdddd, 0, 0);
}
DI unsigned perm_lo(unsigned hi_src, unsigned lo_src) { return __builtin_amdgcn_perm(hi_src, lo_src, 0x05040100u); }
DI unsigned perm_hi(unsigned hi_src, unsigned lo_src) { return __builtin_amdgcn_perm(hi_src, lo_src, 0x07060302u); }

DI bf16x8 frag_lo(const unsigned* w) {
  u32x4 r = {perm_lo(w[1], w[0]), perm_lo(w[3], w[2]), perm_lo(w[5], w[4]), perm_lo(w[7], w[6])};
  return __builtin_bit_cast(bf16x8, r);
}
DI bf16x8 frag_hi(const unsigned* w) {
  u32x4 r = {perm_hi(w[1], w[0]), perm_hi(w[3], w[2]), perm_hi(w[5], w[4]), perm_hi(w[7], w[6])};
  return __builtin_bit_cast(bf16x8, r);
}
DI float ln_elem(float xv, float mb, float den, float inv) {
  const float d = rbf(xv - mb);
  float q = d * inv;
  const float rem = __builtin_fmaf(-q, den, d);
  q = __builtin_fmaf(rem, inv, q);
  return rbf(q);
}

static constexpr int VT_STRIDE = 1056;
static constexpr int VF_BYTES = 64 * VT_STRIDE;
static constexpr size_t OFF_XNF = 0;
static constexpr size_t OFF_OF = OFF_XNF + (size_t)S_ * D_ * 2;
static constexpr size_t OFF_WQF = OFF_OF + (size_t)S_ * D_ * 2;
static constexpr size_t OFF_WOF = OFF_WQF + (size_t)D_ * D_ * 2;
static constexpr size_t OFF_KF = OFF_WOF + (size_t)D_ * D_ * 2;
static constexpr size_t OFF_VF = OFF_KF + 65536;
static constexpr size_t OFF_R = OFF_VF + VF_BYTES;
static constexpr size_t OFF_STATS = OFF_R + S_ * 4;
static constexpr int NHELP = 86;
static constexpr size_t OFF_Y = OFF_STATS + S_ * 8;
static constexpr size_t OFF_SEL = OFF_Y + NHELP * 1024 * 4;

static constexpr int XN_STRIDE = 1040;
__global__ __launch_bounds__(512) void ka_prep(const u16* __restrict__ x, const u16* __restrict__ wq,
                                               const u16* __restrict__ wk, const u16* __restrict__ wv,
                                               const u16* __restrict__ wo, const u16* __restrict__ wr,
                                               u16* __restrict__ xnf, u16* __restrict__ wqf, u16* __restrict__ wof,
                                               u16* __restrict__ kf, u16* __restrict__ vf,
                                               unsigned* __restrict__ rkeys, float2* __restrict__ stats) {
  __shared__ __attribute__((aligned(16))) char smem[16 * XN_STRIDE + 8 * 5 * 64 * 16];
  const int tid = threadIdx.x;
  const int lane = tid & 63, wave = tid >> 6;
  TSR(0, 8); TS(0, 0);
  if (blockIdx.x >= 128) {
    const int pb = blockIdx.x - 128;
    const bool iso = pb >= 32;
    const int cp = tid & 255, ko = (pb & 31) * 2 + (tid >> 8);
    const int ks = ko >> 1, hh = ko & 1;
    const u16* W = (iso ? wo : wq) + 2 * cp;
    unsigned w[8];
#pragma unroll
    for (int j = 0; j < 8; ++j) {
      const int row = 16 * ks + (iso ? (8 * (j >> 2) + 4 * hh + (j & 3)) : (8 * hh + j));
      w[j] = *(const unsigned*)(W + row * D_);
    }
    u16* dst = (iso ? wof : wqf) + (((cp >> 4) * 32 + ks) * 64 + hh * 32 + ((2 * cp) & 31)) * 8;
    *(bf16x8*)dst = frag_lo(w);
    *(bf16x8*)(dst + 8) = frag_hi(w);
    TS(0, 3); TSR(0, 9);
    return;
  }
  const int row0 = blockIdx.x * 16;
  const int rt = blockIdx.x >> 1, th = blockIdx.x & 1;
  const int g = lane >> 4, i = lane & 15;
  const int k0 = wave * 64 + 8 * g;
  u32x4 xr[2];
#pragma unroll
  for (int rr = 0; rr < 2; ++rr) xr[rr] = *(const u32x4*)(x + (size_t)(row0 + 2 * wave + rr) * D_ + 8 * lane);
  const u16* wbase = ((i < 4) ? (wk + 4 * i) : (i < 8 ? wv + 4 * (i - 4) : wk)) + k0 * 16;
  u32x2 w[2][8];
#pragma unroll
  for (int s = 0; s < 2; ++s)
#pragma unroll
    for (int j = 0; j < 8; ++j) w[s][j] = *(const u32x2*)(wbase + (32 * s + j) * 16);
  bf16x8 rf[2];
#pragma unroll
  for (int s = 0; s < 2; ++s) rf[s] = *(const bf16x8*)(wr + k0 + 32 * s);

  float s1[2], s2[2];
#pragma unroll
  for (int rr = 0; rr < 2; ++rr) {
    float a = 0.f, q = 0.f;
#pragma unroll
    for (int e = 0; e < 4; ++e) {
      const float lo = bf2f(xr[rr][e] & 0xffffu), hi = bfhi2f(xr[rr][e]);
      a += lo + hi;
      q = __builtin_fmaf(lo, lo, q);
      q = __builtin_fmaf(hi, hi, q);
    }
    s1[rr] = a; s2[rr] = q;
  }
#pragma unroll
  for (int off = 32; off >= 1; off >>= 1)
#pragma unroll
    for (int rr = 0; rr < 2; ++rr) {
      s1[rr] += __shfl_xor(s1[rr], off);
      s2[rr] += __shfl_xor(s2[rr], off);
    }
#pragma unroll
  for (int rr = 0; rr < 2; ++rr) {
    const float mean = s1[rr] * (1.0f / D_);
    float var = s2[rr] * (1.0f / D_) - mean * mean;
    var = fmaxf(var, 0.f);
    const float mb = rbf(mean);
    const float den = rbf(__builtin_sqrtf(rbf(rbf(var) + 1e-5f)));
    const float inv = 1.0f / den;
    if (lane == rr) stats[row0 + 2 * wave + rr] = make_float2(mb, den);
    u32x4 o;
#pragma unroll
    for (int e = 0; e < 4; ++e)
      o[e] = pack2(ln_elem(bf2f(xr[rr][e] & 0xffffu), mb, den, inv), ln_elem(bfhi2f(xr[rr][e]), mb, den, inv));
    *(u32x4*)(smem + (2 * wave + rr) * XN_STRIDE + lane * 16) = o;
  }
  if (tid < 2 && th == 0) {
    const unsigned cv = tid ? 0u : 0x3f803f80u;
    u32x4 c4 = {cv, cv, cv, cv};
    *(u32x4*)((char*)vf + rt * VT_STRIDE + 1024 + tid * 16) = c4;
  }
  TS(0, 1);
  __syncthreads();
  {
    const int r = lane & 15, hh = (lane >> 4) & 1, kp = lane >> 5;
#pragma unroll
    for (int s = 0; s < 2; ++s) {
      const int ks = 4 * wave + 2 * s + kp;
      const u32x4 v = *(const u32x4*)(smem + r * XN_STRIDE + ks * 32 + hh * 16);
      *(u32x4*)(xnf + ((size_t)(rt * 32 + ks) * 64 + hh * 32 + th * 16 + r) * 8) = v;
    }
  }
  f32x4* red = (f32x4*)(smem + 16 * XN_STRIDE);
  {
    f32x4 acc[4], ar;
    ar = (f32x4){0.f, 0.f, 0.f, 0.f};
#pragma unroll
    for (int c = 0; c < 4; ++c) acc[c] = (f32x4){0.f, 0.f, 0.f, 0.f};
#pragma unroll
    for (int s = 0; s < 2; ++s) {
      unsigned wx[8], wy[8];
#pragma unroll
      for (int j = 0; j < 8; ++j) { wx[j] = w[s][j][0]; wy[j] = w[s][j][1]; }
      bf16x8 b[4] = {frag_lo(wx), frag_hi(wx), frag_lo(wy), frag_hi(wy)};
      const bf16x8 xa = *(const bf16x8*)(smem + i * XN_STRIDE + (k0 + 32 * s) * 2);
#pragma unroll
      for (int c = 0; c < 4; ++c) acc[c] = mfma16(xa, b[c], acc[c]);
      ar = mfma16(xa, rf[s], ar);
    }
#pragma unroll
    for (int c = 0; c < 4; ++c) red[(wave * 5 + c) * 64 + lane] = acc[c];
    red[(wave * 5 + 4) * 64 + lane] = ar;
  }
  __syncthreads();
  TS(0, 2);
  if (wave < 5) {
    f32x4 v = red[wave * 64 + lane];
#pragma unroll
    for (int wv_ = 1; wv_ < 8; ++wv_) v += red[(wv_ * 5 + wave) * 64 + lane];
    const int kk = 16 * th + 4 * g;
    if (wave == 4) {
      unsigned k0 = 0, k1 = 0;
#pragma unroll
      for (int e = 0; e < 4; ++e) {
        const unsigned u = __float_as_uint(rbf(v[e]));
        const unsigned ord = (u & 0x80000000u) ? ~u : (u | 0x80000000u);
        const unsigned key = (ord & 0xffff0000u) | (unsigned)(2047 - (row0 + 4 * g + e));
        k1 = max(k1, min(k0, key));
        k0 = max(k0, key);
      }
#pragma unroll
      for (int off = 16; off < 64; off <<= 1) {
        const unsigned o0 = __shfl_xor(k0, off), o1 = __shfl_xor(k1, off);
        k1 = max(min(k0, o0), max(k1, o1));
        k0 = max(k0, o0);
      }
      if (lane == 0) *(u32x2*)(rkeys + 2 * blockIdx.x) = (u32x2){k0, k1};
    } else if (i < 4) {
      const int hh = i & 1, j = (i >> 1) * 4 + wave;
#pragma unroll
      for (int e = 0; e < 4; ++e)
        kf[rt * 512 + (hh * 32 + kk + e) * 8 + j] = __builtin_bit_cast(u16, (__bf16)v[e]);
    } else if (i < 8) {
      const int d = 4 * (i - 4) + wave;
      const int hh = g >> 1, jh = g & 1;
      u32x2 o = {pack2(v[0], v[1]), pack2(v[2], v[3])};
      *(u32x2*)(vf + rt * (VT_STRIDE / 2) + th * 256 + (hh * 16 + d) * 8 + jh * 4) = o;
    }
  }
  TS(0, 3); TSR(0, 9);
}

#define P1A1 32
#define P1B1 52
#define P1A2 61
#define P2A1 5
#define P2B1 7
#define P2A2U 6
#define PRIO_IF(a, b, p) asm volatile("s_cmp_lg_u32 %0, %1\n\ts_cbranch_scc1 1f\n\ts_setprio " #p "\n1:" :: "s"(a), "s"(b) : "scc")
static constexpr int LDS_K = 0;
static constexpr int LDS_V = 65536;
static constexpr int LDS_H = 65536 + VF_BYTES + 2048;
static constexpr int LDS_F = LDS_H + 1024;
static constexpr int LDS_HH = LDS_F + 64;
static constexpr int LDS_TOTAL = LDS_HH + 256;

DI void glds16(const char* gsrc, unsigned lds_dst) {
  unsigned keep;
  asm volatile("s_mov_b32 %0, m0\n\ts_mov_b32 m0, %2\n\ts_nop 0\n\tglobal_load_lds_dwordx4 %1, off\n\ts_mov_b32 m0, %0"
               : "=&s"(keep) : "v"(gsrc), "s"(lds_dst) : "memory");
}
DI void stage_v(const char* vsrc, unsigned vdst, int hj) {
#pragma unroll
  for (int c = 0; c < 16; ++c) glds16(vsrc + c * 4096, vdst + c * 4096);
  if (hj < 2) glds16(vsrc + 65536, vdst + 65536);
}

__global__ __launch_bounds__(768) void kb_attn(const u16* __restrict__ xnf, const u16* __restrict__ wqf,
                                               const u16* __restrict__ kf, const u16* __restrict__ vf,
                                               const unsigned* __restrict__ rkeys, const u16* __restrict__ wg,
                                               const u16* __restrict__ wu, const u16* __restrict__ wd,
                                               u16* __restrict__ of, float* __restrict__ ypart,
                                               int* __restrict__ sel) {
  __shared__ __attribute__((aligned(16))) char smem[LDS_TOTAL];
  const int tid = threadIdx.x;
  const int lane = tid & 63, wave = tid >> 6;
  const int b = blockIdx.x;
  unsigned* flags = (unsigned*)(smem + LDS_F);
  TSR(1, 8); TS(1, 0);

  if (wave < 8) {
    const int rb = (b & 7) + 8 * ((b >> 3) & 3), hg = b >> 5;
    const int rt = wave >> 2, ks4 = wave & 3;
    const int r = lane & 31, hh = lane >> 5;
    {
      bf16x8 af[2][8], bfr[8];
      const int rtq = wave & 1, kq = wave >> 1;
      const u16* ap = wqf + ((size_t)(2 * hg) * 32 + 8 * kq) * 512 + lane * 8;
      const u16* bp = xnf + ((size_t)(2 * rb + rtq) * 32 + 8 * kq) * 512 + lane * 8;
#pragma unroll
      for (int s = 0; s < 8; ++s) {
        bfr[s] = *(const bf16x8*)(bp + s * 512);
        af[0][s] = *(const bf16x8*)(ap + s * 512);
        af[1][s] = *(const bf16x8*)(ap + 32 * 512 + s * 512);
      }
      f32x16 acc[2];
#pragma unroll
      for (int e = 0; e < 16; ++e) { acc[0][e] = 0.f; acc[1][e] = 0.f; }
#pragma unroll
      for (int s = 0; s < 8; ++s) {
        acc[0] = mfma32(af[0][s], bfr[s], acc[0]);
        acc[1] = mfma32(af[1][s], bfr[s], acc[1]);
      }
#pragma unroll
      for (int dt = 0; dt < 2; ++dt)
#pragma unroll
        for (int q4 = 0; q4 < 4; ++q4) {
          f32x4 p = {acc[dt][4 * q4], acc[dt][4 * q4 + 1], acc[dt][4 * q4 + 2], acc[dt][4 * q4 + 3]};
          *(f32x4*)(smem + LDS_V + ((((rtq * 4 + kq) * 2 + dt) * 4 + q4) * 64 + lane) * 16) = p;
        }
    }
    __builtin_amdgcn_sched_barrier(0);
    TS(1, 1);
    __syncthreads();
    TS(1, 2);
    const int hl = wave & 3;
    const int head = hg * 4 + hl;
    bf16x8 qf;
    {
      f32x4 a0 = {0.f, 0.f, 0.f, 0.f}, a1 = {0.f, 0.f, 0.f, 0.f};
#pragma unroll
      for (int k = 0; k < 4; ++k) {
        const char* p = smem + LDS_V + ((((rt * 4 + k) * 2 + (hl >> 1)) * 4 + 2 * (hl & 1)) * 64 + lane) * 16;
        a0 += *(const f32x4*)p;
        a1 += *(const f32x4*)(p + 1024);
      }
      const float SC = 0.25f * 1.44269504088896340736f;
      u32x4 pk = {pack2(rbf(a0[0]) * SC, rbf(a0[1]) * SC), pack2(rbf(a0[2]) * SC, rbf(a0[3]) * SC),
                  pack2(rbf(a1[0]) * SC, rbf(a1[1]) * SC), pack2(rbf(a1[2]) * SC, rbf(a1[3]) * SC)};
      qf = __builtin_bit_cast(bf16x8, pk);
    }
    __syncthreads();

    const int yng = __builtin_amdgcn_readfirstlane(wave >> 2);
    const unsigned lbase = (unsigned)(uintptr_t)(__attribute__((address_space(3))) char*)smem;
    float m0 = -__builtin_inff(), m1 = -__builtin_inff();
    f32x16 zero16;
#pragma unroll
    for (int e = 0; e < 16; ++e) zero16[e] = 0.f;
    {
      unsigned kq_ = lbase + LDS_K + lane * 16;
      asm volatile("" : "+v"(kq_));
      f32x16 sa = mfma32(LDSF(kq_), qf, zero16);
      f32x16 sb = mfma32(LDSF(kq_ + 1024), qf, zero16);
      bf16x8 k2 = LDSF(kq_ + 2048), k3 = LDSF(kq_ + 3072);
      __builtin_amdgcn_sched_barrier(0);
#pragma unroll
      for (int t = 0; t < 64; ++t) {
        if (t == 0) __builtin_amdgcn_s_setprio(2);
        if (t == P1A1) PRIO_IF(yng, 0, 1);
        if (t == P1B1) PRIO_IF(yng, 1, 1);
        if (t == P1A2) PRIO_IF(yng, 0, 0);
        f32x16 sc = sb;
        bf16x8 k4 = k3;
        if (t + 2 < 64) sc = mfma32(k2, qf, zero16);
        if (t + 4 < 64) k4 = LDSF(kq_ + (t + 4) * 1024);
        if (t & 1) {
#pragma unroll
          for (int e = 0; e < 16; e += 2) m1 = fmaxf(fmaxf(m1, sa[e]), sa[e + 1]);
        } else {
#pragma unroll
          for (int e = 0; e < 16; e += 2) m0 = fmaxf(fmaxf(m0, sa[e]), sa[e + 1]);
        }
        sa = sb; sb = sc; k2 = k3; k3 = k4;
        __builtin_amdgcn_sched_group_barrier(0x008, 1, 0);
        __builtin_amdgcn_sched_group_barrier(0x100, 1, 0);
        __builtin_amdgcn_sched_group_barrier(0x002, 8, 0);
        __builtin_amdgcn_sched_barrier(0);
      }
    }
    float m = fmaxf(m0, m1);
    m = fmaxf(m, __shfl_xor(m, 32));
    TS(1, 3);
    while (__hip_atomic_load(flags, __ATOMIC_ACQUIRE, __HIP_MEMORY_SCOPE_WORKGROUP) < 4) __builtin_amdgcn_s_sleep(1);
    asm volatile("" ::: "memory");
    TS(1, 4);

    f32x16 negm;
#pragma unroll
    for (int e = 0; e < 16; ++e) negm[e] = -m;
    f32x16 o;
#pragma unroll
    for (int e = 0; e < 16; ++e) o[e] = 0.f;
    unsigned ka_ = lbase + LDS_K + lane * 16;
    unsigned va_ = lbase + LDS_V + ((r < 16) ? (hh * 16 + r) * 16 : (r == 16 ? 1024 : 1040));
    unsigned vb_ = lbase + LDS_V + ((r < 16) ? (hh * 16 + r) * 16 + 512 : (r == 16 ? 1024 : 1040));
    f32x16 s = mfma32(LDSF(ka_), qf, negm);
    bf16x8 kn = LDSF(ka_ + 1024);
    bf16x8 v0 = LDSF(va_);
    bf16x8 v1 = LDSF(vb_);
    bf16x8 v1p = v1;
    f32x8 pbp = {0.f, 0.f, 0.f, 0.f, 0.f, 0.f, 0.f, 0.f};
    const int thr1 = P2A1 + (P2B1 - P2A1) * yng, thr2 = 7 + 92 * yng;
    __builtin_amdgcn_s_setprio(2);
#pragma unroll 1
    for (int tb = 0; tb < 8; ++tb) {
      asm volatile("" : "+v"(ka_), "+v"(va_), "+v"(vb_));
      PRIO_IF(tb, thr1, 1);
#pragma unroll
      for (int u = 0; u < 8; ++u) {
        if (u == P2A2U) PRIO_IF(tb, thr2, 0);
        const f32x16 sn = mfma32(kn, qf, negm);
        const bf16x8 k2 = LDSF(ka_ + (u + 2) * 1024);
        const bf16x8 v0n = LDSF(va_ + (u + 1) * VT_STRIDE);
        const bf16x8 v1n = LDSF(vb_ + (u + 1) * VT_STRIDE);
        f32x8 pa, pb;
#pragma unroll
        for (int e = 0; e < 5; ++e) pa[e] = __builtin_amdgcn_exp2f(s[e]);
        o = smfma32(v1p, pbp, o);
#pragma unroll
        for (int e = 5; e < 8; ++e) pa[e] = __builtin_amdgcn_exp2f(s[e]);
#pragma unroll
        for (int e = 8; e < 10; ++e) pb[e - 8] = __builtin_amdgcn_exp2f(s[e]);
        o = smfma32(v0, pa, o);
#pragma unroll
        for (int e = 10; e < 16; ++e) pb[e - 8] = __builtin_amdgcn_exp2f(s[e]);
        pbp = pb;
        v1p = v1;
        s = sn; kn = k2; v0 = v0n; v1 = v1n;
        __builtin_amdgcn_sched_group_barrier(0x008, 1, 0);
        __builtin_amdgcn_sched_group_barrier(0x100, 3, 0);
        __builtin_amdgcn_sched_group_barrier(0x402, 5, 0);
        __builtin_amdgcn_sched_group_barrier(0x008, 1, 0);
        __builtin_amdgcn_sched_group_barrier(0x402, 5, 0);
        __builtin_amdgcn_sched_group_barrier(0x008, 1, 0);
        __builtin_amdgcn_sched_group_barrier(0x402, 6, 0);
        __builtin_amdgcn_sched_barrier(0);
      }
      ka_ += 8 * 1024; va_ += 8 * VT_STRIDE; vb_ += 8 * VT_STRIDE;
    }
    o = smfma32(v1p, pbp, o);
    TS(1, 5);
    const float il = 1.0f / __shfl(o[8], r);
    u32x4 w0 = {pack2(o[0] * il, o[1] * il), pack2(o[2] * il, o[3] * il), pack2(o[4] * il, o[5] * il),
                pack2(o[6] * il, o[7] * il)};
    *(u32x4*)(of + ((size_t)((2 * rb + rt) * 32 + head) * 64 + lane) * 8) = w0;
  } else {
    __builtin_amdgcn_s_setprio(3);
    const int hj = __builtin_amdgcn_readfirstlane(wave - 8);
    const unsigned ldst = (unsigned)(uintptr_t)(__attribute__((address_space(3))) char*)smem + hj * 1024;
    {
      const char* ksrc = (const char*)kf + hj * 1024 + lane * 16;
#pragma unroll
      for (int c = 0; c < 16; ++c) glds16(ksrc + c * 4096, ldst + LDS_K + c * 4096);
    }
    if (hj == 0 && lane < 2) flags[lane] = 0u;
    const char* vsrc = (const char*)vf + hj * 1024 + lane * 16;
    if (b >= NHELP) {
      asm volatile("s_waitcnt vmcnt(0)" ::: "memory");
      __syncthreads();
      __syncthreads();
      stage_v(vsrc, ldst + LDS_V, hj);
      asm volatile("s_waitcnt vmcnt(0)" ::: "memory");
      if (lane == 0) __hip_atomic_fetch_add(flags, 1u, __ATOMIC_RELEASE, __HIP_MEMORY_SCOPE_WORKGROUP);
      return;
    }
    const int g = lane >> 4, i = lane & 15;
    const int f0 = b * 16;
    const u32x4 kv = *(const u32x4*)(rkeys + lane * 4);
    unsigned k0 = max(kv[0], kv[2]), k1 = max(min(kv[0], kv[2]), max(kv[1], kv[3]));
#pragma unroll
    for (int off = 1; off < 64; off <<= 1) {
      const unsigned o0 = __shfl_xor(k0, off), o1 = __shfl_xor(k1, off);
      k1 = max(min(k0, o0), max(k1, o1));
      k0 = max(k0, o0);
    }
    const int i0 = 2047 - (int)(k0 & 0x7ffu), i1 = 2047 - (int)(k1 & 0x7ffu);
    if (b == 0 && hj == 0 && lane == 0) {
      const float v0 = __uint_as_float((k0 & 0x80000000u) ? (k0 & 0x7fff0000u) : (~k0 & 0xffff0000u));
      const float v1 = __uint_as_float((k1 & 0x80000000u) ? (k1 & 0x7fff0000u) : (~k1 & 0xffff0000u));
      sel[0] = i0; sel[1] = i1;
      ((float*)sel)[2] = rbf(1.0f / (1.0f + __expf(-v0)));
      ((float*)sel)[3] = rbf(1.0f / (1.0f + __expf(-v1)));
    }
    const int srow = (i & 1) ? i1 : i0;
    const u16* xp = xnf + ((size_t)((srow >> 5) * 32 + 16 * (hj >> 1) + 8 * ((i >> 1) & 1) + (g >> 1)) * 64 + (g & 1) * 32 + (srow & 31)) * 8;
    bf16x8 xa[4];
#pragma unroll
    for (int s = 0; s < 4; ++s) xa[s] = *(const bf16x8*)(xp + (size_t)s * 1024);
    TS(1, 3);
    asm volatile("s_waitcnt vmcnt(0)" ::: "memory");
    __syncthreads();
    __syncthreads();
    stage_v(vsrc, ldst + LDS_V, hj);
    asm volatile("s_waitcnt vmcnt(0)" ::: "memory");
    if (lane == 0) __hip_atomic_fetch_add(flags, 1u, __ATOMIC_RELEASE, __HIP_MEMORY_SCOPE_WORKGROUP);
    __builtin_amdgcn_sched_barrier(0);
    unsigned wa[4][8];
    const char* ub = (const char*)(((hj & 1) ? wu : wg) + (size_t)(256 * (hj >> 1)) * F_ + f0);
    const unsigned lo1 = (unsigned)((128 * (i >> 3) + 8 * g) * F_ + 2 * (i & 7)) * 2u;
#pragma unroll
    for (int s = 0; s < 4; ++s)
#pragma unroll
      for (int j = 0; j < 8; j += 2) {
        gcp rp = (gcp)(ub + (size_t)((32 * s + j) * F_ * 2));
        asm volatile("" : "+s"(rp));
        wa[s][j] = *(const __attribute__((address_space(1))) unsigned*)(rp + lo1);
        wa[s][j + 1] = *(const __attribute__((address_space(1))) unsigned*)(rp + F_ * 2 + lo1);
      }
    unsigned wdv[4][4];
    const char* db = (const char*)(wd + (size_t)f0 * D_ + hj * 128);
    const unsigned lo2 = (unsigned)(4 * g * D_ + 2 * i) * 2u;
#pragma unroll
    for (int q = 0; q < 4; ++q)
#pragma unroll
      for (int j = 0; j < 4; ++j) wdv[q][j] = *(const unsigned*)(db + (j * D_ + 32 * q) * 2 + lo2);
    f32x4 a0 = {0.f, 0.f, 0.f, 0.f}, a1 = {0.f, 0.f, 0.f, 0.f};
#pragma unroll
    for (int s = 0; s < 4; ++s) {
      a0 = mfma16(xa[s], frag_lo(wa[s]), a0);
      a1 = mfma16(xa[s], frag_hi(wa[s]), a1);
    }
    if (lane < 16)
      *(f32x4*)(smem + LDS_H + (hj * 16 + i) * 16) =
          (i < 8) ? (f32x4){a0[0], a0[1], a1[0], a1[1]} : (f32x4){a0[2], a0[3], a1[2], a1[3]};
    TS(1, 4);
    if (lane == 0) __hip_atomic_fetch_add(flags + 1, 1u, __ATOMIC_RELEASE, __HIP_MEMORY_SCOPE_WORKGROUP);
    while (__hip_atomic_load(flags + 1, __ATOMIC_ACQUIRE, __HIP_MEMORY_SCOPE_WORKGROUP) < 4) __builtin_amdgcn_s_sleep(2);
    asm volatile("" ::: "memory");
    __builtin_amdgcn_sched_barrier(0);
    const float* hp = (const float*)(smem + LDS_H) + ((i >> 3) * 16 + (i & 7)) * 4 + g;
    const float gt = rbf((hp[0] + hp[32]) + (hp[128] + hp[160]));
    const float up = __shfl_xor(gt, 8);
    const float hv = rbf(gt / (1.0f + __expf(-gt))) * up;
    if (i < 8) *(u16*)(smem + LDS_HH + hj * 64 + (g & 1) * 32 + (2 * i + (g >> 1)) * 2) = __builtin_bit_cast(u16, (__bf16)hv);
    const s16x4 ha = *(const s16x4*)(smem + LDS_HH + hj * 64 + (i & 1) * 32 + g * 8);
#pragma unroll
    for (int q = 0; q < 4; ++q) {
      const u32x2 be = {perm_lo(wdv[q][1], wdv[q][0]), perm_lo(wdv[q][3], wdv[q][2])};
      const u32x2 bo = {perm_hi(wdv[q][1], wdv[q][0]), perm_hi(wdv[q][3], wdv[q][2])};
      const f32x4 z = {0.f, 0.f, 0.f, 0.f};
      const f32x4 ye = __builtin_amdgcn_mfma_f32_16x16x16bf16_1k(ha, __builtin_bit_cast(s16x4, be), z, 0, 0, 0);
      const f32x4 yo = __builtin_amdgcn_mfma_f32_16x16x16bf16_1k(ha, __builtin_bit_cast(s16x4, bo), z, 0, 0, 0);
      if (lane < 16) {
        float* yp = ypart + (size_t)b * 1024 + hj * 128 + 32 * q + 2 * i;
        *(float2*)yp = make_float2(ye[0], yo[0]);
        *(float2*)(yp + 512) = make_float2(ye[1], yo[1]);
      }
    }
  }
  TS(1, 6); TSR(1, 9);
}

static constexpr int CH_STRIDE = 1056;
static constexpr int LDS_Y = 64 * CH_STRIDE;
__global__ __launch_bounds__(512) void kc_out(const u16* __restrict__ x, const u16* __restrict__ of,
                                              const u16* __restrict__ wof, const float2* __restrict__ stats,
                                              const float* __restrict__ ypart, const int* __restrict__ sel,
                                              u16* __restrict__ out) {
  __shared__ __attribute__((aligned(16))) char smem[LDS_Y + 2 * 8 * 256];
  const int tid = threadIdx.x;
  const int lane = tid & 63, wave = tid >> 6;
  TSR(2, 8); TS(2, 0);
  const int bb = blockIdx.x;
  const int rb = (bb & 7) + 8 * ((bb >> 3) & 3), cg = bb >> 5;
  const int rt = wave & 1, ks4 = wave >> 1;
  const int idx0 = sel[0], idx1 = sel[1];
  const float rs0 = ((const float*)sel)[2], rs1 = ((const float*)sel)[3];
  bf16x8 af[2][8], bfr[8];
  const u16* ap = wof + ((size_t)(2 * cg) * 32 + 8 * ks4) * 512 + lane * 8;
  const u16* bp = of + ((size_t)(2 * rb + rt) * 32 + 8 * ks4) * 512 + lane * 8;
#pragma unroll
  for (int s = 0; s < 8; ++s) {
    bfr[s] = *(const bf16x8*)(bp + s * 512);
    af[0][s] = *(const bf16x8*)(ap + s * 512);
    af[1][s] = *(const bf16x8*)(ap + 32 * 512 + s * 512);
  }
  const int r64 = tid >> 3, oct = tid & 7;
  const int row = rb * 64 + r64;
  const size_t off = (size_t)row * D_ + cg * 64 + oct * 8;
  const u32x4 xv = *(const u32x4*)(x + off);
  const float2 st = stats[row];
  f32x4 yv[2][3];
#pragma unroll
  for (int tk = 0; tk < 2; ++tk) {
    const int idx = tk ? idx1 : idx0;
#pragma unroll
    for (int q = 0; q < 3; ++q) yv[tk][q] = (f32x4){0.f, 0.f, 0.f, 0.f};
    if ((unsigned)(idx - rb * 64) < 64u) {
      const int pg = tid >> 4;
      const float* yp = ypart + (size_t)(pg * 2 + tk) * 512 + cg * 64 + (tid & 15) * 4;
      yv[tk][0] = *(const f32x4*)yp;
      yv[tk][1] = *(const f32x4*)(yp + 32 * 1024);
      yv[tk][2] = *(const f32x4*)(yp + (pg + 64 < NHELP ? 64 * 1024 : 0));
    }
  }
  f32x16 acc[2];
#pragma unroll
  for (int e = 0; e < 16; ++e) { acc[0][e] = 0.f; acc[1][e] = 0.f; }
#pragma unroll
  for (int s = 0; s < 8; ++s) {
    acc[0] = mfma32(af[0][s], bfr[s], acc[0]);
    acc[1] = mfma32(af[1][s], bfr[s], acc[1]);
  }
  TS(2, 1);
#pragma unroll
  for (int ct = 0; ct < 2; ++ct)
#pragma unroll
    for (int q4 = 0; q4 < 4; ++q4) {
      f32x4 p = {acc[ct][4 * q4], acc[ct][4 * q4 + 1], acc[ct][4 * q4 + 2], acc[ct][4 * q4 + 3]};
      *(f32x4*)(smem + (((rt * 4 + ks4) * 2 + ct) * 4 + q4) * CH_STRIDE + lane * 16) = p;
    }
#pragma unroll
  for (int tk = 0; tk < 2; ++tk) {
    const int idx = tk ? idx1 : idx0;
    if ((unsigned)(idx - rb * 64) < 64u) {
      f32x4 y = yv[tk][0] + yv[tk][1];
      if ((tid >> 4) + 64 < NHELP) y += yv[tk][2];
#pragma unroll
      for (int e = 0; e < 4; ++e) {
        y[e] += __shfl_xor(y[e], 16);
        y[e] += __shfl_xor(y[e], 32);
      }
      if (lane < 16) *(f32x4*)(smem + LDS_Y + (tk * 8 + wave) * 256 + lane * 16) = y;
    }
  }
  __syncthreads();
  TS(2, 2);
  f32x4 lo = {0.f, 0.f, 0.f, 0.f}, hi = {0.f, 0.f, 0.f, 0.f};
#pragma unroll
  for (int k = 0; k < 4; ++k) {
    const char* p = smem + (((r64 >> 5) * 4 + k) * 8 + oct) * CH_STRIDE + (r64 & 31) * 16;
    lo += *(const f32x4*)p;
    hi += *(const f32x4*)(p + 512);
  }
  float upd[8];
#pragma unroll
  for (int e = 0; e < 8; ++e) upd[e] = 0.f;
  if (row == idx0 || row == idx1) {
    const int tk = (row == idx0) ? 0 : 1;
    f32x4 ya = {0.f, 0.f, 0.f, 0.f}, yb = {0.f, 0.f, 0.f, 0.f};
#pragma unroll
    for (int p = 0; p < 8; ++p) {
      const char* yp = smem + LDS_Y + (tk * 8 + p) * 256 + oct * 32;
      ya += *(const f32x4*)yp;
      yb += *(const f32x4*)(yp + 16);
    }
    const float rs = tk ? rs1 : rs0;
#pragma unroll
    for (int e = 0; e < 4; ++e) { upd[e] = rbf(rbf(ya[e]) * rs); upd[4 + e] = rbf(rbf(yb[e]) * rs); }
  }
  const float inv = 1.0f / st.y;
  float res[8];
#pragma unroll
  for (int e = 0; e < 4; ++e) {
    const float x0 = bf2f(xv[e] & 0xffffu), x1 = bfhi2f(xv[e]);
    const float a0 = rbf(e < 2 ? lo[2 * e] : hi[2 * e - 4]);
    const float a1 = rbf(e < 2 ? lo[2 * e + 1] : hi[2 * e - 3]);
    res[2 * e] = rbf(x0 + a0) + rbf(ln_elem(x0, st.x, st.y, inv) + upd[2 * e]);
    res[2 * e + 1] = rbf(x1 + a1) + rbf(ln_elem(x1, st.x, st.y, inv) + upd[2 * e + 1]);
  }
  u32x4 o = {pack2(res[0], res[1]), pack2(res[2], res[3]), pack2(res[4], res[5]), pack2(res[6], res[7])};
  *(u32x4*)(out + off) = o;
  TS(2, 3); TSR(2, 9);
}

extern "C" void kernel_launch(void* const* d_in, const int* in_sizes, int n_in, void* d_out, int out_size,
                              void* d_ws, size_t ws_size, hipStream_t stream) {
  const u16* x = (const u16*)d_in[0];
  const u16* wq = (const u16*)d_in[1];
  const u16* wk = (const u16*)d_in[2];
  const u16* wv = (const u16*)d_in[3];
  const u16* wo = (const u16*)d_in[4];
  const u16* wr = (const u16*)d_in[6];
  const u16* wg = (const u16*)d_in[7];
  const u16* wu = (const u16*)d_in[8];
  const u16* wd = (const u16*)d_in[9];
  char* ws = (char*)d_ws;
  u16* xnf = (u16*)(ws + OFF_XNF);
  u16* of = (u16*)(ws + OFF_OF);
  u16* wqf = (u16*)(ws + OFF_WQF);
  u16* wof = (u16*)(ws + OFF_WOF);
  u16* kf = (u16*)(ws + OFF_KF);
  u16* vf = (u16*)(ws + OFF_VF);
  unsigned* rkeys = (unsigned*)(ws + OFF_R);
  float2* stats = (float2*)(ws + OFF_STATS);
  float* ypart = (float*)(ws + OFF_Y);
  int* sel = (int*)(ws + OFF_SEL);
  u16* out = (u16*)d_out;

  ka_prep<<<192, 512, 0, stream>>>(x, wq, wk, wv, wo, wr, xnf, wqf, wof, kf, vf, rkeys, stats);
  kb_attn<<<256, 768, 0, stream>>>(xnf, wqf, kf, vf, rkeys, wg, wu, wd, of, ypart, sel);
  kc_out<<<256, 512, 0, stream>>>(x, of, wof, stats, ypart, sel, out);
}
```

```cpp
#include <hip/hip_runtime.h>
#include <hip/hip_bf16.h>

typedef unsigned short u16;
typedef __attribute__((ext_vector_type(8))) __bf16 bf16x8;
typedef __attribute__((ext_vector_type(16))) __bf16 bf16x16;
typedef __attribute__((ext_vector_type(2))) __bf16 bf16x2;
typedef __attribute__((ext_vector_type(8))) float f32x8;
typedef __attribute__((ext_vector_type(2))) float f32x2;
typedef __attribute__((ext_vector_type(4))) float f32x4;
typedef __attribute__((ext_vector_type(16))) float f32x16;
typedef __attribute__((ext_vector_type(4))) unsigned u32x4;
typedef __attribute__((ext_vector_type(2))) unsigned u32x2;
typedef __attribute__((ext_vector_type(4))) short s16x4;
typedef const __attribute__((address_space(1))) char* gcp;

#define DI __device__ __forceinline__
#define TS(k, n)
#define TSR(k, n)

static constexpr int S_ = 2048;
static constexpr int D_ = 512;
static constexpr int F_ = 1376;

#define LDSF(a) (*(const __attribute__((address_space(3))) bf16x8*)(uintptr_t)(a))
DI float bf2f(unsigned u) { return __uint_as_float(u << 16); }
DI float bfhi2f(unsigned u) { return __uint_as_float(u & 0xffff0000u); }
DI float rbf(float f) { return (float)(__bf16)f; }
DI unsigned pack2(float lo, float hi) {
  f32x2 p = {lo, hi};
  bf16x2 q = __builtin_convertvector(p, bf16x2);
  return __builtin_bit_cast(unsigned, q);
}
DI f32x4 mfma16(bf16x8 a, bf16x8 b, f32x4 c) { return __builtin_amdgcn_mfma_f32_16x16x32_bf16(a, b, c, 0, 0, 0); }
DI f32x16 mfma32(bf16x8 a, bf16x8 b, f32x16 c) { return __builtin_amdgcn_mfma_f32_32x32x16_bf16(a, b, c, 0, 0, 0); }
DI f32x16 smfma32(bf16x8 a, f32x8 p, f32x16 c) {
  return __builtin_amdgcn_smfmac_f32_32x32x32_bf16(a, __builtin_bit_cast(bf16x16, p), c, 0xdddd, 0, 0);
}
DI unsigned perm_lo(unsigned hi_src, unsigned lo_src) { return __builtin_amdgcn_perm(hi_src, lo_src, 0x05040100u); }
DI unsigned perm_hi(unsigned hi_src, unsigned lo_src) { return __builtin_amdgcn_perm(hi_src, lo_src, 0x07060302u); }

DI bf16x8 frag_lo(const unsigned* w) {
  u32x4 r = {perm_lo(w[1], w[0]), perm_lo(w[3], w[2]), perm_lo(w[5], w[4]), perm_lo(w[7], w[6])};
  return __builtin_bit_cast(bf16x8, r);
}
DI bf16x8 frag_hi(const unsigned* w) {
  u32x4 r = {perm_hi(w[1], w[0]), perm_hi(w[3], w[2]), perm_hi(w[5], w[4]), perm_hi(w[7], w[6])};
  return __builtin_bit_cast(bf16x8, r);
}
DI float ln_elem(float xv, float mb, float den, float inv) {
  const float d = rbf(xv - mb);
  float q = d * inv;
  const float rem = __builtin_fmaf(-q, den, d);
  q = __builtin_fmaf(rem, inv, q);
  return rbf(q);
}

static constexpr int VT_STRIDE = 1056;
static constexpr int VF_BYTES = 64 * VT_STRIDE;
static constexpr size_t OFF_XNF = 0;
static constexpr size_t OFF_OF = OFF_XNF + (size_t)S_ * D_ * 2;
static constexpr size_t OFF_WQF = OFF_OF + (size_t)S_ * D_ * 2;
static constexpr size_t OFF_WOF = OFF_WQF + (size_t)D_ * D_ * 2;
static constexpr size_t OFF_KF = OFF_WOF + (size_t)D_ * D_ * 2;
static constexpr size_t OFF_VF = OFF_KF + 65536;
static constexpr size_t OFF_R = OFF_VF + VF_BYTES;
static constexpr size_t OFF_STATS = OFF_R + S_ * 4;
static constexpr int NHELP = 86;
static constexpr size_t OFF_Y = OFF_STATS + S_ * 8;
static constexpr size_t OFF_CNT = OFF_Y + NHELP * 1024 * 4;

static constexpr int XN_STRIDE = 1040;
__global__ __launch_bounds__(512) void ka_prep(const u16* __restrict__ x, const u16* __restrict__ wq,
                                               const u16* __restrict__ wk, const u16* __restrict__ wv,
                                               const u16* __restrict__ wo, const u16* __restrict__ wr,
                                               u16* __restrict__ xnf, u16* __restrict__ wqf, u16* __restrict__ wof,
                                               u16* __restrict__ kf, u16* __restrict__ vf,
                                               unsigned* __restrict__ rkeys, float2* __restrict__ stats,
                                               unsigned* __restrict__ cnt) {
  __shared__ __attribute__((aligned(16))) char smem[16 * XN_STRIDE + 8 * 5 * 64 * 16];
  const int tid = threadIdx.x;
  const int lane = tid & 63, wave = tid >> 6;
  TSR(0, 8); TS(0, 0);
  if (blockIdx.x >= 128) {
    const int pb = blockIdx.x - 128;
    const bool iso = pb >= 32;
    if (pb == 0 && tid < 33) cnt[tid * 32] = 0u;
    const int cp = tid & 255, ko = (pb & 31) * 2 + (tid >> 8);
    const int ks = ko >> 1, hh = ko & 1;
    const u16* W = (iso ? wo : wq) + 2 * cp;
    unsigned w[8];
#pragma unroll
    for (int j = 0; j < 8; ++j) {
      const int row = 16 * ks + (iso ? (8 * (j >> 2) + 4 * hh + (j & 3)) : (8 * hh + j));
      w[j] = *(const unsigned*)(W + row * D_);
    }
    u16* dst = (iso ? wof : wqf) + (((cp >> 4) * 32 + ks) * 64 + hh * 32 + ((2 * cp) & 31)) * 8;
    *(bf16x8*)dst = frag_lo(w);
    *(bf16x8*)(dst + 8) = frag_hi(w);
    TS(0, 3); TSR(0, 9);
    return;
  }
  const int row0 = blockIdx.x * 16;
  const int rt = blockIdx.x >> 1, th = blockIdx.x & 1;
  const int g = lane >> 4, i = lane & 15;
  const int k0 = wave * 64 + 8 * g;
  u32x4 xr[2];
#pragma unroll
  for (int rr = 0; rr < 2; ++rr) xr[rr] = *(const u32x4*)(x + (size_t)(row0 + 2 * wave + rr) * D_ + 8 * lane);
  const u16* wbase = ((i < 4) ? (wk + 4 * i) : (i < 8 ? wv + 4 * (i - 4) : wk)) + k0 * 16;
  u32x2 w[2][8];
#pragma unroll
  for (int s = 0; s < 2; ++s)
#pragma unroll
    for (int j = 0; j < 8; ++j) w[s][j] = *(const u32x2*)(wbase + (32 * s + j) * 16);
  bf16x8 rf[2];
#pragma unroll
  for (int s = 0; s < 2; ++s) rf[s] = *(const bf16x8*)(wr + k0 + 32 * s);

  float s1[2], s2[2];
#pragma unroll
  for (int rr = 0; rr < 2; ++rr) {
    float a = 0.f, q = 0.f;
#pragma unroll
    for (int e = 0; e < 4; ++e) {
      const float lo = bf2f(xr[rr][e] & 0xffffu), hi = bfhi2f(xr[rr][e]);
      a += lo + hi;
      q = __builtin_fmaf(lo, lo, q);
      q = __builtin_fmaf(hi, hi, q);
    }
    s1[rr] = a; s2[rr] = q;
  }
#pragma unroll
  for (int off = 32; off >= 1; off >>= 1)
#pragma unroll
    for (int rr = 0; rr < 2; ++rr) {
      s1[rr] += __shfl_xor(s1[rr], off);
      s2[rr] += __shfl_xor(s2[rr], off);
    }
#pragma unroll
  for (int rr = 0; rr < 2; ++rr) {
    const float mean = s1[rr] * (1.0f / D_);
    float var = s2[rr] * (1.0f / D_) - mean * mean;
    var = fmaxf(var, 0.f);
    const float mb = rbf(mean);
    const float den = rbf(__builtin_sqrtf(rbf(rbf(var) + 1e-5f)));
    const float inv = 1.0f / den;
    if (lane == rr) stats[row0 + 2 * wave + rr] = make_float2(mb, den);
    u32x4 o;
#pragma unroll
    for (int e = 0; e < 4; ++e)
      o[e] = pack2(ln_elem(bf2f(xr[rr][e] & 0xffffu), mb, den, inv), ln_elem(bfhi2f(xr[rr][e]), mb, den, inv));
    *(u32x4*)(smem + (2 * wave + rr) * XN_STRIDE + lane * 16) = o;
  }
  if (tid < 2 && th == 0) {
    const unsigned cv = tid ? 0u : 0x3f803f80u;
    u32x4 c4 = {cv, cv, cv, cv};
    *(u32x4*)((char*)vf + rt * VT_STRIDE + 1024 + tid * 16) = c4;
  }
  TS(0, 1);
  __syncthreads();
  {
    const int r = lane & 15, hh = (lane >> 4) & 1, kp = lane >> 5;
#pragma unroll
    for (int s = 0; s < 2; ++s) {
      const int ks = 4 * wave + 2 * s + kp;
      const u32x4 v = *(const u32x4*)(smem + r * XN_STRIDE + ks * 32 + hh * 16);
      *(u32x4*)(xnf + ((size_t)(rt * 32 + ks) * 64 + hh * 32 + th * 16 + r) * 8) = v;
    }
  }
  f32x4* red = (f32x4*)(smem + 16 * XN_STRIDE);
  {
    f32x4 acc[4], ar;
    ar = (f32x4){0.f, 0.f, 0.f, 0.f};
#pragma unroll
    for (int c = 0; c < 4; ++c) acc[c] = (f32x4){0.f, 0.f, 0.f, 0.f};
#pragma unroll
    for (int s = 0; s < 2; ++s) {
      unsigned wx[8], wy[8];
#pragma unroll
      for (int j = 0; j < 8; ++j) { wx[j] = w[s][j][0]; wy[j] = w[s][j][1]; }
      bf16x8 b[4] = {frag_lo(wx), frag_hi(wx), frag_lo(wy), frag_hi(wy)};
      const bf16x8 xa = *(const bf16x8*)(smem + i * XN_STRIDE + (k0 + 32 * s) * 2);
#pragma unroll
      for (int c = 0; c < 4; ++c) acc[c] = mfma16(xa, b[c], acc[c]);
      ar = mfma16(xa, rf[s], ar);
    }
#pragma unroll
    for (int c = 0; c < 4; ++c) red[(wave * 5 + c) * 64 + lane] = acc[c];
    red[(wave * 5 + 4) * 64 + lane] = ar;
  }
  __syncthreads();
  TS(0, 2);
  if (wave < 5) {
    f32x4 v = red[wave * 64 + lane];
#pragma unroll
    for (int wv_ = 1; wv_ < 8; ++wv_) v += red[(wv_ * 5 + wave) * 64 + lane];
    const int kk = 16 * th + 4 * g;
    if (wave == 4) {
      unsigned k0 = 0, k1 = 0;
#pragma unroll
      for (int e = 0; e < 4; ++e) {
        const unsigned u = __float_as_uint(rbf(v[e]));
        const unsigned ord = (u & 0x80000000u) ? ~u : (u | 0x80000000u);
        const unsigned key = (ord & 0xffff0000u) | (unsigned)(2047 - (row0 + 4 * g + e));
        k1 = max(k1, min(k0, key));
        k0 = max(k0, key);
      }
#pragma unroll
      for (int off = 16; off < 64; off <<= 1) {
        const unsigned o0 = __shfl_xor(k0, off), o1 = __shfl_xor(k1, off);
        k1 = max(min(k0, o0), max(k1, o1));
        k0 = max(k0, o0);
      }
      if (lane == 0) *(u32x2*)(rkeys + 2 * blockIdx.x) = (u32x2){k0, k1};
    } else if (i < 4) {
      const int hh = i & 1, j = (i >> 1) * 4 + wave;
#pragma unroll
      for (int e = 0; e < 4; ++e)
        kf[rt * 512 + (hh * 32 + kk + e) * 8 + j] = __builtin_bit_cast(u16, (__bf16)v[e]);
    } else if (i < 8) {
      const int d = 4 * (i - 4) + wave;
      const int hh = g >> 1, jh = g & 1;
      u32x2 o = {pack2(v[0], v[1]), pack2(v[2], v[3])};
      *(u32x2*)(vf + rt * (VT_STRIDE / 2) + th * 256 + (hh * 16 + d) * 8 + jh * 4) = o;
    }
  }
  TS(0, 3); TSR(0, 9);
}

#define P1A1 32
#define P1B1 52
#define P1A2 61
#define P2A1 5
#define P2B1 7
#define P2A2U 6
#define PRIO_IF(a, b, p) asm volatile("s_cmp_lg_u32 %0, %1\n\ts_cbranch_scc1 1f\n\ts_setprio " #p "\n1:" :: "s"(a), "s"(b) : "scc")
static constexpr int LDS_K = 0;
static constexpr int LDS_V = 65536;
static constexpr int LDS_H = 65536 + VF_BYTES + 2048;
static constexpr int LDS_F = LDS_H + 1024;
static constexpr int LDS_HH = LDS_F + 64;
static constexpr int LDS_Y = LDS_HH + 256;
static constexpr int LDS_TOTAL = LDS_Y + 2048;
static constexpr int CH_STRIDE = 1056;
typedef __attribute__((address_space(1))) unsigned gu32;
typedef __attribute__((address_space(1))) unsigned long long gu64;
#define RLX_AGENT __ATOMIC_RELAXED, __HIP_MEMORY_SCOPE_AGENT
DI void wait_count(unsigned* word, unsigned want) {
  for (unsigned spins = 0; spins < (1u << 18); ++spins) {
    if ((unsigned)__builtin_amdgcn_readfirstlane(__hip_atomic_load((gu32*)word, RLX_AGENT)) >= want) break;
    __builtin_amdgcn_s_sleep(1);
  }
  __builtin_amdgcn_fence(__ATOMIC_ACQUIRE, "agent");
  asm volatile("s_waitcnt vmcnt(0)" ::: "memory");
}

DI void glds16(const char* gsrc, unsigned lds_dst) {
  unsigned keep;
  asm volatile("s_mov_b32 %0, m0\n\ts_mov_b32 m0, %2\n\ts_nop 0\n\tglobal_load_lds_dwordx4 %1, off\n\ts_mov_b32 m0, %0"
               : "=&s"(keep) : "v"(gsrc), "s"(lds_dst) : "memory");
}
DI void stage_v(const char* vsrc, unsigned vdst, int hj) {
#pragma unroll
  for (int c = 0; c < 16; ++c) glds16(vsrc + c * 4096, vdst + c * 4096);
  if (hj < 2) glds16(vsrc + 65536, vdst + 65536);
}

__global__ __launch_bounds__(768) void kb_attn(const u16* __restrict__ xnf, const u16* __restrict__ wqf,
                                               const u16* __restrict__ kf, const u16* __restrict__ vf,
                                               const unsigned* __restrict__ rkeys, const u16* __restrict__ wg,
                                               const u16* __restrict__ wu, const u16* __restrict__ wd,
                                               u16* of, float* ypart, unsigned* cnt,
                                               const u16* __restrict__ x, const u16* __restrict__ wof,
                                               const float2* __restrict__ stats, u16* __restrict__ out) {
  __shared__ __attribute__((aligned(16))) char smem[LDS_TOTAL];
  const int tid = threadIdx.x;
  const int lane = tid & 63, wave = tid >> 6;
  const int b = blockIdx.x;
  unsigned* flags = (unsigned*)(smem + LDS_F);
  TSR(1, 8); TS(1, 0);

  const int rb = (b & 7) + 8 * (b >> 6), hg = (b >> 3) & 7;
  if (wave < 8) {
    const int rt = wave >> 2, ks4 = wave & 3;
    const int r = lane & 31, hh = lane >> 5;
    {
      bf16x8 af[2][8], bfr[8];
      const int rtq = wave & 1, kq = wave >> 1;
      const u16* ap = wqf + ((size_t)(2 * hg) * 32 + 8 * kq) * 512 + lane * 8;
      const u16* bp = xnf + ((size_t)(2 * rb + rtq) * 32 + 8 * kq) * 512 + lane * 8;
#pragma unroll
      for (int s = 0; s < 8; ++s) {
        bfr[s] = *(const bf16x8*)(bp + s * 512);
        af[0][s] = *(const bf16x8*)(ap + s * 512);
        af[1][s] = *(const bf16x8*)(ap + 32 * 512 + s * 512);
      }
      f32x16 acc[2];
#pragma unroll
      for (int e = 0; e < 16; ++e) { acc[0][e] = 0.f; acc[1][e] = 0.f; }
#pragma unroll
      for (int s = 0; s < 8; ++s) {
        acc[0] = mfma32(af[0][s], bfr[s], acc[0]);
        acc[1] = mfma32(af[1][s], bfr[s], acc[1]);
      }
#pragma unroll
      for (int dt = 0; dt < 2; ++dt)
#pragma unroll
        for (int q4 = 0; q4 < 4; ++q4) {
          f32x4 p = {acc[dt][4 * q4], acc[dt][4 * q4 + 1], acc[dt][4 * q4 + 2], acc[dt][4 * q4 + 3]};
          *(f32x4*)(smem + LDS_V + ((((rtq * 4 + kq) * 2 + dt) * 4 + q4) * 64 + lane) * 16) = p;
        }
    }
    __builtin_amdgcn_sched_barrier(0);
    TS(1, 1);
    __syncthreads();
    TS(1, 2);
    const int hl = wave & 3;
    const int head = hg * 4 + hl;
    bf16x8 qf;
    {
      f32x4 a0 = {0.f, 0.f, 0.f, 0.f}, a1 = {0.f, 0.f, 0.f, 0.f};
#pragma unroll
      for (int k = 0; k < 4; ++k) {
        const char* p = smem + LDS_V + ((((rt * 4 + k) * 2 + (hl >> 1)) * 4 + 2 * (hl & 1)) * 64 + lane) * 16;
        a0 += *(const f32x4*)p;
        a1 += *(const f32x4*)(p + 1024);
      }
      const float SC = 0.25f * 1.44269504088896340736f;
      u32x4 pk = {pack2(rbf(a0[0]) * SC, rbf(a0[1]) * SC), pack2(rbf(a0[2]) * SC, rbf(a0[3]) * SC),
                  pack2(rbf(a1[0]) * SC, rbf(a1[1]) * SC), pack2(rbf(a1[2]) * SC, rbf(a1[3]) * SC)};
      qf = __builtin_bit_cast(bf16x8, pk);
    }
    __syncthreads();

    const int yng = __builtin_amdgcn_readfirstlane(wave >> 2);
    const unsigned lbase = (unsigned)(uintptr_t)(__attribute__((address_space(3))) char*)smem;
    float m0 = -__builtin_inff(), m1 = -__builtin_inff();
    f32x16 zero16;
#pragma unroll
    for (int e = 0; e < 16; ++e) zero16[e] = 0.f;
    {
      unsigned kq_ = lbase + LDS_K + lane * 16;
      asm volatile("" : "+v"(kq_));
      f32x16 sa = mfma32(LDSF(kq_), qf, zero16);
      f32x16 sb = mfma32(LDSF(kq_ + 1024), qf, zero16);
      bf16x8 k2 = LDSF(kq_ + 2048), k3 = LDSF(kq_ + 3072);
      __builtin_amdgcn_sched_barrier(0);
#pragma unroll
      for (int t = 0; t < 64; ++t) {
        if (t == 0) __builtin_amdgcn_s_setprio(2);
        if (t == P1A1) PRIO_IF(yng, 0, 1);
        if (t == P1B1) PRIO_IF(yng, 1, 1);
        if (t == P1A2) PRIO_IF(yng, 0, 0);
        f32x16 sc = sb;
        bf16x8 k4 = k3;
        if (t + 2 < 64) sc = mfma32(k2, qf, zero16);
        if (t + 4 < 64) k4 = LDSF(kq_ + (t + 4) * 1024);
        if (t & 1) {
#pragma unroll
          for (int e = 0; e < 16; e += 2) m1 = fmaxf(fmaxf(m1, sa[e]), sa[e + 1]);
        } else {
#pragma unroll
          for (int e = 0; e < 16; e += 2) m0 = fmaxf(fmaxf(m0, sa[e]), sa[e + 1]);
        }
        sa = sb; sb = sc; k2 = k3; k3 = k4;
        __builtin_amdgcn_sched_group_barrier(0x008, 1, 0);
        __builtin_amdgcn_sched_group_barrier(0x100, 1, 0);
        __builtin_amdgcn_sched_group_barrier(0x002, 8, 0);
        __builtin_amdgcn_sched_barrier(0);
      }
    }
    float m = fmaxf(m0, m1);
    m = fmaxf(m, __shfl_xor(m, 32));
    TS(1, 3);
    while (__hip_atomic_load(flags, __ATOMIC_ACQUIRE, __HIP_MEMORY_SCOPE_WORKGROUP) < 4) __builtin_amdgcn_s_sleep(1);
    asm volatile("" ::: "memory");
    TS(1, 4);
    const int r64 = tid >> 3, oct = tid & 7;
    const int row = rb * 64 + r64;
    const size_t off = (size_t)row * D_ + hg * 64 + oct * 8;
    const u32x4 xv = *(const u32x4*)(x + off);
    const float2 st = stats[row];

    f32x16 negm;
#pragma unroll
    for (int e = 0; e < 16; ++e) negm[e] = -m;
    f32x16 o;
#pragma unroll
    for (int e = 0; e < 16; ++e) o[e] = 0.f;
    unsigned ka_ = lbase + LDS_K + lane * 16;
    unsigned va_ = lbase + LDS_V + ((r < 16) ? (hh * 16 + r) * 16 : (r == 16 ? 1024 : 1040));
    unsigned vb_ = lbase + LDS_V + ((r < 16) ? (hh * 16 + r) * 16 + 512 : (r == 16 ? 1024 : 1040));
    f32x16 s = mfma32(LDSF(ka_), qf, negm);
    bf16x8 kn = LDSF(ka_ + 1024);
    bf16x8 v0 = LDSF(va_);
    bf16x8 v1 = LDSF(vb_);
    bf16x8 v1p = v1;
    f32x8 pbp = {0.f, 0.f, 0.f, 0.f, 0.f, 0.f, 0.f, 0.f};
    const int thr1 = P2A1 + (P2B1 - P2A1) * yng, thr2 = 7 + 92 * yng;
    __builtin_amdgcn_s_setprio(2);
#pragma unroll 1
    for (int tb = 0; tb < 8; ++tb) {
      asm volatile("" : "+v"(ka_), "+v"(va_), "+v"(vb_));
      PRIO_IF(tb, thr1, 1);
#pragma unroll
      for (int u = 0; u < 8; ++u) {
        if (u == P2A2U) PRIO_IF(tb, thr2, 0);
        const f32x16 sn = mfma32(kn, qf, negm);
        const bf16x8 k2 = LDSF(ka_ + (u + 2) * 1024);
        const bf16x8 v0n = LDSF(va_ + (u + 1) * VT_STRIDE);
        const bf16x8 v1n = LDSF(vb_ + (u + 1) * VT_STRIDE);
        f32x8 pa, pb;
#pragma unroll
        for (int e = 0; e < 5; ++e) pa[e] = __builtin_amdgcn_exp2f(s[e]);
        o = smfma32(v1p, pbp, o);
#pragma unroll
        for (int e = 5; e < 8; ++e) pa[e] = __builtin_amdgcn_exp2f(s[e]);
#pragma unroll
        for (int e = 8; e < 10; ++e) pb[e - 8] = __builtin_amdgcn_exp2f(s[e]);
        o = smfma32(v0, pa, o);
#pragma unroll
        for (int e = 10; e < 16; ++e) pb[e - 8] = __builtin_amdgcn_exp2f(s[e]);
        pbp = pb;
        v1p = v1;
        s = sn; kn = k2; v0 = v0n; v1 = v1n;
        __builtin_amdgcn_sched_group_barrier(0x008, 1, 0);
        __builtin_amdgcn_sched_group_barrier(0x100, 3, 0);
        __builtin_amdgcn_sched_group_barrier(0x402, 5, 0);
        __builtin_amdgcn_sched_group_barrier(0x008, 1, 0);
        __builtin_amdgcn_sched_group_barrier(0x402, 5, 0);
        __builtin_amdgcn_sched_group_barrier(0x008, 1, 0);
        __builtin_amdgcn_sched_group_barrier(0x402, 6, 0);
        __builtin_amdgcn_sched_barrier(0);
      }
      ka_ += 8 * 1024; va_ += 8 * VT_STRIDE; vb_ += 8 * VT_STRIDE;
    }
    o = smfma32(v1p, pbp, o);
    TS(1, 5);
    const float il = 1.0f / __shfl(o[8], r);
    u32x4 w0 = {pack2(o[0] * il, o[1] * il), pack2(o[2] * il, o[3] * il), pack2(o[4] * il, o[5] * il),
                pack2(o[6] * il, o[7] * il)};
    {
      const u16* dst = of + ((size_t)((2 * rb + rt) * 32 + head) * 64 + lane) * 8;
      asm volatile("global_store_dwordx4 %0, %1, off sc1\n\ts_nop 1\n\ts_waitcnt vmcnt(0)" :: "v"(dst), "v"(w0) : "memory");
    }
    __syncthreads();
    __syncthreads();
    TS(1, 6);
    {
      const int rto = wave & 1, ko = wave >> 1;
      const u16* bp = of + ((size_t)(2 * rb + rto) * 32 + 8 * ko) * 512 + lane * 8;
      bf16x8 bfr[8];
#pragma unroll
      for (int s8 = 0; s8 < 8; ++s8) bfr[s8] = *(const bf16x8*)(bp + s8 * 512);
      __builtin_amdgcn_sched_barrier(0);
      const unsigned wa_ = lbase + LDS_K + ko * 8192 + lane * 16;
      f32x16 acc[2];
#pragma unroll
      for (int e = 0; e < 16; ++e) { acc[0][e] = 0.f; acc[1][e] = 0.f; }
#pragma unroll
      for (int s8 = 0; s8 < 8; ++s8) {
        acc[0] = mfma32(LDSF(wa_ + s8 * 1024), bfr[s8], acc[0]);
        acc[1] = mfma32(LDSF(wa_ + 32768 + s8 * 1024), bfr[s8], acc[1]);
      }
#pragma unroll
      for (int ct = 0; ct < 2; ++ct)
#pragma unroll
        for (int q4 = 0; q4 < 4; ++q4) {
          f32x4 p = {acc[ct][4 * q4], acc[ct][4 * q4 + 1], acc[ct][4 * q4 + 2], acc[ct][4 * q4 + 3]};
          *(f32x4*)(smem + LDS_V + (((rto * 4 + ko) * 2 + ct) * 4 + q4) * CH_STRIDE + lane * 16) = p;
        }
    }
    __syncthreads();
    f32x4 lo = {0.f, 0.f, 0.f, 0.f}, hi = {0.f, 0.f, 0.f, 0.f};
#pragma unroll
    for (int k = 0; k < 4; ++k) {
      const char* p = smem + LDS_V + (((r64 >> 5) * 4 + k) * 8 + oct) * CH_STRIDE + (r64 & 31) * 16;
      lo += *(const f32x4*)p;
      hi += *(const f32x4*)(p + 512);
    }
    float upd[8];
#pragma unroll
    for (int e = 0; e < 8; ++e) upd[e] = 0.f;
    const u32x4 sl = *(const u32x4*)(smem + LDS_F + 16);
    if (row == (int)sl[0] || row == (int)sl[1]) {
      const int tk = (row == (int)sl[0]) ? 0 : 1;
      f32x4 ya = {0.f, 0.f, 0.f, 0.f}, yb = {0.f, 0.f, 0.f, 0.f};
#pragma unroll
      for (int p = 0; p < 4; ++p) {
        const char* yp = smem + LDS_Y + (tk * 4 + p) * 256 + oct * 32;
        ya += *(const f32x4*)yp;
        yb += *(const f32x4*)(yp + 16);
      }
      const float rs = __uint_as_float(tk ? sl[3] : sl[2]);
#pragma unroll
      for (int e = 0; e < 4; ++e) { upd[e] = rbf(rbf(ya[e]) * rs); upd[4 + e] = rbf(rbf(yb[e]) * rs); }
    }
    const float inv = 1.0f / st.y;
    float res[8];
#pragma unroll
    for (int e = 0; e < 4; ++e) {
      const float x0 = bf2f(xv[e] & 0xffffu), x1 = bfhi2f(xv[e]);
      const float a0 = rbf(e < 2 ? lo[2 * e] : hi[2 * e - 4]);
      const float a1 = rbf(e < 2 ? lo[2 * e + 1] : hi[2 * e - 3]);
      res[2 * e] = rbf(x0 + a0) + rbf(ln_elem(x0, st.x, st.y, inv) + upd[2 * e]);
      res[2 * e + 1] = rbf(x1 + a1) + rbf(ln_elem(x1, st.x, st.y, inv) + upd[2 * e + 1]);
    }
    u32x4 ov = {pack2(res[0], res[1]), pack2(res[2], res[3]), pack2(res[4], res[5]), pack2(res[6], res[7])};
    *(u32x4*)(out + off) = ov;
  } else {
    __builtin_amdgcn_s_setprio(3);
    const int hj = __builtin_amdgcn_readfirstlane(wave - 8);
    const unsigned ldst = (unsigned)(uintptr_t)(__attribute__((address_space(3))) char*)smem + hj * 1024;
    {
      const char* ksrc = (const char*)kf + hj * 1024 + lane * 16;
#pragma unroll
      for (int c = 0; c < 16; ++c) glds16(ksrc + c * 4096, ldst + LDS_K + c * 4096);
    }
    if (hj == 0 && lane < 4) flags[lane] = 0u;
    const char* vsrc = (const char*)vf + hj * 1024 + lane * 16;
    const int g = lane >> 4, i = lane & 15;
    const int f0 = b * 16;
    const bool mod = b < NHELP;
    const u32x4 kv = *(const u32x4*)(rkeys + lane * 4);
    unsigned k0 = max(kv[0], kv[2]), k1 = max(min(kv[0], kv[2]), max(kv[1], kv[3]));
#pragma unroll
    for (int off = 1; off < 64; off <<= 1) {
      const unsigned o0 = __shfl_xor(k0, off), o1 = __shfl_xor(k1, off);
      k1 = max(min(k0, o0), max(k1, o1));
      k0 = max(k0, o0);
    }
    const int i0 = 2047 - (int)(k0 & 0x7ffu), i1 = 2047 - (int)(k1 & 0x7ffu);
    if (hj == 0 && lane == 0) {
      const float v0 = __uint_as_float((k0 & 0x80000000u) ? (k0 & 0x7fff0000u) : (~k0 & 0xffff0000u));
      const float v1 = __uint_as_float((k1 & 0x80000000u) ? (k1 & 0x7fff0000u) : (~k1 & 0xffff0000u));
      *(u32x4*)(smem + LDS_F + 16) = (u32x4){(unsigned)i0, (unsigned)i1, __float_as_uint(rbf(1.0f / (1.0f + __expf(-v0)))),
                                             __float_as_uint(rbf(1.0f / (1.0f + __expf(-v1))))};
    }
    bf16x8 xa[4];
    if (mod) {
      const int srow = (i & 1) ? i1 : i0;
      const u16* xp = xnf + ((size_t)((srow >> 5) * 32 + 16 * (hj >> 1) + 8 * ((i >> 1) & 1) + (g >> 1)) * 64 + (g & 1) * 32 + (srow & 31)) * 8;
#pragma unroll
      for (int s = 0; s < 4; ++s) xa[s] = *(const bf16x8*)(xp + (size_t)s * 1024);
    }
    TS(1, 3);
    asm volatile("s_waitcnt vmcnt(0)" ::: "memory");
    __syncthreads();
    __syncthreads();
    stage_v(vsrc, ldst + LDS_V, hj);
    asm volatile("s_waitcnt vmcnt(0)" ::: "memory");
    if (lane == 0) __hip_atomic_fetch_add(flags, 1u, __ATOMIC_RELEASE, __HIP_MEMORY_SCOPE_WORKGROUP);
    __builtin_amdgcn_sched_barrier(0);
    if (mod) {
      unsigned wa[4][8];
      const char* ub = (const char*)(((hj & 1) ? wu : wg) + (size_t)(256 * (hj >> 1)) * F_ + f0);
      const unsigned lo1 = (unsigned)((128 * (i >> 3) + 8 * g) * F_ + 2 * (i & 7)) * 2u;
#pragma unroll
      for (int s = 0; s < 4; ++s)
#pragma unroll
        for (int j = 0; j < 8; j += 2) {
          gcp rp = (gcp)(ub + (size_t)((32 * s + j) * F_ * 2));
          asm volatile("" : "+s"(rp));
          wa[s][j] = *(const __attribute__((address_space(1))) unsigned*)(rp + lo1);
          wa[s][j + 1] = *(const __attribute__((address_space(1))) unsigned*)(rp + F_ * 2 + lo1);
        }
      unsigned wdv[4][4];
      const char* db = (const char*)(wd + (size_t)f0 * D_ + hj * 128);
      const unsigned lo2 = (unsigned)(4 * g * D_ + 2 * i) * 2u;
#pragma unroll
      for (int q = 0; q < 4; ++q)
#pragma unroll
        for (int j = 0; j < 4; ++j) wdv[q][j] = *(const unsigned*)(db + (j * D_ + 32 * q) * 2 + lo2);
      f32x4 a0 = {0.f, 0.f, 0.f, 0.f}, a1 = {0.f, 0.f, 0.f, 0.f};
#pragma unroll
      for (int s = 0; s < 4; ++s) {
        a0 = mfma16(xa[s], frag_lo(wa[s]), a0);
        a1 = mfma16(xa[s], frag_hi(wa[s]), a1);
      }
      if (lane < 16)
        *(f32x4*)(smem + LDS_H + (hj * 16 + i) * 16) =
            (i < 8) ? (f32x4){a0[0], a0[1], a1[0], a1[1]} : (f32x4){a0[2], a0[3], a1[2], a1[3]};
      TS(1, 4);
      if (lane == 0) __hip_atomic_fetch_add(flags + 1, 1u, __ATOMIC_RELEASE, __HIP_MEMORY_SCOPE_WORKGROUP);
      while (__hip_atomic_load(flags + 1, __ATOMIC_ACQUIRE, __HIP_MEMORY_SCOPE_WORKGROUP) < 4) __builtin_amdgcn_s_sleep(2);
      asm volatile("" ::: "memory");
      __builtin_amdgcn_sched_barrier(0);
      const float* hp = (const float*)(smem + LDS_H) + ((i >> 3) * 16 + (i & 7)) * 4 + g;
      const float gt = rbf((hp[0] + hp[32]) + (hp[128] + hp[160]));
      const float up = __shfl_xor(gt, 8);
      const float hv = rbf(gt / (1.0f + __expf(-gt))) * up;
      if (i < 8) *(u16*)(smem + LDS_HH + hj * 64 + (g & 1) * 32 + (2 * i + (g >> 1)) * 2) = __builtin_bit_cast(u16, (__bf16)hv);
      const s16x4 ha = *(const s16x4*)(smem + LDS_HH + hj * 64 + (i & 1) * 32 + g * 8);
#pragma unroll
      for (int q = 0; q < 4; ++q) {
        const u32x2 be = {perm_lo(wdv[q][1], wdv[q][0]), perm_lo(wdv[q][3], wdv[q][2])};
        const u32x2 bo = {perm_hi(wdv[q][1], wdv[q][0]), perm_hi(wdv[q][3], wdv[q][2])};
        const f32x4 z = {0.f, 0.f, 0.f, 0.f};
        const f32x4 ye = __builtin_amdgcn_mfma_f32_16x16x16bf16_1k(ha, __builtin_bit_cast(s16x4, be), z, 0, 0, 0);
        const f32x4 yo = __builtin_amdgcn_mfma_f32_16x16x16bf16_1k(ha, __builtin_bit_cast(s16x4, bo), z, 0, 0, 0);
        if (lane < 16) {
          gu64* yp = (gu64*)(ypart + (size_t)b * 1024 + hj * 128 + 32 * q + 2 * i);
          __hip_atomic_store(yp, __builtin_bit_cast(unsigned long long, (f32x2){ye[0], yo[0]}), RLX_AGENT);
          __hip_atomic_store(yp + 256, __builtin_bit_cast(unsigned long long, (f32x2){ye[1], yo[1]}), RLX_AGENT);
        }
      }
      asm volatile("s_waitcnt vmcnt(0)" ::: "memory");
      if (lane == 0 && __hip_atomic_fetch_add(flags + 2, 1u, __ATOMIC_ACQ_REL, __HIP_MEMORY_SCOPE_WORKGROUP) == 3u)
        __hip_atomic_fetch_add((gu32*)(cnt + 32 * 32), 1u, RLX_AGENT);
    }
    __builtin_amdgcn_sched_barrier(0);
    const bool own0 = (unsigned)(i0 - rb * 64) < 64u, own1 = (unsigned)(i1 - rb * 64) < 64u;
    if (own0 || own1) {
      wait_count(cnt + 32 * 32, NHELP);
#pragma unroll
      for (int tk = 0; tk < 2; ++tk) {
        if (tk ? own1 : own0) {
          const int p0 = hj * 4 + g;
          const float* yp = ypart + (size_t)(p0 * 2 + tk) * 512 + hg * 64 + i * 4;
          f32x4 yv[6];
#pragma unroll
          for (int q = 0; q < 6; ++q) yv[q] = *(const f32x4*)(yp + (p0 + 16 * q < NHELP ? q * 16 * 1024 : 0));
          f32x4 y = ((yv[0] + yv[1]) + (yv[2] + yv[3])) + yv[4];
          if (p0 + 80 < NHELP) y += yv[5];
#pragma unroll
          for (int e = 0; e < 4; ++e) {
            y[e] += __shfl_xor(y[e], 16);
            y[e] += __shfl_xor(y[e], 32);
          }
          if (lane < 16) *(f32x4*)(smem + LDS_Y + (tk * 4 + hj) * 256 + lane * 16) = y;
        }
      }
    }
    __builtin_amdgcn_sched_barrier(0);
    u32x4 wr_[16];
    const char* wsrc = (const char*)wof + (size_t)hg * 65536 + hj * 1024 + lane * 16;
#pragma unroll
    for (int c = 0; c < 16; ++c) wr_[c] = *(const u32x4*)(wsrc + c * 4096);
    TS(1, 5);
    __syncthreads();
    if (hj == 0 && lane == 0) __hip_atomic_fetch_add((gu32*)(cnt + rb * 32), 1u, RLX_AGENT);
#pragma unroll
    for (int c = 0; c < 16; ++c) *(u32x4*)(smem + LDS_K + hj * 1024 + lane * 16 + c * 4096) = wr_[c];
    if (hj == 0) wait_count(cnt + rb * 32, 8);
    __syncthreads();
    TS(1, 6);
  }
  TS(1, 7); TSR(1, 9);
}

extern "C" void kernel_launch(void* const* d_in, const int* in_sizes, int n_in, void* d_out, int out_size,
                              void* d_ws, size_t ws_size, hipStream_t stream) {
  const u16* x = (const u16*)d_in[0];
  const u16* wq = (const u16*)d_in[1];
  const u16* wk = (const u16*)d_in[2];
  const u16* wv = (const u16*)d_in[3];
  const u16* wo = (const u16*)d_in[4];
  const u16* wr = (const u16*)d_in[6];
  const u16* wg = (const u16*)d_in[7];
  const u16* wu = (const u16*)d_in[8];
  const u16* wd = (const u16*)d_in[9];
  char* ws = (char*)d_ws;
  u16* xnf = (u16*)(ws + OFF_XNF);
  u16* of = (u16*)(ws + OFF_OF);
  u16* wqf = (u16*)(ws + OFF_WQF);
  u16* wof = (u16*)(ws + OFF_WOF);
  u16* kf = (u16*)(ws + OFF_KF);
  u16* vf = (u16*)(ws + OFF_VF);
  unsigned* rkeys = (unsigned*)(ws + OFF_R);
  float2* stats = (float2*)(ws + OFF_STATS);
  float* ypart = (float*)(ws + OFF_Y);
  unsigned* cnt = (unsigned*)(ws + OFF_CNT);
  u16* out = (u16*)d_out;

  ka_prep<<<192, 512, 0, stream>>>(x, wq, wk, wv, wo, wr, xnf, wqf, wof, kf, vf, rkeys, stats, cnt);
  kb_attn<<<256, 768, 0, stream>>>(xnf, wqf, kf, vf, rkeys, wg, wu, wd, of, ypart, cnt, x, wof, stats, out);
}
```

```cpp
#include <hip/hip_runtime.h>
#include <hip/hip_bf16.h>

typedef unsigned short u16;
typedef __attribute__((ext_vector_type(8))) __bf16 bf16x8;
typedef __attribute__((ext_vector_type(16))) __bf16 bf16x16;
typedef __attribute__((ext_vector_type(2))) __bf16 bf16x2;
typedef __attribute__((ext_vector_type(8))) float f32x8;
typedef __attribute__((ext_vector_type(2))) float f32x2;
typedef __attribute__((ext_vector_type(4))) float f32x4;
typedef __attribute__((ext_vector_type(16))) float f32x16;
typedef __attribute__((ext_vector_type(4))) unsigned u32x4;
typedef __attribute__((ext_vector_type(2))) unsigned u32x2;
typedef __attribute__((ext_vector_type(4))) short s16x4;
typedef const __attribute__((address_space(1))) char* gcp;

#define DI __device__ __forceinline__
#define TS(k, n)
#define TSR(k, n)

static constexpr int S_ = 2048;
static constexpr int D_ = 512;
static constexpr int F_ = 1376;

#define LDSF(a) (*(const __attribute__((address_space(3))) bf16x8*)(uintptr_t)(a))
DI float bf2f(unsigned u) { return __uint_as_float(u << 16); }
DI float bfhi2f(unsigned u) { return __uint_as_float(u & 0xffff0000u); }
DI float rbf(float f) { return (float)(__bf16)f; }
DI unsigned pack2(float lo, float hi) {
  f32x2 p = {lo, hi};
  bf16x2 q = __builtin_convertvector(p, bf16x2);
  return __builtin_bit_cast(unsigned, q);
}
DI f32x4 mfma16(bf16x8 a, bf16x8 b, f32x4 c) { return __builtin_amdgcn_mfma_f32_16x16x32_bf16(a, b, c, 0, 0, 0); }
DI f32x16 mfma32(bf16x8 a, bf16x8 b, f32x16 c) { return __builtin_amdgcn_mfma_f32_32x32x16_bf16(a, b, c, 0, 0, 0); }
DI f32x16 smfma32(bf16x8 a, f32x8 p, f32x16 c) {
  return __builtin_amdgcn_smfmac_f32_32x32x32_bf16(a, __builtin_bit_cast(bf16x16, p), c, 0xdddd, 0, 0);
}
DI unsigned perm_lo(unsigned hi_src, unsigned lo_src) { return __builtin_amdgcn_perm(hi_src, lo_src, 0x05040100u); }
DI unsigned perm_hi(unsigned hi_src, unsigned lo_src) { return __builtin_amdgcn_perm(hi_src, lo_src, 0x07060302u); }

DI bf16x8 frag_lo(const unsigned* w) {
  u32x4 r = {perm_lo(w[1], w[0]), perm_lo(w[3], w[2]), perm_lo(w[5], w[4]), perm_lo(w[7], w[6])};
  return __builtin_bit_cast(bf16x8, r);
}
DI bf16x8 frag_hi(const unsigned* w) {
  u32x4 r = {perm_hi(w[1], w[0]), perm_hi(w[3], w[2]), perm_hi(w[5], w[4]), perm_hi(w[7], w[6])};
  return __builtin_bit_cast(bf16x8, r);
}
DI float ln_elem(float xv, float mb, float den, float inv) {
  const float d = rbf(xv - mb);
  float q = d * inv;
  const float rem = __builtin_fmaf(-q, den, d);
  q = __builtin_fmaf(rem, inv, q);
  return rbf(q);
}

static constexpr int VT_STRIDE = 1056;
static constexpr int VF_BYTES = 64 * VT_STRIDE;
static constexpr size_t OFF_XNF = 0;
static constexpr size_t OFF_OF = OFF_XNF + (size_t)S_ * D_ * 2;
static constexpr size_t OFF_WQF = OFF_OF + (size_t)S_ * D_ * 2;
static constexpr size_t OFF_WOF = OFF_WQF + (size_t)D_ * D_ * 2;
static constexpr size_t OFF_KF = OFF_WOF + (size_t)D_ * D_ * 2;
static constexpr size_t OFF_VF = OFF_KF + 65536;
static constexpr size_t OFF_R = OFF_VF + VF_BYTES;
static constexpr size_t OFF_STATS = OFF_R + S_ * 4;
static constexpr int NHELP = 86;
static constexpr size_t OFF_Y = OFF_STATS + S_ * 8;
static constexpr size_t OFF_CNT = OFF_Y + NHELP * 1024 * 4;

static constexpr int XN_STRIDE = 1040;
__global__ __launch_bounds__(512) void ka_prep(const u16* __restrict__ x, const u16* __restrict__ wq,
                                               const u16* __restrict__ wk, const u16* __restrict__ wv,
                                               const u16* __restrict__ wo, const u16* __restrict__ wr,
                                               u16* __restrict__ xnf, u16* __restrict__ wqf, u16* __restrict__ wof,
                                               u16* __restrict__ kf, u16* __restrict__ vf,
                                               unsigned* __restrict__ rkeys, float2* __restrict__ stats,
                                               unsigned* __restrict__ cnt, u16* __restrict__ of) {
  __shared__ __attribute__((aligned(16))) char smem[16 * XN_STRIDE + 8 * 5 * 64 * 16];
  const int tid = threadIdx.x;
  const int lane = tid & 63, wave = tid >> 6;
  TSR(0, 8); TS(0, 0);
  if (blockIdx.x >= 128) {
    const int pb = blockIdx.x - 128;
    const bool iso = pb >= 32;
    if (pb == 0 && tid < 33) cnt[tid * 32] = 0u;
    const int cp = tid & 255, ko = (pb & 31) * 2 + (tid >> 8);
    const int ks = ko >> 1, hh = ko & 1;
    const u16* W = (iso ? wo : wq) + 2 * cp;
    unsigned w[8];
#pragma unroll
    for (int j = 0; j < 8; ++j) {
      const int row = 16 * ks + (iso ? (8 * (j >> 2) + 4 * hh + (j & 3)) : (8 * hh + j));
      w[j] = *(const unsigned*)(W + row * D_);
    }
    u16* dst = (iso ? wof : wqf) + (((cp >> 4) * 32 + ks) * 64 + hh * 32 + ((2 * cp) & 31)) * 8;
    *(bf16x8*)dst = frag_lo(w);
    *(bf16x8*)(dst + 8) = frag_hi(w);
    TS(0, 3); TSR(0, 9);
    return;
  }
  const int row0 = blockIdx.x * 16;
  {
    const u32x4 nr = {~0u, ~0u, ~0u, ~0u};
    u32x4* op = (u32x4*)(of + (size_t)blockIdx.x * 8192) + tid;
    op[0] = nr; op[512] = nr;
  }
  const int rt = blockIdx.x >> 1, th = blockIdx.x & 1;
  const int g = lane >> 4, i = lane & 15;
  const int k0 = wave * 64 + 8 * g;
  u32x4 xr[2];
#pragma unroll
  for (int rr = 0; rr < 2; ++rr) xr[rr] = *(const u32x4*)(x + (size_t)(row0 + 2 * wave + rr) * D_ + 8 * lane);
  const u16* wbase = ((i < 4) ? (wk + 4 * i) : (i < 8 ? wv + 4 * (i - 4) : wk)) + k0 * 16;
  u32x2 w[2][8];
#pragma unroll
  for (int s = 0; s < 2; ++s)
#pragma unroll
    for (int j = 0; j < 8; ++j) w[s][j] = *(const u32x2*)(wbase + (32 * s + j) * 16);
  bf16x8 rf[2];
#pragma unroll
  for (int s = 0; s < 2; ++s) rf[s] = *(const bf16x8*)(wr + k0 + 32 * s);

  float s1[2], s2[2];
#pragma unroll
  for (int rr = 0; rr < 2; ++rr) {
    float a = 0.f, q = 0.f;
#pragma unroll
    for (int e = 0; e < 4; ++e) {
      const float lo = bf2f(xr[rr][e] & 0xffffu), hi = bfhi2f(xr[rr][e]);
      a += lo + hi;
      q = __builtin_fmaf(lo, lo, q);
      q = __builtin_fmaf(hi, hi, q);
    }
    s1[rr] = a; s2[rr] = q;
  }
#pragma unroll
  for (int off = 32; off >= 1; off >>= 1)
#pragma unroll
    for (int rr = 0; rr < 2; ++rr) {
      s1[rr] += __shfl_xor(s1[rr], off);
      s2[rr] += __shfl_xor(s2[rr], off);
    }
#pragma unroll
  for (int rr = 0; rr < 2; ++rr) {
    const float mean = s1[rr] * (1.0f / D_);
    float var = s2[rr] * (1.0f / D_) - mean * mean;
    var = fmaxf(var, 0.f);
    const float mb = rbf(mean);
    const float den = rbf(__builtin_sqrtf(rbf(rbf(var) + 1e-5f)));
    const float inv = 1.0f / den;
    if (lane == rr) stats[row0 + 2 * wave + rr] = make_float2(mb, den);
    u32x4 o;
#pragma unroll
    for (int e = 0; e < 4; ++e)
      o[e] = pack2(ln_elem(bf2f(xr[rr][e] & 0xffffu), mb, den, inv), ln_elem(bfhi2f(xr[rr][e]), mb, den, inv));
    *(u32x4*)(smem + (2 * wave + rr) * XN_STRIDE + lane * 16) = o;
  }
  if (tid < 2 && th == 0) {
    const unsigned cv = tid ? 0u : 0x3f803f80u;
    u32x4 c4 = {cv, cv, cv, cv};
    *(u32x4*)((char*)vf + rt * VT_STRIDE + 1024 + tid * 16) = c4;
  }
  TS(0, 1);
  __syncthreads();
  {
    const int r = lane & 15, hh = (lane >> 4) & 1, kp = lane >> 5;
#pragma unroll
    for (int s = 0; s < 2; ++s) {
      const int ks = 4 * wave + 2 * s + kp;
      const u32x4 v = *(const u32x4*)(smem + r * XN_STRIDE + ks * 32 + hh * 16);
      *(u32x4*)(xnf + ((size_t)(rt * 32 + ks) * 64 + hh * 32 + th * 16 + r) * 8) = v;
    }
  }
  f32x4* red = (f32x4*)(smem + 16 * XN_STRIDE);
  {
    f32x4 acc[4], ar;
    ar = (f32x4){0.f, 0.f, 0.f, 0.f};
#pragma unroll
    for (int c = 0; c < 4; ++c) acc[c] = (f32x4){0.f, 0.f, 0.f, 0.f};
#pragma unroll
    for (int s = 0; s < 2; ++s) {
      unsigned wx[8], wy[8];
#pragma unroll
      for (int j = 0; j < 8; ++j) { wx[j] = w[s][j][0]; wy[j] = w[s][j][1]; }
      bf16x8 b[4] = {frag_lo(wx), frag_hi(wx), frag_lo(wy), frag_hi(wy)};
      const bf16x8 xa = *(const bf16x8*)(smem + i * XN_STRIDE + (k0 + 32 * s) * 2);
#pragma unroll
      for (int c = 0; c < 4; ++c) acc[c] = mfma16(xa, b[c], acc[c]);
      ar = mfma16(xa, rf[s], ar);
    }
#pragma unroll
    for (int c = 0; c < 4; ++c) red[(wave * 5 + c) * 64 + lane] = acc[c];
    red[(wave * 5 + 4) * 64 + lane] = ar;
  }
  __syncthreads();
  TS(0, 2);
  if (wave < 5) {
    f32x4 v = red[wave * 64 + lane];
#pragma unroll
    for (int wv_ = 1; wv_ < 8; ++wv_) v += red[(wv_ * 5 + wave) * 64 + lane];
    const int kk = 16 * th + 4 * g;
    if (wave == 4) {
      unsigned k0 = 0, k1 = 0;
#pragma unroll
      for (int e = 0; e < 4; ++e) {
        const unsigned u = __float_as_uint(rbf(v[e]));
        const unsigned ord = (u & 0x80000000u) ? ~u : (u | 0x80000000u);
        const unsigned key = (ord & 0xffff0000u) | (unsigned)(2047 - (row0 + 4 * g + e));
        k1 = max(k1, min(k0, key));
        k0 = max(k0, key);
      }
#pragma unroll
      for (int off = 16; off < 64; off <<= 1) {
        const unsigned o0 = __shfl_xor(k0, off), o1 = __shfl_xor(k1, off);
        k1 = max(min(k0, o0), max(k1, o1));
        k0 = max(k0, o0);
      }
      if (lane == 0) *(u32x2*)(rkeys + 2 * blockIdx.x) = (u32x2){k0, k1};
    } else if (i < 4) {
      const int hh = i & 1, j = (i >> 1) * 4 + wave;
#pragma unroll
      for (int e = 0; e < 4; ++e)
        kf[rt * 512 + (hh * 32 + kk + e) * 8 + j] = __builtin_bit_cast(u16, (__bf16)v[e]);
    } else if (i < 8) {
      const int d = 4 * (i - 4) + wave;
      const int hh = g >> 1, jh = g & 1;
      u32x2 o = {pack2(v[0], v[1]), pack2(v[2], v[3])};
      *(u32x2*)(vf + rt * (VT_STRIDE / 2) + th * 256 + (hh * 16 + d) * 8 + jh * 4) = o;
    }
  }
  TS(0, 3); TSR(0, 9);
}

#define P1A1 32
#define P1B1 52
#define P1A2 61
#define P2A1 5
#define P2B1 7
#define P2A2U 6
#define PRIO_IF(a, b, p) asm volatile("s_cmp_lg_u32 %0, %1\n\ts_cbranch_scc1 1f\n\ts_setprio " #p "\n1:" :: "s"(a), "s"(b) : "scc")
static constexpr int LDS_K = 0;
static constexpr int LDS_V = 65536;
static constexpr int LDS_H = 65536 + VF_BYTES + 2048;
static constexpr int LDS_F = LDS_H + 1024;
static constexpr int LDS_HH = LDS_F + 64;
static constexpr int LDS_Y = LDS_HH + 256;
static constexpr int LDS_TOTAL = LDS_Y + 2048;
static constexpr int CH_STRIDE = 1056;
typedef __attribute__((address_space(1))) unsigned gu32;
typedef __attribute__((address_space(1))) unsigned long long gu64;
#define RLX_AGENT __ATOMIC_RELAXED, __HIP_MEMORY_SCOPE_AGENT
DI void wait_count(unsigned* word, unsigned want) {
  for (unsigned spins = 0; spins < (1u << 18); ++spins) {
    if ((unsigned)__builtin_amdgcn_readfirstlane(__hip_atomic_load((gu32*)word, RLX_AGENT)) >= want) break;
    __builtin_amdgcn_s_sleep(1);
  }
  __builtin_amdgcn_fence(__ATOMIC_ACQUIRE, "agent");
  asm volatile("s_waitcnt vmcnt(0)" ::: "memory");
}

DI void glds16(const char* gsrc, unsigned lds_dst) {
  unsigned keep;
  asm volatile("s_mov_b32 %0, m0\n\ts_mov_b32 m0, %2\n\ts_nop 0\n\tglobal_load_lds_dwordx4 %1, off\n\ts_mov_b32 m0, %0"
               : "=&s"(keep) : "v"(gsrc), "s"(lds_dst) : "memory");
}
DI void stage_v(const char* vsrc, unsigned vdst, int hj) {
#pragma unroll
  for (int c = 0; c < 16; ++c) glds16(vsrc + c * 4096, vdst + c * 4096);
  if (hj < 2) glds16(vsrc + 65536, vdst + 65536);
}

__global__ __launch_bounds__(768) void kb_attn(const u16* __restrict__ xnf, const u16* __restrict__ wqf,
                                               const u16* __restrict__ kf, const u16* __restrict__ vf,
                                               const unsigned* __restrict__ rkeys, const u16* __restrict__ wg,
                                               const u16* __restrict__ wu, const u16* __restrict__ wd,
                                               u16* of, float* ypart, unsigned* cnt,
                                               const u16* __restrict__ x, const u16* __restrict__ wof,
                                               const float2* __restrict__ stats, u16* __restrict__ out) {
  __shared__ __attribute__((aligned(16))) char smem[LDS_TOTAL];
  const int tid = threadIdx.x;
  const int lane = tid & 63, wave = tid >> 6;
  const int b = blockIdx.x;
  unsigned* flags = (unsigned*)(smem + LDS_F);
  TSR(1, 8); TS(1, 0);

  const int rb = (b & 7) + 8 * (b >> 6), hg = (b >> 3) & 7;
  if (wave < 8) {
    const int rt = wave >> 2, ks4 = wave & 3;
    const int r = lane & 31, hh = lane >> 5;
    {
      bf16x8 af[2][8], bfr[8];
      const int rtq = wave & 1, kq = wave >> 1;
      const u16* ap = wqf + ((size_t)(2 * hg) * 32 + 8 * kq) * 512 + lane * 8;
      const u16* bp = xnf + ((size_t)(2 * rb + rtq) * 32 + 8 * kq) * 512 + lane * 8;
#pragma unroll
      for (int s = 0; s < 8; ++s) {
        bfr[s] = *(const bf16x8*)(bp + s * 512);
        af[0][s] = *(const bf16x8*)(ap + s * 512);
        af[1][s] = *(const bf16x8*)(ap + 32 * 512 + s * 512);
      }
      f32x16 acc[2];
#pragma unroll
      for (int e = 0; e < 16; ++e) { acc[0][e] = 0.f; acc[1][e] = 0.f; }
#pragma unroll
      for (int s = 0; s < 8; ++s) {
        acc[0] = mfma32(af[0][s], bfr[s], acc[0]);
        acc[1] = mfma32(af[1][s], bfr[s], acc[1]);
      }
#pragma unroll
      for (int dt = 0; dt < 2; ++dt)
#pragma unroll
        for (int q4 = 0; q4 < 4; ++q4) {
          f32x4 p = {acc[dt][4 * q4], acc[dt][4 * q4 + 1], acc[dt][4 * q4 + 2], acc[dt][4 * q4 + 3]};
          *(f32x4*)(smem + LDS_V + ((((rtq * 4 + kq) * 2 + dt) * 4 + q4) * 64 + lane) * 16) = p;
        }
    }
    __builtin_amdgcn_sched_barrier(0);
    TS(1, 1);
    __syncthreads();
    TS(1, 2);
    const int hl = wave & 3;
    const int head = hg * 4 + hl;
    bf16x8 qf;
    {
      f32x4 a0 = {0.f, 0.f, 0.f, 0.f}, a1 = {0.f, 0.f, 0.f, 0.f};
#pragma unroll
      for (int k = 0; k < 4; ++k) {
        const char* p = smem + LDS_V + ((((rt * 4 + k) * 2 + (hl >> 1)) * 4 + 2 * (hl & 1)) * 64 + lane) * 16;
        a0 += *(const f32x4*)p;
        a1 += *(const f32x4*)(p + 1024);
      }
      const float SC = 0.25f * 1.44269504088896340736f;
      u32x4 pk = {pack2(rbf(a0[0]) * SC, rbf(a0[1]) * SC), pack2(rbf(a0[2]) * SC, rbf(a0[3]) * SC),
                  pack2(rbf(a1[0]) * SC, rbf(a1[1]) * SC), pack2(rbf(a1[2]) * SC, rbf(a1[3]) * SC)};
      qf = __builtin_bit_cast(bf16x8, pk);
    }
    __syncthreads();

    const int yng = __builtin_amdgcn_readfirstlane(wave >> 2);
    const unsigned lbase = (unsigned)(uintptr_t)(__attribute__((address_space(3))) char*)smem;
    float m0 = -__builtin_inff(), m1 = -__builtin_inff();
    f32x16 zero16;
#pragma unroll
    for (int e = 0; e < 16; ++e) zero16[e] = 0.f;
    {
      unsigned kq_ = lbase + LDS_K + lane * 16;
      asm volatile("" : "+v"(kq_));
      f32x16 sa = mfma32(LDSF(kq_), qf, zero16);
      f32x16 sb = mfma32(LDSF(kq_ + 1024), qf, zero16);
      bf16x8 k2 = LDSF(kq_ + 2048), k3 = LDSF(kq_ + 3072);
      __builtin_amdgcn_sched_barrier(0);
#pragma unroll
      for (int t = 0; t < 64; ++t) {
        if (t == 0) __builtin_amdgcn_s_setprio(2);
        if (t == P1A1) PRIO_IF(yng, 0, 1);
        if (t == P1B1) PRIO_IF(yng, 1, 1);
        if (t == P1A2) PRIO_IF(yng, 0, 0);
        f32x16 sc = sb;
        bf16x8 k4 = k3;
        if (t + 2 < 64) sc = mfma32(k2, qf, zero16);
        if (t + 4 < 64) k4 = LDSF(kq_ + (t + 4) * 1024);
        if (t & 1) {
#pragma unroll
          for (int e = 0; e < 16; e += 2) m1 = fmaxf(fmaxf(m1, sa[e]), sa[e + 1]);
        } else {
#pragma unroll
          for (int e = 0; e < 16; e += 2) m0 = fmaxf(fmaxf(m0, sa[e]), sa[e + 1]);
        }
        sa = sb; sb = sc; k2 = k3; k3 = k4;
        __builtin_amdgcn_sched_group_barrier(0x008, 1, 0);
        __builtin_amdgcn_sched_group_barrier(0x100, 1, 0);
        __builtin_amdgcn_sched_group_barrier(0x002, 8, 0);
        __builtin_amdgcn_sched_barrier(0);
      }
    }
    float m = fmaxf(m0, m1);
    m = fmaxf(m, __shfl_xor(m, 32));
    TS(1, 3);
    while (__hip_atomic_load(flags, __ATOMIC_ACQUIRE, __HIP_MEMORY_SCOPE_WORKGROUP) < 4) __builtin_amdgcn_s_sleep(1);
    asm volatile("" ::: "memory");
    TS(1, 4);
    const int r64 = tid >> 3, oct = tid & 7;
    const int row = rb * 64 + r64;
    const size_t off = (size_t)row * D_ + hg * 64 + oct * 8;
    const u32x4 xv = *(const u32x4*)(x + off);
    const float2 st = stats[row];

    f32x16 negm;
#pragma unroll
    for (int e = 0; e < 16; ++e) negm[e] = -m;
    f32x16 o;
#pragma unroll
    for (int e = 0; e < 16; ++e) o[e] = 0.f;
    unsigned ka_ = lbase + LDS_K + lane * 16;
    unsigned va_ = lbase + LDS_V + ((r < 16) ? (hh * 16 + r) * 16 : (r == 16 ? 1024 : 1040));
    unsigned vb_ = lbase + LDS_V + ((r < 16) ? (hh * 16 + r) * 16 + 512 : (r == 16 ? 1024 : 1040));
    f32x16 s = mfma32(LDSF(ka_), qf, negm);
    bf16x8 kn = LDSF(ka_ + 1024);
    bf16x8 v0 = LDSF(va_);
    bf16x8 v1 = LDSF(vb_);
    bf16x8 v1p = v1;
    f32x8 pbp = {0.f, 0.f, 0.f, 0.f, 0.f, 0.f, 0.f, 0.f};
    const int thr1 = P2A1 + (P2B1 - P2A1) * yng, thr2 = 7 + 92 * yng;
    __builtin_amdgcn_s_setprio(2);
#pragma unroll 1
    for (int tb = 0; tb < 8; ++tb) {
      asm volatile("" : "+v"(ka_), "+v"(va_), "+v"(vb_));
      PRIO_IF(tb, thr1, 1);
#pragma unroll
      for (int u = 0; u < 8; ++u) {
        if (u == P2A2U) PRIO_IF(tb, thr2, 0);
        const f32x16 sn = mfma32(kn, qf, negm);
        const bf16x8 k2 = LDSF(ka_ + (u + 2) * 1024);
        const bf16x8 v0n = LDSF(va_ + (u + 1) * VT_STRIDE);
        const bf16x8 v1n = LDSF(vb_ + (u + 1) * VT_STRIDE);
        f32x8 pa, pb;
#pragma unroll
        for (int e = 0; e < 5; ++e) pa[e] = __builtin_amdgcn_exp2f(s[e]);
        o = smfma32(v1p, pbp, o);
#pragma unroll
        for (int e = 5; e < 8; ++e) pa[e] = __builtin_amdgcn_exp2f(s[e]);
#pragma unroll
        for (int e = 8; e < 10; ++e) pb[e - 8] = __builtin_amdgcn_exp2f(s[e]);
        o = smfma32(v0, pa, o);
#pragma unroll
        for (int e = 10; e < 16; ++e) pb[e - 8] = __builtin_amdgcn_exp2f(s[e]);
        pbp = pb;
        v1p = v1;
        s = sn; kn = k2; v0 = v0n; v1 = v1n;
        __builtin_amdgcn_sched_group_barrier(0x008, 1, 0);
        __builtin_amdgcn_sched_group_barrier(0x100, 3, 0);
        __builtin_amdgcn_sched_group_barrier(0x402, 5, 0);
        __builtin_amdgcn_sched_group_barrier(0x008, 1, 0);
        __builtin_amdgcn_sched_group_barrier(0x402, 5, 0);
        __builtin_amdgcn_sched_group_barrier(0x008, 1, 0);
        __builtin_amdgcn_sched_group_barrier(0x402, 6, 0);
        __builtin_amdgcn_sched_barrier(0);
      }
      ka_ += 8 * 1024; va_ += 8 * VT_STRIDE; vb_ += 8 * VT_STRIDE;
    }
    o = smfma32(v1p, pbp, o);
    TS(1, 5);
    const float il = 1.0f / __shfl(o[8], r);
    u32x4 w0 = {pack2(o[0] * il, o[1] * il), pack2(o[2] * il, o[3] * il), pack2(o[4] * il, o[5] * il),
                pack2(o[6] * il, o[7] * il)};
    const __amdgpu_buffer_rsrc_t ors = __builtin_amdgcn_make_buffer_rsrc(of + (size_t)rb * 32768, 0, 65536, 0x00020000);
#pragma unroll
    for (int e = 0; e < 4; ++e) w0[e] = min(w0[e], 0xfffffffeu);
    __builtin_amdgcn_raw_buffer_store_b128(w0, ors, ((rt * 32 + head) * 64 + lane) * 16, 0, 16);
    __builtin_amdgcn_s_barrier();
    float xn8[8];
    {
      const float inv = 1.0f / st.y;
#pragma unroll
      for (int e = 0; e < 4; ++e) {
        xn8[2 * e] = ln_elem(bf2f(xv[e] & 0xffffu), st.x, st.y, inv);
        xn8[2 * e + 1] = ln_elem(bfhi2f(xv[e]), st.x, st.y, inv);
      }
#pragma unroll
      for (int e = 0; e < 8; ++e) asm volatile("" : "+v"(xn8[e]));
    }
    TS(1, 6);
    {
      const int rto = wave & 1, ko = wave >> 1;
      u32x4 fr[8];
      for (unsigned spins = 0; spins < (1u << 16); ++spins) {
        asm volatile("" ::: "memory");
#pragma unroll
        for (int s8 = 0; s8 < 8; ++s8)
          fr[s8] = __builtin_amdgcn_raw_buffer_load_b128(ors, ((rto * 32 + 8 * ko + s8) * 64 + lane) * 16, 0, 16);
        unsigned mx = 0;
#pragma unroll
        for (int s8 = 0; s8 < 8; ++s8) mx = max(max(mx, max(fr[s8][0], fr[s8][1])), max(fr[s8][2], fr[s8][3]));
        if (!__any(mx == ~0u)) break;
      }
      bf16x8 bfr[8];
#pragma unroll
      for (int s8 = 0; s8 < 8; ++s8) bfr[s8] = __builtin_bit_cast(bf16x8, fr[s8]);
      __syncthreads();
      const unsigned wa_ = lbase + LDS_K + ko * 8192 + lane * 16;
      f32x16 acc[2];
#pragma unroll
      for (int e = 0; e < 16; ++e) { acc[0][e] = 0.f; acc[1][e] = 0.f; }
#pragma unroll
      for (int s8 = 0; s8 < 8; ++s8) {
        acc[0] = mfma32(LDSF(wa_ + s8 * 1024), bfr[s8], acc[0]);
        acc[1] = mfma32(LDSF(wa_ + 32768 + s8 * 1024), bfr[s8], acc[1]);
      }
#pragma unroll
      for (int ct = 0; ct < 2; ++ct)
#pragma unroll
        for (int q4 = 0; q4 < 4; ++q4) {
          f32x4 p = {acc[ct][4 * q4], acc[ct][4 * q4 + 1], acc[ct][4 * q4 + 2], acc[ct][4 * q4 + 3]};
          *(f32x4*)(smem + LDS_V + (((rto * 4 + ko) * 2 + ct) * 4 + q4) * CH_STRIDE + lane * 16) = p;
        }
    }
    __syncthreads();
    f32x4 lo = {0.f, 0.f, 0.f, 0.f}, hi = {0.f, 0.f, 0.f, 0.f};
#pragma unroll
    for (int k = 0; k < 4; ++k) {
      const char* p = smem + LDS_V + (((r64 >> 5) * 4 + k) * 8 + oct) * CH_STRIDE + (r64 & 31) * 16;
      lo += *(const f32x4*)p;
      hi += *(const f32x4*)(p + 512);
    }
    float upd[8];
#pragma unroll
    for (int e = 0; e < 8; ++e) upd[e] = 0.f;
    const u32x4 sl = *(const u32x4*)(smem + LDS_F + 16);
    if (row == (int)sl[0] || row == (int)sl[1]) {
      const int tk = (row == (int)sl[0]) ? 0 : 1;
      f32x4 ya = {0.f, 0.f, 0.f, 0.f}, yb = {0.f, 0.f, 0.f, 0.f};
#pragma unroll
      for (int p = 0; p < 4; ++p) {
        const char* yp = smem + LDS_Y + (tk * 4 + p) * 256 + oct * 32;
        ya += *(const f32x4*)yp;
        yb += *(const f32x4*)(yp + 16);
      }
      const float rs = __uint_as_float(tk ? sl[3] : sl[2]);
#pragma unroll
      for (int e = 0; e < 4; ++e) { upd[e] = rbf(rbf(ya[e]) * rs); upd[4 + e] = rbf(rbf(yb[e]) * rs); }
    }
    float res[8];
#pragma unroll
    for (int e = 0; e < 4; ++e) {
      const float x0 = bf2f(xv[e] & 0xffffu), x1 = bfhi2f(xv[e]);
      const float a0 = rbf(e < 2 ? lo[2 * e] : hi[2 * e - 4]);
      const float a1 = rbf(e < 2 ? lo[2 * e + 1] : hi[2 * e - 3]);
      res[2 * e] = rbf(x0 + a0) + rbf(xn8[2 * e] + upd[2 * e]);
      res[2 * e + 1] = rbf(x1 + a1) + rbf(xn8[2 * e + 1] + upd[2 * e + 1]);
    }
    u32x4 ov = {pack2(res[0], res[1]), pack2(res[2], res[3]), pack2(res[4], res[5]), pack2(res[6], res[7])};
    *(u32x4*)(out + off) = ov;
  } else {
    __builtin_amdgcn_s_setprio(3);
    const int hj = __builtin_amdgcn_readfirstlane(wave - 8);
    const unsigned ldst = (unsigned)(uintptr_t)(__attribute__((address_space(3))) char*)smem + hj * 1024;
    {
      const char* ksrc = (const char*)kf + hj * 1024 + lane * 16;
#pragma unroll
      for (int c = 0; c < 16; ++c) glds16(ksrc + c * 4096, ldst + LDS_K + c * 4096);
    }
    if (hj == 0 && lane < 4) flags[lane] = 0u;
    const char* vsrc = (const char*)vf + hj * 1024 + lane * 16;
    const int g = lane >> 4, i = lane & 15;
    const int f0 = b * 16;
    const bool mod = b < NHELP;
    const u32x4 kv = *(const u32x4*)(rkeys + lane * 4);
    unsigned k0 = max(kv[0], kv[2]), k1 = max(min(kv[0], kv[2]), max(kv[1], kv[3]));
#pragma unroll
    for (int off = 1; off < 64; off <<= 1) {
      const unsigned o0 = __shfl_xor(k0, off), o1 = __shfl_xor(k1, off);
      k1 = max(min(k0, o0), max(k1, o1));
      k0 = max(k0, o0);
    }
    const int i0 = 2047 - (int)(k0 & 0x7ffu), i1 = 2047 - (int)(k1 & 0x7ffu);
    if (hj == 0 && lane == 0) {
      const float v0 = __uint_as_float((k0 & 0x80000000u) ? (k0 & 0x7fff0000u) : (~k0 & 0xffff0000u));
      const float v1 = __uint_as_float((k1 & 0x80000000u) ? (k1 & 0x7fff0000u) : (~k1 & 0xffff0000u));
      *(u32x4*)(smem + LDS_F + 16) = (u32x4){(unsigned)i0, (unsigned)i1, __float_as_uint(rbf(1.0f / (1.0f + __expf(-v0)))),
                                             __float_as_uint(rbf(1.0f / (1.0f + __expf(-v1))))};
    }
    bf16x8 xa[4];
    if (mod) {
      const int srow = (i & 1) ? i1 : i0;
      const u16* xp = xnf + ((size_t)((srow >> 5) * 32 + 16 * (hj >> 1) + 8 * ((i >> 1) & 1) + (g >> 1)) * 64 + (g & 1) * 32 + (srow & 31)) * 8;
#pragma unroll
      for (int s = 0; s < 4; ++s) xa[s] = *(const bf16x8*)(xp + (size_t)s * 1024);
    }
    TS(1, 3);
    asm volatile("s_waitcnt vmcnt(0)" ::: "memory");
    __syncthreads();
    __syncthreads();
    stage_v(vsrc, ldst + LDS_V, hj);
    asm volatile("s_waitcnt vmcnt(0)" ::: "memory");
    if (lane == 0) __hip_atomic_fetch_add(flags, 1u, __ATOMIC_RELEASE, __HIP_MEMORY_SCOPE_WORKGROUP);
    __builtin_amdgcn_sched_barrier(0);
    if (mod) {
      unsigned wa[4][8];
      const char* ub = (const char*)(((hj & 1) ? wu : wg) + (size_t)(256 * (hj >> 1)) * F_ + f0);
      const unsigned lo1 = (unsigned)((128 * (i >> 3) + 8 * g) * F_ + 2 * (i & 7)) * 2u;
#pragma unroll
      for (int s = 0; s < 4; ++s)
#pragma unroll
        for (int j = 0; j < 8; j += 2) {
          gcp rp = (gcp)(ub + (size_t)((32 * s + j) * F_ * 2));
          asm volatile("" : "+s"(rp));
          wa[s][j] = *(const __attribute__((address_space(1))) unsigned*)(rp + lo1);
          wa[s][j + 1] = *(const __attribute__((address_space(1))) unsigned*)(rp + F_ * 2 + lo1);
        }
      unsigned wdv[4][4];
      const char* db = (const char*)(wd + (size_t)f0 * D_ + hj * 128);
      const unsigned lo2 = (unsigned)(4 * g * D_ + 2 * i) * 2u;
#pragma unroll
      for (int q = 0; q < 4; ++q)
#pragma unroll
        for (int j = 0; j < 4; ++j) wdv[q][j] = *(const unsigned*)(db + (j * D_ + 32 * q) * 2 + lo2);
      f32x4 a0 = {0.f, 0.f, 0.f, 0.f}, a1 = {0.f, 0.f, 0.f, 0.f};
#pragma unroll
      for (int s = 0; s < 4; ++s) {
        a0 = mfma16(xa[s], frag_lo(wa[s]), a0);
        a1 = mfma16(xa[s], frag_hi(wa[s]), a1);
      }
      if (lane < 16)
        *(f32x4*)(smem + LDS_H + (hj * 16 + i) * 16) =
            (i < 8) ? (f32x4){a0[0], a0[1], a1[0], a1[1]} : (f32x4){a0[2], a0[3], a1[2], a1[3]};
      TS(1, 4);
      if (lane == 0) __hip_atomic_fetch_add(flags + 1, 1u, __ATOMIC_RELEASE, __HIP_MEMORY_SCOPE_WORKGROUP);
      while (__hip_atomic_load(flags + 1, __ATOMIC_ACQUIRE, __HIP_MEMORY_SCOPE_WORKGROUP) < 4) __builtin_amdgcn_s_sleep(2);
      asm volatile("" ::: "memory");
      __builtin_amdgcn_sched_barrier(0);
      const float* hp = (const float*)(smem + LDS_H) + ((i >> 3) * 16 + (i & 7)) * 4 + g;
      const float gt = rbf((hp[0] + hp[32]) + (hp[128] + hp[160]));
      const float up = __shfl_xor(gt, 8);
      const float hv = rbf(gt / (1.0f + __expf(-gt))) * up;
      if (i < 8) *(u16*)(smem + LDS_HH + hj * 64 + (g & 1) * 32 + (2 * i + (g >> 1)) * 2) = __builtin_bit_cast(u16, (__bf16)hv);
      const s16x4 ha = *(const s16x4*)(smem + LDS_HH + hj * 64 + (i & 1) * 32 + g * 8);
#pragma unroll
      for (int q = 0; q < 4; ++q) {
        const u32x2 be = {perm_lo(wdv[q][1], wdv[q][0]), perm_lo(wdv[q][3], wdv[q][2])};
        const u32x2 bo = {perm_hi(wdv[q][1], wdv[q][0]), perm_hi(wdv[q][3], wdv[q][2])};
        const f32x4 z = {0.f, 0.f, 0.f, 0.f};
        const f32x4 ye = __builtin_amdgcn_mfma_f32_16x16x16bf16_1k(ha, __builtin_bit_cast(s16x4, be), z, 0, 0, 0);
        const f32x4 yo = __builtin_amdgcn_mfma_f32_16x16x16bf16_1k(ha, __builtin_bit_cast(s16x4, bo), z, 0, 0, 0);
        if (lane < 16) {
          gu64* yp = (gu64*)(ypart + (size_t)b * 1024 + hj * 128 + 32 * q + 2 * i);
          __hip_atomic_store(yp, __builtin_bit_cast(unsigned long long, (f32x2){ye[0], yo[0]}), RLX_AGENT);
          __hip_atomic_store(yp + 256, __builtin_bit_cast(unsigned long long, (f32x2){ye[1], yo[1]}), RLX_AGENT);
        }
      }
      asm volatile("s_waitcnt vmcnt(0)" ::: "memory");
      if (lane == 0 && __hip_atomic_fetch_add(flags + 2, 1u, __ATOMIC_ACQ_REL, __HIP_MEMORY_SCOPE_WORKGROUP) == 3u)
        __hip_atomic_fetch_add((gu32*)(cnt + 32 * 32), 1u, RLX_AGENT);
    }
    __builtin_amdgcn_sched_barrier(0);
    const bool own0 = (unsigned)(i0 - rb * 64) < 64u, own1 = (unsigned)(i1 - rb * 64) < 64u;
    if (own0 || own1) {
      wait_count(cnt + 32 * 32, NHELP);
#pragma unroll
      for (int tk = 0; tk < 2; ++tk) {
        if (tk ? own1 : own0) {
          const int p0 = hj * 4 + g;
          const float* yp = ypart + (size_t)(p0 * 2 + tk) * 512 + hg * 64 + i * 4;
          f32x4 yv[6];
#pragma unroll
          for (int q = 0; q < 6; ++q) yv[q] = *(const f32x4*)(yp + (p0 + 16 * q < NHELP ? q * 16 * 1024 : 0));
          f32x4 y = ((yv[0] + yv[1]) + (yv[2] + yv[3])) + yv[4];
          if (p0 + 80 < NHELP) y += yv[5];
#pragma unroll
          for (int e = 0; e < 4; ++e) {
            y[e] += __shfl_xor(y[e], 16);
            y[e] += __shfl_xor(y[e], 32);
          }
          if (lane < 16) *(f32x4*)(smem + LDS_Y + (tk * 4 + hj) * 256 + lane * 16) = y;
        }
      }
    }
    __builtin_amdgcn_sched_barrier(0);
    u32x4 wr_[16];
    const char* wsrc = (const char*)wof + (size_t)hg * 65536 + hj * 1024 + lane * 16;
#pragma unroll
    for (int c = 0; c < 16; ++c) wr_[c] = *(const u32x4*)(wsrc + c * 4096);
    TS(1, 5);
    __syncthreads();
#pragma unroll
    for (int c = 0; c < 16; ++c) *(u32x4*)(smem + LDS_K + hj * 1024 + lane * 16 + c * 4096) = wr_[c];
    __syncthreads();
    TS(1, 6);
  }
  TS(1, 7); TSR(1, 9);
}

extern "C" void kernel_launch(void* const* d_in, const int* in_sizes, int n_in, void* d_out, int out_size,
                              void* d_ws, size_t ws_size, hipStream_t stream) {
  const u16* x = (const u16*)d_in[0];
  const u16* wq = (const u16*)d_in[1];
  const u16* wk = (const u16*)d_in[2];
  const u16* wv = (const u16*)d_in[3];
  const u16* wo = (const u16*)d_in[4];
  const u16* wr = (const u16*)d_in[6];
  const u16* wg = (const u16*)d_in[7];
  const u16* wu = (const u16*)d_in[8];
  const u16* wd = (const u16*)d_in[9];
  char* ws = (char*)d_ws;
  u16* xnf = (u16*)(ws + OFF_XNF);
  u16* of = (u16*)(ws + OFF_OF);
  u16* wqf = (u16*)(ws + OFF_WQF);
  u16* wof = (u16*)(ws + OFF_WOF);
  u16* kf = (u16*)(ws + OFF_KF);
  u16* vf = (u16*)(ws + OFF_VF);
  unsigned* rkeys = (unsigned*)(ws + OFF_R);
  float2* stats = (float2*)(ws + OFF_STATS);
  float* ypart = (float*)(ws + OFF_Y);
  unsigned* cnt = (unsigned*)(ws + OFF_CNT);
  u16* out = (u16*)d_out;

  ka_prep<<<192, 512, 0, stream>>>(x, wq, wk, wv, wo, wr, xnf, wqf, wof, kf, vf, rkeys, stats, cnt, of);
  kb_attn<<<256, 768, 0, stream>>>(xnf, wqf, kf, vf, rkeys, wg, wu, wd, of, ypart, cnt, x, wof, stats, out);
}
```

```cpp
#include <hip/hip_runtime.h>
#include <hip/hip_bf16.h>

typedef unsigned short u16;
typedef __attribute__((ext_vector_type(8))) __bf16 bf16x8;
typedef __attribute__((ext_vector_type(16))) __bf16 bf16x16;
typedef __attribute__((ext_vector_type(2))) __bf16 bf16x2;
typedef __attribute__((ext_vector_type(8))) float f32x8;
typedef __attribute__((ext_vector_type(2))) float f32x2;
typedef __attribute__((ext_vector_type(4))) float f32x4;
typedef __attribute__((ext_vector_type(16))) float f32x16;
typedef __attribute__((ext_vector_type(4))) unsigned u32x4;
typedef __attribute__((ext_vector_type(2))) unsigned u32x2;
typedef __attribute__((ext_vector_type(4))) short s16x4;
typedef const __attribute__((address_space(1))) char* gcp;

#define DI __device__ __forceinline__
#define TS(k, n)
#define TSR(k, n)

static constexpr int S_ = 2048;
static constexpr int D_ = 512;
static constexpr int F_ = 1376;

#define LDSF(a) (*(const __attribute__((address_space(3))) bf16x8*)(uintptr_t)(a))
DI float bf2f(unsigned u) { return __uint_as_float(u << 16); }
DI float bfhi2f(unsigned u) { return __uint_as_float(u & 0xffff0000u); }
DI float rbf(float f) { return (float)(__bf16)f; }
DI unsigned pack2(float lo, float hi) {
  f32x2 p = {lo, hi};
  bf16x2 q = __builtin_convertvector(p, bf16x2);
  return __builtin_bit_cast(unsigned, q);
}
DI f32x4 mfma16(bf16x8 a, bf16x8 b, f32x4 c) { return __builtin_amdgcn_mfma_f32_16x16x32_bf16(a, b, c, 0, 0, 0); }
DI f32x16 mfma32(bf16x8 a, bf16x8 b, f32x16 c) { return __builtin_amdgcn_mfma_f32_32x32x16_bf16(a, b, c, 0, 0, 0); }
DI f32x16 smfma32(bf16x8 a, f32x8 p, f32x16 c) {
  return __builtin_amdgcn_smfmac_f32_32x32x32_bf16(a, __builtin_bit_cast(bf16x16, p), c, 0xdddd, 0, 0);
}
DI unsigned perm_lo(unsigned hi_src, unsigned lo_src) { return __builtin_amdgcn_perm(hi_src, lo_src, 0x05040100u); }
DI unsigned perm_hi(unsigned hi_src, unsigned lo_src) { return __builtin_amdgcn_perm(hi_src, lo_src, 0x07060302u); }

DI bf16x8 frag_lo(const unsigned* w) {
  u32x4 r = {perm_lo(w[1], w[0]), perm_lo(w[3], w[2]), perm_lo(w[5], w[4]), perm_lo(w[7], w[6])};
  return __builtin_bit_cast(bf16x8, r);
}
DI bf16x8 frag_hi(const unsigned* w) {
  u32x4 r = {perm_hi(w[1], w[0]), perm_hi(w[3], w[2]), perm_hi(w[5], w[4]), perm_hi(w[7], w[6])};
  return __builtin_bit_cast(bf16x8, r);
}
DI float u2f(unsigned u) { return __uint_as_float(u); }
DI unsigned f2u(float f) { return __float_as_uint(f); }
DI unsigned ln_pair(unsigned xp, float mb, float inv) {
  const unsigned db = pack2(bf2f(xp & 0xffffu) - mb, bfhi2f(xp) - mb);
  return pack2(bf2f(db & 0xffffu) * inv, bfhi2f(db) * inv);
}
DI void glds16(const char* gsrc, unsigned lds_dst) {
  unsigned keep;
  asm volatile("s_mov_b32 %0, m0\n\ts_mov_b32 m0, %2\n\ts_nop 0\n\tglobal_load_lds_dwordx4 %1, off\n\ts_mov_b32 m0, %0"
               : "=&s"(keep) : "v"(gsrc), "s"(lds_dst) : "memory");
}
typedef __attribute__((ext_vector_type(4))) short v4i16_t;
DI u32x2 lds_tr(unsigned a) {
  return __builtin_bit_cast(u32x2, __builtin_amdgcn_ds_read_tr16_b64_v4i16((__attribute__((address_space(3))) v4i16_t*)(uintptr_t)a));
}

static constexpr int VT_STRIDE = 1056;
static constexpr int VF_BYTES = 64 * VT_STRIDE;
static constexpr size_t OFF_XNF = 0;
static constexpr size_t OFF_OF = OFF_XNF + (size_t)S_ * D_ * 2;
static constexpr size_t OFF_WQF = OFF_OF + (size_t)S_ * D_ * 2;
static constexpr size_t OFF_WOF = OFF_WQF + (size_t)D_ * D_ * 2;
static constexpr size_t OFF_KF = OFF_WOF + (size_t)D_ * D_ * 2;
static constexpr size_t OFF_VF = OFF_KF + 65536;
static constexpr size_t OFF_R = OFF_VF + VF_BYTES;
static constexpr size_t OFF_STATS = OFF_R + S_ * 4;
static constexpr int NHELP = 86;
static constexpr size_t OFF_Y = OFF_STATS + S_ * 8;
static constexpr size_t OFF_CNT = OFF_Y + NHELP * 1024 * 4;

static constexpr int KA_EXT = 1024, KA_RED = KA_EXT + 8 * 4096;
__global__ __launch_bounds__(512) void ka_prep(const u16* __restrict__ x, const u16* __restrict__ wq,
                                               const u16* __restrict__ wk, const u16* __restrict__ wv,
                                               const u16* __restrict__ wo, const u16* __restrict__ wr,
                                               u16* __restrict__ xnf, u16* __restrict__ wqf, u16* __restrict__ wof,
                                               u16* __restrict__ kf, u16* __restrict__ vf,
                                               unsigned* __restrict__ rkeys, float2* __restrict__ stats,
                                               unsigned* __restrict__ cnt, u16* __restrict__ of) {
  __shared__ __attribute__((aligned(16))) char smem[KA_RED + 8 * 3 * 1024];
  const int tid = threadIdx.x;
  const int lane = tid & 63, wave = __builtin_amdgcn_readfirstlane(tid >> 6);
  TSR(0, 8); TS(0, 0);
  asm volatile("" :: "s"(x), "s"(wq), "s"(wk), "s"(wv), "s"(wo), "s"(wr), "s"(xnf), "s"(wqf), "s"(wof), "s"(kf), "s"(vf),
               "s"(rkeys), "s"(stats), "s"(cnt), "s"(of));
  if (blockIdx.x >= 192) {
    u32x4* op = (u32x4*)(of + (size_t)(blockIdx.x - 192) * 16384) + tid;
#pragma unroll
    for (int c = 0; c < 4; ++c) op[c * 512] = (u32x4){~0u, ~0u, ~0u, ~0u};
    TS(0, 3); TSR(0, 9);
    return;
  }
  if (blockIdx.x >= 128) {
    const int pb = blockIdx.x - 128;
    const bool iso = pb >= 32;
    if (pb == 0 && tid == 0) cnt[0] = 0u;
    const int cp = tid & 255, ko = (pb & 31) * 2 + (tid >> 8);
    const int ks = ko >> 1, hh = ko & 1;
    const u16* W = (iso ? wo : wq) + 2 * cp;
    unsigned w[8];
#pragma unroll
    for (int j = 0; j < 8; ++j) {
      const int row = 16 * ks + (iso ? (8 * (j >> 2) + 4 * hh + (j & 3)) : (8 * hh + j));
      w[j] = *(const unsigned*)(W + row * D_);
    }
    u16* dst = (iso ? wof : wqf) + (((cp >> 4) * 32 + ks) * 64 + hh * 32 + ((2 * cp) & 31)) * 8;
    *(bf16x8*)dst = frag_lo(w);
    *(bf16x8*)(dst + 8) = frag_hi(w);
    TS(0, 3); TSR(0, 9);
    return;
  }
  const int row0 = blockIdx.x * 16;
  const int rt = blockIdx.x >> 1, th = blockIdx.x & 1;
  const int g = lane >> 4, i = lane & 15;
  const int k0 = wave * 64 + 8 * g;
  const unsigned lbase = (unsigned)(uintptr_t)(__attribute__((address_space(3))) char*)smem;
  u32x4 xr[2];
#pragma unroll
  for (int s = 0; s < 2; ++s) xr[s] = *(const u32x4*)(x + (size_t)(row0 + i) * D_ + k0 + 32 * s);
  const unsigned eb = lbase + KA_EXT + wave * 4096;
#pragma unroll
  for (int s = 0; s < 2; ++s) {
    glds16((const char*)wk + wave * 2048 + s * 1024 + lane * 16, eb + s * 1024);
    glds16((const char*)wv + wave * 2048 + s * 1024 + lane * 16, eb + 2048 + s * 1024);
  }
  bf16x8 rf[2];
#pragma unroll
  for (int s = 0; s < 2; ++s) rf[s] = *(const bf16x8*)(wr + k0 + 32 * s);
  __builtin_amdgcn_sched_barrier(0);
  {
    float a = 0.f, q = 0.f, a2 = 0.f, q2 = 0.f;
#pragma unroll
    for (int s = 0; s < 2; ++s)
#pragma unroll
      for (int e = 0; e < 4; ++e) {
        const float lo = bf2f(xr[s][e] & 0xffffu), hi = bfhi2f(xr[s][e]);
        a += lo; a2 += hi;
        q = __builtin_fmaf(lo, lo, q);
        q2 = __builtin_fmaf(hi, hi, q2);
      }
    const auto p1 = __builtin_amdgcn_permlane32_swap(f2u(a + a2), f2u(q + q2), false, false);
    const unsigned t = f2u(u2f(p1[0]) + u2f(p1[1]));
    const auto p2 = __builtin_amdgcn_permlane16_swap(t, t, false, false);
    *(float*)(smem + ((lane >> 5) * 16 + i) * 32 + wave * 4) = u2f(p2[0]) + u2f(p2[1]);
  }
  TS(0, 1);
  asm volatile("s_waitcnt lgkmcnt(0)" ::: "memory");
  __builtin_amdgcn_s_barrier();
  asm volatile("" ::: "memory");
  bf16x8 xb[2];
  float2 st;
  {
    const char* sp = smem + ((lane >> 5) * 16 + i) * 32;
    const f32x4 t0 = *(const f32x4*)sp, t1 = *(const f32x4*)(sp + 16);
    const unsigned t = f2u(((t0[0] + t0[1]) + (t0[2] + t0[3])) + ((t1[0] + t1[1]) + (t1[2] + t1[3])));
    const auto p = __builtin_amdgcn_permlane32_swap(t, t, false, false);
    const float mean = u2f(p[0]) * (1.0f / D_);
    const float var = fmaxf(u2f(p[1]) * (1.0f / D_) - mean * mean, 0.f);
    const float mb = rbf(mean);
    const float inv = 1.0f / rbf(__builtin_sqrtf(rbf(rbf(var) + 1e-5f)));
#pragma unroll
    for (int s = 0; s < 2; ++s) {
      u32x4 o;
#pragma unroll
      for (int e = 0; e < 4; ++e) o[e] = ln_pair(xr[s][e], mb, inv);
      xb[s] = __builtin_bit_cast(bf16x8, o);
    }
    asm volatile("s_waitcnt vmcnt(0)" ::: "memory");
    st = make_float2(mb, inv);
  }
#pragma unroll
  for (int s = 0; s < 2; ++s)
    *(bf16x8*)(xnf + ((size_t)(rt * 32 + 4 * wave + 2 * s + (g >> 1)) * 64 + (g & 1) * 32 + th * 16 + i) * 8) = xb[s];
  f32x4* red = (f32x4*)(smem + KA_RED);
  {
    const unsigned ta = eb + (8 * g + (i >> 2)) * 32 + (i & 3) * 8;
    f32x4 acc[3];
#pragma unroll
    for (int c = 0; c < 3; ++c) acc[c] = (f32x4){0.f, 0.f, 0.f, 0.f};
#pragma unroll
    for (int s = 0; s < 2; ++s) {
#pragma unroll
      for (int c = 0; c < 2; ++c) {
        const u32x2 lo = lds_tr(ta + c * 2048 + s * 1024), hi = lds_tr(ta + c * 2048 + s * 1024 + 128);
        const u32x4 f = {lo[0], lo[1], hi[0], hi[1]};
        acc[c] = mfma16(xb[s], __builtin_bit_cast(bf16x8, f), acc[c]);
      }
      acc[2] = mfma16(xb[s], rf[s], acc[2]);
    }
#pragma unroll
    for (int c = 0; c < 3; ++c) red[(wave * 3 + c) * 64 + lane] = acc[c];
  }
  if (wave == 7 && lane < 16) stats[row0 + lane] = st;
  asm volatile("s_waitcnt lgkmcnt(0)" ::: "memory");
  __builtin_amdgcn_s_barrier();
  asm volatile("" ::: "memory");
  TS(0, 2);
  if (wave < 3) {
    f32x4 v = red[wave * 64 + lane];
#pragma unroll
    for (int k = 1; k < 8; ++k) v += red[(k * 3 + wave) * 64 + lane];
    if (wave == 0) {
      const int hh = (i >> 2) & 1, j = (i >> 3) * 4 + (i & 3);
#pragma unroll
      for (int e = 0; e < 4; ++e)
        kf[rt * 512 + (hh * 32 + 16 * th + 4 * g + e) * 8 + j] = __builtin_bit_cast(u16, (__bf16)v[e]);
    } else if (wave == 1) {
      const u32x2 o = {pack2(v[0], v[1]), pack2(v[2], v[3])};
      *(u32x2*)(vf + rt * (VT_STRIDE / 2) + th * 256 + ((g >> 1) * 16 + i) * 8 + (g & 1) * 4) = o;
      if (lane == 0) {
        const unsigned cv = th ? 0u : 0x3f803f80u;
        *(u32x4*)((char*)vf + rt * VT_STRIDE + 1024 + th * 16) = (u32x4){cv, cv, cv, cv};
      }
    } else {
      unsigned k0_ = 0, k1_ = 0;
#pragma unroll
      for (int e = 0; e < 4; ++e) {
        const unsigned u = __float_as_uint(rbf(v[e]));
        const unsigned ord = (u & 0x80000000u) ? ~u : (u | 0x80000000u);
        const unsigned key = (ord & 0xffff0000u) | (unsigned)(2047 - (row0 + 4 * g + e));
        k1_ = max(k1_, min(k0_, key));
        k0_ = max(k0_, key);
      }
#pragma unroll
      for (int off = 16; off < 64; off <<= 1) {
        const unsigned o0 = __shfl_xor(k0_, off), o1 = __shfl_xor(k1_, off);
        k1_ = max(min(k0_, o0), max(k1_, o1));
        k0_ = max(k0_, o0);
      }
      if (lane == 0) *(u32x2*)(rkeys + 2 * blockIdx.x) = (u32x2){k0_, k1_};
    }
  }
  TS(0, 3); TSR(0, 9);
}

#define P1A1 32
#define P1B1 52
#define P1A2 61
#define P2A1 5
#define P2B1 7
#define P2A2U 6
#define PRIO_IF(a, b, p) asm volatile("s_cmp_lg_u32 %0, %1\n\ts_cbranch_scc1 1f\n\ts_setprio " #p "\n1:" :: "s"(a), "s"(b) : "scc")
static constexpr int LDS_K = 0;
static constexpr int LDS_V = 65536;
static constexpr int LDS_H = 65536 + VF_BYTES + 2048;
static constexpr int LDS_F = LDS_H + 1024;
static constexpr int LDS_HH = LDS_F + 64;
static constexpr int LDS_Y = LDS_HH + 256;
static constexpr int LDS_TOTAL = LDS_Y + 2048;
static constexpr int CH_STRIDE = 1056;
typedef __attribute__((address_space(1))) unsigned gu32;
typedef __attribute__((address_space(1))) unsigned long long gu64;
#define RLX_AGENT __ATOMIC_RELAXED, __HIP_MEMORY_SCOPE_AGENT
DI void wait_count(unsigned* word, unsigned want) {
  for (unsigned spins = 0; spins < (1u << 18); ++spins) {
    if ((unsigned)__builtin_amdgcn_readfirstlane(__hip_atomic_load((gu32*)word, RLX_AGENT)) >= want) break;
    __builtin_amdgcn_s_sleep(1);
  }
  __builtin_amdgcn_fence(__ATOMIC_ACQUIRE, "agent");
  asm volatile("s_waitcnt vmcnt(0)" ::: "memory");
}

DI void stage_v(const char* vsrc, unsigned vdst, int hj) {
#pragma unroll
  for (int c = 0; c < 16; ++c) glds16(vsrc + c * 4096, vdst + c * 4096);
  if (hj < 2) glds16(vsrc + 65536, vdst + 65536);
}

__global__ __launch_bounds__(768) void kb_attn(const u16* __restrict__ xnf, const u16* __restrict__ wqf,
                                               const u16* __restrict__ kf, const u16* __restrict__ vf,
                                               const unsigned* __restrict__ rkeys, const u16* __restrict__ wg,
                                               const u16* __restrict__ wu, const u16* __restrict__ wd,
                                               u16* of, float* ypart, unsigned* cnt,
                                               const u16* __restrict__ x, const u16* __restrict__ wof,
                                               const float2* __restrict__ stats, u16* __restrict__ out) {
  __shared__ __attribute__((aligned(16))) char smem[LDS_TOTAL];
  const int tid = threadIdx.x;
  const int lane = tid & 63, wave = tid >> 6;
  const int b = blockIdx.x;
  unsigned* flags = (unsigned*)(smem + LDS_F);
  TSR(1, 8); TS(1, 0);

  const int rb = (b & 7) + 8 * (b >> 6), hg = (b >> 3) & 7;
  if (wave < 8) {
    const int rt = wave >> 2, ks4 = wave & 3;
    const int r = lane & 31, hh = lane >> 5;
    {
      bf16x8 af[2][8], bfr[8];
      const int rtq = wave & 1, kq = wave >> 1;
      const u16* ap = wqf + ((size_t)(2 * hg) * 32 + 8 * kq) * 512 + lane * 8;
      const u16* bp = xnf + ((size_t)(2 * rb + rtq) * 32 + 8 * kq) * 512 + lane * 8;
#pragma unroll
      for (int s = 0; s < 8; ++s) {
        bfr[s] = *(const bf16x8*)(bp + s * 512);
        af[0][s] = *(const bf16x8*)(ap + s * 512);
        af[1][s] = *(const bf16x8*)(ap + 32 * 512 + s * 512);
      }
      f32x16 acc[2];
#pragma unroll
      for (int e = 0; e < 16; ++e) { acc[0][e] = 0.f; acc[1][e] = 0.f; }
#pragma unroll
      for (int s = 0; s < 8; ++s) {
        acc[0] = mfma32(af[0][s], bfr[s], acc[0]);
        acc[1] = mfma32(af[1][s], bfr[s], acc[1]);
      }
#pragma unroll
      for (int dt = 0; dt < 2; ++dt)
#pragma unroll
        for (int q4 = 0; q4 < 4; ++q4) {
          f32x4 p = {acc[dt][4 * q4], acc[dt][4 * q4 + 1], acc[dt][4 * q4 + 2], acc[dt][4 * q4 + 3]};
          *(f32x4*)(smem + LDS_V + ((((rtq * 4 + kq) * 2 + dt) * 4 + q4) * 64 + lane) * 16) = p;
        }
    }
    __builtin_amdgcn_sched_barrier(0);
    TS(1, 1);
    __syncthreads();
    TS(1, 2);
    const int hl = wave & 3;
    const int head = hg * 4 + hl;
    bf16x8 qf;
    {
      f32x4 a0 = {0.f, 0.f, 0.f, 0.f}, a1 = {0.f, 0.f, 0.f, 0.f};
#pragma unroll
      for (int k = 0; k < 4; ++k) {
        const char* p = smem + LDS_V + ((((rt * 4 + k) * 2 + (hl >> 1)) * 4 + 2 * (hl & 1)) * 64 + lane) * 16;
        a0 += *(const f32x4*)p;
        a1 += *(const f32x4*)(p + 1024);
      }
      const float SC = 0.25f * 1.44269504088896340736f;
      u32x4 pk = {pack2(rbf(a0[0]) * SC, rbf(a0[1]) * SC), pack2(rbf(a0[2]) * SC, rbf(a0[3]) * SC),
                  pack2(rbf(a1[0]) * SC, rbf(a1[1]) * SC), pack2(rbf(a1[2]) * SC, rbf(a1[3]) * SC)};
      qf = __builtin_bit_cast(bf16x8, pk);
    }
    __syncthreads();

    const int yng = __builtin_amdgcn_readfirstlane(wave >> 2);
    const unsigned lbase = (unsigned)(uintptr_t)(__attribute__((address_space(3))) char*)smem;
    float m0 = -__builtin_inff(), m1 = -__builtin_inff();
    f32x16 zero16;
#pragma unroll
    for (int e = 0; e < 16; ++e) zero16[e] = 0.f;
    {
      unsigned kq_ = lbase + LDS_K + lane * 16;
      asm volatile("" : "+v"(kq_));
      f32x16 sa = mfma32(LDSF(kq_), qf, zero16);
      f32x16 sb = mfma32(LDSF(kq_ + 1024), qf, zero16);
      bf16x8 k2 = LDSF(kq_ + 2048), k3 = LDSF(kq_ + 3072);
      __builtin_amdgcn_sched_barrier(0);
#pragma unroll
      for (int t = 0; t < 64; ++t) {
        if (t == 0) __builtin_amdgcn_s_setprio(2);
        if (t == P1A1) PRIO_IF(yng, 0, 1);
        if (t == P1B1) PRIO_IF(yng, 1, 1);
        if (t == P1A2) PRIO_IF(yng, 0, 0);
        f32x16 sc = sb;
        bf16x8 k4 = k3;
        if (t + 2 < 64) sc = mfma32(k2, qf, zero16);
        if (t + 4 < 64) k4 = LDSF(kq_ + (t + 4) * 1024);
        if (t & 1) {
#pragma unroll
          for (int e = 0; e < 16; e += 2) m1 = fmaxf(fmaxf(m1, sa[e]), sa[e + 1]);
        } else {
#pragma unroll
          for (int e = 0; e < 16; e += 2) m0 = fmaxf(fmaxf(m0, sa[e]), sa[e + 1]);
        }
        sa = sb; sb = sc; k2 = k3; k3 = k4;
        __builtin_amdgcn_sched_group_barrier(0x008, 1, 0);
        __builtin_amdgcn_sched_group_barrier(0x100, 1, 0);
        __builtin_amdgcn_sched_group_barrier(0x002, 8, 0);
        __builtin_amdgcn_sched_barrier(0);
      }
    }
    float m = fmaxf(m0, m1);
    m = fmaxf(m, __shfl_xor(m, 32));
    TS(1, 3);
    while (__hip_atomic_load(flags, __ATOMIC_ACQUIRE, __HIP_MEMORY_SCOPE_WORKGROUP) < 4) __builtin_amdgcn_s_sleep(1);
    asm volatile("" ::: "memory");
    TS(1, 4);
    const int r64 = tid >> 3, oct = tid & 7;
    const int row = rb * 64 + r64;
    const size_t off = (size_t)row * D_ + hg * 64 + oct * 8;
    const u32x4 xv = *(const u32x4*)(x + off);
    const float2 st = stats[row];

    f32x16 negm;
#pragma unroll
    for (int e = 0; e < 16; ++e) negm[e] = -m;
    f32x16 o;
#pragma unroll
    for (int e = 0; e < 16; ++e) o[e] = 0.f;
    unsigned ka_ = lbase + LDS_K + lane * 16;
    unsigned va_ = lbase + LDS_V + ((r < 16) ? (hh * 16 + r) * 16 : (r == 16 ? 1024 : 1040));
    unsigned vb_ = lbase + LDS_V + ((r < 16) ? (hh * 16 + r) * 16 + 512 : (r == 16 ? 1024 : 1040));
    f32x16 s = mfma32(LDSF(ka_), qf, negm);
    bf16x8 kn = LDSF(ka_ + 1024);
    bf16x8 v0 = LDSF(va_);
    bf16x8 v1 = LDSF(vb_);
    bf16x8 v1p = v1;
    f32x8 pbp = {0.f, 0.f, 0.f, 0.f, 0.f, 0.f, 0.f, 0.f};
    const int thr1 = P2A1 + (P2B1 - P2A1) * yng, thr2 = 7 + 92 * yng;
    __builtin_amdgcn_s_setprio(2);
#pragma unroll 1
    for (int tb = 0; tb < 8; ++tb) {
      asm volatile("" : "+v"(ka_), "+v"(va_), "+v"(vb_));
      PRIO_IF(tb, thr1, 1);
#pragma unroll
      for (int u = 0; u < 8; ++u) {
        if (u == P2A2U) PRIO_IF(tb, thr2, 0);
        const f32x16 sn = mfma32(kn, qf, negm);
        const bf16x8 k2 = LDSF(ka_ + (u + 2) * 1024);
        const bf16x8 v0n = LDSF(va_ + (u + 1) * VT_STRIDE);
        const bf16x8 v1n = LDSF(vb_ + (u + 1) * VT_STRIDE);
        f32x8 pa, pb;
#pragma unroll
        for (int e = 0; e < 5; ++e) pa[e] = __builtin_amdgcn_exp2f(s[e]);
        o = smfma32(v1p, pbp, o);
#pragma unroll
        for (int e = 5; e < 8; ++e) pa[e] = __builtin_amdgcn_exp2f(s[e]);
#pragma unroll
        for (int e = 8; e < 10; ++e) pb[e - 8] = __builtin_amdgcn_exp2f(s[e]);
        o = smfma32(v0, pa, o);
#pragma unroll
        for (int e = 10; e < 16; ++e) pb[e - 8] = __builtin_amdgcn_exp2f(s[e]);
        pbp = pb;
        v1p = v1;
        s = sn; kn = k2; v0 = v0n; v1 = v1n;
        __builtin_amdgcn_sched_group_barrier(0x008, 1, 0);
        __builtin_amdgcn_sched_group_barrier(0x100, 3, 0);
        __builtin_amdgcn_sched_group_barrier(0x402, 5, 0);
        __builtin_amdgcn_sched_group_barrier(0x008, 1, 0);
        __builtin_amdgcn_sched_group_barrier(0x402, 5, 0);
        __builtin_amdgcn_sched_group_barrier(0x008, 1, 0);
        __builtin_amdgcn_sched_group_barrier(0x402, 6, 0);
        __builtin_amdgcn_sched_barrier(0);
      }
      ka_ += 8 * 1024; va_ += 8 * VT_STRIDE; vb_ += 8 * VT_STRIDE;
    }
    o = smfma32(v1p, pbp, o);
    TS(1, 5);
    const float il = 1.0f / __shfl(o[8], r);
    u32x4 w0 = {pack2(o[0] * il, o[1] * il), pack2(o[2] * il, o[3] * il), pack2(o[4] * il, o[5] * il),
                pack2(o[6] * il, o[7] * il)};
    const __amdgpu_buffer_rsrc_t ors = __builtin_amdgcn_make_buffer_rsrc(of + (size_t)rb * 32768, 0, 65536, 0x00020000);
#pragma unroll
    for (int e = 0; e < 4; ++e) w0[e] = min(w0[e], 0xfffffffeu);
    __builtin_amdgcn_raw_buffer_store_b128(w0, ors, ((rt * 32 + head) * 64 + lane) * 16, 0, 16);
    __builtin_amdgcn_s_barrier();
    u32x4 xnv;
#pragma unroll
    for (int e = 0; e < 4; ++e) xnv[e] = ln_pair(xv[e], st.x, st.y);
    asm volatile("" : "+v"(xnv));
    TS(1, 6);
    {
      const int rto = wave & 1, ko = wave >> 1;
      u32x4 fr[8];
      for (unsigned spins = 0; spins < (1u << 16); ++spins) {
        asm volatile("" ::: "memory");
#pragma unroll
        for (int s8 = 0; s8 < 8; ++s8)
          fr[s8] = __builtin_amdgcn_raw_buffer_load_b128(ors, ((rto * 32 + 8 * ko + s8) * 64 + lane) * 16, 0, 16);
        unsigned mx = 0;
#pragma unroll
        for (int s8 = 0; s8 < 8; ++s8) mx = max(max(mx, max(fr[s8][0], fr[s8][1])), max(fr[s8][2], fr[s8][3]));
        if (!__any(mx == ~0u)) break;
      }
      bf16x8 bfr[8];
#pragma unroll
      for (int s8 = 0; s8 < 8; ++s8) bfr[s8] = __builtin_bit_cast(bf16x8, fr[s8]);
      __syncthreads();
      const unsigned wa_ = lbase + LDS_K + ko * 8192 + lane * 16;
      f32x16 acc[2];
#pragma unroll
      for (int e = 0; e < 16; ++e) { acc[0][e] = 0.f; acc[1][e] = 0.f; }
#pragma unroll
      for (int s8 = 0; s8 < 8; ++s8) {
        acc[0] = mfma32(LDSF(wa_ + s8 * 1024), bfr[s8], acc[0]);
        acc[1] = mfma32(LDSF(wa_ + 32768 + s8 * 1024), bfr[s8], acc[1]);
      }
#pragma unroll
      for (int ct = 0; ct < 2; ++ct)
#pragma unroll
        for (int q4 = 0; q4 < 4; ++q4) {
          f32x4 p = {acc[ct][4 * q4], acc[ct][4 * q4 + 1], acc[ct][4 * q4 + 2], acc[ct][4 * q4 + 3]};
          *(f32x4*)(smem + LDS_V + (((rto * 4 + ko) * 2 + ct) * 4 + q4) * CH_STRIDE + lane * 16) = p;
        }
    }
    __syncthreads();
    f32x4 lo = {0.f, 0.f, 0.f, 0.f}, hi = {0.f, 0.f, 0.f, 0.f};
#pragma unroll
    for (int k = 0; k < 4; ++k) {
      const char* p = smem + LDS_V + (((r64 >> 5) * 4 + k) * 8 + oct) * CH_STRIDE + (r64 & 31) * 16;
      lo += *(const f32x4*)p;
      hi += *(const f32x4*)(p + 512);
    }
    float upd[8];
#pragma unroll
    for (int e = 0; e < 8; ++e) upd[e] = 0.f;
    const u32x4 sl = *(const u32x4*)(smem + LDS_F + 16);
    if (row == (int)sl[0] || row == (int)sl[1]) {
      const int tk = (row == (int)sl[0]) ? 0 : 1;
      f32x4 ya = {0.f, 0.f, 0.f, 0.f}, yb = {0.f, 0.f, 0.f, 0.f};
#pragma unroll
      for (int p = 0; p < 4; ++p) {
        const char* yp = smem + LDS_Y + (tk * 4 + p) * 256 + oct * 32;
        ya += *(const f32x4*)yp;
        yb += *(const f32x4*)(yp + 16);
      }
      const float rs = __uint_as_float(tk ? sl[3] : sl[2]);
#pragma unroll
      for (int e = 0; e < 4; ++e) { upd[e] = rbf(rbf(ya[e]) * rs); upd[4 + e] = rbf(rbf(yb[e]) * rs); }
    }
    float res[8];
#pragma unroll
    for (int e = 0; e < 4; ++e) {
      const float x0 = bf2f(xv[e] & 0xffffu), x1 = bfhi2f(xv[e]);
      const float a0 = rbf(e < 2 ? lo[2 * e] : hi[2 * e - 4]);
      const float a1 = rbf(e < 2 ? lo[2 * e + 1] : hi[2 * e - 3]);
      res[2 * e] = rbf(x0 + a0) + rbf(bf2f(xnv[e] & 0xffffu) + upd[2 * e]);
      res[2 * e + 1] = rbf(x1 + a1) + rbf(bfhi2f(xnv[e]) + upd[2 * e + 1]);
    }
    u32x4 ov = {pack2(res[0], res[1]), pack2(res[2], res[3]), pack2(res[4], res[5]), pack2(res[6], res[7])};
    *(u32x4*)(out + off) = ov;
  } else {
    __builtin_amdgcn_s_setprio(3);
    const int hj = __builtin_amdgcn_readfirstlane(wave - 8);
    const unsigned ldst = (unsigned)(uintptr_t)(__attribute__((address_space(3))) char*)smem + hj * 1024;
    {
      const char* ksrc = (const char*)kf + hj * 1024 + lane * 16;
#pragma unroll
      for (int c = 0; c < 16; ++c) glds16(ksrc + c * 4096, ldst + LDS_K + c * 4096);
    }
    if (hj == 0 && lane < 4) flags[lane] = 0u;
    const char* vsrc = (const char*)vf + hj * 1024 + lane * 16;
    const int g = lane >> 4, i = lane & 15;
    const int f0 = b * 16;
    const bool mod = b < NHELP;
    const u32x4 kv = *(const u32x4*)(rkeys + lane * 4);
    unsigned k0 = max(kv[0], kv[2]), k1 = max(min(kv[0], kv[2]), max(kv[1], kv[3]));
#pragma unroll
    for (int off = 1; off < 64; off <<= 1) {
      const unsigned o0 = __shfl_xor(k0, off), o1 = __shfl_xor(k1, off);
      k1 = max(min(k0, o0), max(k1, o1));
      k0 = max(k0, o0);
    }
    const int i0 = 2047 - (int)(k0 & 0x7ffu), i1 = 2047 - (int)(k1 & 0x7ffu);
    if (hj == 0 && lane == 0) {
      const float v0 = __uint_as_float((k0 & 0x80000000u) ? (k0 & 0x7fff0000u) : (~k0 & 0xffff0000u));
      const float v1 = __uint_as_float((k1 & 0x80000000u) ? (k1 & 0x7fff0000u) : (~k1 & 0xffff0000u));
      *(u32x4*)(smem + LDS_F + 16) = (u32x4){(unsigned)i0, (unsigned)i1, __float_as_uint(rbf(1.0f / (1.0f + __expf(-v0)))),
                                             __float_as_uint(rbf(1.0f / (1.0f + __expf(-v1))))};
    }
    bf16x8 xa[4];
    if (mod) {
      const int srow = (i & 1) ? i1 : i0;
      const u16* xp = xnf + ((size_t)((srow >> 5) * 32 + 16 * (hj >> 1) + 8 * ((i >> 1) & 1) + (g >> 1)) * 64 + (g & 1) * 32 + (srow & 31)) * 8;
#pragma unroll
      for (int s = 0; s < 4; ++s) xa[s] = *(const bf16x8*)(xp + (size_t)s * 1024);
    }
    TS(1, 3);
    asm volatile("s_waitcnt vmcnt(0)" ::: "memory");
    __syncthreads();
    __syncthreads();
    stage_v(vsrc, ldst + LDS_V, hj);
    asm volatile("s_waitcnt vmcnt(0)" ::: "memory");
    if (lane == 0) __hip_atomic_fetch_add(flags, 1u, __ATOMIC_RELEASE, __HIP_MEMORY_SCOPE_WORKGROUP);
    __builtin_amdgcn_sched_barrier(0);
    if (mod) {
      unsigned wa[4][8];
      const char* ub = (const char*)(((hj & 1) ? wu : wg) + (size_t)(256 * (hj >> 1)) * F_ + f0);
      const unsigned lo1 = (unsigned)((128 * (i >> 3) + 8 * g) * F_ + 2 * (i & 7)) * 2u;
#pragma unroll
      for (int s = 0; s < 4; ++s)
#pragma unroll
        for (int j = 0; j < 8; j += 2) {
          gcp rp = (gcp)(ub + (size_t)((32 * s + j) * F_ * 2));
          asm volatile("" : "+s"(rp));
          wa[s][j] = *(const __attribute__((address_space(1))) unsigned*)(rp + lo1);
          wa[s][j + 1] = *(const __attribute__((address_space(1))) unsigned*)(rp + F_ * 2 + lo1);
        }
      unsigned wdv[4][4];
      const char* db = (const char*)(wd + (size_t)f0 * D_ + hj * 128);
      const unsigned lo2 = (unsigned)(4 * g * D_ + 2 * i) * 2u;
#pragma unroll
      for (int q = 0; q < 4; ++q)
#pragma unroll
        for (int j = 0; j < 4; ++j) wdv[q][j] = *(const unsigned*)(db + (j * D_ + 32 * q) * 2 + lo2);
      f32x4 a0 = {0.f, 0.f, 0.f, 0.f}, a1 = {0.f, 0.f, 0.f, 0.f};
#pragma unroll
      for (int s = 0; s < 4; ++s) {
        a0 = mfma16(xa[s], frag_lo(wa[s]), a0);
        a1 = mfma16(xa[s], frag_hi(wa[s]), a1);
      }
      if (lane < 16)
        *(f32x4*)(smem + LDS_H + (hj * 16 + i) * 16) =
            (i < 8) ? (f32x4){a0[0], a0[1], a1[0], a1[1]} : (f32x4){a0[2], a0[3], a1[2], a1[3]};
      TS(1, 4);
      if (lane == 0) __hip_atomic_fetch_add(flags + 1, 1u, __ATOMIC_RELEASE, __HIP_MEMORY_SCOPE_WORKGROUP);
      while (__hip_atomic_load(flags + 1, __ATOMIC_ACQUIRE, __HIP_MEMORY_SCOPE_WORKGROUP) < 4) __builtin_amdgcn_s_sleep(2);
      asm volatile("" ::: "memory");
      __builtin_amdgcn_sched_barrier(0);
      const float* hp = (const float*)(smem + LDS_H) + ((i >> 3) * 16 + (i & 7)) * 4 + g;
      const float gt = rbf((hp[0] + hp[32]) + (hp[128] + hp[160]));
      const float up = __shfl_xor(gt, 8);
      const float hv = rbf(gt / (1.0f + __expf(-gt))) * up;
      if (i < 8) *(u16*)(smem + LDS_HH + hj * 64 + (g & 1) * 32 + (2 * i + (g >> 1)) * 2) = __builtin_bit_cast(u16, (__bf16)hv);
      const s16x4 ha = *(const s16x4*)(smem + LDS_HH + hj * 64 + (i & 1) * 32 + g * 8);
#pragma unroll
      for (int q = 0; q < 4; ++q) {
        const u32x2 be = {perm_lo(wdv[q][1], wdv[q][0]), perm_lo(wdv[q][3], wdv[q][2])};
        const u32x2 bo = {perm_hi(wdv[q][1], wdv[q][0]), perm_hi(wdv[q][3], wdv[q][2])};
        const f32x4 z = {0.f, 0.f, 0.f, 0.f};
        const f32x4 ye = __builtin_amdgcn_mfma_f32_16x16x16bf16_1k(ha, __builtin_bit_cast(s16x4, be), z, 0, 0, 0);
        const f32x4 yo = __builtin_amdgcn_mfma_f32_16x16x16bf16_1k(ha, __builtin_bit_cast(s16x4, bo), z, 0, 0, 0);
        if (lane < 16) {
          gu64* yp = (gu64*)(ypart + (size_t)b * 1024 + hj * 128 + 32 * q + 2 * i);
          __hip_atomic_store(yp, __builtin_bit_cast(unsigned long long, (f32x2){ye[0], yo[0]}), RLX_AGENT);
          __hip_atomic_store(yp + 256, __builtin_bit_cast(unsigned long long, (f32x2){ye[1], yo[1]}), RLX_AGENT);
        }
      }
      asm volatile("s_waitcnt vmcnt(0)" ::: "memory");
      if (lane == 0 && __hip_atomic_fetch_add(flags + 2, 1u, __ATOMIC_ACQ_REL, __HIP_MEMORY_SCOPE_WORKGROUP) == 3u)
        __hip_atomic_fetch_add((gu32*)(cnt), 1u, RLX_AGENT);
    }
    __builtin_amdgcn_sched_barrier(0);
    const bool own0 = (unsigned)(i0 - rb * 64) < 64u, own1 = (unsigned)(i1 - rb * 64) < 64u;
    if (own0 || own1) {
      wait_count(cnt, NHELP);
#pragma unroll
      for (int tk = 0; tk < 2; ++tk) {
        if (tk ? own1 : own0) {
          const int p0 = hj * 4 + g;
          const float* yp = ypart + (size_t)(p0 * 2 + tk) * 512 + hg * 64 + i * 4;
          f32x4 yv[6];
#pragma unroll
          for (int q = 0; q < 6; ++q) yv[q] = *(const f32x4*)(yp + (p0 + 16 * q < NHELP ? q * 16 * 1024 : 0));
          f32x4 y = ((yv[0] + yv[1]) + (yv[2] + yv[3])) + yv[4];
          if (p0 + 80 < NHELP) y += yv[5];
#pragma unroll
          for (int e = 0; e < 4; ++e) {
            y[e] += __shfl_xor(y[e], 16);
            y[e] += __shfl_xor(y[e], 32);
          }
          if (lane < 16) *(f32x4*)(smem + LDS_Y + (tk * 4 + hj) * 256 + lane * 16) = y;
        }
      }
    }
    __builtin_amdgcn_sched_barrier(0);
    u32x4 wr_[16];
    const char* wsrc = (const char*)wof + (size_t)hg * 65536 + hj * 1024 + lane * 16;
#pragma unroll
    for (int c = 0; c < 16; ++c) wr_[c] = *(const u32x4*)(wsrc + c * 4096);
    TS(1, 5);
    __syncthreads();
#pragma unroll
    for (int c = 0; c < 16; ++c) *(u32x4*)(smem + LDS_K + hj * 1024 + lane * 16 + c * 4096) = wr_[c];
    __syncthreads();
    TS(1, 6);
  }
  TS(1, 7); TSR(1, 9);
}

extern "C" void kernel_launch(void* const* d_in, const int* in_sizes, int n_in, void* d_out, int out_size,
                              void* d_ws, size_t ws_size, hipStream_t stream) {
  const u16* x = (const u16*)d_in[0];
  const u16* wq = (const u16*)d_in[1];
  const u16* wk = (const u16*)d_in[2];
  const u16* wv = (const u16*)d_in[3];
  const u16* wo = (const u16*)d_in[4];
  const u16* wr = (const u16*)d_in[6];
  const u16* wg = (const u16*)d_in[7];
  const u16* wu = (const u16*)d_in[8];
  const u16* wd = (const u16*)d_in[9];
  char* ws = (char*)d_ws;
  u16* xnf = (u16*)(ws + OFF_XNF);
  u16* of = (u16*)(ws + OFF_OF);
  u16* wqf = (u16*)(ws + OFF_WQF);
  u16* wof = (u16*)(ws + OFF_WOF);
  u16* kf = (u16*)(ws + OFF_KF);
  u16* vf = (u16*)(ws + OFF_VF);
  unsigned* rkeys = (unsigned*)(ws + OFF_R);
  float2* stats = (float2*)(ws + OFF_STATS);
  float* ypart = (float*)(ws + OFF_Y);
  unsigned* cnt = (unsigned*)(ws + OFF_CNT);
  u16* out = (u16*)d_out;

  ka_prep<<<256, 512, 0, stream>>>(x, wq, wk, wv, wo, wr, xnf, wqf, wof, kf, vf, rkeys, stats, cnt, of);
  kb_attn<<<256, 768, 0, stream>>>(xnf, wqf, kf, vf, rkeys, wg, wu, wd, of, ypart, cnt, x, wof, stats, out);
}
```

```cpp
#include <hip/hip_runtime.h>
#include <hip/hip_bf16.h>

typedef unsigned short u16;
typedef __attribute__((ext_vector_type(8))) __bf16 bf16x8;
typedef __attribute__((ext_vector_type(16))) __bf16 bf16x16;
typedef __attribute__((ext_vector_type(2))) __bf16 bf16x2;
typedef __attribute__((ext_vector_type(8))) float f32x8;
typedef __attribute__((ext_vector_type(2))) float f32x2;
typedef __attribute__((ext_vector_type(4))) float f32x4;
typedef __attribute__((ext_vector_type(16))) float f32x16;
typedef __attribute__((ext_vector_type(4))) unsigned u32x4;
typedef __attribute__((ext_vector_type(2))) unsigned u32x2;
typedef __attribute__((ext_vector_type(4))) short s16x4;
typedef const __attribute__((address_space(1))) char* gcp;

#define DI __device__ __forceinline__
#define TSDECL
#define TS(k, n)
#define TSR(k, n)
#define TSFLUSH(k)

static constexpr int S_ = 2048;
static constexpr int D_ = 512;
static constexpr int F_ = 1376;

#define LDSF(a) (*(const __attribute__((address_space(3))) bf16x8*)(uintptr_t)(a))
DI float bf2f(unsigned u) { return __uint_as_float(u << 16); }
DI float bfhi2f(unsigned u) { return __uint_as_float(u & 0xffff0000u); }
DI float rbf(float f) { return (float)(__bf16)f; }
DI unsigned pack2(float lo, float hi) {
  f32x2 p = {lo, hi};
  bf16x2 q = __builtin_convertvector(p, bf16x2);
  return __builtin_bit_cast(unsigned, q);
}
DI f32x4 mfma16(bf16x8 a, bf16x8 b, f32x4 c) { return __builtin_amdgcn_mfma_f32_16x16x32_bf16(a, b, c, 0, 0, 0); }
DI f32x16 mfma32(bf16x8 a, bf16x8 b, f32x16 c) { return __builtin_amdgcn_mfma_f32_32x32x16_bf16(a, b, c, 0, 0, 0); }
DI f32x16 smfma32(bf16x8 a, f32x8 p, f32x16 c) {
  return __builtin_amdgcn_smfmac_f32_32x32x32_bf16(a, __builtin_bit_cast(bf16x16, p), c, 0xdddd, 0, 0);
}
DI unsigned perm_lo(unsigned hi_src, unsigned lo_src) { return __builtin_amdgcn_perm(hi_src, lo_src, 0x05040100u); }
DI unsigned perm_hi(unsigned hi_src, unsigned lo_src) { return __builtin_amdgcn_perm(hi_src, lo_src, 0x07060302u); }

DI bf16x8 frag_lo(const unsigned* w) {
  u32x4 r = {perm_lo(w[1], w[0]), perm_lo(w[3], w[2]), perm_lo(w[5], w[4]), perm_lo(w[7], w[6])};
  return __builtin_bit_cast(bf16x8, r);
}
DI bf16x8 frag_hi(const unsigned* w) {
  u32x4 r = {perm_hi(w[1], w[0]), perm_hi(w[3], w[2]), perm_hi(w[5], w[4]), perm_hi(w[7], w[6])};
  return __builtin_bit_cast(bf16x8, r);
}
DI float u2f(unsigned u) { return __uint_as_float(u); }
DI unsigned f2u(float f) { return __float_as_uint(f); }
DI unsigned ln_pair(unsigned xp, float mb, float inv) {
  const unsigned db = pack2(bf2f(xp & 0xffffu) - mb, bfhi2f(xp) - mb);
  return pack2(bf2f(db & 0xffffu) * inv, bfhi2f(db) * inv);
}
DI void glds16(const char* gsrc, unsigned lds_dst) {
  unsigned keep;
  asm volatile("s_mov_b32 %0, m0\n\ts_mov_b32 m0, %2\n\ts_nop 0\n\tglobal_load_lds_dwordx4 %1, off\n\ts_mov_b32 m0, %0"
               : "=&s"(keep) : "v"(gsrc), "s"(lds_dst) : "memory");
}
DI void st16_wt(void* base, unsigned byte_off, u32x4 v) {
  const __amdgpu_buffer_rsrc_t rs = __builtin_amdgcn_make_buffer_rsrc(base, 0, 0x7fffffff, 0x00020000);
  __builtin_amdgcn_raw_buffer_store_b128(v, rs, byte_off, 0, 16);
}
typedef __attribute__((ext_vector_type(4))) short v4i16_t;
DI u32x2 lds_tr(unsigned a) {
  return __builtin_bit_cast(u32x2, __builtin_amdgcn_ds_read_tr16_b64_v4i16((__attribute__((address_space(3))) v4i16_t*)(uintptr_t)a));
}

DI void glds16s(unsigned voff, const void* sbase, unsigned lds_dst) {
  unsigned keep;
  asm volatile("s_mov_b32 %0, m0\n\ts_mov_b32 m0, %3\n\ts_nop 0\n\tglobal_load_lds_dwordx4 %1, %2\n\ts_mov_b32 m0, %0"
               : "=&s"(keep) : "v"(voff), "s"(sbase), "s"(lds_dst) : "memory");
}
DI void glds16c(unsigned voff, const void* sbase, unsigned lds_dst) {
  unsigned keep;
  asm volatile("s_mov_b32 %0, m0\n\ts_mov_b32 m0, %3\n\ts_nop 0\n\tglobal_load_lds_dwordx4 %1, %2 sc1\n\ts_mov_b32 m0, %0"
               : "=&s"(keep) : "v"(voff), "s"(sbase), "s"(lds_dst) : "memory");
}
extern "C" __device__ unsigned long long dispatch_id_() __asm("llvm.amdgcn.dispatch.id");

#define FL2 128
static constexpr int VT_STRIDE = 1056;
static constexpr int VF_BYTES = 64 * VT_STRIDE;
static constexpr unsigned OFF_OF = 0;
static constexpr unsigned OFF_XN = OFF_OF + S_ * D_ * 2;
static constexpr unsigned OFF_KF = OFF_XN + S_ * D_ * 2;
static constexpr unsigned OFF_VF = OFF_KF + 65536;
static constexpr unsigned OFF_R = OFF_VF + 65536;
static constexpr unsigned OFF_ST = OFF_R + 2048;
static constexpr unsigned OFF_FLAG = OFF_ST + S_ * 8;
static constexpr int NHELP = 86;
static constexpr unsigned OFF_FLG2 = OFF_FLAG + 4096;
static constexpr unsigned OFF_FLG3 = OFF_FLG2 + 256 * FL2;
static constexpr unsigned OFF_FLG4 = OFF_FLG3 + 4096;
static constexpr unsigned OFF_Y = OFF_FLG4 + 4096;
static constexpr unsigned OFF_CNT = OFF_Y + NHELP * 1024 * 4;
static constexpr unsigned WS_END = OFF_CNT + 256;

#define TWAIT 25
#define PRET
#define FSLEEP 4
#define SSLEEP 4
#define KSLEEP 2
#define WQD 0
#define QPRIO 2
#define P2A1 5
#define P2B1 7
#define P2A2U 6
#define PRIO_IF(a, b, p) asm volatile("s_cmp_lg_u32 %0, %1\n\ts_cbranch_scc1 1f\n\ts_setprio " #p "\n1:" :: "s"(a), "s"(b) : "scc")
static constexpr int LDS_K = 0;
static constexpr int LDS_V = 65536;
static constexpr int LDS_H = 65536 + VF_BYTES + 2048;
static constexpr int LDS_F = LDS_H + 1024;
static constexpr int LDS_HH = LDS_F + 128;
static constexpr int LDS_Y = LDS_HH + 256;
static constexpr int LDS_X = LDS_Y + 2048;
static constexpr int LDS_SP = LDS_X + 8192;
static constexpr int LDS_SQ = LDS_SP + 256;
static constexpr int LDS_SF = LDS_SQ + 2048;
static constexpr int LDS_TOTAL = LDS_SF + 512;
static constexpr int LDS_P0 = LDS_K + 32768, LDS_P1 = LDS_V + 32 * VT_STRIDE;
static constexpr int LDS_Q0 = LDS_K + 16384, LDS_Q1 = LDS_V + 16 * VT_STRIDE;
#define SM_DELTA 88.0f
#define SM_LIMIT 0x7d800000u
typedef __attribute__((address_space(1))) unsigned gu32;
typedef __attribute__((address_space(1))) unsigned long long gu64;
#define RLX_AGENT __ATOMIC_RELAXED, __HIP_MEMORY_SCOPE_AGENT
DI void wait_count(unsigned* word, unsigned want) {
  for (unsigned spins = 0; spins < (1u << 18); ++spins) {
    if ((unsigned)__builtin_amdgcn_readfirstlane(__hip_atomic_load((gu32*)word, RLX_AGENT)) >= want) break;
    __builtin_amdgcn_s_sleep(1);
  }
  asm volatile("s_waitcnt vmcnt(0)" ::: "memory");
}

DI unsigned lds_peek(unsigned* word) {
  return __builtin_amdgcn_readfirstlane(__hip_atomic_load(word, __ATOMIC_RELAXED, __HIP_MEMORY_SCOPE_WORKGROUP));
}
DI void lds_wait(unsigned* word, unsigned want) {
  while (lds_peek(word) < want) __builtin_amdgcn_s_sleep(1);
  asm volatile("" ::: "memory");
}

DI void lds_spin(unsigned* word, unsigned want) {
  while (lds_peek(word) < want) {}
  asm volatile("" ::: "memory");
}
DI void lds_add(unsigned* word, int lane) {
  asm volatile("s_waitcnt lgkmcnt(0)" ::: "memory");
  if (lane == 0) __hip_atomic_fetch_add(word, 1u, __ATOMIC_RELAXED, __HIP_MEMORY_SCOPE_WORKGROUP);
}
DI void ln_stats(float sum, float sq, float& mb, float& inv) {
  const float mean = sum * (1.0f / D_);
  const float var = fmaxf(sq * (1.0f / D_) - mean * mean, 0.f);
  mb = rbf(mean);
  inv = __builtin_amdgcn_rcpf(rbf(__builtin_amdgcn_sqrtf(rbf(rbf(var) + 1e-5f))));
}
DI bf16x8 ln_frag(u32x4 v, float mb, float inv) {
  u32x4 o;
#pragma unroll
  for (int e = 0; e < 4; ++e) o[e] = ln_pair(v[e], mb, inv);
  return __builtin_bit_cast(bf16x8, o);
}
DI bf16x8 tr_frag(unsigned a0, unsigned a1) {
  const u32x2 lo = lds_tr(a0), hi = lds_tr(a1);
  return __builtin_bit_cast(bf16x8, (u32x4){lo[0], lo[1], hi[0], hi[1]});
}
DI unsigned max4(u32x4 v) { return max(max(v[0], v[1]), max(v[2], v[3])); }
DI unsigned wave_max(unsigned v) {
  v = max(v, (unsigned)__builtin_amdgcn_update_dpp(0, (int)v, 0x111, 0xf, 0xf, false));
  v = max(v, (unsigned)__builtin_amdgcn_update_dpp(0, (int)v, 0x112, 0xf, 0xf, false));
  v = max(v, (unsigned)__builtin_amdgcn_update_dpp(0, (int)v, 0x114, 0xf, 0xf, false));
  v = max(v, (unsigned)__builtin_amdgcn_update_dpp(0, (int)v, 0x118, 0xf, 0xf, false));
  v = max(v, (unsigned)__builtin_amdgcn_update_dpp(0, (int)v, 0x142, 0xa, 0xf, false));
  v = max(v, (unsigned)__builtin_amdgcn_update_dpp(0, (int)v, 0x143, 0xc, 0xf, false));
  return (unsigned)__builtin_amdgcn_readlane((int)v, 63);
}
DI float ror8(float v) { return u2f(__builtin_amdgcn_update_dpp(0, f2u(v), 0x128, 0xf, 0xf, false)); }

__global__ __launch_bounds__(768) void fused(const u16* __restrict__ x, const u16* __restrict__ wq,
                                             const u16* __restrict__ wk, const u16* __restrict__ wv,
                                             const u16* __restrict__ wo, const u16* __restrict__ wr,
                                             const u16* __restrict__ wg, const u16* __restrict__ wu,
                                             const u16* __restrict__ wd, char* ws, u16* __restrict__ out) {
  __shared__ __attribute__((aligned(256))) char smem[LDS_TOTAL];
  const int tid = threadIdx.x;
  const int lane = tid & 63, wave = __builtin_amdgcn_readfirstlane(tid >> 6);
  const int b = blockIdx.x;
  unsigned* flags = (unsigned*)(smem + LDS_F);
  TSDECL;
  TSR(1, 8); TS(1, 0);

  const int rb = (b & 7) + 8 * (b >> 6), hg = (b >> 3) & 7;
  const int g = lane >> 4, i = lane & 15;
  const unsigned lbase = (unsigned)(uintptr_t)(__attribute__((address_space(3))) char*)smem;
  const __amdgpu_buffer_rsrc_t wrs = __builtin_amdgcn_make_buffer_rsrc(ws, 0, WS_END, 0x00020000);
  const int kp = ((b >> 5) << 3) | (b & 7), kc = (b >> 3) & 3;
  const unsigned long long did = dispatch_id_(), qp = (unsigned long long)__builtin_amdgcn_queue_ptr();
  const unsigned t0 = (unsigned)__builtin_amdgcn_s_memrealtime();
  const unsigned tg0 = (unsigned)did, tg1 = (unsigned)qp ^ 0x6b43a9b5u, tg2 = (unsigned)(qp >> 32) ^ ((unsigned)(did >> 32) << 16) ^ 0x1d8e4e27u;
  auto valid = [&](u32x4 f) { return f[0] == tg0 && f[1] == tg1 && f[2] == tg2 && f[3] - t0 + (1u << 21) < (1u << 22); };
  if (wave < 8) {
    const int rt = wave >> 2, ks4 = wave & 3;
    const int r = lane & 31, hh = lane >> 5;
    const int so = (ks4 < 2 ? LDS_P0 : LDS_P1) + (ks4 & 1) * 16384;
    if (wave >= 4) {
      __builtin_amdgcn_s_setprio(QPRIO);
      __builtin_amdgcn_s_barrier();
      asm volatile("" ::: "memory");
      {
        const int k8 = lane >> 3;
        const unsigned wo0 = (unsigned)(k8 * 1024 + (((lane >> 1) & 3) ^ (k8 & 2)) * 32 + (lane & 1) * 16);
        const char* wb = (const char*)wq + (size_t)(128 * ks4) * 1024 + hg * 128;
#pragma unroll
        for (int u = 0; u < 16; ++u) {
          const int uu = (u + 4 * (b >> 6)) & 15;
          glds16s(wo0, wb + uu * 8192, lbase + so + uu * 1024);
        }
      }
      asm volatile("s_waitcnt vmcnt(0)" ::: "memory");
      lds_add(flags + 31, lane);
      TS(1, 10);
    } else {
      __builtin_amdgcn_s_setprio(3);
      const int row0 = rb * 64 + hg * 8;
      const int k0 = ks4 * 128 + 32 * (i >> 3) + 8 * g;
      u32x4 xr[2];
#pragma unroll
      for (int u = 0; u < 2; ++u)
        glds16s((unsigned)(g * 1024 + (i ^ (u * 8 + g * 2)) * 16), (const char*)x + (size_t)(row0 + 4 * u) * 1024 + ks4 * 256, lbase + LDS_X + ks4 * 2048 + u * 1024);
      u32x4 rf[2];
      asm volatile("global_load_dwordx4 %0, %2, off\n\tglobal_load_dwordx4 %1, %2, off offset:128"
                   : "=&v"(rf[0]), "=&v"(rf[1]) : "v"(wr + k0) : "memory");
      const int ebo = (ks4 < 2 ? LDS_Q0 : LDS_Q1) + (ks4 & 1) * 8192;
      const unsigned eb = lbase + ebo;
#pragma unroll
      for (int s = 0; s < 4; ++s) {
        glds16s(lane * 16, (const char*)wk + ks4 * 4096 + s * 1024, eb + s * 1024);
        glds16s(lane * 16, (const char*)wv + ks4 * 4096 + s * 1024, eb + 4096 + s * 1024);
      }
      __builtin_amdgcn_s_barrier();
      asm volatile("s_waitcnt vmcnt(8)" : "+v"(rf[0]), "+v"(rf[1]) :: "memory");
#pragma unroll
      for (int s = 0; s < 2; ++s)
        xr[s] = *(const u32x4*)(smem + LDS_X + ks4 * 2048 + (i & 7) * 256 + ((8 * s + 4 * (i >> 3) + g) ^ (2 * (i & 7))) * 16);
      {
        float a = 0.f, q = 0.f, a2 = 0.f, q2 = 0.f;
#pragma unroll
        for (int s = 0; s < 2; ++s)
#pragma unroll
          for (int e = 0; e < 4; ++e) {
            const float lo = bf2f(xr[s][e] & 0xffffu), hi = bfhi2f(xr[s][e]);
            a += lo; a2 += hi;
            q = __builtin_fmaf(lo, lo, q);
            q2 = __builtin_fmaf(hi, hi, q2);
          }
        const auto p1 = __builtin_amdgcn_permlane32_swap(f2u(a + a2), f2u(q + q2), false, false);
        const unsigned t = f2u(u2f(p1[0]) + u2f(p1[1]));
        const auto p2 = __builtin_amdgcn_permlane16_swap(t, t, false, false);
        const float t2 = u2f(p2[0]) + u2f(p2[1]);
        *(float*)(smem + LDS_SP + (hh * 8 + (i & 7)) * 16 + ks4 * 4) = t2 + ror8(t2);
      }
      lds_add(flags + 0, lane);
      TS(1, 10);
      lds_spin(flags + 0, 4);
      bf16x8 xb[2];
      float mb, inv;
      {
        const f32x4 t0 = *(const f32x4*)(smem + LDS_SP + (hh * 8 + (i & 7)) * 16);
        const unsigned t = f2u((t0[0] + t0[1]) + (t0[2] + t0[3]));
        const auto p = __builtin_amdgcn_permlane32_swap(t, t, false, false);
        ln_stats(u2f(p[0]), u2f(p[1]), mb, inv);
        xb[0] = ln_frag(xr[0], mb, inv);
        xb[1] = ln_frag(xr[1], mb, inv);
      }
      asm volatile("s_waitcnt vmcnt(0)" : "+v"(xb[0]), "+v"(xb[1]) :: "memory");
      TS(1, 11);
      {
        const unsigned ta = eb + (8 * g + (i >> 2)) * 32 + (i & 3) * 8;
        f32x4 ak[2], av[2], ar = {0.f, 0.f, 0.f, 0.f};
#pragma unroll
        for (int c = 0; c < 2; ++c) { ak[c] = (f32x4){0.f, 0.f, 0.f, 0.f}; av[c] = (f32x4){0.f, 0.f, 0.f, 0.f}; }
#pragma unroll
        for (int s = 0; s < 2; ++s) {
#pragma unroll
          for (int c = 0; c < 2; ++c) {
            const unsigned a = ta + (2 * s + c) * 1024;
            ak[c] = mfma16(tr_frag(a, a + 128), xb[s], ak[c]);
            av[c] = mfma16(xb[s], tr_frag(a + 4096, a + 4096 + 128), av[c]);
          }
          ar = mfma16(xb[s], __builtin_bit_cast(bf16x8, rf[s]), ar);
        }
        __builtin_amdgcn_sched_barrier(0);
        *(f32x4*)(smem + ebo + lane * 16) = (i < 8) ? ak[0] : ak[1];
        *(f32x4*)(smem + ebo + 1024 + lane * 16) = (g < 2) ? av[0] : av[1];
        *(f32x4*)(smem + ebo + 2048 + lane * 16) = ar;
      }
      lds_add(flags + 24, lane);
      TS(1, 15);
      lds_spin(flags + 26, 4);
      if (ks4 == 3) {
        if (lane < 8)
          __builtin_amdgcn_raw_buffer_store_b64((u32x2){min(f2u(mb), 0xfffffffeu), min(f2u(inv), 0xfffffffeu)}, wrs, OFF_ST + (row0 + lane) * 8, 0, 16);
      } else {
        lds_spin(flags + 24, 4);
        TS(1, 14);
        const char* pp = smem + ks4 * 1024 + lane * 16;
        f32x4 v = (*(const f32x4*)(pp + LDS_Q0) + *(const f32x4*)(pp + LDS_Q0 + 8192)) + (*(const f32x4*)(pp + LDS_Q1) + *(const f32x4*)(pp + LDS_Q1 + 8192));
        if (ks4 == 0) {
#pragma unroll
          for (int e = 0; e < 4; ++e) v[e] += ror8(v[e]);
          const unsigned w0 = min(pack2(v[0], v[1]), 0xfffffffeu), w1 = min(pack2(v[2], v[3]), 0xfffffffeu);
          const auto s0 = __builtin_amdgcn_permlane32_swap(w0, w0, false, false), s1 = __builtin_amdgcn_permlane32_swap(w1, w1, false, false);
          if (g < 2 && i < 8)
            __builtin_amdgcn_raw_buffer_store_b128((u32x4){w0, w1, s0[1], s1[1]}, wrs, OFF_KF + kp * 1024 + (g * 32 + 8 * kc + i) * 16, 0, 16);
        } else if (ks4 == 1) {
#pragma unroll
          for (int e = 0; e < 4; ++e) {
            const auto sw = __builtin_amdgcn_permlane32_swap(f2u(v[e]), f2u(v[e]), false, false);
            v[e] = u2f(sw[0]) + u2f(sw[1]);
          }
          const unsigned w0 = min(pack2(v[0], v[1]), 0xfffffffeu), w1 = min(pack2(v[2], v[3]), 0xfffffffeu);
          const auto s0 = __builtin_amdgcn_permlane16_swap(w0, w0, false, false), s1 = __builtin_amdgcn_permlane16_swap(w1, w1, false, false);
          if (lane < 16)
            __builtin_amdgcn_raw_buffer_store_b128((u32x4){w0, w1, s0[1], s1[1]}, wrs, OFF_VF + kp * 1024 + kc * 256 + i * 16, 0, 16);
        } else {
          unsigned k0_ = 0, k1_ = 0;
#pragma unroll
          for (int e = 0; e < 4; ++e) {
            const auto sw = __builtin_amdgcn_permlane32_swap(f2u(v[e]), f2u(v[e]), false, false);
            const unsigned u = __float_as_uint(rbf(u2f(sw[0]) + ror8(u2f(sw[1]))));
            const unsigned ord = (u & 0x80000000u) ? ~u : (u | 0x80000000u);
            const unsigned key = (ord & 0xffff0000u) | (unsigned)(2047 - (row0 + 4 * (g & 1) + e));
            k1_ = max(k1_, min(k0_, key));
            k0_ = max(k0_, key);
          }
          const auto o0 = __builtin_amdgcn_permlane16_swap(k0_, k0_, false, false), o1 = __builtin_amdgcn_permlane16_swap(k1_, k1_, false, false);
          k1_ = max(min(k0_, o0[1]), max(k1_, o1[1]));
          k0_ = max(k0_, o0[1]);
          if (lane == 0) __builtin_amdgcn_raw_buffer_store_b64((u32x2){k0_, k1_}, wrs, OFF_R + b * 8, 0, 16);
        }
        lds_add(flags + 25, lane);
      }
    }
    __builtin_amdgcn_sched_barrier(0);
    TS(1, 1);
    {
      if (wave < 4) lds_spin(flags + 31, 4);
      bf16x8 wa[2][8];
      const unsigned wa_ = lbase + so + (8 * hh + (i >> 2)) * 128 + ((g & 1) ^ ((i >> 2) & 2)) * 32 + (i & 3) * 8;
#pragma unroll
      for (int s = 0; s < 8; ++s) {
        wa[0][s] = tr_frag(wa_ + s * 2048, wa_ + s * 2048 + 512);
        wa[1][s] = tr_frag((wa_ ^ 64) + s * 2048, (wa_ ^ 64) + s * 2048 + 512);
      }
      lds_add(flags + 27, lane);
      __builtin_amdgcn_sched_barrier(0);
      if (wave < 4) {
        asm volatile("s_waitcnt vmcnt(0)" ::: "memory");
        unsigned n = 0;
        if (lane == 0) n = __hip_atomic_fetch_add(flags + 26, 1u, __ATOMIC_RELAXED, __HIP_MEMORY_SCOPE_WORKGROUP);
        if (__builtin_amdgcn_readfirstlane(n) == 7u && lane == 0)
          __builtin_amdgcn_raw_buffer_store_b128((u32x4){tg0, tg1, tg2, t0}, wrs, OFF_FLG4 + ((kp & 3) * 64 + (kp >> 2) * 4 + kc) * 16, 0, 16);
      }
      lds_spin(flags + 30, 1);
      TS(1, 4);
      u32x4 xf[8];
      const unsigned xa_ = OFF_XN + (unsigned)((2 * rb + rt) * 32 + 8 * ks4) * 1024 + lane * 16;
#pragma unroll
      for (int s = 0; s < 8; ++s) xf[s] = __builtin_amdgcn_raw_buffer_load_b128(wrs, xa_ + s * 1024, 0, 16);
      __builtin_amdgcn_sched_barrier(0);
      lds_spin(flags + 27, 8);
      f32x16 acc[2];
#pragma unroll
      for (int e = 0; e < 16; ++e) { acc[0][e] = 0.f; acc[1][e] = 0.f; }
#pragma unroll
      for (int s = 0; s < 8; ++s) {
        acc[0] = mfma32(wa[0][s], __builtin_bit_cast(bf16x8, xf[s]), acc[0]);
        acc[1] = mfma32(wa[1][s], __builtin_bit_cast(bf16x8, xf[s]), acc[1]);
        __builtin_amdgcn_sched_barrier(0);
      }
      if (wave >= 4) TS(1, 11);
      char* pq = smem + so + rt * 8192 + lane * 16;
#pragma unroll
      for (int dt = 0; dt < 2; ++dt)
#pragma unroll
        for (int q4 = 0; q4 < 4; ++q4)
          *(f32x4*)(pq + (dt * 4 + q4) * 1024) = (f32x4){acc[dt][4 * q4], acc[dt][4 * q4 + 1], acc[dt][4 * q4 + 2], acc[dt][4 * q4 + 3]};
      lds_add(flags + 20 + rt, lane);
      if (wave >= 4) TS(1, 14);
      __builtin_amdgcn_sched_barrier(0);
    }
    lds_spin(flags + 20 + rt, 4);
    TS(1, 12);
    const int hl = wave & 3;
    const int head = hg * 4 + hl;
    bf16x8 qf;
    {
      f32x4 a0 = {0.f, 0.f, 0.f, 0.f}, a1 = {0.f, 0.f, 0.f, 0.f};
      const char* p = smem + rt * 8192 + (((hl >> 1) * 4 + 2 * (hl & 1)) * 64 + lane) * 16;
#pragma unroll
      for (int k = 0; k < 4; ++k) {
        const int o = (k < 2 ? LDS_P0 : LDS_P1) + (k & 1) * 16384;
        a0 += *(const f32x4*)(p + o);
        a1 += *(const f32x4*)(p + o + 1024);
      }
      const float SC = 0.25f * 1.44269504088896340736f;
      u32x4 pk = {pack2(rbf(a0[0]) * SC, rbf(a0[1]) * SC), pack2(rbf(a0[2]) * SC, rbf(a0[3]) * SC),
                  pack2(rbf(a1[0]) * SC, rbf(a1[1]) * SC), pack2(rbf(a1[2]) * SC, rbf(a1[3]) * SC)};
      qf = __builtin_bit_cast(bf16x8, pk);
      asm volatile("" : "+v"(qf) :: "memory");
    }
    TS(1, 13);
    if (lane == 0) __hip_atomic_fetch_add(flags + 22 + rt, 1u, __ATOMIC_RELAXED, __HIP_MEMORY_SCOPE_WORKGROUP);
    lds_wait(flags + 16, 4);
    TS(1, 2);

    const int yng = __builtin_amdgcn_readfirstlane(wave >> 2);
    f32x16 zero16;
#pragma unroll
    for (int e = 0; e < 16; ++e) zero16[e] = 0.f;
    const unsigned kbase = lbase + LDS_K + lane * 16;
    const unsigned vabase = lbase + LDS_V + ((r < 16) ? (hh * 16 + r) * 16 : (r == 16 ? 1024 : 1040));
    const unsigned vbbase = lbase + LDS_V + ((r < 16) ? (hh * 16 + r) * 16 + 512 : (r == 16 ? 1024 : 1040));
    __builtin_amdgcn_s_setprio(2);
    float cref;
    {
      const f32x16 sa = mfma32(LDSF(kbase), qf, zero16);
      const f32x16 sb = mfma32(LDSF(kbase + 1024), qf, zero16);
      float m0 = fmaxf(sa[0], sa[1]), m1 = fmaxf(sb[0], sb[1]);
#pragma unroll
      for (int e = 2; e < 16; e += 2) {
        m0 = fmaxf(fmaxf(m0, sa[e]), sa[e + 1]);
        m1 = fmaxf(fmaxf(m1, sb[e]), sb[e + 1]);
      }
      m0 = fmaxf(m0, m1);
      const auto sw = __builtin_amdgcn_permlane32_swap(f2u(m0), f2u(m0), false, false);
      cref = fmaxf(u2f(sw[0]), u2f(sw[1])) + SM_DELTA;
    }
    TS(1, 3);
    const int r64 = tid >> 3, oct = tid & 7;
    const int row = rb * 64 + r64;
    const size_t off = (size_t)row * D_ + hg * 64 + oct * 8;

    f32x16 o = zero16;
    float ck[9];
#pragma unroll
    for (int e = 0; e < 9; ++e) ck[e] = 0.f;
    const int thr1 = P2A1 + (P2B1 - P2A1) * yng, thr2 = 7 + 92 * yng;
    const unsigned prog = lbase + LDS_F + 32 + wave * 4;
    int tb = 0;
    bool redo = false;
#pragma unroll 1
    for (;;) {
      f32x16 negm;
#pragma unroll
      for (int e = 0; e < 16; ++e) negm[e] = -cref;
      unsigned ka_ = kbase + tb * 8192, va_ = vabase + tb * (8 * VT_STRIDE), vb_ = vbbase + tb * (8 * VT_STRIDE);
      f32x16 s = mfma32(LDSF(ka_), qf, negm);
      bf16x8 kn = LDSF(ka_ + 1024);
      bf16x8 v0 = LDSF(va_);
      bf16x8 v1 = LDSF(vb_);
      bf16x8 v1p = v1;
      f32x8 pbp = {0.f, 0.f, 0.f, 0.f, 0.f, 0.f, 0.f, 0.f};
      bool bad = false;
#pragma unroll 1
      for (; tb < 8; ++tb) {
        asm volatile("" : "+v"(ka_), "+v"(va_), "+v"(vb_));
        PRIO_IF(tb, thr1, 1);
        const unsigned sta = lbase + LDS_F + 68 + 4 * min(2, (tb + 1) >> 1);
        unsigned kst = 0;
#pragma unroll
        for (int u = 0; u < 8; ++u) {
          if (u == P2A2U) PRIO_IF(tb, thr2, 0);
          if (u == 4) kst = *(const volatile __attribute__((address_space(3))) unsigned*)(uintptr_t)sta;
          if (u == 6) {
            unsigned sv;
            asm volatile("v_readfirstlane_b32 %1, %0\n\ts_cmp_gt_u32 %1, 3\n\ts_cbranch_scc1 2f\n"
                         "1:\n\tds_read_b32 %0, %2\n\ts_waitcnt lgkmcnt(0)\n\tv_readfirstlane_b32 %1, %0\n\t"
                         "s_cmp_gt_u32 %1, 3\n\ts_cbranch_scc1 2f\n\ts_sleep 1\n\ts_branch 1b\n2:"
                         : "+v"(kst), "=&s"(sv) : "v"(sta) : "scc", "memory");
          }
          const f32x16 sn = mfma32(kn, qf, negm);
          const bf16x8 k2 = LDSF(ka_ + (u + 2) * 1024);
          const bf16x8 v0n = LDSF(va_ + (u + 1) * VT_STRIDE);
          const bf16x8 v1n = LDSF(vb_ + (u + 1) * VT_STRIDE);
          f32x8 pa, pb;
#pragma unroll
          for (int e = 0; e < 5; ++e) pa[e] = __builtin_amdgcn_exp2f(s[e]);
          o = smfma32(v1p, pbp, o);
#pragma unroll
          for (int e = 5; e < 8; ++e) pa[e] = __builtin_amdgcn_exp2f(s[e]);
#pragma unroll
          for (int e = 8; e < 10; ++e) pb[e - 8] = __builtin_amdgcn_exp2f(s[e]);
          o = smfma32(v0, pa, o);
#pragma unroll
          for (int e = 10; e < 16; ++e) pb[e - 8] = __builtin_amdgcn_exp2f(s[e]);
          pbp = pb;
          v1p = v1;
          s = sn; kn = k2; v0 = v0n; v1 = v1n;
          __builtin_amdgcn_sched_group_barrier(0x008, 1, 0);
          if (u == 4) __builtin_amdgcn_sched_group_barrier(0x100, 4, 0); else __builtin_amdgcn_sched_group_barrier(0x100, 3, 0);
          __builtin_amdgcn_sched_group_barrier(0x402, 5, 0);
          __builtin_amdgcn_sched_group_barrier(0x008, 1, 0);
          __builtin_amdgcn_sched_group_barrier(0x402, 5, 0);
          __builtin_amdgcn_sched_group_barrier(0x008, 1, 0);
          __builtin_amdgcn_sched_group_barrier(0x402, 6, 0);
          __builtin_amdgcn_sched_barrier(0);
        }
        ka_ += 8 * 1024; va_ += 8 * VT_STRIDE; vb_ += 8 * VT_STRIDE;
        if ((tb & 3) == 3) {
          o = smfma32(v1p, pbp, o);
#pragma unroll
          for (int e = 0; e < 8; ++e) pbp[e] = 0.f;
          unsigned mx = f2u(o[8]) & 0x7fffffffu;
#pragma unroll
          for (int e = 0; e < 8; e += 2) mx = max(max(mx, f2u(o[e]) & 0x7fffffffu), f2u(o[e + 1]) & 0x7fffffffu);
          if (__builtin_amdgcn_ballot_w64(mx >= SM_LIMIT) != 0ull && !redo) { bad = true; break; }
          redo = false;
#pragma unroll
          for (int e = 0; e < 9; ++e) ck[e] = o[e];
          if (tb == 3) asm volatile("ds_write_b32 %0, %0" :: "v"(prog) : "memory");
        }
      }
      if (!bad) break;
      tb -= 3;
      float mm = -__builtin_inff();
#pragma unroll 1
      for (int t = 0; t < 32; ++t) {
        const f32x16 sc = mfma32(LDSF(kbase + tb * 8192 + t * 1024), qf, zero16);
#pragma unroll
        for (int e = 0; e < 16; e += 2) mm = fmaxf(fmaxf(mm, sc[e]), sc[e + 1]);
      }
      const auto sw = __builtin_amdgcn_permlane32_swap(f2u(mm), f2u(mm), false, false);
      const float cnew = fmaxf(cref, fmaxf(u2f(sw[0]), u2f(sw[1])) + SM_DELTA);
      const float fh = __builtin_amdgcn_exp2f(0.5f * (cref - cnew));
      o = zero16;
#pragma unroll
      for (int e = 0; e < 9; ++e) { ck[e] = (ck[e] * fh) * fh; o[e] = ck[e]; }
      cref = cnew;
      redo = true;
    }
    TS(1, 5);
    const float il = __builtin_amdgcn_rcpf(u2f(__builtin_amdgcn_permlane32_swap(f2u(o[8]), f2u(o[8]), false, false)[0]));
    u32x4 w0 = {pack2(o[0] * il, o[1] * il), pack2(o[2] * il, o[3] * il), pack2(o[4] * il, o[5] * il),
                pack2(o[6] * il, o[7] * il)};
    const __amdgpu_buffer_rsrc_t ors = __builtin_amdgcn_make_buffer_rsrc(ws + OFF_OF + (size_t)rb * 65536, 0, 65536, 0x00020000);
#pragma unroll
    for (int e = 0; e < 4; ++e) w0[e] = min(w0[e], 0xfffffffeu);
    __builtin_amdgcn_raw_buffer_store_b128(w0, ors, ((rt * 32 + head) * 64 + lane) * 16, 0, 16);
    __builtin_amdgcn_sched_barrier(0);
    while (__hip_atomic_load(flags + 3, __ATOMIC_ACQUIRE, __HIP_MEMORY_SCOPE_WORKGROUP) < 4) __builtin_amdgcn_s_sleep(1);
    asm volatile("" ::: "memory");
    const int rto = wave & 1, ko = wave >> 1;
    bf16x8 wf[2][8];
    {
      const unsigned wa_ = lbase + (ko < 2 ? LDS_K : LDS_V) + (ko & 1) * 16384 + (4 * hh + (i >> 2)) * 128 + ((g & 1) ^ ((i >> 2) & 2)) * 32 + (i & 3) * 8;
#pragma unroll
      for (int s8 = 0; s8 < 8; ++s8) {
        wf[0][s8] = tr_frag(wa_ + s8 * 2048, wa_ + s8 * 2048 + 1024);
        wf[1][s8] = tr_frag((wa_ ^ 64) + s8 * 2048, (wa_ ^ 64) + s8 * 2048 + 1024);
      }
    }
    __builtin_amdgcn_sched_barrier(0);
    const int fo = ((rto * 32 + 8 * ko) * 64 + lane) * 16;
    u32x4 fr[8];
    __builtin_amdgcn_sched_barrier(0);
    __builtin_amdgcn_s_barrier();
    asm volatile("" ::: "memory");
    TS(1, 6);
#pragma unroll
    for (int s8 = 0; s8 < 8; ++s8) fr[s8] = __builtin_amdgcn_raw_buffer_load_b128(ors, fo + s8 * 1024, 0, 16);
    __builtin_amdgcn_sched_barrier(0);
    const u32x4 xv = *(const u32x4*)(smem + LDS_X + tid * 16);
    u32x4 xnv;
    {
      const f32x2 st = *(const f32x2*)(smem + LDS_SF + r64 * 8);
      xnv = __builtin_bit_cast(u32x4, ln_frag(xv, st[0], st[1]));
    }
    {
      const u32x4 sl = *(const u32x4*)(smem + LDS_F + 16);
      if (row == (int)sl[0] || row == (int)sl[1]) {
        const int tk = (row == (int)sl[0]) ? 0 : 1;
        f32x4 y[2] = {{0.f, 0.f, 0.f, 0.f}, {0.f, 0.f, 0.f, 0.f}};
#pragma unroll
        for (int p = 0; p < 4; ++p) {
          const char* yp = smem + LDS_Y + (tk * 4 + p) * 256 + oct * 32;
          y[0] += *(const f32x4*)yp;
          y[1] += *(const f32x4*)(yp + 16);
        }
        const float rs = __uint_as_float(tk ? sl[3] : sl[2]);
#pragma unroll
        for (int e = 0; e < 4; ++e)
          xnv[e] = pack2(bf2f(xnv[e] & 0xffffu) + rbf(rbf(y[e >> 1][(2 * e) & 3]) * rs),
                         bfhi2f(xnv[e]) + rbf(rbf(y[e >> 1][(2 * e + 1) & 3]) * rs));
      }
    }
    __builtin_amdgcn_sched_barrier(0);
    {
      f32x16 acc[2];
#pragma unroll
      for (int e = 0; e < 16; ++e) { acc[0][e] = 0.f; acc[1][e] = 0.f; }
      auto retry = [&](int h, unsigned spins) -> bool {
        unsigned mx = 0;
#pragma unroll
        for (int s8 = 4 * h; s8 < 4 * h + 4; ++s8) mx = max(max(mx, max(fr[s8][0], fr[s8][1])), max(fr[s8][2], fr[s8][3]));
        if (!__any(mx == ~0u) || spins >= (1u << 16)) return false;
        asm volatile("" ::: "memory");
#pragma unroll
        for (int s8 = 4 * h; s8 < 4 * h + 4; ++s8) fr[s8] = __builtin_amdgcn_raw_buffer_load_b128(ors, fo + s8 * 1024, 0, 16);
        return true;
      };
      if (retry(0, 0)) {
#pragma unroll 1
        for (unsigned spins = 0;; ++spins) {
          retry(1, 0);
          if (!retry(0, spins)) break;
        }
      }
#pragma unroll
      for (int s8 = 0; s8 < 4; ++s8) {
        acc[0] = mfma32(wf[0][s8], __builtin_bit_cast(bf16x8, fr[s8]), acc[0]);
        acc[1] = mfma32(wf[1][s8], __builtin_bit_cast(bf16x8, fr[s8]), acc[1]);
      }
      __builtin_amdgcn_sched_barrier(0);
#pragma unroll 1
      for (unsigned spins = 0;; ++spins)
        if (!retry(1, spins)) break;
#pragma unroll
      for (int s8 = 4; s8 < 8; ++s8) {
        acc[0] = mfma32(wf[0][s8], __builtin_bit_cast(bf16x8, fr[s8]), acc[0]);
        acc[1] = mfma32(wf[1][s8], __builtin_bit_cast(bf16x8, fr[s8]), acc[1]);
      }
      char* pw = smem + (rto ? LDS_P1 : LDS_P0) + ko * 8192;
#pragma unroll
      for (int ct = 0; ct < 2; ++ct)
#pragma unroll
        for (int q4 = 0; q4 < 4; ++q4) {
          f32x4 p = {acc[ct][4 * q4], acc[ct][4 * q4 + 1], acc[ct][4 * q4 + 2], acc[ct][4 * q4 + 3]};
          *(f32x4*)(pw + (ct * 4 + q4) * 1024 + ((lane * 16) ^ ((ct * 4 + q4) * 32))) = p;
        }
    }
    float xs[8], xb[8];
#pragma unroll
    for (int e = 0; e < 4; ++e) {
      xs[2 * e] = bf2f(xv[e] & 0xffffu); xs[2 * e + 1] = bfhi2f(xv[e]);
      xb[2 * e] = bf2f(xnv[e] & 0xffffu); xb[2 * e + 1] = bfhi2f(xnv[e]);
    }
#pragma unroll
    for (int e = 0; e < 8; ++e) asm volatile("" : "+v"(xs[e]), "+v"(xb[e]));
    __syncthreads();
    f32x4 lo, hi;
    {
      const char* p = smem + ((r64 >> 5) ? LDS_P1 : LDS_P0) + oct * 1024 + (((r64 & 31) * 16) ^ (oct * 32));
      lo = (*(const f32x4*)p + *(const f32x4*)(p + 8192)) + (*(const f32x4*)(p + 16384) + *(const f32x4*)(p + 24576));
      hi = (*(const f32x4*)(p + 512) + *(const f32x4*)(p + 8704)) + (*(const f32x4*)(p + 16896) + *(const f32x4*)(p + 25088));
    }
    float res[8];
#pragma unroll
    for (int e = 0; e < 4; ++e) {
      res[e] = rbf(xs[e] + rbf(lo[e])) + xb[e];
      res[4 + e] = rbf(xs[4 + e] + rbf(hi[e])) + xb[4 + e];
    }
    u32x4 ov = {pack2(res[0], res[1]), pack2(res[2], res[3]), pack2(res[4], res[5]), pack2(res[6], res[7])};
    st16_wt(out, (unsigned)off * 2u, ov);
  } else {
    __builtin_amdgcn_s_setprio(3);
    const int hj = wave - 8;
    const unsigned ldst = lbase + hj * 1024;
    if (hj == 0 && lane < 32) flags[lane] = 0u;
    const int sl = b;
    const bool mod = b < NHELP;
    const int f0 = sl * 16;
    float* ypart = (float*)(ws + OFF_Y);
    unsigned* cnt = (unsigned*)(ws + OFF_CNT);
    const u32x4 e4 = {~0u, ~0u, ~0u, ~0u};
    {
      if (hj == 0 && lane < 8) glds16s(OFF_XN + (unsigned)(((2 * rb + (hg >> 2)) * 32) * 64 + 8 * (hg & 3) + lane) * 16, ws, lbase + LDS_H);
      if (hj == 1) {
        const unsigned a = lane < 16 ? OFF_KF + kp * 1024 + ((lane >> 3) * 32 + 8 * kc + (lane & 7)) * 16
                                     : (lane < 32 ? OFF_VF + kp * 1024 + kc * 256 + i * 16 : OFF_ST + (rb * 64 + hg * 8) * 8 + (lane & 3) * 16);
        if (lane < 36) __builtin_amdgcn_raw_buffer_store_b128(e4, wrs, a, 0, 16);
      } else if (hj == 2) {
        if (lane == 0) __builtin_amdgcn_raw_buffer_store_b64((u32x2){~0u, ~0u}, wrs, OFF_R + b * 8, 0, 16);
      } else if (hj == 3) {
        if (lane == 0 && b == 0) __builtin_amdgcn_raw_buffer_store_b32(0u, wrs, OFF_CNT, 0, 16);
      }
    }
    asm volatile("s_waitcnt lgkmcnt(0)" ::: "memory");
    __builtin_amdgcn_s_barrier();
    lds_spin(flags + 0, 4);
    {
      const f32x4 p0 = *(const f32x4*)(smem + LDS_SP + ((lane >> 5) * 8 + (i & 7)) * 16);
      u32x4 xr[2];
#pragma unroll
      for (int s = 0; s < 2; ++s)
        xr[s] = *(const u32x4*)(smem + LDS_X + hj * 2048 + (i & 7) * 256 + ((8 * s + 4 * (i >> 3) + g) ^ (2 * (i & 7))) * 16);
      const unsigned t = f2u((p0[0] + p0[1]) + (p0[2] + p0[3]));
      const auto p = __builtin_amdgcn_permlane32_swap(t, t, false, false);
      float mb, inv;
      ln_stats(u2f(p[0]), u2f(p[1]), mb, inv);
      const unsigned a = OFF_XN + (unsigned)(((2 * rb + (hg >> 2)) * 32 + 8 * hj + 2 * (i >> 3) + (g >> 1)) * 64 + (g & 1) * 32 + 8 * (hg & 3) + (i & 7)) * 16;
#pragma unroll
      for (int s = 0; s < 2; ++s)
        __builtin_amdgcn_raw_buffer_store_b128(__builtin_bit_cast(u32x4, ln_frag(xr[s], mb, inv)), wrs, a + s * 4096, 0, 16);
    }
    TS(1, 14);
    asm volatile("s_waitcnt vmcnt(0)" ::: "memory");
    {
      unsigned n = 0;
      if (lane == 0) n = __hip_atomic_fetch_add(flags + 26, 1u, __ATOMIC_RELAXED, __HIP_MEMORY_SCOPE_WORKGROUP);
      if (__builtin_amdgcn_readfirstlane(n) == 3u && lane < 2)
        __builtin_amdgcn_raw_buffer_store_b128((u32x4){tg0, tg1, tg2, t0}, wrs,
                                               lane ? OFF_FLAG + ((kp & 3) * 64 + (kp >> 2) * 4 + kc) * 16 : OFF_FLG2 + (rb * 8 + hg) * FL2, 0, 16);
    }
    TS(1, 1);
    if (hj == 3) {
      const unsigned fa = OFF_FLG2 + (rb * 8 + (lane & 7)) * FL2;
      unsigned ta, tb_, tc, left = 1u << 14;
#define PLD(r) "global_load_dwordx4 v[" #r ":" #r "+3], %4, %5 sc1\n\t"
#define PCK(r) "s_waitcnt vmcnt(3)\n\tv_xor_b32 %0, %6, v[" #r "]\n\tv_xor_b32 %1, %7, v[" #r "+1]\n\tv_xor_b32 %2, %8, v[" #r "+2]\n\t" \
               "v_or3_b32 %0, %0, %1, %2\n\tv_subrev_u32 %1, %9, v[" #r "+3]\n\tv_lshrrev_b32 %1, 22, %1\n\tv_or_b32 %0, %0, %1\n\t" \
               "v_cmp_ne_u32 vcc, 0, %0\n\ts_cbranch_vccz 2f\n\t" PLD(r)
#define STR_(x) #x
#define STR(x) STR_(x)
#define PEND "s_waitcnt vmcnt(0)"
#define PSL "s_sleep " STR(SSLEEP) "\n\t"
      asm volatile(PLD(100) PSL PLD(104) PSL PLD(108) PSL PLD(112)
                   "1:\n\t" PCK(100) PCK(104) PCK(108) PCK(112)
                   "s_sub_u32 %3, %3, 1\n\ts_cbranch_scc0 1b\n"
                   "2:\n\tds_write_b32 %10, %10\n\t" PEND
                   : "=&v"(ta), "=&v"(tb_), "=&v"(tc), "+s"(left)
                   : "v"(fa), "s"(ws), "s"(tg0), "s"(tg1), "s"(tg2), "s"(t0 - (1u << 21)), "v"(lbase + LDS_F + 120)
                   : "vcc", "scc", "memory", "v100", "v101", "v102", "v103", "v104", "v105", "v106", "v107", "v108", "v109",
                     "v110", "v111", "v112", "v113", "v114", "v115");
      TS(1, 15);
    }
    lds_wait(flags + TWAIT, 3);
    unsigned long long seen = 0;
    auto tags = [&](unsigned long long need, unsigned base) {
#pragma unroll 1
      for (unsigned spins = 0; (~seen & need) != 0ull; ++spins) {
        if (spins) __builtin_amdgcn_s_sleep(FSLEEP);
        const unsigned long long ask = ~seen & need;
        bool ok = false;
        if ((ask >> lane) & 1ull) ok = valid(__builtin_amdgcn_raw_buffer_load_b128(wrs, base + hj * 1024 + lane * 16, 0, 16));
        seen |= __builtin_amdgcn_ballot_w64(ok);
        if (spins >= (1u << 16)) seen = ~0ull;
      }
      asm volatile("" ::: "memory");
    };
    tags(0xffull, OFF_FLAG);
    TS(1, 2);
    {
      const unsigned ga = OFF_KF + hj * 1024 + lane * 16;
      char* kd = smem + LDS_K + hj * 1024 + lane * 16;
      char* vd = smem + LDS_V + hj * VT_STRIDE + lane * 16;
      u32x4 kv[4][2];
      auto ld = [&](int c0, int c1) {
#pragma unroll
        for (int c = c0; c < c1; ++c) {
          kv[c][0] = __builtin_amdgcn_raw_buffer_load_b128(wrs, ga + c * 4096, 0, 16);
          kv[c][1] = __builtin_amdgcn_raw_buffer_load_b128(wrs, ga + 65536 + c * 4096, 0, 16);
        }
      };
      auto settle = [&](int c0, int c1) {
#pragma unroll 1
        for (unsigned spins = 0; spins < (1u << 16); ++spins) {
          unsigned mx = 0;
#pragma unroll
          for (int c = c0; c < c1; ++c) mx = max(mx, max(max4(kv[c][0]), max4(kv[c][1])));
          if (!__any(mx == ~0u)) break;
          asm volatile("" ::: "memory");
          __builtin_amdgcn_s_sleep(KSLEEP);
          if (mx == ~0u) ld(c0, c1);
        }
      };
      auto put = [&](int c0, int c1, int fl) {
#pragma unroll
        for (int c = c0; c < c1; ++c) {
          *(u32x4*)(kd + c * 4096) = kv[c][0];
          *(u32x4*)(vd + c * 4 * VT_STRIDE) = kv[c][1];
        }
        const unsigned cv = (lane & 1) ? 0u : 0x3f803f80u;
        if (lane < 2 * (c1 - c0))
          *(u32x4*)(smem + LDS_V + (hj + 4 * (c0 + (lane >> 1))) * VT_STRIDE + 1024 + (lane & 1) * 16) = (u32x4){cv, cv, cv, cv};
        lds_add(flags + fl, lane);
      };
      ld(0, 2);
      settle(0, 2);
      put(0, 2, 16);
      TS(1, 3);
      tags(0xffffull, OFF_FLAG);
      ld(2, 4);
      const u32x4 t4 = __builtin_amdgcn_raw_buffer_load_b128(wrs, OFF_FLG4 + hj * 1024 + lane * 16, 0, 16);
      TS(1, 10);
      settle(2, 4);
      put(2, 4, 17);
      TS(1, 11);
      seen = __builtin_amdgcn_ballot_w64(valid(t4));
      tags(~0ull, OFF_FLG4);
      asm volatile("s_waitcnt vmcnt(0)" ::: "memory");
      lds_add(flags + 28, lane);
      auto dma = [&](int c0, int c1) {
#pragma unroll
        for (int c = c0; c < c1; ++c) {
          glds16c(ga, ws + c * 4096, ldst + LDS_K + c * 4096);
          glds16c(ga, ws + 65536 + c * 4096, lbase + LDS_V + (hj + 4 * c) * VT_STRIDE);
        }
        const unsigned cv = (lane & 1) ? 0u : 0x3f803f80u;
        if (lane < 2 * (c1 - c0))
          *(u32x4*)(smem + LDS_V + (hj + 4 * (c0 + (lane >> 1))) * VT_STRIDE + 1024 + (lane & 1) * 16) = (u32x4){cv, cv, cv, cv};
      };
      lds_wait(flags + 25, 3);
      dma(4, 8);
      lds_wait(flags + 22, 4);
      lds_wait(flags + 23, 4);
      dma(8, 16);
      asm volatile("s_waitcnt vmcnt(16)" ::: "memory");
      lds_add(flags + 18, lane);
#pragma unroll
      for (int c = 0; c < 2; ++c)
        __builtin_amdgcn_raw_buffer_store_b128(e4, wrs, OFF_OF + rb * 65536 + ((c * 32 + hg * 4 + hj) * 64 + lane) * 16, 0, 16);
      asm volatile("s_waitcnt vmcnt(0)" ::: "memory");
      TS(1, 12);
      if (lane == 0 && __hip_atomic_fetch_add(flags + 19, 1u, __ATOMIC_RELAXED, __HIP_MEMORY_SCOPE_WORKGROUP) == 3u)
        __builtin_amdgcn_raw_buffer_store_b128((u32x4){tg0, tg1, tg2, t0}, wrs, OFF_FLG3 + (rb * 8 + hg) * 16, 0, 16);
      TS(1, 13);
    }
    lds_wait(flags + 28, 4);
    unsigned k0, k1;
    {
      u32x4 ka, kb;
#pragma unroll 1
      for (unsigned spins = 0; spins < (1u << 16); ++spins) {
        ka = __builtin_amdgcn_raw_buffer_load_b128(wrs, OFF_R + lane * 32, 0, 16);
        kb = __builtin_amdgcn_raw_buffer_load_b128(wrs, OFF_R + lane * 32 + 16, 0, 16);
        if (!__any(max(max4(ka), max4(kb)) == ~0u)) break;
        asm volatile("" ::: "memory");
      }
      k0 = max(ka[0], ka[2]); k1 = max(min(ka[0], ka[2]), max(ka[1], ka[3]));
      const unsigned o0 = max(kb[0], kb[2]), o1 = max(min(kb[0], kb[2]), max(kb[1], kb[3]));
      k1 = max(min(k0, o0), max(k1, o1));
      k0 = max(k0, o0);
    }
    {
      const unsigned m0 = wave_max(k0);
      k1 = wave_max(k0 == m0 ? k1 : k0);
      k0 = m0;
    }
    const int i0 = 2047 - (int)(k0 & 0x7ffu), i1 = 2047 - (int)(k1 & 0x7ffu);
    if (hj == 0 && lane == 0) *(u32x2*)(smem + LDS_F + 16) = (u32x2){(unsigned)i0, (unsigned)i1};
    bf16x8 xa[4];
    if (mod) {
      const int srow = (i & 1) ? i1 : i0;
      const unsigned xp = OFF_XN + (unsigned)(((srow >> 5) * 32 + 16 * (hj >> 1) + 8 * ((i >> 1) & 1) + (g >> 1)) * 64 + (g & 1) * 32 + (srow & 31)) * 16;
#pragma unroll
      for (int s = 0; s < 4; ++s) xa[s] = __builtin_bit_cast(bf16x8, __builtin_amdgcn_raw_buffer_load_b128(wrs, xp + s * 2048, 0, 16));
    }
    __builtin_amdgcn_sched_barrier(0);
    if (mod) {
      unsigned wa[4][8];
      const char* ub = (const char*)(((hj & 1) ? wu : wg) + (size_t)(256 * (hj >> 1)) * F_ + f0);
      const unsigned lo1 = (unsigned)((128 * (i >> 3) + 8 * g) * F_ + 2 * (i & 7)) * 2u;
#pragma unroll
      for (int s = 0; s < 4; ++s)
#pragma unroll
        for (int j = 0; j < 8; j += 2) {
          gcp rp = (gcp)(ub + (size_t)((32 * s + j) * F_ * 2));
          asm volatile("" : "+s"(rp));
          wa[s][j] = *(const __attribute__((address_space(1))) unsigned*)(rp + lo1);
          wa[s][j + 1] = *(const __attribute__((address_space(1))) unsigned*)(rp + F_ * 2 + lo1);
        }
      unsigned wdv[4][4];
      const char* db = (const char*)(wd + (size_t)f0 * D_ + hj * 128);
      const unsigned lo2 = (unsigned)(4 * g * D_ + 2 * i) * 2u;
#pragma unroll
      for (int q = 0; q < 4; ++q)
#pragma unroll
        for (int j = 0; j < 4; ++j) wdv[q][j] = *(const unsigned*)(db + (j * D_ + 32 * q) * 2 + lo2);
      f32x4 a0 = {0.f, 0.f, 0.f, 0.f}, a1 = {0.f, 0.f, 0.f, 0.f};
#pragma unroll
      for (int s = 0; s < 4; ++s) {
        a0 = mfma16(xa[s], frag_lo(wa[s]), a0);
        a1 = mfma16(xa[s], frag_hi(wa[s]), a1);
      }
      if (lane < 16)
        *(f32x4*)(smem + LDS_H + (hj * 16 + i) * 16) =
            (i < 8) ? (f32x4){a0[0], a0[1], a1[0], a1[1]} : (f32x4){a0[2], a0[3], a1[2], a1[3]};
      TS(1, 4);
      if (lane == 0) __hip_atomic_fetch_add(flags + 1, 1u, __ATOMIC_RELEASE, __HIP_MEMORY_SCOPE_WORKGROUP);
      while (__hip_atomic_load(flags + 1, __ATOMIC_ACQUIRE, __HIP_MEMORY_SCOPE_WORKGROUP) < 4) __builtin_amdgcn_s_sleep(2);
      asm volatile("" ::: "memory");
      __builtin_amdgcn_sched_barrier(0);
      const float* hp = (const float*)(smem + LDS_H) + ((i >> 3) * 16 + (i & 7)) * 4 + g;
      const float gt = rbf((hp[0] + hp[32]) + (hp[128] + hp[160]));
      const float up = ror8(gt);
      const float hv = rbf(gt * __builtin_amdgcn_rcpf(1.0f + __expf(-gt))) * up;
      if (i < 8) *(u16*)(smem + LDS_HH + hj * 64 + (g & 1) * 32 + (2 * i + (g >> 1)) * 2) = __builtin_bit_cast(u16, (__bf16)hv);
      const s16x4 ha = *(const s16x4*)(smem + LDS_HH + hj * 64 + (i & 1) * 32 + g * 8);
#pragma unroll
      for (int q = 0; q < 4; ++q) {
        const u32x2 be = {perm_lo(wdv[q][1], wdv[q][0]), perm_lo(wdv[q][3], wdv[q][2])};
        const u32x2 bo = {perm_hi(wdv[q][1], wdv[q][0]), perm_hi(wdv[q][3], wdv[q][2])};
        const f32x4 z = {0.f, 0.f, 0.f, 0.f};
        const f32x4 ye = __builtin_amdgcn_mfma_f32_16x16x16bf16_1k(ha, __builtin_bit_cast(s16x4, be), z, 0, 0, 0);
        const f32x4 yo = __builtin_amdgcn_mfma_f32_16x16x16bf16_1k(ha, __builtin_bit_cast(s16x4, bo), z, 0, 0, 0);
        if (lane < 16) {
          gu64* yp = (gu64*)(ypart + (size_t)sl * 1024 + hj * 128 + 32 * q + 2 * i);
          __hip_atomic_store(yp, __builtin_bit_cast(unsigned long long, (f32x2){ye[0], yo[0]}), RLX_AGENT);
          __hip_atomic_store(yp + 256, __builtin_bit_cast(unsigned long long, (f32x2){ye[1], yo[1]}), RLX_AGENT);
        }
      }
      asm volatile("s_waitcnt vmcnt(0)" ::: "memory");
      if (lane == 0 && __hip_atomic_fetch_add(flags + 2, 1u, __ATOMIC_ACQ_REL, __HIP_MEMORY_SCOPE_WORKGROUP) == 3u)
        __hip_atomic_fetch_add((gu32*)(cnt), 1u, RLX_AGENT);
    }
    __builtin_amdgcn_sched_barrier(0);
    if (hj == 3) {
      u32x2 st;
#pragma unroll 1
      for (unsigned spins = 0; spins < (1u << 16); ++spins) {
        st = __builtin_amdgcn_raw_buffer_load_b64(wrs, OFF_ST + (rb * 64 + lane) * 8, 0, 16);
        if (!__any(max(st[0], st[1]) == ~0u)) break;
        asm volatile("" ::: "memory");
      }
      *(u32x2*)(smem + LDS_SF + lane * 8) = st;
    }
#pragma unroll
    for (int c = 0; c < 2; ++c)
      glds16((const char*)(x + (size_t)(rb * 64 + (hj * 2 + c) * 8 + (lane >> 3)) * D_ + hg * 64 + (lane & 7) * 8),
             lbase + LDS_X + (hj * 2 + c) * 1024);
    {
#pragma unroll
      for (int c = 0; c < 2; ++c) {
        const unsigned t = __builtin_amdgcn_raw_buffer_load_b32(wrs, OFF_OF + rb * 65536 + ((c * 32 + hg * 4 + hj) * 64 + lane) * 16, 0, 16);
        asm volatile("" :: "v"(t));
      }
    }
    TS(1, 5);
    while (!__all(__hip_atomic_load(flags + 8 + (lane & 7), __ATOMIC_ACQUIRE, __HIP_MEMORY_SCOPE_WORKGROUP) != 0u))
      __builtin_amdgcn_s_sleep(32);
    asm volatile("" ::: "memory");
    {
      const int k8 = lane >> 3;
      const unsigned wo0 = (unsigned)(k8 * 1024 + (((lane >> 1) & 3) ^ (k8 & 2)) * 32 + (lane & 1) * 16);
      const char* wb = (const char*)wo + hj * 8192 + hg * 128;
#pragma unroll
      for (int c = 0; c < 8; ++c) {
        glds16s(wo0, wb + c * 32768, ldst + LDS_K + c * 4096);
        glds16s(wo0, wb + 262144 + c * 32768, ldst + LDS_V + c * 4096);
      }
    }
    if (hj == 3) {
#pragma unroll 1
      for (unsigned spins = 0; spins < (1u << 16); ++spins) {
        const u32x4 f = __builtin_amdgcn_raw_buffer_load_b128(wrs, OFF_FLG3 + (rb * 8 + (lane & 7)) * 16, 0, 16);
        if (__all(valid(f))) break;
        __builtin_amdgcn_s_sleep(4);
      }
    }
    asm volatile("s_waitcnt vmcnt(0)" ::: "memory");
    if (lane == 0) __hip_atomic_fetch_add(flags + 3, 1u, __ATOMIC_RELEASE, __HIP_MEMORY_SCOPE_WORKGROUP);
    __builtin_amdgcn_sched_barrier(0);
    const bool own0 = (unsigned)(i0 - rb * 64) < 64u, own1 = (unsigned)(i1 - rb * 64) < 64u;
    if (own0 || own1) {
      if (hj == 1 && lane < 2) {
        const unsigned kk = lane ? k1 : k0;
        const float v = __uint_as_float((kk & 0x80000000u) ? (kk & 0x7fff0000u) : (~kk & 0xffff0000u));
        *(float*)(smem + LDS_F + 24 + lane * 4) = rbf(1.0f / (1.0f + __expf(-v)));
      }
      wait_count(cnt, NHELP);
      const __amdgpu_buffer_rsrc_t yrs = __builtin_amdgcn_make_buffer_rsrc(ypart, 0, NHELP * 4096, 0x00020000);
#pragma unroll
      for (int tk = 0; tk < 2; ++tk) {
        if (tk ? own1 : own0) {
          const int p0 = hj * 4 + g;
          const int yo_ = ((p0 * 2 + tk) * 512 + hg * 64 + i * 4) * 4;
          f32x4 yv[6];
#pragma unroll
          for (int q = 0; q < 6; ++q)
            yv[q] = __builtin_bit_cast(f32x4, __builtin_amdgcn_raw_buffer_load_b128(yrs, yo_ + (p0 + 16 * q < NHELP ? q * 65536 : 0), 0, 16));
          f32x4 y = ((yv[0] + yv[1]) + (yv[2] + yv[3])) + yv[4];
          if (p0 + 80 < NHELP) y += yv[5];
#pragma unroll
          for (int e = 0; e < 4; ++e) {
            y[e] += __shfl_xor(y[e], 16);
            y[e] += __shfl_xor(y[e], 32);
          }
          if (lane < 16) *(f32x4*)(smem + LDS_Y + (tk * 4 + hj) * 256 + lane * 16) = y;
        }
      }
    }
    __builtin_amdgcn_sched_barrier(0);
    asm volatile("s_waitcnt vmcnt(0)" ::: "memory");
    __syncthreads();
    TS(1, 6);
  }
  TS(1, 7); TSR(1, 9); TSFLUSH(1);
}

extern "C" void kernel_launch(void* const* d_in, const int* in_sizes, int n_in, void* d_out, int out_size,
                              void* d_ws, size_t ws_size, hipStream_t stream) {
  if (ws_size < WS_END) return;
  fused<<<256, 768, 0, stream>>>((const u16*)d_in[0], (const u16*)d_in[1], (const u16*)d_in[2], (const u16*)d_in[3],
                                 (const u16*)d_in[4], (const u16*)d_in[6], (const u16*)d_in[7], (const u16*)d_in[8],
                                 (const u16*)d_in[9], (char*)d_ws, (u16*)d_out);
}
```
